```python
import torch

def _softmax(x, axis=-1):
    return torch.softmax(x, dim=axis)

def _gelu(x):
    return torch.nn.functional.gelu(x, approximate='tanh')

def _silu(x):
    return torch.nn.functional.silu(x)

def _sigmoid(x):
    return torch.sigmoid(x)

def _layernorm(x, axis=-1, eps=1e-05):
    m = x.mean(dim=axis, keepdim=True)
    v = x.var(dim=axis, keepdim=True, unbiased=False)
    return (x - m) / torch.sqrt(v + eps)

def _scatter_add(tgt, idx, src, axis=0):
    return tgt.clone().index_add_(axis, idx, src)

def _segment_sum(src, seg, n):
    out = torch.zeros((n,) + src.shape[1:], dtype=src.dtype)
    return out.index_add_(0, seg, src)

def _pad(x, pw):
    flat = tuple((v for pair in reversed(pw) for v in pair))
    return torch.nn.functional.pad(x, flat)
kernel_name = 'concat_heads__grouped_kv_mqa__memory_aug_recurrent_mem_tr__prompt'

def setup_inputs():

    _w = lambda a: (lambda t: t.to(torch.bfloat16) if t.is_floating_point() else t)(torch.as_tensor(a))

    def _A_setup():
        B, S, D, H = (1, 2048, 1024, 32)
        Dh = D // H
        return {'x': _w((lambda *s: torch.randn(*s, dtype=torch.bfloat16) * 0.05)(B, S, D)), 'wq': _w((lambda *s: torch.randn(*s, dtype=torch.bfloat16) * 3.2)(D, H * Dh)), 'wk': _w((lambda *s: torch.randn(*s, dtype=torch.bfloat16) * 3.2)(D, Dh)), 'wv': _w((lambda *s: torch.randn(*s, dtype=torch.bfloat16) * 0.1)(D, Dh)), 'wo': _w((lambda *s: torch.randn(*s, dtype=torch.bfloat16) * 0.1)(H * Dh, D))}
    _A_in = _A_setup()

    def _B_setup():
        B, S, Dm = (1, 2048, 1024)
        H, Dh = (32, 128)
        M, seg = (8, 32)
        seg = min(seg, S)
        S = S // seg * seg
        return {'x': _w((lambda *s: torch.randn(*s, dtype=torch.bfloat16) * 0.05)(B, S, Dm)), 'mem0': _w((lambda *s: torch.randn(*s, dtype=torch.bfloat16) * 1.6)(B, M, Dm)), 'Wq': _w((lambda *s: torch.randn(*s, dtype=torch.bfloat16) * 0.8)(Dm, H, Dh) * Dm ** (-0.5)), 'Wk': _w((lambda *s: torch.randn(*s, dtype=torch.bfloat16) * 0.8)(Dm, H, Dh) * Dm ** (-0.5)), 'Wv': _w((lambda *s: torch.randn(*s, dtype=torch.bfloat16) * 1.6)(Dm, H, Dh) * Dm ** (-0.5)), 'Wo': _w((lambda *s: torch.randn(*s, dtype=torch.bfloat16) * 1.6)(H, Dh, Dm) * (H * Dh) ** (-0.5)), 'seg_len': seg}
    _B_in = _B_setup()
    return {'A_x': _A_in['x'], 'A_wq': _A_in['wq'], 'A_wk': _A_in['wk'], 'A_wv': _A_in['wv'], 'A_wo': _A_in['wo'], 'B_x': _B_in['x'], 'B_mem0': _B_in['mem0'], 'B_Wq': _B_in['Wq'], 'B_Wk': _B_in['Wk'], 'B_Wv': _B_in['Wv'], 'B_Wo': _B_in['Wo'], 'B_seg_len': _B_in['seg_len']}

def reference(A_x, A_wq, A_wk, A_wv, A_wo, B_x, B_mem0, B_Wq, B_Wk, B_Wv, B_Wo, B_seg_len):

    def _A_ref(x, wq, wk, wv, wo):
        B, S, D = x.shape
        H = 32
        Dh = D // H
        q = (lambda x, *d: x.permute(*d))((x @ wq).reshape(B, S, H, Dh), 0, 2, 1, 3)
        k = x @ wk
        v = x @ wv
        scores = torch.einsum('bhqd,bkd->bhqk', q, k) * Dh ** (-0.5)
        p = _softmax(scores, axis=-1)
        o = torch.einsum('bhqk,bkd->bhqd', p, v)
        return (lambda x, *d: x.permute(*d))(o, 0, 2, 1, 3).reshape(B, S, H * Dh) @ wo

    def _B_ref(x, mem0, Wq, Wk, Wv, Wo, seg_len):
        B, S, Dm = x.shape
        H, Dh = (Wq.shape[1], Wq.shape[2])
        M = mem0.shape[1]
        scale = Dh ** (-0.5)
        n_seg = S // seg_len
        L = M + seg_len
        idx = torch.arange(L)
        mask = (idx[:, None] < M) | (idx[:, None] >= idx[None, :])
        mem = mem0
        outs = []
        for s in range(n_seg):
            xs = x[:, s * seg_len:(s + 1) * seg_len, :].clone()
            h = torch.concatenate([mem, xs], axis=1)
            q = torch.einsum('btd,dhe->bhte', h, Wq)
            k = torch.einsum('btd,dhe->bhte', h, Wk)
            v = torch.einsum('btd,dhe->bhte', h, Wv)
            scores = torch.einsum('bhqe,bhke->bhqk', q, k) * scale
            scores = torch.where(mask[None, None], scores, -1000000000.0)
            p = _softmax(scores, axis=-1)
            o = torch.einsum('bhqk,bhke->bhqe', p, v)
            o = torch.einsum('bhte,hed->btd', o, Wo)
            mem = o[:, :M, :].clone()
            outs.append(o[:, M:, :])
        return torch.concatenate(outs, axis=1)
    a = _A_ref(A_x, A_wq, A_wk, A_wv, A_wo)
    b = _B_ref(B_x, B_mem0, B_Wq, B_Wk, B_Wv, B_Wo, B_seg_len)
    return torch.concatenate([a, b], axis=-1)
```

```cpp
#include <hip/hip_runtime.h>
#include <hip/hip_bf16.h>
#define MIMIC 0
typedef unsigned short u16;
typedef short bf16x8 __attribute__((ext_vector_type(8)));
typedef float f32x4 __attribute__((ext_vector_type(4)));
__device__ __forceinline__ float b2f(u16 x) { return __uint_as_float((unsigned)x << 16); }
__device__ __forceinline__ u16 f2b(float f) { __hip_bfloat16 h = __float2bfloat16(f); return *(u16*)&h; }
__device__ __forceinline__ float rb(float f) { return b2f(f2b(f)); }

__global__ void gemm(const u16* A, int lda, const u16* B, int ldb, u16* C, int ldc, int M, int N, int K) {
  __shared__ u16 As[64][40];
  __shared__ u16 Bs[64][40];
  const int bm = blockIdx.y * 64, bn = blockIdx.x * 64, t = threadIdx.x, lane = t & 63, w = t >> 6;
  const int wm = (w >> 1) * 32, wn = (w & 1) * 32;
  f32x4 acc[2][2] = {};
  for (int k0 = 0; k0 < K; k0 += 32) {
    for (int i = t; i < 64 * 32; i += 256) { int r = i >> 5, c = i & 31; As[r][c] = (bm + r < M) ? A[(size_t)(bm + r) * lda + k0 + c] : (u16)0; }
    for (int i = t; i < 64 * 32; i += 256) { int kk = i >> 6, n = i & 63; Bs[n][kk] = (bn + n < N) ? B[(size_t)(k0 + kk) * ldb + bn + n] : (u16)0; }
    __syncthreads();
    bf16x8 a[2], b[2];
    for (int i = 0; i < 2; ++i) { a[i] = *(const bf16x8*)&As[wm + i * 16 + (lane & 15)][(lane >> 4) * 8]; b[i] = *(const bf16x8*)&Bs[wn + i * 16 + (lane & 15)][(lane >> 4) * 8]; }
    for (int i = 0; i < 2; ++i) for (int j = 0; j < 2; ++j) acc[i][j] = __builtin_amdgcn_mfma_f32_16x16x32_bf16(a[i], b[j], acc[i][j], 0, 0, 0);
    __syncthreads();
  }
  for (int i = 0; i < 2; ++i) for (int j = 0; j < 2; ++j) for (int r = 0; r < 4; ++r) {
    int m = bm + wm + i * 16 + (lane >> 4) * 4 + r, n = bn + wn + j * 16 + (lane & 15);
    if (m < M && n < N) C[(size_t)m * ldc + n] = f2b(acc[i][j][r]);
  }
}

__global__ void attnA(const u16* Q, const u16* Kx, const u16* Vx, u16* O) {
  __shared__ float ks[64][32], vs[64][32];
  const int idx = blockIdx.x * 256 + threadIdx.x;
  float q[32], o[32];
  for (int d = 0; d < 32; ++d) { q[d] = b2f(Q[(size_t)idx * 32 + d]); o[d] = 0.f; }
  const float scale = 0.17677669529663687f;
  float m = -1e30f, l = 0.f;
  for (int k0 = 0; k0 < 2048; k0 += 64) {
    __syncthreads();
    for (int i = threadIdx.x; i < 2048; i += 256) { ks[i >> 5][i & 31] = b2f(Kx[k0 * 32 + i]); vs[i >> 5][i & 31] = b2f(Vx[k0 * 32 + i]); }
    __syncthreads();
    for (int j = 0; j < 64; ++j) { float s = 0; for (int d = 0; d < 32; ++d) s += q[d] * ks[j][d]; s *= scale;
      float mn = fmaxf(m, s); float a = __expf(m - mn), p = __expf(s - mn); l = l * a + p; m = mn; float pb = rb(p);
      for (int d = 0; d < 32; ++d) o[d] = o[d] * a + pb * vs[j][d]; }
  }
  for (int d = 0; d < 32; ++d) O[(size_t)idx * 32 + d] = f2b(o[d] / l);
}

__global__ void attnB(const u16* qm, const u16* km, const u16* vm, const u16* qx, const u16* kx, const u16* vx, u16* om, u16* ox, int s) {
  __shared__ float q[40][129], k[40][129], v[40][129], p[40][41];
  const int h = blockIdx.x, t = threadIdx.x;
  for (int i = t; i < 40 * 128; i += 256) { int r = i >> 7, e = i & 127;
    if (r < 8) { q[r][e] = b2f(qm[r * 4096 + h * 128 + e]); k[r][e] = b2f(km[r * 4096 + h * 128 + e]); v[r][e] = b2f(vm[r * 4096 + h * 128 + e]); }
    else { size_t g = (size_t)(s * 32 + r - 8) * 4096 + h * 128 + e; q[r][e] = b2f(qx[g]); k[r][e] = b2f(kx[g]); v[r][e] = b2f(vx[g]); } }
  __syncthreads();
  const float scale = 0.08838834764831845f;
  for (int i = t; i < 1600; i += 256) { int r = i / 40, c = i % 40; float a = 0; for (int e = 0; e < 128; ++e) a += q[r][e] * k[c][e];
    a *= scale;
    p[r][c] = (r < 8 || r >= c) ? a : -1e9f; }
  __syncthreads();
  if (t < 40) { float m = -1e30f; for (int c = 0; c < 40; ++c) m = fmaxf(m, p[t][c]); float l = 0; for (int c = 0; c < 40; ++c) { float e = __expf(p[t][c] - m); p[t][c] = e; l += e; }
    for (int c = 0; c < 40; ++c) p[t][c] = rb(p[t][c] / l); }
  __syncthreads();
  for (int i = t; i < 40 * 128; i += 256) { int r = i >> 7, e = i & 127; float a = 0; for (int c = 0; c < 40; ++c) a += p[r][c] * v[c][e];
    if (r < 8) om[r * 4096 + h * 128 + e] = f2b(a); else ox[(size_t)(s * 32 + r - 8) * 4096 + h * 128 + e] = f2b(a); }
}

extern "C" void kernel_launch(void* const* d_in, const int* in_sizes, int n_in, void* d_out, int out_size, void* d_ws, size_t ws_size, hipStream_t stream) {
  const u16 *Ax = (const u16*)d_in[0], *Awq = (const u16*)d_in[1], *Awk = (const u16*)d_in[2], *Awv = (const u16*)d_in[3], *Awo = (const u16*)d_in[4];
  const u16 *Bx = (const u16*)d_in[5], *Bm = (const u16*)d_in[6], *Wq = (const u16*)d_in[7], *Wk = (const u16*)d_in[8], *Wv = (const u16*)d_in[9], *Wo = (const u16*)d_in[10];
  u16* out = (u16*)d_out; u16* ws = (u16*)d_ws;
  u16 *qa = ws, *ka = qa + 2048 * 1024, *va = ka + 2048 * 32, *oa = va + 2048 * 32;
  u16 *qx = oa + 2048 * 1024, *kx = qx + 2048 * 4096, *vx = kx + 2048 * 4096, *ox = vx + 2048 * 4096;
  u16 *qm = ox + 2048 * 4096, *km = qm + 8 * 4096, *vm = km + 8 * 4096, *om = vm + 8 * 4096, *mem = om + 8 * 4096;
  auto G = [&](const u16* A, int lda, const u16* B, int ldb, u16* C, int ldc, int M, int N, int K) {
    gemm<<<dim3((N + 63) / 64, (M + 63) / 64), 256, 0, stream>>>(A, lda, B, ldb, C, ldc, M, N, K); };
  G(Ax, 1024, Awq, 1024, qa, 1024, 2048, 1024, 1024);
  G(Ax, 1024, Awk, 32, ka, 32, 2048, 32, 1024);
  G(Ax, 1024, Awv, 32, va, 32, 2048, 32, 1024);
  attnA<<<256, 256, 0, stream>>>(qa, ka, va, oa);
  G(oa, 1024, Awo, 1024, out, 2048, 2048, 1024, 1024);
  G(Bx, 1024, Wq, 4096, qx, 4096, 2048, 4096, 1024);
  G(Bx, 1024, Wk, 4096, kx, 4096, 2048, 4096, 1024);
  G(Bx, 1024, Wv, 4096, vx, 4096, 2048, 4096, 1024);
  for (int s = 0; s < 64; ++s) {
    const u16* m = s == 0 ? Bm : mem;
    G(m, 1024, Wq, 4096, qm, 4096, 8, 4096, 1024);
    G(m, 1024, Wk, 4096, km, 4096, 8, 4096, 1024);
    G(m, 1024, Wv, 4096, vm, 4096, 8, 4096, 1024);
    attnB<<<32, 256, 0, stream>>>(qm, km, vm, qx, kx, vx, om, ox, s);
    G(om, 4096, Wo, 1024, mem, 1024, 8, 1024, 4096);
  }
  G(ox, 4096, Wo, 1024, out + 1024, 2048, 2048, 1024, 4096);
}
```

```cpp
#include <hip/hip_runtime.h>

#define CLOBBERS \
    "v1", "v2", "v3", "v4", "v5", "v6", "v7", "v8", "v9", "v10", "v11", "v12", "v13", "v14", "v15", "v16", "v17", "v18", "v19", "v20", "v21", "v22", "v23", "v24", \
    "v25", "v26", "v27", "v28", "v29", "v30", "v31", "v32", "v33", "v34", "v35", "v36", "v37", "v38", "v39", "v40", "v41", "v42", "v43", "v44", "v45", "v46", "v47", "v48", \
    "v49", "v50", "v51", "v52", "v53", "v54", "v55", "v56", "v57", "v58", "v59", "v60", "v61", "v62", "v63", "v64", "v65", "v66", "v67", "v68", "v69", "v70", "v71", "v72", \
    "v73", "v74", "v75", "v76", "v77", "v78", "v79", "v80", "v81", "v82", "v83", "v84", "v85", "v86", "v87", "v88", "v89", "v90", "v91", "v92", "v93", "v94", "v95", "v96", \
    "v97", "v98", "v99", "v100", "v101", "v102", "v103", "v104", "v105", "v106", "v107", "v108", "v109", "v110", "v111", "v112", "v113", "v114", "v115", "v116", "v117", "v118", "v119", "v120", \
    "v121", "v122", "v123", "v124", "v125", "v126", "v127", "v128", "v129", "v130", "v131", "v132", "v133", "v134", "v135", "v136", "v137", "v138", "v139", "v140", "v141", "v142", "v143", "v144", \
    "v145", "v146", "v147", "v148", "v149", "v150", "v151", "v152", "v153", "v154", "v155", "v156", "v157", "v158", "v159", "v160", "v161", "v162", "v163", "v164", "v165", "v166", "v167", "v168", \
    "v169", "v170", "v171", "v172", "v173", "v174", "v175", "v176", "v177", "v178", "v179", "v180", "v181", "v182", "v183", "v184", "v185", "v186", "v187", "v188", "v189", "v190", "v191", "v192", \
    "v193", "v194", "v195", "v196", "v197", "v198", "v199", "v200", "v201", "v202", "v203", "v204", "v205", "v206", "v207", "v208", "v209", "v210", "v211", "v212", "v213", "v214", "v215", "v216", \
    "v217", "v218", "v219", "v220", "v221", "v222", "v223", "v224", "v225", "v226", "v227", "v228", "v229", "v230", "v231", "v232", "v233", "v234", "v235", "v236", "v237", "v238", "v239", "v240", \
    "v241", "v242", "v243", "v244", "v245", "v246", "v247", "v248", "v249", "v250", "v251", "v252", "v253", "v254", "v255", "a0", "a1", "a2", "a3", "a4", "a5", "a6", "a7", "a8", \
    "a9", "a10", "a11", "a12", "a13", "a14", "a15", "a16", "a17", "a18", "a19", "a20", "a21", "a22", "a23", "a24", "a25", "a26", "a27", "a28", "a29", "a30", "a31", "a32", \
    "a33", "a34", "a35", "a36", "a37", "a38", "a39", "a40", "a41", "a42", "a43", "a44", "a45", "a46", "a47", "a48", "a49", "a50", "a51", "a52", "a53", "a54", "a55", "a56", \
    "a57", "a58", "a59", "a60", "a61", "a62", "a63", "a64", "a65", "a66", "a67", "a68", "a69", "a70", "a71", "a72", "a73", "a74", "a75", "a76", "a77", "a78", "a79", "a80", \
    "a81", "a82", "a83", "a84", "a85", "a86", "a87", "a88", "a89", "a90", "a91", "a92", "a93", "a94", "a95", "a96", "a97", "a98", "a99", "a100", "a101", "a102", "a103", "a104", \
    "a105", "a106", "a107", "a108", "a109", "a110", "a111", "a112", "a113", "a114", "a115", "a116", "a117", "a118", "a119", "a120", "a121", "a122", "a123", "a124", "a125", "a126", "a127", "a128", \
    "a129", "a130", "a131", "a132", "a133", "a134", "a135", "a136", "a137", "a138", "a139", "a140", "a141", "a142", "a143", "a144", "a145", "a146", "a147", "a148", "a149", "a150", "a151", "a152", \
    "a153", "a154", "a155", "a156", "a157", "a158", "a159", "a160", "a161", "a162", "a163", "a164", "a165", "a166", "a167", "a168", "a169", "a170", "a171", "a172", "a173", "a174", "a175", "a176", \
    "a177", "a178", "a179", "a180", "a181", "a182", "a183", "a184", "a185", "a186", "a187", "a188", "a189", "a190", "a191", "a192", "a193", "a194", "a195", "a196", "a197", "a198", "a199", "a200", \
    "a201", "a202", "a203", "a204", "a205", "a206", "a207", "a208", "a209", "a210", "a211", "a212", "a213", "a214", "a215", "a216", "a217", "a218", "a219", "a220", "a221", "a222", "a223", "a224", \
    "a225", "a226", "a227", "a228", "a229", "a230", "a231", "a232", "a233", "a234", "a235", "a236", "a237", "a238", "a239", "a240", "a241", "a242", "a243", "a244", "a245", "a246", "a247", "a248", \
    "a249", "a250", "a251", "a252", "a253", "a254", "a255", "s8", "s9", "s10", "s11", "s12", "s13", "s14", "s15", "s16", "s17", "s18", "s19", "s20", "s21", "s22", "s23", "s24", \
    "s25", "s26", "s27", "s28", "s29", "s30", "s31", "s33", "s34", "s35", "s36", "s37", "s38", "s39", "s40", "s41", "s42", "s43", "s44", "s45", "s46", "s47", "s48", "s49", \
    "s50", "s51", "s52", "s53", "s54", "s55", "s56", "s57", "s58", "s59", "s60", "s61", "s62", "s63", "s64", "s65", "s66", "s67", "s68", "s69", "s70", "s71", "s72", "s73", \
    "s74", "s75", "s76", "s77", "s78", "s79", "s80", "s81", "s82", "s83", "s84", "s85", "s86", "s87", "s88", "s89", "s90", "s91", "s92", "s93", "s94", "s95", "s96", "s97", \
    "s98", "s99", "vcc", "scc", "memory"
typedef const unsigned short* cp16;
__global__ void __launch_bounds__(256, 1) fused(cp16 Ax, cp16 Awq, cp16 Awk, cp16 Awv, cp16 Awo, cp16 Bx, cp16 Bm, cp16 Wq, cp16 Wk, cp16 Wv, cp16 Wo,
                                                unsigned short* out, unsigned* ws) {
  const unsigned long long ka = (unsigned long long)__builtin_amdgcn_kernarg_segment_ptr();
  asm volatile(
    ".Lk_start_%=:\n"
    "s_mov_b64 exec, -1\n"
    "s_mov_b64 s[28:29], %0\n"
    "s_sub_u32 s17, %1, 0\n"
    "s_and_b32 s17, s17, 7\n"
    "s_andn2_b32 s16, %1, 7\n"
    "s_or_b32 s16, s16, s17\n"
    "s_load_dwordx2 s[30:31], s[28:29], 96\n"
    "v_and_b32 v1, 63, %2\n"
    "v_lshrrev_b32 v2, 6, %2\n"
    "s_mov_b32 s26, 524288\n"
    "v_readfirstlane_b32 s25, v2\n"
    "s_getreg_b32 s17, hwreg(HW_REG_XCC_ID, 0, 4)\n"
    "s_sub_u32 s17, s17, s16\n"
    "s_and_b32 s17, s17, 7\n"
    "s_lshl_b32 s19, s16, 2\n"
    "s_add_u32 s19, s19, s25\n"
    "s_lshl_b32 s19, s19, 2\n"
    "s_getpc_b64 s[0:1]\n"
    ".Lk_pc_%=:\n"
    "s_and_b32 s1, s1, 0xffff\n"
    "s_mov_b32 s2, .Lk_end_%=-.Lk_pc_%=\n"
    "s_mov_b32 s3, 0x20000\n"
    "s_lshr_b32 s4, s16, 3\n"
    "s_lshl_b32 s4, s4, 2\n"
    "s_add_u32 s4, s4, s25\n"
    "s_lshl_b32 s4, s4, 10\n"
    "v_lshlrev_b32 v2, 4, v1\n"
    "buffer_load_dword a252, v2, s[0:3], s4 offen\n"
    "s_waitcnt lgkmcnt(0)\n"
    "s_mov_b32 s12, s30\n"
    "s_and_b32 s13, s31, 0xffff\n"
    "s_mov_b32 s14, 2097152\n"
    "s_mov_b32 s15, 0x20000\n"
    ".Lk_dispatch_%=:\n"
    "s_add_u32 s10, s19, 69632\n"
    "s_mov_b64 exec, 1\n"
    "buffer_load_dword v0, off, s[12:15], s10 sc1\n"
    "s_mov_b64 exec, -1\n"
    "s_add_u32 s0, s16, s17\n"
    "s_and_b32 s0, s0, 7\n"
    "s_and_b32 s1, s17, 4\n"
    "s_xor_b32 s0, s0, s1\n"
    "s_lshr_b32 s1, s16, 3\n"
    "s_add_u32 s1, s1, 31\n"
    "s_and_b32 s1, s1, 31\n"
    "s_lshl_b32 s2, s0, 3\n"
    "s_cmp_ge_u32 s1, 24\n"
    "s_cbranch_scc1 .Lk_toa_%=\n"
    "s_cmp_ge_u32 s0, 4\n"
    "s_cbranch_scc1 .Lk_togx_%=\n"
    "s_add_u32 s24, s2, s1\n"
    "s_cmp_lt_u32 s1, 8\n"
    "s_cbranch_scc1 .Lqk_entry_%=\n"
    "s_add_u32 s24, s24, s2\n"
    "s_sub_u32 s24, s24, 8\n"
    "s_branch .Lvo_entry_%=\n"
    ".Lk_togx_%=:\n"
    "s_lshr_b32 s3, s1, 3\n"
    "s_lshl_b32 s3, s3, 5\n"
    "s_and_b32 s1, s1, 7\n"
    "s_add_u32 s3, s3, s1\n"
    "s_add_u32 s3, s3, s2\n"
    "s_sub_u32 s24, s3, 32\n"
    "s_branch .Lgx_entry_%=\n"
    ".Lk_toa_%=:\n"
    "s_xor_b32 s2, s2, 32\n"
    "s_add_u32 s2, s2, s1\n"
    "s_sub_u32 s24, s2, 24\n"
    "s_cmp_lg_u32 s16, 0\n"
    "s_cbranch_scc1 .Lat_entry_%=\n"
    "v_lshlrev_b32 v2, 4, v1\n"
    "v_mov_b64 v[4:5], 0\n"
    "v_mov_b64 v[6:7], 0\n"
    "s_cmp_lg_u32 s25, 0\n"
    "s_cbranch_scc1 .Lk_nocal_%=\n"
    "v_lshlrev_b32 v3, 8, v1\n"
    "s_mov_b32 s8, 131072\n"
    "buffer_store_dword v4, v3, s[12:15], s8 offen sc1\n"
    "s_mov_b32 s8, 147456\n"
    "buffer_store_dword v4, v3, s[12:15], s8 offen sc1\n"
    "s_mov_b64 exec, 1\n"
    "s_mov_b32 s8, 131072\n"
    "s_waitcnt vmcnt(0)\n"
    "s_mov_b32 s9, 0\n"
    "s_xor_b32 s10, s8, 0\n"
    "s_xor_b32 s11, s10, 8192\n"
    "buffer_load_dword v8, off, s[12:15], s10 sc1\n"
    "buffer_load_dword v9, off, s[12:15], s11 sc1\n"
    "s_waitcnt vmcnt(1)\n"
    "s_memtime s[0:1]\n"
    "s_waitcnt vmcnt(0)\n"
    "s_memtime s[2:3]\n"
    "s_waitcnt lgkmcnt(0)\n"
    "s_sub_u32 s0, s2, s0\n"
    "s_min_u32 s0, s0, 200\n"
    "s_sub_u32 s9, s9, s0\n"
    "s_xor_b32 s10, s8, 8448\n"
    "s_xor_b32 s11, s10, 8192\n"
    "buffer_load_dword v8, off, s[12:15], s10 sc1\n"
    "buffer_load_dword v9, off, s[12:15], s11 sc1\n"
    "s_waitcnt vmcnt(1)\n"
    "s_memtime s[0:1]\n"
    "s_waitcnt vmcnt(0)\n"
    "s_memtime s[2:3]\n"
    "s_waitcnt lgkmcnt(0)\n"
    "s_sub_u32 s0, s2, s0\n"
    "s_min_u32 s0, s0, 200\n"
    "s_add_u32 s9, s9, s0\n"
    "s_xor_b32 s10, s8, 512\n"
    "s_xor_b32 s11, s10, 8192\n"
    "buffer_load_dword v8, off, s[12:15], s10 sc1\n"
    "buffer_load_dword v9, off, s[12:15], s11 sc1\n"
    "s_waitcnt vmcnt(1)\n"
    "s_memtime s[0:1]\n"
    "s_waitcnt vmcnt(0)\n"
    "s_memtime s[2:3]\n"
    "s_waitcnt lgkmcnt(0)\n"
    "s_sub_u32 s0, s2, s0\n"
    "s_min_u32 s0, s0, 200\n"
    "s_sub_u32 s9, s9, s0\n"
    "s_xor_b32 s10, s8, 8960\n"
    "s_xor_b32 s11, s10, 8192\n"
    "buffer_load_dword v8, off, s[12:15], s10 sc1\n"
    "buffer_load_dword v9, off, s[12:15], s11 sc1\n"
    "s_waitcnt vmcnt(1)\n"
    "s_memtime s[0:1]\n"
    "s_waitcnt vmcnt(0)\n"
    "s_memtime s[2:3]\n"
    "s_waitcnt lgkmcnt(0)\n"
    "s_sub_u32 s0, s2, s0\n"
    "s_min_u32 s0, s0, 200\n"
    "s_add_u32 s9, s9, s0\n"
    "s_xor_b32 s10, s8, 1024\n"
    "s_xor_b32 s11, s10, 8192\n"
    "buffer_load_dword v8, off, s[12:15], s10 sc1\n"
    "buffer_load_dword v9, off, s[12:15], s11 sc1\n"
    "s_waitcnt vmcnt(1)\n"
    "s_memtime s[0:1]\n"
    "s_waitcnt vmcnt(0)\n"
    "s_memtime s[2:3]\n"
    "s_waitcnt lgkmcnt(0)\n"
    "s_sub_u32 s0, s2, s0\n"
    "s_min_u32 s0, s0, 200\n"
    "s_sub_u32 s9, s9, s0\n"
    "s_xor_b32 s10, s8, 9472\n"
    "s_xor_b32 s11, s10, 8192\n"
    "buffer_load_dword v8, off, s[12:15], s10 sc1\n"
    "buffer_load_dword v9, off, s[12:15], s11 sc1\n"
    "s_waitcnt vmcnt(1)\n"
    "s_memtime s[0:1]\n"
    "s_waitcnt vmcnt(0)\n"
    "s_memtime s[2:3]\n"
    "s_waitcnt lgkmcnt(0)\n"
    "s_sub_u32 s0, s2, s0\n"
    "s_min_u32 s0, s0, 200\n"
    "s_add_u32 s9, s9, s0\n"
    "s_cmp_gt_i32 s9, 0\n"
    "s_cselect_b32 s0, 8192, 0\n"
    "s_xor_b32 s8, s8, s0\n"
    "s_mov_b32 s20, s8\n"
    "s_mov_b64 exec, -1\n"
    "s_bfe_u32 s9, s8, 65549\n"
    "s_lshl_b32 s9, s9, 3\n"
    "v_mov_b32 v8, s9\n"
    "ds_write_b32 v4, v8\n"
    "s_waitcnt lgkmcnt(0)\n"
    ".Lk_nocal_%=:\n"
    "s_barrier\n"
    "ds_read_b32 v8, v4\n"
    "s_lshl_b32 s8, s25, 10\n"
    "v_add_u32 v2, s8, v2\n"
    "s_waitcnt lgkmcnt(0)\n"
    "v_readfirstlane_b32 s9, v8\n"
    "s_nop 1\n"
    "s_and_b32 s27, s9, 8\n"
    "s_lshl_b32 s27, s27, 10\n"
    "s_or_b32 s9, s9, s17\n"
    "s_add_u32 s8, 0, 0\n"
    "buffer_store_dwordx4 v[4:7], v2, s[12:15], s8 offen sc1\n"
    "s_add_u32 s8, 0, 4096\n"
    "buffer_store_dwordx4 v[4:7], v2, s[12:15], s8 offen sc1\n"
    "s_add_u32 s8, 0, 8192\n"
    "buffer_store_dwordx4 v[4:7], v2, s[12:15], s8 offen sc1\n"
    "s_add_u32 s8, 0, 12288\n"
    "buffer_store_dwordx4 v[4:7], v2, s[12:15], s8 offen sc1\n"
    "s_add_u32 s8, s27, 16384\n"
    "buffer_store_dwordx4 v[4:7], v2, s[12:15], s8 offen sc1\n"
    "s_add_u32 s8, s27, 20480\n"
    "buffer_store_dwordx4 v[4:7], v2, s[12:15], s8 offen sc1\n"
    "s_add_u32 s8, s27, 32768\n"
    "buffer_store_dwordx4 v[4:7], v2, s[12:15], s8 offen sc1\n"
    "s_add_u32 s8, s27, 36864\n"
    "buffer_store_dwordx4 v[4:7], v2, s[12:15], s8 offen sc1\n"
    "s_add_u32 s8, s27, 49152\n"
    "buffer_store_dwordx4 v[4:7], v2, s[12:15], s8 offen sc1\n"
    "s_mov_b32 s8, 65536\n"
    "s_mov_b32 s10, 69632\n"
    "s_mov_b32 s11, 12288\n"
    "buffer_load_dwordx4 v[8:11], v2, s[12:15], s8 offen sc1\n"
    "v_mov_b32 v3, 0xffffff00\n"
    "v_mov_b32 v20, s9\n"
    "v_cmp_ne_u32 s[0:1], 0, v2\n"
    "s_waitcnt vmcnt(0)\n"
    "s_barrier\n"
    "v_bfi_b32 v12, v3, v8, v20\n"
    "v_bfi_b32 v13, v3, v9, v20\n"
    "v_bfi_b32 v14, v3, v10, v20\n"
    "v_bfi_b32 v15, v3, v11, v20\n"
    "buffer_store_dwordx4 v[12:15], v2, s[12:15], s10 offen sc1\n"
    ".Lk_serve_%=:\n"
    "buffer_load_dwordx4 v[8:11], v2, s[12:15], s8 offen sc1\n"
    "buffer_load_dwordx4 v[16:19], v2, s[12:15], s11 offen sc1\n"
    "s_waitcnt vmcnt(0)\n"
    "v_bfi_b32 v8, v3, v8, v20\n"
    "v_bfi_b32 v9, v3, v9, v20\n"
    "v_bfi_b32 v10, v3, v10, v20\n"
    "v_bfi_b32 v11, v3, v11, v20\n"
    "v_cmp_ne_u64 vcc, v[8:9], v[12:13]\n"
    "v_cmp_ne_u64 s[2:3], v[10:11], v[14:15]\n"
    "v_min3_u32 v16, v16, v17, v18\n"
    "v_min_u32 v16, v16, v19\n"
    "v_mov_b64 v[12:13], v[8:9]\n"
    "v_mov_b64 v[14:15], v[10:11]\n"
    "s_or_b64 s[2:3], s[2:3], vcc\n"
    "v_cmp_eq_u32 vcc, 0, v16\n"
    "s_cbranch_scc0 .Lk_same_%=\n"
    "s_mov_b64 exec, s[2:3]\n"
    "buffer_store_dwordx4 v[12:15], v2, s[12:15], s10 offen sc1\n"
    "s_mov_b64 exec, -1\n"
    ".Lk_same_%=:\n"
    "s_and_b64 s[2:3], s[0:1], vcc\n"
    "s_cbranch_scc0 .Lk_served_%=\n"
    "s_sleep 4\n"
    "s_sub_u32 s26, s26, 1\n"
    "s_cbranch_scc0 .Lk_serve_%=\n"
    "s_endpgm\n"
    ".Lk_served_%=:\n"
    "s_barrier\n"
    "s_cmp_lg_u32 s25, 0\n"
    "s_cbranch_scc1 .Lat_entry_%=\n"
    "s_sleep 36\n"
    "s_sleep 36\n"
    "s_sleep 36\n"
    "s_mov_b32 s8, s20\n"
    "s_mov_b64 exec, 1\n"
    "s_mov_b32 s9, 0\n"
    "s_xor_b32 s10, s8, 1536\n"
    "s_xor_b32 s11, s10, 16384\n"
    "buffer_load_dword v8, off, s[12:15], s10 sc1\n"
    "buffer_load_dword v9, off, s[12:15], s11 sc1\n"
    "s_waitcnt vmcnt(1)\n"
    "s_memtime s[0:1]\n"
    "s_waitcnt vmcnt(0)\n"
    "s_memtime s[2:3]\n"
    "s_waitcnt lgkmcnt(0)\n"
    "s_sub_u32 s0, s2, s0\n"
    "s_cmp_gt_u32 s0, 100\n"
    "s_cselect_b32 s0, 8, s0\n"
    "s_sub_u32 s9, s9, s0\n"
    "s_xor_b32 s10, s8, 18176\n"
    "s_xor_b32 s11, s10, 16384\n"
    "buffer_load_dword v8, off, s[12:15], s10 sc1\n"
    "buffer_load_dword v9, off, s[12:15], s11 sc1\n"
    "s_waitcnt vmcnt(1)\n"
    "s_memtime s[0:1]\n"
    "s_waitcnt vmcnt(0)\n"
    "s_memtime s[2:3]\n"
    "s_waitcnt lgkmcnt(0)\n"
    "s_sub_u32 s0, s2, s0\n"
    "s_cmp_gt_u32 s0, 100\n"
    "s_cselect_b32 s0, 8, s0\n"
    "s_add_u32 s9, s9, s0\n"
    "s_xor_b32 s10, s8, 2048\n"
    "s_xor_b32 s11, s10, 16384\n"
    "buffer_load_dword v8, off, s[12:15], s10 sc1\n"
    "buffer_load_dword v9, off, s[12:15], s11 sc1\n"
    "s_waitcnt vmcnt(1)\n"
    "s_memtime s[0:1]\n"
    "s_waitcnt vmcnt(0)\n"
    "s_memtime s[2:3]\n"
    "s_waitcnt lgkmcnt(0)\n"
    "s_sub_u32 s0, s2, s0\n"
    "s_cmp_gt_u32 s0, 100\n"
    "s_cselect_b32 s0, 8, s0\n"
    "s_sub_u32 s9, s9, s0\n"
    "s_xor_b32 s10, s8, 18688\n"
    "s_xor_b32 s11, s10, 16384\n"
    "buffer_load_dword v8, off, s[12:15], s10 sc1\n"
    "buffer_load_dword v9, off, s[12:15], s11 sc1\n"
    "s_waitcnt vmcnt(1)\n"
    "s_memtime s[0:1]\n"
    "s_waitcnt vmcnt(0)\n"
    "s_memtime s[2:3]\n"
    "s_waitcnt lgkmcnt(0)\n"
    "s_sub_u32 s0, s2, s0\n"
    "s_cmp_gt_u32 s0, 100\n"
    "s_cselect_b32 s0, 8, s0\n"
    "s_add_u32 s9, s9, s0\n"
    "s_xor_b32 s10, s8, 2560\n"
    "s_xor_b32 s11, s10, 16384\n"
    "buffer_load_dword v8, off, s[12:15], s10 sc1\n"
    "buffer_load_dword v9, off, s[12:15], s11 sc1\n"
    "s_waitcnt vmcnt(1)\n"
    "s_memtime s[0:1]\n"
    "s_waitcnt vmcnt(0)\n"
    "s_memtime s[2:3]\n"
    "s_waitcnt lgkmcnt(0)\n"
    "s_sub_u32 s0, s2, s0\n"
    "s_cmp_gt_u32 s0, 100\n"
    "s_cselect_b32 s0, 8, s0\n"
    "s_sub_u32 s9, s9, s0\n"
    "s_xor_b32 s10, s8, 19200\n"
    "s_xor_b32 s11, s10, 16384\n"
    "buffer_load_dword v8, off, s[12:15], s10 sc1\n"
    "buffer_load_dword v9, off, s[12:15], s11 sc1\n"
    "s_waitcnt vmcnt(1)\n"
    "s_memtime s[0:1]\n"
    "s_waitcnt vmcnt(0)\n"
    "s_memtime s[2:3]\n"
    "s_waitcnt lgkmcnt(0)\n"
    "s_sub_u32 s0, s2, s0\n"
    "s_cmp_gt_u32 s0, 100\n"
    "s_cselect_b32 s0, 8, s0\n"
    "s_add_u32 s9, s9, s0\n"
    "s_cmp_gt_i32 s9, 0\n"
    "s_cselect_b32 s0, 16384, 0\n"
    "s_xor_b32 s8, s8, s0\n"
    "s_mov_b32 s9, 0\n"
    "s_xor_b32 s10, s8, 3072\n"
    "s_xor_b32 s11, s10, 4096\n"
    "buffer_load_dword v8, off, s[12:15], s10 sc1\n"
    "buffer_load_dword v9, off, s[12:15], s11 sc1\n"
    "s_waitcnt vmcnt(1)\n"
    "s_memtime s[0:1]\n"
    "s_waitcnt vmcnt(0)\n"
    "s_memtime s[2:3]\n"
    "s_waitcnt lgkmcnt(0)\n"
    "s_sub_u32 s0, s2, s0\n"
    "s_cmp_gt_u32 s0, 60\n"
    "s_cselect_b32 s0, 8, s0\n"
    "s_sub_u32 s9, s9, s0\n"
    "s_xor_b32 s10, s8, 7424\n"
    "s_xor_b32 s11, s10, 4096\n"
    "buffer_load_dword v8, off, s[12:15], s10 sc1\n"
    "buffer_load_dword v9, off, s[12:15], s11 sc1\n"
    "s_waitcnt vmcnt(1)\n"
    "s_memtime s[0:1]\n"
    "s_waitcnt vmcnt(0)\n"
    "s_memtime s[2:3]\n"
    "s_waitcnt lgkmcnt(0)\n"
    "s_sub_u32 s0, s2, s0\n"
    "s_cmp_gt_u32 s0, 60\n"
    "s_cselect_b32 s0, 8, s0\n"
    "s_add_u32 s9, s9, s0\n"
    "s_xor_b32 s10, s8, 3584\n"
    "s_xor_b32 s11, s10, 4096\n"
    "buffer_load_dword v8, off, s[12:15], s10 sc1\n"
    "buffer_load_dword v9, off, s[12:15], s11 sc1\n"
    "s_waitcnt vmcnt(1)\n"
    "s_memtime s[0:1]\n"
    "s_waitcnt vmcnt(0)\n"
    "s_memtime s[2:3]\n"
    "s_waitcnt lgkmcnt(0)\n"
    "s_sub_u32 s0, s2, s0\n"
    "s_cmp_gt_u32 s0, 60\n"
    "s_cselect_b32 s0, 8, s0\n"
    "s_sub_u32 s9, s9, s0\n"
    "s_xor_b32 s10, s8, 7936\n"
    "s_xor_b32 s11, s10, 4096\n"
    "buffer_load_dword v8, off, s[12:15], s10 sc1\n"
    "buffer_load_dword v9, off, s[12:15], s11 sc1\n"
    "s_waitcnt vmcnt(1)\n"
    "s_memtime s[0:1]\n"
    "s_waitcnt vmcnt(0)\n"
    "s_memtime s[2:3]\n"
    "s_waitcnt lgkmcnt(0)\n"
    "s_sub_u32 s0, s2, s0\n"
    "s_cmp_gt_u32 s0, 60\n"
    "s_cselect_b32 s0, 8, s0\n"
    "s_add_u32 s9, s9, s0\n"
    "s_cmp_gt_i32 s9, 0\n"
    "s_cselect_b32 s0, 4096, 0\n"
    "s_xor_b32 s8, s8, s0\n"
    "s_bfe_u32 s9, s8, 65550\n"
    "s_bfe_u32 s0, s8, 65548\n"
    "s_lshl_b32 s0, s0, 1\n"
    "s_or_b32 s9, s9, s0\n"
    "s_xor_b32 s9, s9, s17\n"
    "s_and_b32 s9, s9, 3\n"
    "s_xor_b32 s9, s9, 6\n"
    "v_mov_b32 v8, s9\n"
    "s_add_u32 s8, s27, 39936\n"
    "buffer_store_dword v8, off, s[12:15], s8 sc1\n"
    "s_mov_b64 exec, -1\n"
    "s_branch .Lat_entry_%=\n"
    ".Lqk_entry_%=:\n"
    "s_load_dwordx4 s[76:79], s[28:29], 56\n"
    "s_waitcnt lgkmcnt(0)\n"
    "s_add_u32 s40, s30, 2097152\n"
    "s_addc_u32 s41, s31, 0\n"
    "s_and_b32 s41, s41, 0xffff\n"
    "s_mov_b32 s42, 50331648\n"
    "s_mov_b32 s43, 0x20000\n"
    "s_add_u32 s44, s30, 52428800\n"
    "s_addc_u32 s45, s31, 0\n"
    "s_and_b32 s45, s45, 0xffff\n"
    "s_mov_b32 s46, 16777216\n"
    "s_mov_b32 s47, 0x20000\n"
    "s_add_u32 s48, s30, 0\n"
    "s_addc_u32 s49, s31, 0\n"
    "s_and_b32 s49, s49, 0xffff\n"
    "s_mov_b32 s50, 65536\n"
    "s_mov_b32 s51, 0x20000\n"
    "s_cmp_gt_u32 s25, 1\n"
    "s_cselect_b32 s76, s78, s76\n"
    "s_cselect_b32 s77, s79, s77\n"
    "s_mov_b32 s36, s76\n"
    "s_and_b32 s37, s77, 0xffff\n"
    "s_mov_b32 s38, 8388608\n"
    "s_mov_b32 s39, 0x20000\n"
    "s_and_b32 s54, s25, 1\n"
    "s_lshl_b32 s54, s54, 7\n"
    "s_lshl_b32 s52, s24, 8\n"
    "s_add_u32 s52, s52, s54\n"
    "s_mul_i32 s53, s25, 40960\n"
    "s_add_u32 s62, s53, 20480\n"
    "s_and_b32 s75, s24, 0\n"
    "v_lshlrev_b32 v72, 4, v1\n"
    "v_add_u32 v72, s62, v72\n"
    "v_bfe_u32 v2, v1, 3, 1\n"
    "v_lshlrev_b32 v2, 14, v2\n"
    "v_bfe_u32 v3, v1, 4, 1\n"
    "v_lshl_add_u32 v2, v3, 15, v2\n"
    "v_bfe_u32 v3, v1, 5, 1\n"
    "v_lshl_add_u32 v2, v3, 17, v2\n"
    "v_bfe_u32 v3, v1, 0, 3\n"
    "v_lshl_add_u32 v2, v3, 4, v2\n"
    "v_bfe_u32 v4, v1, 4, 2\n"
    "v_lshlrev_b32 v4, 5, v4\n"
    "v_xor_b32 v2, v2, v4\n"
    "v_bfe_u32 v4, v1, 5, 1\n"
    "v_lshlrev_b32 v4, 11, v4\n"
    "v_bfe_u32 v3, v1, 4, 1\n"
    "v_lshl_add_u32 v4, v3, 7, v4\n"
    "v_bfe_u32 v3, v1, 3, 1\n"
    "v_lshl_add_u32 v4, v3, 8, v4\n"
    "v_bfe_u32 v3, v1, 2, 1\n"
    "v_lshl_add_u32 v4, v3, 9, v4\n"
    "v_bfe_u32 v3, v1, 3, 1\n"
    "v_lshl_add_u32 v4, v3, 5, v4\n"
    "v_bfe_u32 v3, v1, 2, 1\n"
    "v_lshl_add_u32 v4, v3, 6, v4\n"
    "v_bfe_u32 v3, v1, 0, 2\n"
    "v_lshl_add_u32 v4, v3, 3, v4\n"
    "v_add_u32 v4, s53, v4\n"
    "v_xor_b32 v5, 32, v4\n"
    "v_xor_b32 v6, 64, v4\n"
    "v_xor_b32 v7, 96, v4\n"
    "s_xor_b32 s54, s75, 0\n"
    "s_lshl_b32 s54, s54, 19\n"
    "s_add_u32 s54, s54, s52\n"
    "s_add_u32 m0, s53, 8192\n"
    "s_add_u32 s55, s54, 0\n"
    "buffer_load_dwordx4 v2, s[36:39], s55 offen lds\n"
    "s_add_u32 m0, s53, 9216\n"
    "s_add_u32 s55, s54, 8192\n"
    "buffer_load_dwordx4 v2, s[36:39], s55 offen lds\n"
    "s_add_u32 m0, s53, 10240\n"
    "s_add_u32 s55, s54, 65536\n"
    "buffer_load_dwordx4 v2, s[36:39], s55 offen lds\n"
    "s_add_u32 m0, s53, 11264\n"
    "s_add_u32 s55, s54, 73728\n"
    "buffer_load_dwordx4 v2, s[36:39], s55 offen lds\n"
    "s_add_u32 m0, s53, 12288\n"
    "s_add_u32 s55, s54, 262144\n"
    "buffer_load_dwordx4 v2, s[36:39], s55 offen lds\n"
    "s_add_u32 m0, s53, 13312\n"
    "s_add_u32 s55, s54, 270336\n"
    "buffer_load_dwordx4 v2, s[36:39], s55 offen lds\n"
    "s_add_u32 m0, s53, 14336\n"
    "s_add_u32 s55, s54, 327680\n"
    "buffer_load_dwordx4 v2, s[36:39], s55 offen lds\n"
    "s_add_u32 m0, s53, 15360\n"
    "s_add_u32 s55, s54, 335872\n"
    "buffer_load_dwordx4 v2, s[36:39], s55 offen lds\n"
    "s_xor_b32 s54, s75, 4\n"
    "s_lshl_b32 s54, s54, 19\n"
    "s_add_u32 s54, s54, s52\n"
    "s_add_u32 m0, s53, 16384\n"
    "s_add_u32 s55, s54, 0\n"
    "buffer_load_dwordx4 v2, s[36:39], s55 offen lds\n"
    "s_add_u32 m0, s53, 17408\n"
    "s_add_u32 s55, s54, 8192\n"
    "buffer_load_dwordx4 v2, s[36:39], s55 offen lds\n"
    "s_add_u32 m0, s53, 18432\n"
    "s_add_u32 s55, s54, 65536\n"
    "buffer_load_dwordx4 v2, s[36:39], s55 offen lds\n"
    "s_add_u32 m0, s53, 19456\n"
    "s_add_u32 s55, s54, 73728\n"
    "buffer_load_dwordx4 v2, s[36:39], s55 offen lds\n"
    "s_add_u32 m0, s53, 20480\n"
    "s_add_u32 s55, s54, 262144\n"
    "buffer_load_dwordx4 v2, s[36:39], s55 offen lds\n"
    "s_add_u32 m0, s53, 21504\n"
    "s_add_u32 s55, s54, 270336\n"
    "buffer_load_dwordx4 v2, s[36:39], s55 offen lds\n"
    "s_add_u32 m0, s53, 22528\n"
    "s_add_u32 s55, s54, 327680\n"
    "buffer_load_dwordx4 v2, s[36:39], s55 offen lds\n"
    "s_add_u32 m0, s53, 23552\n"
    "s_add_u32 s55, s54, 335872\n"
    "buffer_load_dwordx4 v2, s[36:39], s55 offen lds\n"
    "s_xor_b32 s54, s75, 8\n"
    "s_lshl_b32 s54, s54, 19\n"
    "s_add_u32 s54, s54, s52\n"
    "s_add_u32 m0, s53, 24576\n"
    "s_add_u32 s55, s54, 0\n"
    "buffer_load_dwordx4 v2, s[36:39], s55 offen lds\n"
    "s_add_u32 m0, s53, 25600\n"
    "s_add_u32 s55, s54, 8192\n"
    "buffer_load_dwordx4 v2, s[36:39], s55 offen lds\n"
    "s_add_u32 m0, s53, 26624\n"
    "s_add_u32 s55, s54, 65536\n"
    "buffer_load_dwordx4 v2, s[36:39], s55 offen lds\n"
    "s_add_u32 m0, s53, 27648\n"
    "s_add_u32 s55, s54, 73728\n"
    "buffer_load_dwordx4 v2, s[36:39], s55 offen lds\n"
    "s_add_u32 m0, s53, 28672\n"
    "s_add_u32 s55, s54, 262144\n"
    "buffer_load_dwordx4 v2, s[36:39], s55 offen lds\n"
    "s_add_u32 m0, s53, 29696\n"
    "s_add_u32 s55, s54, 270336\n"
    "buffer_load_dwordx4 v2, s[36:39], s55 offen lds\n"
    "s_add_u32 m0, s53, 30720\n"
    "s_add_u32 s55, s54, 327680\n"
    "buffer_load_dwordx4 v2, s[36:39], s55 offen lds\n"
    "s_add_u32 m0, s53, 31744\n"
    "s_add_u32 s55, s54, 335872\n"
    "buffer_load_dwordx4 v2, s[36:39], s55 offen lds\n"
    "s_xor_b32 s54, s75, 12\n"
    "s_lshl_b32 s54, s54, 19\n"
    "s_add_u32 s54, s54, s52\n"
    "s_add_u32 m0, s53, 32768\n"
    "s_add_u32 s55, s54, 0\n"
    "buffer_load_dwordx4 v2, s[36:39], s55 offen lds\n"
    "s_add_u32 m0, s53, 33792\n"
    "s_add_u32 s55, s54, 8192\n"
    "buffer_load_dwordx4 v2, s[36:39], s55 offen lds\n"
    "s_add_u32 m0, s53, 34816\n"
    "s_add_u32 s55, s54, 65536\n"
    "buffer_load_dwordx4 v2, s[36:39], s55 offen lds\n"
    "s_add_u32 m0, s53, 35840\n"
    "s_add_u32 s55, s54, 73728\n"
    "buffer_load_dwordx4 v2, s[36:39], s55 offen lds\n"
    "s_add_u32 m0, s53, 36864\n"
    "s_add_u32 s55, s54, 262144\n"
    "buffer_load_dwordx4 v2, s[36:39], s55 offen lds\n"
    "s_add_u32 m0, s53, 37888\n"
    "s_add_u32 s55, s54, 270336\n"
    "buffer_load_dwordx4 v2, s[36:39], s55 offen lds\n"
    "s_add_u32 m0, s53, 38912\n"
    "s_add_u32 s55, s54, 327680\n"
    "buffer_load_dwordx4 v2, s[36:39], s55 offen lds\n"
    "s_add_u32 m0, s53, 39936\n"
    "s_add_u32 s55, s54, 335872\n"
    "buffer_load_dwordx4 v2, s[36:39], s55 offen lds\n"
    "s_xor_b32 s54, s75, 11\n"
    "s_lshl_b32 s54, s54, 19\n"
    "s_add_u32 s54, s54, s52\n"
    "s_add_u32 m0, s53, 0\n"
    "s_add_u32 s55, s54, 0\n"
    "buffer_load_dwordx4 v2, s[36:39], s55 offen lds\n"
    "s_add_u32 m0, s53, 1024\n"
    "s_add_u32 s55, s54, 8192\n"
    "buffer_load_dwordx4 v2, s[36:39], s55 offen lds\n"
    "s_add_u32 m0, s53, 2048\n"
    "s_add_u32 s55, s54, 65536\n"
    "buffer_load_dwordx4 v2, s[36:39], s55 offen lds\n"
    "s_add_u32 m0, s53, 3072\n"
    "s_add_u32 s55, s54, 73728\n"
    "buffer_load_dwordx4 v2, s[36:39], s55 offen lds\n"
    "s_add_u32 m0, s53, 4096\n"
    "s_add_u32 s55, s54, 262144\n"
    "buffer_load_dwordx4 v2, s[36:39], s55 offen lds\n"
    "s_add_u32 m0, s53, 5120\n"
    "s_add_u32 s55, s54, 270336\n"
    "buffer_load_dwordx4 v2, s[36:39], s55 offen lds\n"
    "s_add_u32 m0, s53, 6144\n"
    "s_add_u32 s55, s54, 327680\n"
    "buffer_load_dwordx4 v2, s[36:39], s55 offen lds\n"
    "s_add_u32 m0, s53, 7168\n"
    "s_add_u32 s55, s54, 335872\n"
    "buffer_load_dwordx4 v2, s[36:39], s55 offen lds\n"
    "s_waitcnt vmcnt(40)\n"
    "v_readfirstlane_b32 s18, v0\n"
    "s_add_u32 s10, s19, 65536\n"
    "s_or_b32 s18, s18, 0xff\n"
    "s_add_u32 s18, s18, 1\n"
    "v_mov_b32 v0, s18\n"
    "s_mov_b64 exec, 1\n"
    "buffer_store_dword v0, off, s[12:15], s10 sc1\n"
    "s_mov_b64 exec, -1\n"
    "s_waitcnt vmcnt(33)\n"
    "ds_read_b64_tr_b16 a[0:1], v4 offset:8192\n"
    "ds_read_b64_tr_b16 a[2:3], v4 offset:9216\n"
    "ds_read_b64_tr_b16 a[4:5], v4 offset:12288\n"
    "ds_read_b64_tr_b16 a[6:7], v4 offset:13312\n"
    "ds_read_b64_tr_b16 a[8:9], v5 offset:8192\n"
    "ds_read_b64_tr_b16 a[10:11], v5 offset:9216\n"
    "ds_read_b64_tr_b16 a[12:13], v5 offset:12288\n"
    "ds_read_b64_tr_b16 a[14:15], v5 offset:13312\n"
    "ds_read_b64_tr_b16 a[16:17], v6 offset:8192\n"
    "ds_read_b64_tr_b16 a[18:19], v6 offset:9216\n"
    "ds_read_b64_tr_b16 a[20:21], v6 offset:12288\n"
    "ds_read_b64_tr_b16 a[22:23], v6 offset:13312\n"
    "ds_read_b64_tr_b16 a[24:25], v7 offset:8192\n"
    "ds_read_b64_tr_b16 a[26:27], v7 offset:9216\n"
    "ds_read_b64_tr_b16 a[28:29], v7 offset:12288\n"
    "ds_read_b64_tr_b16 a[30:31], v7 offset:13312\n"
    "s_waitcnt lgkmcnt(0)\n"
    "s_xor_b32 s54, s75, 15\n"
    "s_lshl_b32 s54, s54, 19\n"
    "s_add_u32 s54, s54, s52\n"
    "s_add_u32 m0, s53, 8192\n"
    "s_add_u32 s55, s54, 0\n"
    "buffer_load_dwordx4 v2, s[36:39], s55 offen lds\n"
    "s_add_u32 m0, s53, 9216\n"
    "s_add_u32 s55, s54, 8192\n"
    "buffer_load_dwordx4 v2, s[36:39], s55 offen lds\n"
    "s_add_u32 m0, s53, 10240\n"
    "s_add_u32 s55, s54, 65536\n"
    "buffer_load_dwordx4 v2, s[36:39], s55 offen lds\n"
    "s_add_u32 m0, s53, 11264\n"
    "s_add_u32 s55, s54, 73728\n"
    "buffer_load_dwordx4 v2, s[36:39], s55 offen lds\n"
    "s_add_u32 m0, s53, 12288\n"
    "s_add_u32 s55, s54, 262144\n"
    "buffer_load_dwordx4 v2, s[36:39], s55 offen lds\n"
    "s_add_u32 m0, s53, 13312\n"
    "s_add_u32 s55, s54, 270336\n"
    "buffer_load_dwordx4 v2, s[36:39], s55 offen lds\n"
    "s_add_u32 m0, s53, 14336\n"
    "s_add_u32 s55, s54, 327680\n"
    "buffer_load_dwordx4 v2, s[36:39], s55 offen lds\n"
    "s_add_u32 m0, s53, 15360\n"
    "s_add_u32 s55, s54, 335872\n"
    "buffer_load_dwordx4 v2, s[36:39], s55 offen lds\n"
    "s_waitcnt vmcnt(33)\n"
    "ds_read_b64_tr_b16 a[32:33], v4 offset:16384\n"
    "ds_read_b64_tr_b16 a[34:35], v4 offset:17408\n"
    "ds_read_b64_tr_b16 a[36:37], v4 offset:20480\n"
    "ds_read_b64_tr_b16 a[38:39], v4 offset:21504\n"
    "ds_read_b64_tr_b16 a[40:41], v5 offset:16384\n"
    "ds_read_b64_tr_b16 a[42:43], v5 offset:17408\n"
    "ds_read_b64_tr_b16 a[44:45], v5 offset:20480\n"
    "ds_read_b64_tr_b16 a[46:47], v5 offset:21504\n"
    "ds_read_b64_tr_b16 a[48:49], v6 offset:16384\n"
    "ds_read_b64_tr_b16 a[50:51], v6 offset:17408\n"
    "ds_read_b64_tr_b16 a[52:53], v6 offset:20480\n"
    "ds_read_b64_tr_b16 a[54:55], v6 offset:21504\n"
    "ds_read_b64_tr_b16 a[56:57], v7 offset:16384\n"
    "ds_read_b64_tr_b16 a[58:59], v7 offset:17408\n"
    "ds_read_b64_tr_b16 a[60:61], v7 offset:20480\n"
    "ds_read_b64_tr_b16 a[62:63], v7 offset:21504\n"
    "s_waitcnt lgkmcnt(0)\n"
    "s_xor_b32 s54, s75, 1\n"
    "s_lshl_b32 s54, s54, 19\n"
    "s_add_u32 s54, s54, s52\n"
    "s_add_u32 m0, s53, 16384\n"
    "s_add_u32 s55, s54, 0\n"
    "buffer_load_dwordx4 v2, s[36:39], s55 offen lds\n"
    "s_add_u32 m0, s53, 17408\n"
    "s_add_u32 s55, s54, 8192\n"
    "buffer_load_dwordx4 v2, s[36:39], s55 offen lds\n"
    "s_add_u32 m0, s53, 18432\n"
    "s_add_u32 s55, s54, 65536\n"
    "buffer_load_dwordx4 v2, s[36:39], s55 offen lds\n"
    "s_add_u32 m0, s53, 19456\n"
    "s_add_u32 s55, s54, 73728\n"
    "buffer_load_dwordx4 v2, s[36:39], s55 offen lds\n"
    "s_add_u32 m0, s53, 20480\n"
    "s_add_u32 s55, s54, 262144\n"
    "buffer_load_dwordx4 v2, s[36:39], s55 offen lds\n"
    "s_add_u32 m0, s53, 21504\n"
    "s_add_u32 s55, s54, 270336\n"
    "buffer_load_dwordx4 v2, s[36:39], s55 offen lds\n"
    "s_add_u32 m0, s53, 22528\n"
    "s_add_u32 s55, s54, 327680\n"
    "buffer_load_dwordx4 v2, s[36:39], s55 offen lds\n"
    "s_add_u32 m0, s53, 23552\n"
    "s_add_u32 s55, s54, 335872\n"
    "buffer_load_dwordx4 v2, s[36:39], s55 offen lds\n"
    "s_waitcnt vmcnt(33)\n"
    "ds_read_b64_tr_b16 a[64:65], v4 offset:24576\n"
    "ds_read_b64_tr_b16 a[66:67], v4 offset:25600\n"
    "ds_read_b64_tr_b16 a[68:69], v4 offset:28672\n"
    "ds_read_b64_tr_b16 a[70:71], v4 offset:29696\n"
    "ds_read_b64_tr_b16 a[72:73], v5 offset:24576\n"
    "ds_read_b64_tr_b16 a[74:75], v5 offset:25600\n"
    "ds_read_b64_tr_b16 a[76:77], v5 offset:28672\n"
    "ds_read_b64_tr_b16 a[78:79], v5 offset:29696\n"
    "ds_read_b64_tr_b16 a[80:81], v6 offset:24576\n"
    "ds_read_b64_tr_b16 a[82:83], v6 offset:25600\n"
    "ds_read_b64_tr_b16 a[84:85], v6 offset:28672\n"
    "ds_read_b64_tr_b16 a[86:87], v6 offset:29696\n"
    "ds_read_b64_tr_b16 a[88:89], v7 offset:24576\n"
    "ds_read_b64_tr_b16 a[90:91], v7 offset:25600\n"
    "ds_read_b64_tr_b16 a[92:93], v7 offset:28672\n"
    "ds_read_b64_tr_b16 a[94:95], v7 offset:29696\n"
    "s_waitcnt lgkmcnt(0)\n"
    "s_xor_b32 s54, s75, 5\n"
    "s_lshl_b32 s54, s54, 19\n"
    "s_add_u32 s54, s54, s52\n"
    "s_add_u32 m0, s53, 24576\n"
    "s_add_u32 s55, s54, 0\n"
    "buffer_load_dwordx4 v2, s[36:39], s55 offen lds\n"
    "s_add_u32 m0, s53, 25600\n"
    "s_add_u32 s55, s54, 8192\n"
    "buffer_load_dwordx4 v2, s[36:39], s55 offen lds\n"
    "s_add_u32 m0, s53, 26624\n"
    "s_add_u32 s55, s54, 65536\n"
    "buffer_load_dwordx4 v2, s[36:39], s55 offen lds\n"
    "s_add_u32 m0, s53, 27648\n"
    "s_add_u32 s55, s54, 73728\n"
    "buffer_load_dwordx4 v2, s[36:39], s55 offen lds\n"
    "s_add_u32 m0, s53, 28672\n"
    "s_add_u32 s55, s54, 262144\n"
    "buffer_load_dwordx4 v2, s[36:39], s55 offen lds\n"
    "s_add_u32 m0, s53, 29696\n"
    "s_add_u32 s55, s54, 270336\n"
    "buffer_load_dwordx4 v2, s[36:39], s55 offen lds\n"
    "s_add_u32 m0, s53, 30720\n"
    "s_add_u32 s55, s54, 327680\n"
    "buffer_load_dwordx4 v2, s[36:39], s55 offen lds\n"
    "s_add_u32 m0, s53, 31744\n"
    "s_add_u32 s55, s54, 335872\n"
    "buffer_load_dwordx4 v2, s[36:39], s55 offen lds\n"
    "s_waitcnt vmcnt(33)\n"
    "ds_read_b64_tr_b16 a[96:97], v4 offset:32768\n"
    "ds_read_b64_tr_b16 a[98:99], v4 offset:33792\n"
    "ds_read_b64_tr_b16 a[100:101], v4 offset:36864\n"
    "ds_read_b64_tr_b16 a[102:103], v4 offset:37888\n"
    "ds_read_b64_tr_b16 a[104:105], v5 offset:32768\n"
    "ds_read_b64_tr_b16 a[106:107], v5 offset:33792\n"
    "ds_read_b64_tr_b16 a[108:109], v5 offset:36864\n"
    "ds_read_b64_tr_b16 a[110:111], v5 offset:37888\n"
    "ds_read_b64_tr_b16 a[112:113], v6 offset:32768\n"
    "ds_read_b64_tr_b16 a[114:115], v6 offset:33792\n"
    "ds_read_b64_tr_b16 a[116:117], v6 offset:36864\n"
    "ds_read_b64_tr_b16 a[118:119], v6 offset:37888\n"
    "ds_read_b64_tr_b16 a[120:121], v7 offset:32768\n"
    "ds_read_b64_tr_b16 a[122:123], v7 offset:33792\n"
    "ds_read_b64_tr_b16 a[124:125], v7 offset:36864\n"
    "ds_read_b64_tr_b16 a[126:127], v7 offset:37888\n"
    "s_waitcnt lgkmcnt(0)\n"
    "s_xor_b32 s54, s75, 9\n"
    "s_lshl_b32 s54, s54, 19\n"
    "s_add_u32 s54, s54, s52\n"
    "s_add_u32 m0, s53, 32768\n"
    "s_add_u32 s55, s54, 0\n"
    "buffer_load_dwordx4 v2, s[36:39], s55 offen lds\n"
    "s_add_u32 m0, s53, 33792\n"
    "s_add_u32 s55, s54, 8192\n"
    "buffer_load_dwordx4 v2, s[36:39], s55 offen lds\n"
    "s_add_u32 m0, s53, 34816\n"
    "s_add_u32 s55, s54, 65536\n"
    "buffer_load_dwordx4 v2, s[36:39], s55 offen lds\n"
    "s_add_u32 m0, s53, 35840\n"
    "s_add_u32 s55, s54, 73728\n"
    "buffer_load_dwordx4 v2, s[36:39], s55 offen lds\n"
    "s_add_u32 m0, s53, 36864\n"
    "s_add_u32 s55, s54, 262144\n"
    "buffer_load_dwordx4 v2, s[36:39], s55 offen lds\n"
    "s_add_u32 m0, s53, 37888\n"
    "s_add_u32 s55, s54, 270336\n"
    "buffer_load_dwordx4 v2, s[36:39], s55 offen lds\n"
    "s_add_u32 m0, s53, 38912\n"
    "s_add_u32 s55, s54, 327680\n"
    "buffer_load_dwordx4 v2, s[36:39], s55 offen lds\n"
    "s_add_u32 m0, s53, 39936\n"
    "s_add_u32 s55, s54, 335872\n"
    "buffer_load_dwordx4 v2, s[36:39], s55 offen lds\n"
    "s_waitcnt vmcnt(33)\n"
    "ds_read_b64_tr_b16 v[192:193], v4 offset:0\n"
    "ds_read_b64_tr_b16 v[194:195], v4 offset:1024\n"
    "ds_read_b64_tr_b16 v[196:197], v4 offset:4096\n"
    "ds_read_b64_tr_b16 v[198:199], v4 offset:5120\n"
    "ds_read_b64_tr_b16 v[200:201], v5 offset:0\n"
    "ds_read_b64_tr_b16 v[202:203], v5 offset:1024\n"
    "ds_read_b64_tr_b16 v[204:205], v5 offset:4096\n"
    "ds_read_b64_tr_b16 v[206:207], v5 offset:5120\n"
    "ds_read_b64_tr_b16 v[208:209], v6 offset:0\n"
    "ds_read_b64_tr_b16 v[210:211], v6 offset:1024\n"
    "ds_read_b64_tr_b16 v[212:213], v6 offset:4096\n"
    "ds_read_b64_tr_b16 v[214:215], v6 offset:5120\n"
    "ds_read_b64_tr_b16 v[216:217], v7 offset:0\n"
    "ds_read_b64_tr_b16 v[218:219], v7 offset:1024\n"
    "ds_read_b64_tr_b16 v[220:221], v7 offset:4096\n"
    "ds_read_b64_tr_b16 v[222:223], v7 offset:5120\n"
    "s_waitcnt lgkmcnt(0)\n"
    "s_xor_b32 s54, s75, 13\n"
    "s_lshl_b32 s54, s54, 19\n"
    "s_add_u32 s54, s54, s52\n"
    "s_add_u32 m0, s53, 0\n"
    "s_add_u32 s55, s54, 0\n"
    "buffer_load_dwordx4 v2, s[36:39], s55 offen lds\n"
    "s_add_u32 m0, s53, 1024\n"
    "s_add_u32 s55, s54, 8192\n"
    "buffer_load_dwordx4 v2, s[36:39], s55 offen lds\n"
    "s_add_u32 m0, s53, 2048\n"
    "s_add_u32 s55, s54, 65536\n"
    "buffer_load_dwordx4 v2, s[36:39], s55 offen lds\n"
    "s_add_u32 m0, s53, 3072\n"
    "s_add_u32 s55, s54, 73728\n"
    "buffer_load_dwordx4 v2, s[36:39], s55 offen lds\n"
    "s_add_u32 m0, s53, 4096\n"
    "s_add_u32 s55, s54, 262144\n"
    "buffer_load_dwordx4 v2, s[36:39], s55 offen lds\n"
    "s_add_u32 m0, s53, 5120\n"
    "s_add_u32 s55, s54, 270336\n"
    "buffer_load_dwordx4 v2, s[36:39], s55 offen lds\n"
    "s_add_u32 m0, s53, 6144\n"
    "s_add_u32 s55, s54, 327680\n"
    "buffer_load_dwordx4 v2, s[36:39], s55 offen lds\n"
    "s_add_u32 m0, s53, 7168\n"
    "s_add_u32 s55, s54, 335872\n"
    "buffer_load_dwordx4 v2, s[36:39], s55 offen lds\n"
    "s_waitcnt vmcnt(32)\n"
    "ds_read_b64_tr_b16 v[224:225], v4 offset:8192\n"
    "ds_read_b64_tr_b16 v[226:227], v4 offset:9216\n"
    "ds_read_b64_tr_b16 v[228:229], v4 offset:12288\n"
    "ds_read_b64_tr_b16 v[230:231], v4 offset:13312\n"
    "ds_read_b64_tr_b16 v[232:233], v5 offset:8192\n"
    "ds_read_b64_tr_b16 v[234:235], v5 offset:9216\n"
    "ds_read_b64_tr_b16 v[236:237], v5 offset:12288\n"
    "ds_read_b64_tr_b16 v[238:239], v5 offset:13312\n"
    "ds_read_b64_tr_b16 v[240:241], v6 offset:8192\n"
    "ds_read_b64_tr_b16 v[242:243], v6 offset:9216\n"
    "ds_read_b64_tr_b16 v[244:245], v6 offset:12288\n"
    "ds_read_b64_tr_b16 v[246:247], v6 offset:13312\n"
    "ds_read_b64_tr_b16 v[248:249], v7 offset:8192\n"
    "ds_read_b64_tr_b16 v[250:251], v7 offset:9216\n"
    "ds_read_b64_tr_b16 v[252:253], v7 offset:12288\n"
    "ds_read_b64_tr_b16 v[254:255], v7 offset:13312\n"
    "s_waitcnt lgkmcnt(0)\n"
    "s_xor_b32 s54, s75, 2\n"
    "s_lshl_b32 s54, s54, 19\n"
    "s_add_u32 s54, s54, s52\n"
    "s_add_u32 m0, s53, 8192\n"
    "s_add_u32 s55, s54, 0\n"
    "buffer_load_dwordx4 v2, s[36:39], s55 offen lds\n"
    "s_add_u32 m0, s53, 9216\n"
    "s_add_u32 s55, s54, 8192\n"
    "buffer_load_dwordx4 v2, s[36:39], s55 offen lds\n"
    "s_add_u32 m0, s53, 10240\n"
    "s_add_u32 s55, s54, 65536\n"
    "buffer_load_dwordx4 v2, s[36:39], s55 offen lds\n"
    "s_add_u32 m0, s53, 11264\n"
    "s_add_u32 s55, s54, 73728\n"
    "buffer_load_dwordx4 v2, s[36:39], s55 offen lds\n"
    "s_add_u32 m0, s53, 12288\n"
    "s_add_u32 s55, s54, 262144\n"
    "buffer_load_dwordx4 v2, s[36:39], s55 offen lds\n"
    "s_add_u32 m0, s53, 13312\n"
    "s_add_u32 s55, s54, 270336\n"
    "buffer_load_dwordx4 v2, s[36:39], s55 offen lds\n"
    "s_add_u32 m0, s53, 14336\n"
    "s_add_u32 s55, s54, 327680\n"
    "buffer_load_dwordx4 v2, s[36:39], s55 offen lds\n"
    "s_add_u32 m0, s53, 15360\n"
    "s_add_u32 s55, s54, 335872\n"
    "buffer_load_dwordx4 v2, s[36:39], s55 offen lds\n"
    "s_waitcnt vmcnt(32)\n"
    "ds_read_b64_tr_b16 a[128:129], v4 offset:16384\n"
    "ds_read_b64_tr_b16 a[130:131], v4 offset:17408\n"
    "ds_read_b64_tr_b16 a[132:133], v4 offset:20480\n"
    "ds_read_b64_tr_b16 a[134:135], v4 offset:21504\n"
    "ds_read_b64_tr_b16 a[136:137], v5 offset:16384\n"
    "ds_read_b64_tr_b16 a[138:139], v5 offset:17408\n"
    "ds_read_b64_tr_b16 a[140:141], v5 offset:20480\n"
    "ds_read_b64_tr_b16 a[142:143], v5 offset:21504\n"
    "ds_read_b64_tr_b16 a[144:145], v6 offset:16384\n"
    "ds_read_b64_tr_b16 a[146:147], v6 offset:17408\n"
    "ds_read_b64_tr_b16 a[148:149], v6 offset:20480\n"
    "ds_read_b64_tr_b16 a[150:151], v6 offset:21504\n"
    "ds_read_b64_tr_b16 v[8:9], v7 offset:16384\n"
    "ds_read_b64_tr_b16 v[10:11], v7 offset:17408\n"
    "ds_read_b64_tr_b16 v[12:13], v7 offset:20480\n"
    "ds_read_b64_tr_b16 v[14:15], v7 offset:21504\n"
    "s_waitcnt lgkmcnt(0)\n"
    "s_xor_b32 s54, s75, 6\n"
    "s_lshl_b32 s54, s54, 19\n"
    "s_add_u32 s54, s54, s52\n"
    "s_add_u32 m0, s53, 16384\n"
    "s_add_u32 s55, s54, 0\n"
    "buffer_load_dwordx4 v2, s[36:39], s55 offen lds\n"
    "s_add_u32 m0, s53, 17408\n"
    "s_add_u32 s55, s54, 8192\n"
    "buffer_load_dwordx4 v2, s[36:39], s55 offen lds\n"
    "s_add_u32 m0, s53, 18432\n"
    "s_add_u32 s55, s54, 65536\n"
    "buffer_load_dwordx4 v2, s[36:39], s55 offen lds\n"
    "s_add_u32 m0, s53, 19456\n"
    "s_add_u32 s55, s54, 73728\n"
    "buffer_load_dwordx4 v2, s[36:39], s55 offen lds\n"
    "s_add_u32 m0, s53, 20480\n"
    "s_add_u32 s55, s54, 262144\n"
    "buffer_load_dwordx4 v2, s[36:39], s55 offen lds\n"
    "s_add_u32 m0, s53, 21504\n"
    "s_add_u32 s55, s54, 270336\n"
    "buffer_load_dwordx4 v2, s[36:39], s55 offen lds\n"
    "s_add_u32 m0, s53, 22528\n"
    "s_add_u32 s55, s54, 327680\n"
    "buffer_load_dwordx4 v2, s[36:39], s55 offen lds\n"
    "s_add_u32 m0, s53, 23552\n"
    "s_add_u32 s55, s54, 335872\n"
    "buffer_load_dwordx4 v2, s[36:39], s55 offen lds\n"
    "s_waitcnt vmcnt(32)\n"
    "ds_read_b64_tr_b16 a[152:153], v4 offset:24576\n"
    "ds_read_b64_tr_b16 a[154:155], v4 offset:25600\n"
    "ds_read_b64_tr_b16 a[156:157], v4 offset:28672\n"
    "ds_read_b64_tr_b16 a[158:159], v4 offset:29696\n"
    "ds_read_b64_tr_b16 a[160:161], v5 offset:24576\n"
    "ds_read_b64_tr_b16 a[162:163], v5 offset:25600\n"
    "ds_read_b64_tr_b16 a[164:165], v5 offset:28672\n"
    "ds_read_b64_tr_b16 a[166:167], v5 offset:29696\n"
    "ds_read_b64_tr_b16 a[168:169], v6 offset:24576\n"
    "ds_read_b64_tr_b16 a[170:171], v6 offset:25600\n"
    "ds_read_b64_tr_b16 a[172:173], v6 offset:28672\n"
    "ds_read_b64_tr_b16 a[174:175], v6 offset:29696\n"
    "ds_read_b64_tr_b16 v[16:17], v7 offset:24576\n"
    "ds_read_b64_tr_b16 v[18:19], v7 offset:25600\n"
    "ds_read_b64_tr_b16 v[20:21], v7 offset:28672\n"
    "ds_read_b64_tr_b16 v[22:23], v7 offset:29696\n"
    "s_waitcnt lgkmcnt(0)\n"
    "s_xor_b32 s54, s75, 10\n"
    "s_lshl_b32 s54, s54, 19\n"
    "s_add_u32 s54, s54, s52\n"
    "s_add_u32 m0, s53, 24576\n"
    "s_add_u32 s55, s54, 0\n"
    "buffer_load_dwordx4 v2, s[36:39], s55 offen lds\n"
    "s_add_u32 m0, s53, 25600\n"
    "s_add_u32 s55, s54, 8192\n"
    "buffer_load_dwordx4 v2, s[36:39], s55 offen lds\n"
    "s_add_u32 m0, s53, 26624\n"
    "s_add_u32 s55, s54, 65536\n"
    "buffer_load_dwordx4 v2, s[36:39], s55 offen lds\n"
    "s_add_u32 m0, s53, 27648\n"
    "s_add_u32 s55, s54, 73728\n"
    "buffer_load_dwordx4 v2, s[36:39], s55 offen lds\n"
    "s_add_u32 m0, s53, 28672\n"
    "s_add_u32 s55, s54, 262144\n"
    "buffer_load_dwordx4 v2, s[36:39], s55 offen lds\n"
    "s_add_u32 m0, s53, 29696\n"
    "s_add_u32 s55, s54, 270336\n"
    "buffer_load_dwordx4 v2, s[36:39], s55 offen lds\n"
    "s_add_u32 m0, s53, 30720\n"
    "s_add_u32 s55, s54, 327680\n"
    "buffer_load_dwordx4 v2, s[36:39], s55 offen lds\n"
    "s_add_u32 m0, s53, 31744\n"
    "s_add_u32 s55, s54, 335872\n"
    "buffer_load_dwordx4 v2, s[36:39], s55 offen lds\n"
    "s_waitcnt vmcnt(32)\n"
    "ds_read_b64_tr_b16 a[176:177], v4 offset:32768\n"
    "ds_read_b64_tr_b16 a[178:179], v4 offset:33792\n"
    "ds_read_b64_tr_b16 a[180:181], v4 offset:36864\n"
    "ds_read_b64_tr_b16 a[182:183], v4 offset:37888\n"
    "ds_read_b64_tr_b16 a[184:185], v5 offset:32768\n"
    "ds_read_b64_tr_b16 a[186:187], v5 offset:33792\n"
    "ds_read_b64_tr_b16 a[188:189], v5 offset:36864\n"
    "ds_read_b64_tr_b16 a[190:191], v5 offset:37888\n"
    "ds_read_b64_tr_b16 a[192:193], v6 offset:32768\n"
    "ds_read_b64_tr_b16 a[194:195], v6 offset:33792\n"
    "ds_read_b64_tr_b16 a[196:197], v6 offset:36864\n"
    "ds_read_b64_tr_b16 a[198:199], v6 offset:37888\n"
    "ds_read_b64_tr_b16 v[24:25], v7 offset:32768\n"
    "ds_read_b64_tr_b16 v[26:27], v7 offset:33792\n"
    "ds_read_b64_tr_b16 v[28:29], v7 offset:36864\n"
    "ds_read_b64_tr_b16 v[30:31], v7 offset:37888\n"
    "s_waitcnt lgkmcnt(0)\n"
    "s_xor_b32 s54, s75, 14\n"
    "s_lshl_b32 s54, s54, 19\n"
    "s_add_u32 s54, s54, s52\n"
    "s_add_u32 m0, s53, 32768\n"
    "s_add_u32 s55, s54, 0\n"
    "buffer_load_dwordx4 v2, s[36:39], s55 offen lds\n"
    "s_add_u32 m0, s53, 33792\n"
    "s_add_u32 s55, s54, 8192\n"
    "buffer_load_dwordx4 v2, s[36:39], s55 offen lds\n"
    "s_add_u32 m0, s53, 34816\n"
    "s_add_u32 s55, s54, 65536\n"
    "buffer_load_dwordx4 v2, s[36:39], s55 offen lds\n"
    "s_add_u32 m0, s53, 35840\n"
    "s_add_u32 s55, s54, 73728\n"
    "buffer_load_dwordx4 v2, s[36:39], s55 offen lds\n"
    "s_add_u32 m0, s53, 36864\n"
    "s_add_u32 s55, s54, 262144\n"
    "buffer_load_dwordx4 v2, s[36:39], s55 offen lds\n"
    "s_add_u32 m0, s53, 37888\n"
    "s_add_u32 s55, s54, 270336\n"
    "buffer_load_dwordx4 v2, s[36:39], s55 offen lds\n"
    "s_add_u32 m0, s53, 38912\n"
    "s_add_u32 s55, s54, 327680\n"
    "buffer_load_dwordx4 v2, s[36:39], s55 offen lds\n"
    "s_add_u32 m0, s53, 39936\n"
    "s_add_u32 s55, s54, 335872\n"
    "buffer_load_dwordx4 v2, s[36:39], s55 offen lds\n"
    "s_waitcnt vmcnt(32)\n"
    "ds_read_b64_tr_b16 a[200:201], v4 offset:0\n"
    "ds_read_b64_tr_b16 a[202:203], v4 offset:1024\n"
    "ds_read_b64_tr_b16 a[204:205], v4 offset:4096\n"
    "ds_read_b64_tr_b16 a[206:207], v4 offset:5120\n"
    "ds_read_b64_tr_b16 a[208:209], v5 offset:0\n"
    "ds_read_b64_tr_b16 a[210:211], v5 offset:1024\n"
    "ds_read_b64_tr_b16 a[212:213], v5 offset:4096\n"
    "ds_read_b64_tr_b16 a[214:215], v5 offset:5120\n"
    "ds_read_b64_tr_b16 a[216:217], v6 offset:0\n"
    "ds_read_b64_tr_b16 a[218:219], v6 offset:1024\n"
    "ds_read_b64_tr_b16 a[220:221], v6 offset:4096\n"
    "ds_read_b64_tr_b16 a[222:223], v6 offset:5120\n"
    "ds_read_b64_tr_b16 v[32:33], v7 offset:0\n"
    "ds_read_b64_tr_b16 v[34:35], v7 offset:1024\n"
    "ds_read_b64_tr_b16 v[36:37], v7 offset:4096\n"
    "ds_read_b64_tr_b16 v[38:39], v7 offset:5120\n"
    "s_waitcnt lgkmcnt(0)\n"
    "s_xor_b32 s54, s75, 3\n"
    "s_lshl_b32 s54, s54, 19\n"
    "s_add_u32 s54, s54, s52\n"
    "s_add_u32 m0, s53, 0\n"
    "s_add_u32 s55, s54, 0\n"
    "buffer_load_dwordx4 v2, s[36:39], s55 offen lds\n"
    "s_add_u32 m0, s53, 1024\n"
    "s_add_u32 s55, s54, 8192\n"
    "buffer_load_dwordx4 v2, s[36:39], s55 offen lds\n"
    "s_add_u32 m0, s53, 2048\n"
    "s_add_u32 s55, s54, 65536\n"
    "buffer_load_dwordx4 v2, s[36:39], s55 offen lds\n"
    "s_add_u32 m0, s53, 3072\n"
    "s_add_u32 s55, s54, 73728\n"
    "buffer_load_dwordx4 v2, s[36:39], s55 offen lds\n"
    "s_add_u32 m0, s53, 4096\n"
    "s_add_u32 s55, s54, 262144\n"
    "buffer_load_dwordx4 v2, s[36:39], s55 offen lds\n"
    "s_add_u32 m0, s53, 5120\n"
    "s_add_u32 s55, s54, 270336\n"
    "buffer_load_dwordx4 v2, s[36:39], s55 offen lds\n"
    "s_add_u32 m0, s53, 6144\n"
    "s_add_u32 s55, s54, 327680\n"
    "buffer_load_dwordx4 v2, s[36:39], s55 offen lds\n"
    "s_add_u32 m0, s53, 7168\n"
    "s_add_u32 s55, s54, 335872\n"
    "buffer_load_dwordx4 v2, s[36:39], s55 offen lds\n"
    "s_getpc_b64 s[0:1]\n"
    ".Lqk_pc2_%=:\n"
    "s_and_b32 s1, s1, 0xffff\n"
    "s_mov_b32 s2, .Lk_end_%=-.Lqk_pc2_%=\n"
    "s_mov_b32 s3, 0x20000\n"
    "s_lshl_b32 s4, s25, 13\n"
    "v_lshlrev_b32 v74, 7, v1\n"
    "buffer_load_dword v74, v74, s[0:3], s4 offen\n"
    "s_add_u32 s10, s19, 69632\n"
    "s_mov_b64 exec, 1\n"
    "buffer_load_dword v0, off, s[12:15], s10 sc1\n"
    "s_mov_b64 exec, -1\n"
    "s_waitcnt vmcnt(34)\n"
    "ds_read_b64_tr_b16 a[224:225], v4 offset:8192\n"
    "ds_read_b64_tr_b16 a[226:227], v4 offset:9216\n"
    "ds_read_b64_tr_b16 a[228:229], v4 offset:12288\n"
    "ds_read_b64_tr_b16 a[230:231], v4 offset:13312\n"
    "ds_read_b64_tr_b16 a[232:233], v5 offset:8192\n"
    "ds_read_b64_tr_b16 a[234:235], v5 offset:9216\n"
    "ds_read_b64_tr_b16 a[236:237], v5 offset:12288\n"
    "ds_read_b64_tr_b16 a[238:239], v5 offset:13312\n"
    "ds_read_b64_tr_b16 a[240:241], v6 offset:8192\n"
    "ds_read_b64_tr_b16 a[242:243], v6 offset:9216\n"
    "ds_read_b64_tr_b16 a[244:245], v6 offset:12288\n"
    "ds_read_b64_tr_b16 a[246:247], v6 offset:13312\n"
    "ds_read_b64_tr_b16 v[40:41], v7 offset:8192\n"
    "ds_read_b64_tr_b16 v[42:43], v7 offset:9216\n"
    "ds_read_b64_tr_b16 v[44:45], v7 offset:12288\n"
    "ds_read_b64_tr_b16 v[46:47], v7 offset:13312\n"
    "s_waitcnt lgkmcnt(0)\n"
    "s_xor_b32 s54, s75, 7\n"
    "s_lshl_b32 s54, s54, 19\n"
    "s_add_u32 s54, s54, s52\n"
    "s_add_u32 m0, s53, 8192\n"
    "s_add_u32 s55, s54, 0\n"
    "buffer_load_dwordx4 v2, s[36:39], s55 offen lds\n"
    "s_add_u32 m0, s53, 9216\n"
    "s_add_u32 s55, s54, 8192\n"
    "buffer_load_dwordx4 v2, s[36:39], s55 offen lds\n"
    "s_add_u32 m0, s53, 10240\n"
    "s_add_u32 s55, s54, 65536\n"
    "buffer_load_dwordx4 v2, s[36:39], s55 offen lds\n"
    "s_add_u32 m0, s53, 11264\n"
    "s_add_u32 s55, s54, 73728\n"
    "buffer_load_dwordx4 v2, s[36:39], s55 offen lds\n"
    "s_add_u32 m0, s53, 12288\n"
    "s_add_u32 s55, s54, 262144\n"
    "buffer_load_dwordx4 v2, s[36:39], s55 offen lds\n"
    "s_add_u32 m0, s53, 13312\n"
    "s_add_u32 s55, s54, 270336\n"
    "buffer_load_dwordx4 v2, s[36:39], s55 offen lds\n"
    "s_add_u32 m0, s53, 14336\n"
    "s_add_u32 s55, s54, 327680\n"
    "buffer_load_dwordx4 v2, s[36:39], s55 offen lds\n"
    "s_add_u32 m0, s53, 15360\n"
    "s_add_u32 s55, s54, 335872\n"
    "buffer_load_dwordx4 v2, s[36:39], s55 offen lds\n"
    "s_waitcnt vmcnt(34)\n"
    "ds_read_b64_tr_b16 a[248:249], v4 offset:16384\n"
    "ds_read_b64_tr_b16 a[250:251], v4 offset:17408\n"
    "ds_read_b64_tr_b16 a[252:253], v4 offset:20480\n"
    "ds_read_b64_tr_b16 a[254:255], v4 offset:21504\n"
    "ds_read_b64_tr_b16 v[80:81], v5 offset:16384\n"
    "ds_read_b64_tr_b16 v[82:83], v5 offset:17408\n"
    "ds_read_b64_tr_b16 v[84:85], v5 offset:20480\n"
    "ds_read_b64_tr_b16 v[86:87], v5 offset:21504\n"
    "ds_read_b64_tr_b16 v[88:89], v6 offset:16384\n"
    "ds_read_b64_tr_b16 v[90:91], v6 offset:17408\n"
    "ds_read_b64_tr_b16 v[92:93], v6 offset:20480\n"
    "ds_read_b64_tr_b16 v[94:95], v6 offset:21504\n"
    "ds_read_b64_tr_b16 v[48:49], v7 offset:16384\n"
    "ds_read_b64_tr_b16 v[50:51], v7 offset:17408\n"
    "ds_read_b64_tr_b16 v[52:53], v7 offset:20480\n"
    "ds_read_b64_tr_b16 v[54:55], v7 offset:21504\n"
    "s_waitcnt lgkmcnt(0)\n"
    "s_waitcnt vmcnt(26)\n"
    "ds_read_b64_tr_b16 v[96:97], v4 offset:24576\n"
    "ds_read_b64_tr_b16 v[98:99], v4 offset:25600\n"
    "ds_read_b64_tr_b16 v[100:101], v4 offset:28672\n"
    "ds_read_b64_tr_b16 v[102:103], v4 offset:29696\n"
    "ds_read_b64_tr_b16 v[104:105], v5 offset:24576\n"
    "ds_read_b64_tr_b16 v[106:107], v5 offset:25600\n"
    "ds_read_b64_tr_b16 v[108:109], v5 offset:28672\n"
    "ds_read_b64_tr_b16 v[110:111], v5 offset:29696\n"
    "ds_read_b64_tr_b16 v[112:113], v6 offset:24576\n"
    "ds_read_b64_tr_b16 v[114:115], v6 offset:25600\n"
    "ds_read_b64_tr_b16 v[116:117], v6 offset:28672\n"
    "ds_read_b64_tr_b16 v[118:119], v6 offset:29696\n"
    "ds_read_b64_tr_b16 v[56:57], v7 offset:24576\n"
    "ds_read_b64_tr_b16 v[58:59], v7 offset:25600\n"
    "ds_read_b64_tr_b16 v[60:61], v7 offset:28672\n"
    "ds_read_b64_tr_b16 v[62:63], v7 offset:29696\n"
    "s_waitcnt lgkmcnt(0)\n"
    "s_waitcnt vmcnt(18)\n"
    "ds_read_b64_tr_b16 v[120:121], v4 offset:32768\n"
    "ds_read_b64_tr_b16 v[122:123], v4 offset:33792\n"
    "ds_read_b64_tr_b16 v[124:125], v4 offset:36864\n"
    "ds_read_b64_tr_b16 v[126:127], v4 offset:37888\n"
    "ds_read_b64_tr_b16 v[128:129], v5 offset:32768\n"
    "ds_read_b64_tr_b16 v[130:131], v5 offset:33792\n"
    "ds_read_b64_tr_b16 v[132:133], v5 offset:36864\n"
    "ds_read_b64_tr_b16 v[134:135], v5 offset:37888\n"
    "ds_read_b64_tr_b16 v[136:137], v6 offset:32768\n"
    "ds_read_b64_tr_b16 v[138:139], v6 offset:33792\n"
    "ds_read_b64_tr_b16 v[140:141], v6 offset:36864\n"
    "ds_read_b64_tr_b16 v[142:143], v6 offset:37888\n"
    "ds_read_b64_tr_b16 v[64:65], v7 offset:32768\n"
    "ds_read_b64_tr_b16 v[66:67], v7 offset:33792\n"
    "ds_read_b64_tr_b16 v[68:69], v7 offset:36864\n"
    "ds_read_b64_tr_b16 v[70:71], v7 offset:37888\n"
    "s_waitcnt lgkmcnt(0)\n"
    "ds_write_b128 v72, v[8:11] offset:0\n"
    "ds_write_b128 v72, v[12:15] offset:1024\n"
    "ds_write_b128 v72, v[16:19] offset:2048\n"
    "ds_write_b128 v72, v[20:23] offset:3072\n"
    "ds_write_b128 v72, v[24:27] offset:4096\n"
    "ds_write_b128 v72, v[28:31] offset:5120\n"
    "ds_write_b128 v72, v[32:35] offset:6144\n"
    "ds_write_b128 v72, v[36:39] offset:7168\n"
    "ds_write_b128 v72, v[40:43] offset:8192\n"
    "ds_write_b128 v72, v[44:47] offset:9216\n"
    "ds_write_b128 v72, v[48:51] offset:10240\n"
    "ds_write_b128 v72, v[52:55] offset:11264\n"
    "ds_write_b128 v72, v[56:59] offset:12288\n"
    "ds_write_b128 v72, v[60:63] offset:13312\n"
    "ds_write_b128 v72, v[64:67] offset:14336\n"
    "ds_write_b128 v72, v[68:71] offset:15360\n"
    "s_waitcnt lgkmcnt(0)\n"
    "s_waitcnt vmcnt(10)\n"
    "ds_read_b64_tr_b16 v[8:9], v4 offset:0\n"
    "ds_read_b64_tr_b16 v[10:11], v4 offset:1024\n"
    "ds_read_b64_tr_b16 v[12:13], v4 offset:4096\n"
    "ds_read_b64_tr_b16 v[14:15], v4 offset:5120\n"
    "ds_read_b64_tr_b16 v[144:145], v5 offset:0\n"
    "ds_read_b64_tr_b16 v[146:147], v5 offset:1024\n"
    "ds_read_b64_tr_b16 v[148:149], v5 offset:4096\n"
    "ds_read_b64_tr_b16 v[150:151], v5 offset:5120\n"
    "ds_read_b64_tr_b16 v[152:153], v6 offset:0\n"
    "ds_read_b64_tr_b16 v[154:155], v6 offset:1024\n"
    "ds_read_b64_tr_b16 v[156:157], v6 offset:4096\n"
    "ds_read_b64_tr_b16 v[158:159], v6 offset:5120\n"
    "ds_read_b64_tr_b16 v[160:161], v7 offset:0\n"
    "ds_read_b64_tr_b16 v[162:163], v7 offset:1024\n"
    "ds_read_b64_tr_b16 v[164:165], v7 offset:4096\n"
    "ds_read_b64_tr_b16 v[166:167], v7 offset:5120\n"
    "s_waitcnt lgkmcnt(0)\n"
    "ds_write_b128 v72, v[8:11] offset:16384\n"
    "ds_write_b128 v72, v[12:15] offset:17408\n"
    "s_waitcnt lgkmcnt(0)\n"
    "s_waitcnt vmcnt(8)\n"
    ".Lqk_hsc_%=:\n"
    "v_readfirstlane_b32 s10, v0\n"
    "s_nop 1\n"
    "s_andn2_b32 s11, s10, 0xff\n"
    "s_cmp_lg_u32 s11, s18\n"
    "s_cbranch_scc1 .Lqk_hsp_%=\n"
    "s_and_b32 s11, s10, 7\n"
    "s_cmp_lg_u32 s11, s17\n"
    "s_cbranch_scc1 .Lqk_hsr_%=\n"
    "s_and_b32 s10, s10, 8\n"
    "s_lshl_b32 s27, s10, 10\n"
    "s_add_u32 s10, s19, 12288\n"
    "v_mov_b32 v0, 1\n"
    "s_mov_b64 exec, 1\n"
    "buffer_store_dword v0, off, s[12:15], s10 sc1\n"
    "s_mov_b64 exec, -1\n"
    "s_waitcnt vmcnt(1)\n"
    "ds_read_b64_tr_b16 v[16:17], v4 offset:8192\n"
    "ds_read_b64_tr_b16 v[18:19], v4 offset:9216\n"
    "ds_read_b64_tr_b16 v[20:21], v4 offset:12288\n"
    "ds_read_b64_tr_b16 v[22:23], v4 offset:13312\n"
    "ds_read_b64_tr_b16 v[168:169], v5 offset:8192\n"
    "ds_read_b64_tr_b16 v[170:171], v5 offset:9216\n"
    "ds_read_b64_tr_b16 v[172:173], v5 offset:12288\n"
    "ds_read_b64_tr_b16 v[174:175], v5 offset:13312\n"
    "ds_read_b64_tr_b16 v[176:177], v6 offset:8192\n"
    "ds_read_b64_tr_b16 v[178:179], v6 offset:9216\n"
    "ds_read_b64_tr_b16 v[180:181], v6 offset:12288\n"
    "ds_read_b64_tr_b16 v[182:183], v6 offset:13312\n"
    "ds_read_b64_tr_b16 v[184:185], v7 offset:8192\n"
    "ds_read_b64_tr_b16 v[186:187], v7 offset:9216\n"
    "ds_read_b64_tr_b16 v[188:189], v7 offset:12288\n"
    "ds_read_b64_tr_b16 v[190:191], v7 offset:13312\n"
    "s_waitcnt lgkmcnt(0)\n"
    "ds_write_b128 v72, v[16:19] offset:18432\n"
    "ds_write_b128 v72, v[20:23] offset:19456\n"
    "s_waitcnt lgkmcnt(0)\n"
    "s_add_u32 s54, s27, 0\n"
    "s_add_u32 s32, s30, s54\n"
    "s_addc_u32 s33, s31, 0\n"
    "s_and_b32 s33, s33, 0xffff\n"
    "s_mov_b32 s34, 1572864\n"
    "s_mov_b32 s35, 0x20000\n"
    "v_and_b32 v7, 1, v1\n"
    "v_mul_u32_u24 v7, 0xaaaa, v7\n"
    "v_add_u32 v2, 0x4444, v7\n"
    "v_lshlrev_b32 v3, 4, v1\n"
    "s_and_b32 s54, s25, 1\n"
    "s_lshl_b32 s54, s54, 11\n"
    "s_lshr_b32 s55, s25, 1\n"
    "s_lshl_b32 s55, s55, 4\n"
    "s_add_u32 s54, s54, s55\n"
    "s_add_u32 s54, s54, 16384\n"
    "v_bfe_u32 v4, v1, 0, 4\n"
    "v_lshlrev_b32 v4, 5, v4\n"
    "v_bfe_u32 v7, v1, 4, 2\n"
    "v_lshl_add_u32 v4, v7, 2, v4\n"
    "v_add_u32 v4, s54, v4\n"
    "v_bfe_u32 v7, v1, 2, 2\n"
    "v_lshlrev_b32 v7, 3, v7\n"
    "v_xor_b32 v4, v4, v7\n"
    "s_lshr_b32 s63, s24, 3\n"
    "s_xor_b32 s66, s63, s25\n"
    "s_and_b32 s54, s66, 1\n"
    "s_lshl_b32 s54, s54, 14\n"
    "s_bfe_u32 s55, s66, 65537\n"
    "s_lshl_b32 s55, s55, 12\n"
    "s_add_u32 s54, s54, s55\n"
    "s_lshl_b32 s55, s25, 15\n"
    "s_add_u32 s54, s54, s55\n"
    "s_add_u32 s54, s54, 1048576\n"
    "v_bfe_u32 v5, v1, 5, 1\n"
    "v_lshlrev_b32 v5, 9, v5\n"
    "v_bfe_u32 v7, v1, 3, 1\n"
    "v_lshl_add_u32 v5, v7, 8, v5\n"
    "v_bfe_u32 v7, v1, 1, 2\n"
    "v_lshl_add_u32 v5, v7, 6, v5\n"
    "v_bfe_u32 v7, v1, 4, 1\n"
    "v_lshl_add_u32 v5, v7, 5, v5\n"
    "v_bfe_u32 v7, v1, 0, 1\n"
    "v_lshl_add_u32 v5, v7, 4, v5\n"
    "v_add_u32 v5, s54, v5\n"
    "s_lshl_b32 s54, s63, 15\n"
    "s_and_b32 s55, s24, 7\n"
    "s_lshl_b32 s55, s55, 10\n"
    "s_add_u32 s54, s54, s55\n"
    "s_lshl_b32 s55, s25, 8\n"
    "s_add_u32 s54, s54, s55\n"
    "s_and_b32 s55, s63, 1\n"
    "s_lshl_b32 s55, s55, 14\n"
    "s_add_u32 s54, s54, s55\n"
    "v_bfe_u32 v6, v1, 2, 1\n"
    "v_lshlrev_b32 v6, 14, v6\n"
    "v_bfe_u32 v7, v1, 3, 1\n"
    "v_lshl_add_u32 v6, v7, 8, v6\n"
    "v_bfe_u32 v7, v1, 4, 2\n"
    "v_lshl_add_u32 v6, v7, 5, v6\n"
    "v_bfe_u32 v7, v1, 0, 1\n"
    "v_lshl_add_u32 v6, v7, 4, v6\n"
    "v_bfe_u32 v7, v1, 1, 1\n"
    "v_lshl_add_u32 v6, v7, 7, v6\n"
    "v_xor_b32 v6, s54, v6\n"
    "s_lshr_b32 s54, s24, 2\n"
    "s_lshl_b32 s54, s54, 14\n"
    "s_and_b32 s55, s24, 3\n"
    "s_lshl_b32 s55, s55, 11\n"
    "s_add_u32 s54, s54, s55\n"
    "s_lshl_b32 s55, s25, 9\n"
    "s_add_u32 s54, s54, s55\n"
    "s_add_u32 s72, s54, 262144\n"
    "s_lshl_b32 s63, s25, 10\n"
    "s_mov_b32 s68, 1040356334\n"
    "s_mov_b32 s69, 1040356334\n"
    "s_and_b32 s54, s25, 1\n"
    "s_lshl_b32 s54, s54, 4\n"
    "v_bfe_u32 v7, v1, 0, 4\n"
    "v_add_u32 v7, s54, v7\n"
    "v_bfe_u32 v8, v1, 4, 2\n"
    "v_lshlrev_b32 v8, 2, v8\n"
    "v_sub_u32 v7, v7, v8\n"
    "v_cmp_le_i32 s[76:77], 0, v7\n"
    "v_cmp_le_i32 s[78:79], 1, v7\n"
    "v_cmp_le_i32 s[80:81], 2, v7\n"
    "v_cmp_le_i32 s[82:83], 3, v7\n"
    "v_cmp_le_i32 s[84:85], 16, v7\n"
    "v_cmp_le_i32 s[86:87], 17, v7\n"
    "v_cmp_le_i32 s[88:89], 18, v7\n"
    "v_cmp_le_i32 s[90:91], 19, v7\n"
    "s_mov_b32 s92, 0\n"
    "s_mov_b32 s93, -1\n"
    "s_mov_b32 s94, 0x00ff00ff\n"
    "s_mov_b32 s95, 0x00ff00ff\n"
    "s_lshl_b32 s54, s25, 10\n"
    "s_add_u32 s96, s27, 16384\n"
    "s_add_u32 s96, s96, s54\n"
    "s_lshl_b32 s54, s24, 7\n"
    "s_add_u32 s97, s27, 49152\n"
    "s_add_u32 s97, s97, s54\n"
    "s_add_u32 s99, s54, 4096\n"
    "s_add_u32 s20, s30, s97\n"
    "s_addc_u32 s21, s31, 0\n"
    "s_and_b32 s21, s21, 0xffff\n"
    "s_mov_b32 s22, 16\n"
    "s_mov_b32 s23, 0x20000\n"
    "s_cmp_lg_u32 s25, 0\n"
    "s_cselect_b32 s22, 0, 16\n"
    "s_cselect_b32 s97, 0x100000, s97\n"
    "s_lshl_b32 s54, s25, 2\n"
    "s_add_u32 s99, s99, s54\n"
    "v_bfe_u32 v0, v1, 2, 2\n"
    "v_lshlrev_b32 v0, 11, v0\n"
    "v_bfe_u32 v7, v1, 0, 2\n"
    "v_lshl_add_u32 v0, v7, 2, v0\n"
    "s_lshl_b32 s54, s24, 6\n"
    "s_add_u32 s54, s54, s27\n"
    "s_add_u32 s70, s54, 32768\n"
    "s_mov_b32 s56, 0\n"
    "s_mov_b32 s64, 0\n"
    "v_mov_b64 v[60:61], -1\n"
    "v_mov_b64 v[62:63], -1\n"
    "s_mov_b32 s54, 524288\n"
    "buffer_store_dwordx4 v[60:63], v6, s[32:35], s54 offen sc1\n"
    "s_waitcnt vmcnt(0)\n"
    "s_barrier\n"
    "s_mov_b32 s98, 1\n"
    "v_bfe_u32 v7, v3, 4, 3\n"
    "v_lshlrev_b32 v7, 7, v7\n"
    "v_bfe_u32 v8, v3, 7, 1\n"
    "v_lshl_add_u32 v7, v8, 12, v7\n"
    "s_mov_b64 exec, 65535\n"
    "buffer_load_dword v65, v7, s[48:51], s96 offen sc1\n"
    ".Lqk_ibl_%=:\n"
    "s_waitcnt vmcnt(0)\n"
    "v_cmp_gt_u32 vcc, s98, v65\n"
    "s_cbranch_vccz .Lqk_ibok_%=\n"
    "buffer_load_dword v65, v7, s[48:51], s96 offen sc1\n"
    "s_sub_u32 s26, s26, 1\n"
    "s_cbranch_scc0 .Lqk_ibl_%=\n"
    "s_endpgm\n"
    ".Lqk_ibok_%=:\n"
    "s_mov_b64 exec, -1\n"
    "s_add_u32 s96, s27, 49152\n"
    "s_and_b32 s54, s56, 3\n"
    "s_lshl_b32 s57, s54, 17\n"
    "s_lshl_b32 s54, s54, 17\n"
    "s_add_u32 s58, s54, 524288\n"
    "s_add_u32 s54, s54, 393216\n"
    "s_and_b32 s54, s54, 393216\n"
    "s_add_u32 s59, s54, 524288\n"
    "s_mov_b32 s59, 786432\n"
    "s_lshl_b32 s54, s75, 10\n"
    "s_add_u32 s57, s57, s54\n"
    "s_xor_b32 s52, s57, 1024\n"
    "s_xor_b32 s53, s57, 2048\n"
    "s_xor_b32 s71, s57, 3072\n"
    "s_and_b32 s54, s56, 1\n"
    "s_lshl_b32 s54, s54, 17\n"
    "s_add_u32 s60, s54, s72\n"
    "s_xor_b32 s61, s60, 512\n"
    "s_add_u32 s66, s64, 81920\n"
    "s_and_b32 s54, s25, 1\n"
    "s_lshl_b32 s54, s54, 12\n"
    "s_add_u32 s54, s54, s64\n"
    "s_add_u32 s65, s54, 40960\n"
    "s_lshl_b32 s54, s25, 11\n"
    "s_add_u32 s54, s54, s64\n"
    "s_add_u32 s67, s54, 122880\n"
    "s_add_u32 s98, s56, 1\n"
    "s_mov_b32 s28, 0x10000000\n"
    "s_mov_b32 s29, 0x10000000\n"
    "s_and_b32 s54, s25, 1\n"
    "s_lshl_b32 s54, s54, 2\n"
    "s_add_u32 s54, s54, s25\n"
    "s_lshl_b32 s55, s24, 3\n"
    "s_add_u32 s54, s54, s55\n"
    "s_lshl_b32 s72, s54, 10\n"
    "s_cmp_eq_u32 s25, 0\n"
    "s_cbranch_scc1 .Lqk_i0_%=\n"
    "s_cmp_eq_u32 s25, 3\n"
    "s_cselect_b32 s54, 0, s25\n"
    "s_mul_i32 s73, s54, 40960\n"
    "s_add_u32 s73, s73, 40960\n"
    "s_lshl_b32 s54, s54, 13\n"
    "s_mul_i32 s74, s24, 24576\n"
    "s_add_u32 s74, s74, s54\n"
    "s_mov_b32 s36, 1\n"
    "s_add_u32 s54, s56, 0\n"
    ".Lqk_f0chk_%=:\n"
    "s_and_b32 s36, s36, 0xfff\n"
    "s_cbranch_scc1 .Lqk_f0poll_%=\n"
    "s_mul_i32 s54, s54, 786432\n"
    "s_add_u32 s54, s54, s74\n"
    "s_add_u32 s55, s64, s73\n"
    "s_add_u32 m0, s55, 0\n"
    "s_nop 0\n"
    "buffer_load_dwordx4 v3, s[40:43], s54 offen offset:0 sc1 lds\n"
    "buffer_load_dwordx4 v3, s[40:43], s54 offen offset:1024 sc1 lds\n"
    "buffer_load_dwordx4 v3, s[40:43], s54 offen offset:2048 sc1 lds\n"
    "buffer_load_dwordx4 v3, s[40:43], s54 offen offset:3072 sc1 lds\n"
    "s_add_u32 m0, s55, 4096\n"
    "s_add_u32 s54, s54, 4096\n"
    "buffer_load_dwordx4 v3, s[40:43], s54 offen offset:0 sc1 lds\n"
    "buffer_load_dwordx4 v3, s[40:43], s54 offen offset:1024 sc1 lds\n"
    "buffer_load_dwordx4 v3, s[40:43], s54 offen offset:2048 sc1 lds\n"
    "buffer_load_dwordx4 v3, s[40:43], s54 offen offset:3072 sc1 lds\n"
    "s_mov_b64 exec, 0xfff\n"
    "buffer_load_dword v11, v0, s[48:51], s70 offen sc1\n"
    "s_mov_b64 exec, -1\n"
    "s_barrier\n"
    "s_branch .Lqk_i1_%=\n"
    ".Lqk_i0_%=:\n"
    "s_barrier\n"
    "s_memtime s[16:17]\n"
    "s_memrealtime s[0:1]\n"
    "s_nop 2\n"
    "s_memrealtime s[2:3]\n"
    "s_nop 2\n"
    "s_memrealtime s[4:5]\n"
    "s_nop 2\n"
    "s_memrealtime s[6:7]\n"
    "s_nop 2\n"
    "s_memrealtime s[8:9]\n"
    "s_nop 2\n"
    "s_memrealtime s[10:11]\n"
    "s_nop 2\n"
    "s_memrealtime s[12:13]\n"
    "s_nop 2\n"
    "s_memrealtime s[14:15]\n"
    "s_mov_b32 s18, 0\n"
    "s_waitcnt lgkmcnt(0)\n"
    "s_cmp_eq_u32 s2, s0\n"
    "s_addc_u32 s18, s18, 0\n"
    "s_cmp_eq_u32 s4, s0\n"
    "s_addc_u32 s18, s18, 0\n"
    "s_cmp_eq_u32 s6, s0\n"
    "s_addc_u32 s18, s18, 0\n"
    "s_cmp_eq_u32 s8, s0\n"
    "s_addc_u32 s18, s18, 0\n"
    "s_cmp_eq_u32 s10, s0\n"
    "s_addc_u32 s18, s18, 0\n"
    "s_cmp_eq_u32 s12, s0\n"
    "s_addc_u32 s18, s18, 0\n"
    "s_cmp_eq_u32 s14, s0\n"
    "s_addc_u32 s18, s18, 0\n"
    "s_lshl_b32 s18, s18, 4\n"
    "s_add_u32 s16, s16, s18\n"
    "s_add_u32 s0, s0, 4\n"
    "s_lshl_b32 s0, s0, 4\n"
    "s_sub_u32 s18, s0, s38\n"
    "s_sub_u32 s19, s16, s39\n"
    "s_mov_b32 s38, s0\n"
    "s_mov_b32 s39, s16\n"
    "s_mov_b32 s27, 45590\n"
    "s_mov_b32 s75, 94207\n"
    "s_mov_b32 s18, 1600\n"
    "s_mov_b32 s19, 2300\n"
    ".Lqk_i1_%=:\n"
    ".Lqk_step_%=:\n"
    "v_add_u32 v10, s62, v3\n"
    "ds_read_b128 v[44:47], v10 offset:0\n"
    "ds_read_b128 v[48:51], v10 offset:1024\n"
    "ds_read_b128 v[52:55], v10 offset:2048\n"
    "ds_read_b128 v[56:59], v10 offset:3072\n"
    "s_cmp_lg_u32 s25, 0\n"
    "s_cbranch_scc1 .Lqk_mae_%=\n"
    "s_cmp_eq_u32 s56, 0\n"
    "s_cbranch_scc1 .Lqk_mae_%=\n"
    "s_lshl_b32 s6, s56, 20\n"
    ".Lqk_mac_%=:\n"
    "s_waitcnt vmcnt(0)\n"
    "v_cmp_gt_u32 vcc, s6, v1\n"
    "v_subrev_u32 v0, s38, v0\n"
    "v_and_b32 v1, 0xfffff, v1\n"
    "s_cmp_lg_u32 vcc_lo, 0\n"
    "s_cbranch_scc1 .Lqk_map_%=\n"
    "v_max_i32_dpp v0, v0, v0 row_ror:1 row_mask:0xf bank_mask:0xf\n"
    "v_max_i32_dpp v1, v1, v1 row_ror:1 row_mask:0xf bank_mask:0xf\n"
    "s_nop 0\n"
    "v_max_i32_dpp v0, v0, v0 row_ror:2 row_mask:0xf bank_mask:0xf\n"
    "v_max_i32_dpp v1, v1, v1 row_ror:2 row_mask:0xf bank_mask:0xf\n"
    "s_nop 0\n"
    "v_max_i32_dpp v0, v0, v0 row_ror:4 row_mask:0xf bank_mask:0xf\n"
    "v_max_i32_dpp v1, v1, v1 row_ror:4 row_mask:0xf bank_mask:0xf\n"
    "s_nop 0\n"
    "v_max_i32_dpp v0, v0, v0 row_ror:8 row_mask:0xf bank_mask:0xf\n"
    "v_max_i32_dpp v1, v1, v1 row_ror:8 row_mask:0xf bank_mask:0xf\n"
    "v_readlane_b32 s0, v0, 0\n"
    "v_readlane_b32 s1, v0, 16\n"
    "v_readlane_b32 s2, v1, 0\n"
    "v_readlane_b32 s3, v1, 16\n"
    "s_nop 1\n"
    "s_max_i32 s0, s0, s1\n"
    "s_max_i32 s2, s2, s3\n"
    "s_add_u32 s0, s0, s2\n"
    "s_max_i32 s0, s0, 0\n"
    "s_min_i32 s0, s0, 16000\n"
    "s_mul_i32 s0, s0, s75\n"
    "s_lshr_b32 s0, s0, 16\n"
    "s_add_u32 s0, s0, s39\n"
    "s_sub_u32 s0, s0, 100\n"
    "s_getpc_b64 s[4:5]\n"
    ".Lqk_maw_%=:\n"
    "s_memtime s[2:3]\n"
    "s_waitcnt lgkmcnt(0)\n"
    "s_sub_u32 s2, s0, s2\n"
    "s_cmp_lt_i32 s2, 512\n"
    "s_cbranch_scc1 .Lqk_maf_%=\n"
    "s_sleep 4\n"
    "s_sub_u32 s26, s26, 1\n"
    "s_cbranch_scc0 .Lqk_maw_%=\n"
    "s_endpgm\n"
    ".Lqk_map_%=:\n"
    "s_sleep 1\n"
    "v_lshlrev_b32 v7, 3, v3\n"
    "s_mov_b32 exec_hi, 0\n"
    "buffer_load_dwordx2 v[0:1], v7, s[48:51], s96 offen sc1\n"
    "s_mov_b64 exec, -1\n"
    "s_sub_u32 s26, s26, 1\n"
    "s_cbranch_scc0 .Lqk_mac_%=\n"
    "s_endpgm\n"
    ".Lqk_maf_%=:\n"
    "s_max_i32 s2, s2, 0\n"
    "s_lshr_b32 s2, s2, 3\n"
    "s_lshl_b32 s2, s2, 2\n"
    "s_sub_u32 s2, .Lqk_mae_%=-.Lqk_maw_%=, s2\n"
    "s_add_u32 s4, s4, s2\n"
    "s_addc_u32 s5, s5, 0\n"
    "s_setpc_b64 s[4:5]\n"
    "s_nop 1\n"
    "s_nop 1\n"
    "s_nop 1\n"
    "s_nop 1\n"
    "s_nop 1\n"
    "s_nop 1\n"
    "s_nop 1\n"
    "s_nop 1\n"
    "s_nop 1\n"
    "s_nop 1\n"
    "s_nop 1\n"
    "s_nop 1\n"
    "s_nop 1\n"
    "s_nop 1\n"
    "s_nop 1\n"
    "s_nop 1\n"
    "s_nop 1\n"
    "s_nop 1\n"
    "s_nop 1\n"
    "s_nop 1\n"
    "s_nop 1\n"
    "s_nop 1\n"
    "s_nop 1\n"
    "s_nop 1\n"
    "s_nop 1\n"
    "s_nop 1\n"
    "s_nop 1\n"
    "s_nop 1\n"
    "s_nop 1\n"
    "s_nop 1\n"
    "s_nop 1\n"
    "s_nop 1\n"
    "s_nop 1\n"
    "s_nop 1\n"
    "s_nop 1\n"
    "s_nop 1\n"
    "s_nop 1\n"
    "s_nop 1\n"
    "s_nop 1\n"
    "s_nop 1\n"
    "s_nop 1\n"
    "s_nop 1\n"
    "s_nop 1\n"
    "s_nop 1\n"
    "s_nop 1\n"
    "s_nop 1\n"
    "s_nop 1\n"
    "s_nop 1\n"
    "s_nop 1\n"
    "s_nop 1\n"
    "s_nop 1\n"
    "s_nop 1\n"
    "s_nop 1\n"
    "s_nop 1\n"
    "s_nop 1\n"
    "s_nop 1\n"
    "s_nop 1\n"
    "s_nop 1\n"
    "s_nop 1\n"
    "s_nop 1\n"
    "s_nop 1\n"
    "s_nop 1\n"
    "s_nop 1\n"
    "s_nop 1\n"
    ".Lqk_mae_%=:\n"
    "s_mov_b32 m0, s63\n"
    "s_barrier\n"
    "buffer_load_dwordx4 v5, s[32:35], s57 offen sc1 lds\n"
    "s_add_u32 m0, s63, 4096\n"
    "v_mov_b64 v[12:13], 0\n"
    "v_mov_b64 v[14:15], 0\n"
    "v_mov_b64 v[16:17], 0\n"
    "v_mov_b64 v[18:19], 0\n"
    "v_mov_b64 v[20:21], 0\n"
    "v_mov_b64 v[22:23], 0\n"
    "v_mov_b64 v[24:25], 0\n"
    "v_mov_b64 v[26:27], 0\n"
    "s_nop 4\n"
    "buffer_load_dwordx4 v5, s[32:35], s52 offen sc1 lds\n"
    "s_add_u32 m0, s63, 8192\n"
    "v_lshrrev_b32 v9, 4, v3\n"
    "v_and_b32 v9, 0x30, v9\n"
    "v_xor_b32 v9, v9, v3\n"
    "v_lshrrev_b32 v9, 1, v9\n"
    "v_mov_b32 v7, 4048155338\n"
    "v_cndmask_b32 v68, v7, 0, s[92:93]\n"
    "v_cndmask_b32 v69, v7, 0, s[92:93]\n"
    "v_cndmask_b32 v70, v7, 0, s[92:93]\n"
    "v_cndmask_b32 v71, v7, 0, s[92:93]\n"
    "v_and_b32 v8, 0x1ff, v3\n"
    "s_nop 2\n"
    "buffer_load_dwordx4 v5, s[32:35], s53 offen sc1 lds\n"
    "s_add_u32 m0, s63, 12288\n"
    "v_cvt_f32_u32 v61, s19\n"
    "v_cvt_f32_u32 v60, s18\n"
    "v_rcp_f32 v61, v61\n"
    "s_nop 0\n"
    "v_mul_f32 v60, v60, v61\n"
    "v_med3_f32 v60, v60, 0.5, 1.0\n"
    "v_rcp_f32 v61, v60\n"
    "v_mul_f32 v60, 0x47800000, v60\n"
    "v_mul_f32 v61, 0x47800000, v61\n"
    "v_cvt_u32_f32 v60, v60\n"
    "v_cvt_u32_f32 v61, v61\n"
    "v_readfirstlane_b32 s27, v60\n"
    "v_readfirstlane_b32 s75, v61\n"
    "buffer_load_dwordx4 v5, s[32:35], s71 offen sc1 lds\n"
    "s_barrier\n"
    "buffer_store_dwordx4 v[72:75], v3, s[44:47], s28 offen sc1\n"
    "s_waitcnt vmcnt(4)\n"
    ".Lqk_mjoin_%=:\n"
    "s_barrier\n"
    "ds_read_b128 v[28:31], v3 offset:0\n"
    "ds_read_b128 v[32:35], v3 offset:1024\n"
    "ds_read_b128 v[36:39], v3 offset:2048\n"
    "ds_read_b128 v[40:43], v3 offset:3072\n"
    "buffer_store_dwordx4 v[76:79], v3, s[44:47], s29 offen sc1\n"
    "s_waitcnt lgkmcnt(3)\n"
    "v_smfmac_f32_16x16x64_bf16 v[12:15], v[28:31], a[0:7], v2\n"
    "s_min_u32 m0, s56, 2\n"
    "s_sub_u32 m0, 2, m0\n"
    "v_max3_u32 v7, v28, v29, v30\n"
    "v_smfmac_f32_16x16x64_bf16 v[16:19], v[28:31], a[8:15], v2\n"
    "s_mul_i32 m0, m0, 112\n"
    "v_max_u32 v7, v7, v31\n"
    "v_smfmac_f32_16x16x64_bf16 v[20:23], v[28:31], a[16:23], v2\n"
    "s_waitcnt vmcnt(4)\n"
    "s_barrier\n"
    "s_memtime s[0:1]\n"
    "v_smfmac_f32_16x16x64_bf16 v[24:27], v[28:31], a[24:31], v2\n"
    "ds_read_b128 v[28:31], v3 offset:4096\n"
    "s_waitcnt lgkmcnt(3)\n"
    "v_smfmac_f32_16x16x64_bf16 v[12:15], v[32:35], a[32:39], v2\n"
    "s_lshl_b32 s54, s98, 20\n"
    "s_mul_i32 s55, s27, 1000\n"
    "v_max3_u32 v7, v32, v33, v7\n"
    "v_smfmac_f32_16x16x64_bf16 v[16:19], v[32:35], a[40:47], v2\n"
    "s_lshr_b32 s55, s55, 16\n"
    "s_add_u32 s55, s55, 408\n"
    "v_max3_u32 v7, v34, v35, v7\n"
    "v_smfmac_f32_16x16x64_bf16 v[20:23], v[32:35], a[48:55], v2\n"
    "s_or_b32 s55, s55, s54\n"
    "v_cmp_ge_u32 s[36:37], s98, v11\n"
    "v_smfmac_f32_16x16x64_bf16 v[24:27], v[32:35], a[56:63], v2\n"
    "ds_read_b128 v[32:35], v3 offset:5120\n"
    "s_waitcnt lgkmcnt(3)\n"
    "v_smfmac_f32_16x16x64_bf16 v[12:15], v[36:39], a[64:71], v2\n"
    "s_add_u32 s55, s55, m0\n"
    "v_max3_u32 v7, v36, v37, v7\n"
    "v_smfmac_f32_16x16x64_bf16 v[16:19], v[36:39], a[72:79], v2\n"
    "v_max3_u32 v7, v38, v39, v7\n"
    "v_smfmac_f32_16x16x64_bf16 v[20:23], v[36:39], a[80:87], v2\n"
    "s_sub_u32 s54, s0, s39\n"
    "s_add_u32 s54, s54, 1900\n"
    "s_mul_i32 s54, s54, s27\n"
    "v_smfmac_f32_16x16x64_bf16 v[24:27], v[36:39], a[88:95], v2\n"
    "ds_read_b128 v[36:39], v3 offset:6144\n"
    "s_waitcnt lgkmcnt(3)\n"
    "v_smfmac_f32_16x16x64_bf16 v[12:15], v[40:43], a[96:103], v2\n"
    "s_lshr_b32 s54, s54, 16\n"
    "s_add_u32 s54, s54, s38\n"
    "v_max3_u32 v7, v40, v41, v7\n"
    "v_smfmac_f32_16x16x64_bf16 v[16:19], v[40:43], a[104:111], v2\n"
    "s_add_u32 s54, s54, 184\n"
    "s_add_u32 s54, s54, m0\n"
    "v_max3_u32 v7, v42, v43, v7\n"
    "v_smfmac_f32_16x16x64_bf16 v[20:23], v[40:43], a[112:119], v2\n"
    "v_cmp_eq_u32 vcc, -1, v7\n"
    "v_smfmac_f32_16x16x64_bf16 v[24:27], v[40:43], a[120:127], v2\n"
    "ds_read_b128 v[40:43], v3 offset:7168\n"
    "s_waitcnt lgkmcnt(3)\n"
    "v_mov_b64 v[66:67], s[54:55]\n"
    "v_smfmac_f32_16x16x64_bf16 v[12:15], v[28:31], a[128:135], v2\n"
    "s_cbranch_vccnz .Lqk_mretry_%=\n"
    "v_max3_u32 v7, v28, v29, v7\n"
    "v_smfmac_f32_16x16x64_bf16 v[16:19], v[28:31], a[136:143], v2\n"
    "v_max3_u32 v7, v30, v31, v7\n"
    "v_smfmac_f32_16x16x64_bf16 v[20:23], v[28:31], a[144:151], v2\n"
    "s_waitcnt vmcnt(2)\n"
    "s_barrier\n"
    "v_smfmac_f32_16x16x64_bf16 v[24:27], v[28:31], v[44:51], v2\n"
    "ds_read_b128 v[44:47], v10 offset:4096\n"
    "s_waitcnt lgkmcnt(3)\n"
    "v_smfmac_f32_16x16x64_bf16 v[12:15], v[32:35], a[152:159], v2\n"
    "ds_read_b128 v[48:51], v10 offset:5120\n"
    "v_max3_u32 v7, v32, v33, v7\n"
    "v_smfmac_f32_16x16x64_bf16 v[16:19], v[32:35], a[160:167], v2\n"
    "ds_read_b128 v[28:31], v3 offset:8192\n"
    "v_max3_u32 v7, v34, v35, v7\n"
    "v_smfmac_f32_16x16x64_bf16 v[20:23], v[32:35], a[168:175], v2\n"
    "buffer_store_dwordx2 v[66:67], v3, s[20:23], 0 offen sc1\n"
    "v_smfmac_f32_16x16x64_bf16 v[24:27], v[32:35], v[52:59], v2\n"
    "ds_read_b128 v[52:55], v10 offset:6144\n"
    "s_waitcnt lgkmcnt(5)\n"
    "v_smfmac_f32_16x16x64_bf16 v[12:15], v[36:39], a[176:183], v2\n"
    "ds_read_b128 v[56:59], v10 offset:7168\n"
    "v_max3_u32 v7, v36, v37, v7\n"
    "v_smfmac_f32_16x16x64_bf16 v[16:19], v[36:39], a[184:191], v2\n"
    "ds_read_b128 v[32:35], v3 offset:9216\n"
    "v_max3_u32 v7, v38, v39, v7\n"
    "v_smfmac_f32_16x16x64_bf16 v[20:23], v[36:39], a[192:199], v2\n"
    "s_waitcnt lgkmcnt(4)\n"
    "v_smfmac_f32_16x16x64_bf16 v[24:27], v[36:39], v[44:51], v2\n"
    "ds_read_b128 v[44:47], v10 offset:8192\n"
    "v_smfmac_f32_16x16x64_bf16 v[12:15], v[40:43], a[200:207], v2\n"
    "ds_read_b128 v[48:51], v10 offset:9216\n"
    "v_max3_u32 v7, v40, v41, v7\n"
    "v_smfmac_f32_16x16x64_bf16 v[16:19], v[40:43], a[208:215], v2\n"
    "ds_read_b128 v[36:39], v3 offset:10240\n"
    "v_max3_u32 v7, v42, v43, v7\n"
    "v_smfmac_f32_16x16x64_bf16 v[20:23], v[40:43], a[216:223], v2\n"
    "s_waitcnt lgkmcnt(4)\n"
    "v_smfmac_f32_16x16x64_bf16 v[24:27], v[40:43], v[52:59], v2\n"
    "ds_read_b128 v[52:55], v10 offset:10240\n"
    "v_smfmac_f32_16x16x64_bf16 v[12:15], v[28:31], a[224:231], v2\n"
    "ds_read_b128 v[56:59], v10 offset:11264\n"
    "v_max3_u32 v7, v28, v29, v7\n"
    "v_smfmac_f32_16x16x64_bf16 v[16:19], v[28:31], a[232:239], v2\n"
    "ds_read_b128 v[40:43], v3 offset:11264\n"
    "v_max3_u32 v7, v30, v31, v7\n"
    "v_smfmac_f32_16x16x64_bf16 v[20:23], v[28:31], a[240:247], v2\n"
    "s_waitcnt lgkmcnt(4)\n"
    "v_smfmac_f32_16x16x64_bf16 v[24:27], v[28:31], v[44:51], v2\n"
    "ds_read_b128 v[44:47], v10 offset:12288\n"
    "v_smfmac_f32_16x16x64_bf16 v[12:15], v[32:35], a[248:255], v2\n"
    "ds_read_b128 v[48:51], v10 offset:13312\n"
    "v_max3_u32 v7, v32, v33, v7\n"
    "v_smfmac_f32_16x16x64_bf16 v[16:19], v[32:35], v[80:87], v2\n"
    "ds_read_b128 v[28:31], v3 offset:12288\n"
    "v_max3_u32 v7, v34, v35, v7\n"
    "v_smfmac_f32_16x16x64_bf16 v[20:23], v[32:35], v[88:95], v2\n"
    "s_waitcnt lgkmcnt(4)\n"
    "v_smfmac_f32_16x16x64_bf16 v[24:27], v[32:35], v[52:59], v2\n"
    "ds_read_b128 v[52:55], v10 offset:14336\n"
    "v_smfmac_f32_16x16x64_bf16 v[12:15], v[36:39], v[96:103], v2\n"
    "ds_read_b128 v[56:59], v10 offset:15360\n"
    "v_max3_u32 v7, v36, v37, v7\n"
    "v_smfmac_f32_16x16x64_bf16 v[16:19], v[36:39], v[104:111], v2\n"
    "ds_read_b128 v[32:35], v3 offset:13312\n"
    "v_max3_u32 v7, v38, v39, v7\n"
    "v_smfmac_f32_16x16x64_bf16 v[20:23], v[36:39], v[112:119], v2\n"
    "s_waitcnt lgkmcnt(4)\n"
    "v_smfmac_f32_16x16x64_bf16 v[24:27], v[36:39], v[44:51], v2\n"
    "ds_read_b128 v[44:47], v10 offset:16384\n"
    "v_smfmac_f32_16x16x64_bf16 v[12:15], v[40:43], v[120:127], v2\n"
    "ds_read_b128 v[48:51], v10 offset:17408\n"
    "v_max3_u32 v7, v40, v41, v7\n"
    "v_smfmac_f32_16x16x64_bf16 v[16:19], v[40:43], v[128:135], v2\n"
    "ds_read_b128 v[36:39], v3 offset:14336\n"
    "v_max3_u32 v7, v42, v43, v7\n"
    "v_smfmac_f32_16x16x64_bf16 v[20:23], v[40:43], v[136:143], v2\n"
    "s_waitcnt lgkmcnt(4)\n"
    "v_smfmac_f32_16x16x64_bf16 v[24:27], v[40:43], v[52:59], v2\n"
    "ds_read_b128 v[52:55], v10 offset:18432\n"
    "v_smfmac_f32_16x16x64_bf16 v[16:19], v[28:31], v[144:151], v2\n"
    "ds_read_b128 v[56:59], v10 offset:19456\n"
    "s_waitcnt lgkmcnt(5)\n"
    "v_max3_u32 v7, v28, v29, v7\n"
    "v_smfmac_f32_16x16x64_bf16 v[16:19], v[32:35], v[168:175], v2\n"
    "ds_read_b128 v[40:43], v3 offset:15360\n"
    "s_waitcnt lgkmcnt(4)\n"
    "v_max3_u32 v7, v30, v31, v7\n"
    "v_smfmac_f32_16x16x64_bf16 v[12:15], v[28:31], v[44:51], v2\n"
    "s_waitcnt lgkmcnt(3)\n"
    "v_add_u32 v10, s66, v3\n"
    "v_smfmac_f32_16x16x64_bf16 v[16:19], v[36:39], v[200:207], v2\n"
    "ds_read_b128 v[44:47], v10 offset:0\n"
    "v_max3_u32 v7, v32, v33, v7\n"
    "v_smfmac_f32_16x16x64_bf16 v[12:15], v[36:39], v[192:199], v2\n"
    "ds_read_b128 v[48:51], v10 offset:1024\n"
    "v_max3_u32 v7, v34, v35, v7\n"
    "v_smfmac_f32_16x16x64_bf16 v[20:23], v[28:31], v[152:159], v2\n"
    "s_waitcnt lgkmcnt(3)\n"
    "v_max3_u32 v7, v36, v37, v7\n"
    "v_smfmac_f32_16x16x64_bf16 v[12:15], v[32:35], v[52:59], v2\n"
    "ds_read_b128 v[52:55], v10 offset:2048\n"
    "s_waitcnt lgkmcnt(3)\n"
    "v_max3_u32 v7, v38, v39, v7\n"
    "v_smfmac_f32_16x16x64_bf16 v[16:19], v[40:43], v[232:239], v2\n"
    "ds_read_b128 v[56:59], v10 offset:3072\n"
    "v_max3_u32 v7, v40, v41, v7\n"
    "v_smfmac_f32_16x16x64_bf16 v[12:15], v[40:43], v[224:231], v2\n"
    "v_max3_u32 v7, v42, v43, v7\n"
    "v_smfmac_f32_16x16x64_bf16 v[20:23], v[32:35], v[176:183], v2\n"
    "v_cmp_eq_u32 vcc, -1, v7\n"
    "v_smfmac_f32_16x16x64_bf16 v[20:23], v[36:39], v[208:215], v2\n"
    "v_smfmac_f32_16x16x64_bf16 v[20:23], v[40:43], v[240:247], v2\n"
    "s_cbranch_vccnz .Lqk_mretry_%=\n"
    "v_smfmac_f32_16x16x64_bf16 v[24:27], v[28:31], v[160:167], v2\n"
    "v_add_f32 v16, v16, v17\n"
    "v_smfmac_f32_16x16x64_bf16 v[24:27], v[32:35], v[184:191], v2\n"
    "v_add_f32 v18, v18, v19\n"
    "v_smfmac_f32_16x16x64_bf16 v[24:27], v[36:39], v[216:223], v2\n"
    "v_cvt_pk_bf16_f32 v17, v16, v18\n"
    "v_smfmac_f32_16x16x64_bf16 v[24:27], v[40:43], v[248:255], v2\n"
    "v_add_f32 v12, v12, v13\n"
    "v_add_f32 v14, v14, v15\n"
    "v_cvt_pk_bf16_f32 v13, v12, v14\n"
    "ds_write2_b32 v4, v13, v17 offset0:0 offset1:128\n"
    "v_add_f32 v20, v20, v21\n"
    "v_add_f32 v22, v22, v23\n"
    "v_cvt_pk_bf16_f32 v21, v20, v22\n"
    "s_nop 0\n"
    "v_add_f32 v24, v24, v25\n"
    "v_add_f32 v26, v26, v27\n"
    "v_cvt_pk_bf16_f32 v25, v24, v26\n"
    "ds_write2st64_b32 v4, v21, v25 offset0:4 offset1:6\n"
    "buffer_load_dwordx4 v[72:75], v8, s[32:35], s60 offen sc1\n"
    "s_cmp_eq_u32 s56, 63\n"
    "s_waitcnt lgkmcnt(0)\n"
    "s_barrier\n"
    "ds_read_b64_tr_b16 v[12:13], v9 offset:16384\n"
    "ds_read_b64_tr_b16 v[14:15], v9 offset:16896\n"
    "ds_read_b64_tr_b16 v[16:17], v9 offset:17408\n"
    "ds_read_b64_tr_b16 v[18:19], v9 offset:17920\n"
    "buffer_load_dwordx4 v[76:79], v8, s[32:35], s61 offen sc1\n"
    "s_cbranch_scc1 .Lqk_last_%=\n"
    "s_waitcnt lgkmcnt(2)\n"
    "v_mfma_f32_16x16x32_bf16 v[60:63], v[44:47], v[12:15], 0\n"
    "ds_read_b64_tr_b16 v[20:21], v9 offset:18432\n"
    "ds_read_b64_tr_b16 v[22:23], v9 offset:18944\n"
    "v_mfma_f32_16x16x32_bf16 v[68:71], v[12:15], v[12:15], v[68:71]\n"
    "ds_read_b64_tr_b16 v[24:25], v9 offset:19456\n"
    "ds_read_b64_tr_b16 v[26:27], v9 offset:19968\n"
    "s_waitcnt lgkmcnt(4)\n"
    "v_mfma_f32_16x16x32_bf16 v[60:63], v[48:51], v[16:19], v[60:63]\n"
    "ds_read_b128 v[28:31], v10 offset:4096\n"
    "v_add_u32 v8, s67, v3\n"
    "v_mfma_f32_16x16x32_bf16 v[68:71], v[16:19], v[16:19], v[68:71]\n"
    "ds_read_b128 v[32:35], v10 offset:5120\n"
    "s_waitcnt lgkmcnt(4)\n"
    "v_mfma_f32_16x16x32_bf16 v[60:63], v[52:55], v[20:23], v[60:63]\n"
    "ds_read_b128 v[36:39], v10 offset:6144\n"
    "v_mov_b32 v52, 0x3f803f80\n"
    "v_mfma_f32_16x16x32_bf16 v[68:71], v[20:23], v[20:23], v[68:71]\n"
    "ds_read_b128 v[40:43], v10 offset:7168\n"
    "s_waitcnt lgkmcnt(4)\n"
    "v_mfma_f32_16x16x32_bf16 v[60:63], v[56:59], v[24:27], v[60:63]\n"
    "ds_read_b128 v[44:47], v8 offset:0\n"
    "s_waitcnt lgkmcnt(4)\n"
    "v_mfma_f32_16x16x32_bf16 v[64:67], v[28:31], v[12:15], 0\n"
    "ds_read_b128 v[48:51], v8 offset:1024\n"
    "v_mov_b32 v53, 0x3f803f80\n"
    "v_mfma_f32_16x16x32_bf16 v[68:71], v[24:27], v[24:27], v[68:71]\n"
    "s_waitcnt lgkmcnt(2)\n"
    "v_mov_b32 v54, 0x3f803f80\n"
    "v_mfma_f32_16x16x32_bf16 v[64:67], v[32:35], v[16:19], v[64:67]\n"
    "v_mov_b32 v55, 0x3f803f80\n"
    "v_mov_b32 v29, 0\n"
    "v_mfma_f32_16x16x32_bf16 v[64:67], v[36:39], v[20:23], v[64:67]\n"
    "v_max3_f32 v7, v60, v61, v62\n"
    "v_mov_b32 v31, 0\n"
    "v_mfma_f32_16x16x32_bf16 v[64:67], v[40:43], v[24:27], v[64:67]\n"
    "v_mov_b32 v32, 0\n"
    "v_mov_b32 v34, 0\n"
    "v_max3_f32 v7, v7, v63, v68\n"
    "v_max3_f32 v7, v7, v69, v70\n"
    "v_max_f32 v7, v7, v71\n"
    "s_nop 2\n"
    "v_max3_f32 v7, v7, v64, v65\n"
    "v_max3_f32 v8, v7, v66, v67\n"
    "v_max3_f32 v7, v7, v66, v67\n"
    "s_nop 1\n"
    "v_permlane32_swap_b32 v7, v8\n"
    "v_max_f32 v9, v7, v8\n"
    "v_max_f32 v7, v7, v8\n"
    "s_nop 1\n"
    "v_permlane16_swap_b32 v7, v9\n"
    "v_max_f32 v7, v7, v9\n"
    "v_mul_f32 v8, s68, v7\n"
    "v_pk_fma_f32 v[60:61], v[60:61], s[68:69], v[8:9] op_sel_hi:[1,1,0] neg_lo:[0,0,1] neg_hi:[0,0,1]\n"
    "v_pk_fma_f32 v[62:63], v[62:63], s[68:69], v[8:9] op_sel_hi:[1,1,0] neg_lo:[0,0,1] neg_hi:[0,0,1]\n"
    "v_pk_fma_f32 v[64:65], v[64:65], s[68:69], v[8:9] op_sel_hi:[1,1,0] neg_lo:[0,0,1] neg_hi:[0,0,1]\n"
    "v_pk_fma_f32 v[66:67], v[66:67], s[68:69], v[8:9] op_sel_hi:[1,1,0] neg_lo:[0,0,1] neg_hi:[0,0,1]\n"
    "v_exp_f32 v60, v60\n"
    "v_exp_f32 v61, v61\n"
    "v_exp_f32 v62, v62\n"
    "v_cvt_pk_bf16_f32 v56, v60, v61\n"
    "v_exp_f32 v63, v63\n"
    "v_exp_f32 v64, v64\n"
    "v_cvt_pk_bf16_f32 v57, v62, v63\n"
    "v_exp_f32 v65, v65\n"
    "v_exp_f32 v66, v66\n"
    "v_cvt_pk_bf16_f32 v58, v64, v65\n"
    "v_exp_f32 v67, v67\n"
    "v_pk_fma_f32 v[68:69], v[68:69], s[68:69], v[8:9] op_sel_hi:[1,1,0] neg_lo:[0,0,1] neg_hi:[0,0,1]\n"
    "v_cvt_pk_bf16_f32 v59, v66, v67\n"
    "v_pk_fma_f32 v[70:71], v[70:71], s[68:69], v[8:9] op_sel_hi:[1,1,0] neg_lo:[0,0,1] neg_hi:[0,0,1]\n"
    "v_exp_f32 v68, v68\n"
    "v_mfma_f32_16x16x32_bf16 v[60:63], v[52:55], v[56:59], 0\n"
    "v_exp_f32 v69, v69\n"
    "s_waitcnt lgkmcnt(0)\n"
    "v_mfma_f32_16x16x32_bf16 v[36:39], v[44:47], v[56:59], 0\n"
    "v_exp_f32 v70, v70\n"
    "v_mfma_f32_16x16x32_bf16 v[40:43], v[48:51], v[56:59], 0\n"
    "v_exp_f32 v71, v71\n"
    "v_cvt_pk_bf16_f32 v28, v68, v69\n"
    "v_cvt_pk_bf16_f32 v30, v70, v71\n"
    "v_cvt_pk_bf16_f32 v33, v68, v69\n"
    "v_cvt_pk_bf16_f32 v35, v70, v71\n"
    "v_mfma_f32_16x16x32_bf16 v[60:63], v[52:55], v[28:31], v[60:63]\n"
    "s_waitcnt vmcnt(0)\n"
    ".Lqk_vmuse_%=:\n"
    "v_mfma_f32_16x16x32_bf16 v[36:39], v[72:75], v[28:31], v[36:39]\n"
    "v_max3_u32 v7, v72, v73, v74\n"
    "v_max3_u32 v7, v7, v75, v76\n"
    "v_mfma_f32_16x16x32_bf16 v[40:43], v[72:75], v[32:35], v[40:43]\n"
    "v_max3_u32 v7, v7, v77, v78\n"
    "v_max_u32 v7, v7, v79\n"
    "v_cmp_eq_u32 vcc, -1, v7\n"
    "v_rcp_f32 v8, v60\n"
    "s_cbranch_vccnz .Lqk_vmretry_%=\n"
    "s_mov_b64 exec, s[94:95]\n"
    "v_pk_mul_f32 v[36:37], v[36:37], v[8:9] op_sel_hi:[1,0]\n"
    "v_pk_mul_f32 v[38:39], v[38:39], v[8:9] op_sel_hi:[1,0]\n"
    "v_cvt_pk_bf16_f32 v56, v36, v37\n"
    "v_cvt_pk_bf16_f32 v57, v38, v39\n"
    "v_pk_mul_f32 v[40:41], v[40:41], v[8:9] op_sel_hi:[1,0]\n"
    "v_pk_mul_f32 v[42:43], v[42:43], v[8:9] op_sel_hi:[1,0]\n"
    "v_cvt_pk_bf16_f32 v58, v40, v41\n"
    "v_cvt_pk_bf16_f32 v59, v42, v43\n"
    "buffer_store_dwordx4 v[56:59], v6, s[32:35], s58 offen sc1\n"
    "s_mov_b64 exec, -1\n"
    "v_add_u32 v10, s65, v3\n"
    "v_add_u32 v9, s66, v3\n"
    "ds_read_b128 v[28:31], v10 offset:0\n"
    "ds_read_b128 v[32:35], v9 offset:0\n"
    "ds_read_b128 v[36:39], v9 offset:4096\n"
    "ds_read_b128 v[44:47], v10 offset:1024\n"
    "ds_read_b128 v[48:51], v9 offset:1024\n"
    "ds_read_b128 v[52:55], v9 offset:5120\n"
    "v_mov_b32 v7, s56\n"
    "s_cmp_eq_u32 s56, 3\n"
    "s_cbranch_scc1 .Lqk_near_%=\n"
    ".Lqk_nearb_%=:\n"
    "s_mov_b64 exec, 1\n"
    "buffer_store_dword v7, off, s[48:51], s99 sc1\n"
    "s_mov_b64 exec, -1\n"
    "s_cmp_eq_u32 s25, 0\n"
    "s_cbranch_scc1 .Lqk_h0_%=\n"
    ".Lqk_h1_%=:\n"
    "v_mov_b32 v7, 4048155338\n"
    "v_cndmask_b32 v60, v7, 0, s[76:77]\n"
    "v_cndmask_b32 v61, v7, 0, s[78:79]\n"
    "v_cndmask_b32 v62, v7, 0, s[80:81]\n"
    "v_cndmask_b32 v63, v7, 0, s[82:83]\n"
    "v_cndmask_b32 v64, v7, 0, s[84:85]\n"
    "v_cndmask_b32 v65, v7, 0, s[86:87]\n"
    "v_cndmask_b32 v66, v7, 0, s[88:89]\n"
    "v_cndmask_b32 v67, v7, 0, s[90:91]\n"
    "v_cndmask_b32 v68, v7, 0, s[92:93]\n"
    "v_cndmask_b32 v69, v7, 0, s[92:93]\n"
    "v_cndmask_b32 v70, v7, 0, s[92:93]\n"
    "v_cndmask_b32 v71, v7, 0, s[92:93]\n"
    "s_waitcnt lgkmcnt(3)\n"
    "v_mfma_f32_16x16x32_bf16 v[60:63], v[32:35], v[28:31], v[60:63]\n"
    "s_mov_b32 s28, s72\n"
    "s_xor_b32 s29, s72, 1024\n"
    "s_add_u32 s72, s72, 262144\n"
    "v_mfma_f32_16x16x32_bf16 v[64:67], v[36:39], v[28:31], v[64:67]\n"
    "s_mov_b32 s59, s58\n"
    "s_add_u32 s57, s57, 131072\n"
    "s_and_b32 s57, s57, 524287\n"
    "v_mfma_f32_16x16x32_bf16 v[68:71], v[12:15], v[28:31], v[68:71]\n"
    "ds_read_b128 v[28:31], v10 offset:2048\n"
    "ds_read_b128 v[32:35], v9 offset:2048\n"
    "ds_read_b128 v[36:39], v9 offset:6144\n"
    "s_waitcnt lgkmcnt(3)\n"
    "v_mfma_f32_16x16x32_bf16 v[60:63], v[48:51], v[44:47], v[60:63]\n"
    "s_xor_b32 s58, s58, 262144\n"
    "s_xor_b32 s52, s57, 1024\n"
    "s_xor_b32 s53, s57, 2048\n"
    "v_mfma_f32_16x16x32_bf16 v[64:67], v[52:55], v[44:47], v[64:67]\n"
    "s_xor_b32 s71, s57, 3072\n"
    "s_xor_b32 s60, s60, 131072\n"
    "s_xor_b32 s61, s61, 131072\n"
    "v_mfma_f32_16x16x32_bf16 v[68:71], v[16:19], v[44:47], v[68:71]\n"
    "ds_read_b128 v[44:47], v10 offset:3072\n"
    "ds_read_b128 v[48:51], v9 offset:3072\n"
    "ds_read_b128 v[52:55], v9 offset:7168\n"
    "v_add_u32 v10, s67, v3\n"
    "v_xor_b32 v9, 2048, v10\n"
    "s_xor_b32 s67, s67, 253952\n"
    "s_waitcnt lgkmcnt(3)\n"
    "v_mfma_f32_16x16x32_bf16 v[60:63], v[32:35], v[28:31], v[60:63]\n"
    "s_xor_b32 s64, s64, 8192\n"
    "s_xor_b32 s66, s66, 8192\n"
    "s_xor_b32 s65, s65, 24576\n"
    "v_mfma_f32_16x16x32_bf16 v[64:67], v[36:39], v[28:31], v[64:67]\n"
    "s_add_u32 s98, s98, 1\n"
    "v_mfma_f32_16x16x32_bf16 v[68:71], v[20:23], v[28:31], v[68:71]\n"
    "ds_read_b128 v[12:15], v10 offset:0\n"
    "ds_read_b128 v[16:19], v10 offset:1024\n"
    "s_waitcnt lgkmcnt(2)\n"
    "v_mfma_f32_16x16x32_bf16 v[60:63], v[48:51], v[44:47], v[60:63]\n"
    "ds_read_b128 v[20:23], v9 offset:0\n"
    "v_mov_b32 v29, 0\n"
    "v_mov_b32 v31, 0\n"
    "v_mfma_f32_16x16x32_bf16 v[64:67], v[52:55], v[44:47], v[64:67]\n"
    "v_mov_b32 v32, 0\n"
    "v_mov_b32 v34, 0\n"
    "v_mfma_f32_16x16x32_bf16 v[68:71], v[24:27], v[44:47], v[68:71]\n"
    "ds_read_b128 v[24:27], v9 offset:1024\n"
    "v_mov_b32 v52, 0x3f803f80\n"
    "v_mov_b32 v53, 0x3f803f80\n"
    "v_mov_b32 v54, 0x3f803f80\n"
    "v_mov_b32 v55, 0x3f803f80\n"
    "s_cmp_eq_u32 s25, 0\n"
    "s_cbranch_scc1 .Lqk_h3_%=\n"
    "s_cmp_eq_u32 s56, 63\n"
    "s_cbranch_scc1 .Lqk_h3_%=\n"
    "s_add_u32 s54, s56, 1\n"
    ".Lqk_f1chk_%=:\n"
    "s_and_b32 s36, s36, 0xfff\n"
    "s_cbranch_scc1 .Lqk_f1poll_%=\n"
    "s_mul_i32 s54, s54, 786432\n"
    "s_add_u32 s54, s54, s74\n"
    "s_add_u32 s55, s64, s73\n"
    "s_add_u32 m0, s55, 0\n"
    "s_nop 0\n"
    "buffer_load_dwordx4 v3, s[40:43], s54 offen offset:0 sc1 lds\n"
    "buffer_load_dwordx4 v3, s[40:43], s54 offen offset:1024 sc1 lds\n"
    "buffer_load_dwordx4 v3, s[40:43], s54 offen offset:2048 sc1 lds\n"
    "buffer_load_dwordx4 v3, s[40:43], s54 offen offset:3072 sc1 lds\n"
    "s_add_u32 m0, s55, 4096\n"
    "s_add_u32 s54, s54, 4096\n"
    "buffer_load_dwordx4 v3, s[40:43], s54 offen offset:0 sc1 lds\n"
    "buffer_load_dwordx4 v3, s[40:43], s54 offen offset:1024 sc1 lds\n"
    "buffer_load_dwordx4 v3, s[40:43], s54 offen offset:2048 sc1 lds\n"
    "buffer_load_dwordx4 v3, s[40:43], s54 offen offset:3072 sc1 lds\n"
    "v_mov_b64 v[36:37], -1\n"
    "v_mov_b64 v[38:39], -1\n"
    "buffer_store_dwordx4 v[36:39], v6, s[32:35], s58 offen sc1\n"
    "s_mov_b64 exec, 0xfff\n"
    "buffer_load_dword v11, v0, s[48:51], s70 offen sc1\n"
    "s_mov_b64 exec, -1\n"
    ".Lqk_h3_%=:\n"
    "v_max3_f32 v7, v60, v61, v62\n"
    "v_max3_f32 v7, v7, v63, v68\n"
    "v_max3_f32 v7, v7, v69, v70\n"
    "v_max_f32 v7, v7, v71\n"
    "v_max3_f32 v7, v7, v64, v65\n"
    "v_max3_f32 v8, v7, v66, v67\n"
    "v_max3_f32 v7, v7, v66, v67\n"
    "s_nop 1\n"
    "v_permlane32_swap_b32 v7, v8\n"
    "v_max_f32 v9, v7, v8\n"
    "v_max_f32 v7, v7, v8\n"
    "s_nop 1\n"
    "v_permlane16_swap_b32 v7, v9\n"
    "v_max_f32 v7, v7, v9\n"
    "v_mul_f32 v8, s68, v7\n"
    "v_pk_fma_f32 v[60:61], v[60:61], s[68:69], v[8:9] op_sel_hi:[1,1,0] neg_lo:[0,0,1] neg_hi:[0,0,1]\n"
    "v_pk_fma_f32 v[62:63], v[62:63], s[68:69], v[8:9] op_sel_hi:[1,1,0] neg_lo:[0,0,1] neg_hi:[0,0,1]\n"
    "v_pk_fma_f32 v[64:65], v[64:65], s[68:69], v[8:9] op_sel_hi:[1,1,0] neg_lo:[0,0,1] neg_hi:[0,0,1]\n"
    "v_pk_fma_f32 v[66:67], v[66:67], s[68:69], v[8:9] op_sel_hi:[1,1,0] neg_lo:[0,0,1] neg_hi:[0,0,1]\n"
    "v_exp_f32 v60, v60\n"
    "v_exp_f32 v61, v61\n"
    "v_exp_f32 v62, v62\n"
    "v_cvt_pk_bf16_f32 v56, v60, v61\n"
    "v_exp_f32 v63, v63\n"
    "v_exp_f32 v64, v64\n"
    "v_cvt_pk_bf16_f32 v57, v62, v63\n"
    "v_exp_f32 v65, v65\n"
    "v_exp_f32 v66, v66\n"
    "v_cvt_pk_bf16_f32 v58, v64, v65\n"
    "v_exp_f32 v67, v67\n"
    "v_pk_fma_f32 v[68:69], v[68:69], s[68:69], v[8:9] op_sel_hi:[1,1,0] neg_lo:[0,0,1] neg_hi:[0,0,1]\n"
    "v_cvt_pk_bf16_f32 v59, v66, v67\n"
    "v_pk_fma_f32 v[70:71], v[70:71], s[68:69], v[8:9] op_sel_hi:[1,1,0] neg_lo:[0,0,1] neg_hi:[0,0,1]\n"
    "v_exp_f32 v68, v68\n"
    "v_mfma_f32_16x16x32_bf16 v[60:63], v[52:55], v[56:59], 0\n"
    "v_exp_f32 v69, v69\n"
    "s_waitcnt lgkmcnt(0)\n"
    "v_mfma_f32_16x16x32_bf16 v[36:39], v[12:15], v[56:59], 0\n"
    "v_exp_f32 v70, v70\n"
    "v_mfma_f32_16x16x32_bf16 v[40:43], v[16:19], v[56:59], 0\n"
    "v_exp_f32 v71, v71\n"
    "v_mfma_f32_16x16x32_bf16 v[44:47], v[20:23], v[56:59], 0\n"
    "v_cvt_pk_bf16_f32 v28, v68, v69\n"
    "v_cvt_pk_bf16_f32 v30, v70, v71\n"
    "v_mfma_f32_16x16x32_bf16 v[48:51], v[24:27], v[56:59], 0\n"
    "v_cvt_pk_bf16_f32 v33, v68, v69\n"
    "v_cvt_pk_bf16_f32 v35, v70, v71\n"
    "v_mfma_f32_16x16x32_bf16 v[60:63], v[52:55], v[28:31], v[60:63]\n"
    "v_mfma_f32_16x16x32_bf16 v[36:39], v[72:75], v[28:31], v[36:39]\n"
    "v_mfma_f32_16x16x32_bf16 v[40:43], v[72:75], v[32:35], v[40:43]\n"
    "v_mfma_f32_16x16x32_bf16 v[44:47], v[76:79], v[28:31], v[44:47]\n"
    "v_mfma_f32_16x16x32_bf16 v[48:51], v[76:79], v[32:35], v[48:51]\n"
    "s_nop 3\n"
    "v_rcp_f32 v8, v60\n"
    "s_nop 0\n"
    "v_pk_mul_f32 v[36:37], v[36:37], v[8:9] op_sel_hi:[1,0]\n"
    "v_pk_mul_f32 v[38:39], v[38:39], v[8:9] op_sel_hi:[1,0]\n"
    "v_cvt_pk_bf16_f32 v72, v36, v37\n"
    "v_cvt_pk_bf16_f32 v73, v38, v39\n"
    "v_pk_mul_f32 v[40:41], v[40:41], v[8:9] op_sel_hi:[1,0]\n"
    "v_pk_mul_f32 v[42:43], v[42:43], v[8:9] op_sel_hi:[1,0]\n"
    "v_cvt_pk_bf16_f32 v74, v40, v41\n"
    "v_cvt_pk_bf16_f32 v75, v42, v43\n"
    "v_pk_mul_f32 v[44:45], v[44:45], v[8:9] op_sel_hi:[1,0]\n"
    "v_pk_mul_f32 v[46:47], v[46:47], v[8:9] op_sel_hi:[1,0]\n"
    "v_cvt_pk_bf16_f32 v76, v44, v45\n"
    "v_cvt_pk_bf16_f32 v77, v46, v47\n"
    "v_pk_mul_f32 v[48:49], v[48:49], v[8:9] op_sel_hi:[1,0]\n"
    "v_pk_mul_f32 v[50:51], v[50:51], v[8:9] op_sel_hi:[1,0]\n"
    "v_cvt_pk_bf16_f32 v78, v48, v49\n"
    "v_cvt_pk_bf16_f32 v79, v50, v51\n"
    "s_add_u32 s56, s56, 1\n"
    "s_cmp_lt_u32 s56, 64\n"
    "s_cbranch_scc1 .Lqk_step_%=\n"
    "buffer_store_dwordx4 v[72:75], v3, s[44:47], s28 offen sc1\n"
    "buffer_store_dwordx4 v[76:79], v3, s[44:47], s29 offen sc1\n"
    "v_mov_b32 v7, 64\n"
    "s_waitcnt vmcnt(0)\n"
    "s_mov_b64 exec, 1\n"
    "buffer_store_dword v7, off, s[48:51], s99 sc1\n"
    "s_mov_b64 exec, -1\n"
    "s_endpgm\n"
    ".Lqk_mretry_%=:\n"
    "s_waitcnt vmcnt(2) lgkmcnt(0)\n"
    "s_barrier\n"
    "v_add_u32 v10, s62, v3\n"
    "ds_read_b128 v[44:47], v10 offset:0\n"
    "ds_read_b128 v[48:51], v10 offset:1024\n"
    "ds_read_b128 v[52:55], v10 offset:2048\n"
    "ds_read_b128 v[56:59], v10 offset:3072\n"
    ".Lqk_mspin_%=:\n"
    "buffer_load_dwordx4 v[28:31], v5, s[32:35], s57 offen sc1\n"
    "buffer_load_dwordx4 v[32:35], v5, s[32:35], s52 offen sc1\n"
    "buffer_load_dwordx4 v[36:39], v5, s[32:35], s53 offen sc1\n"
    "buffer_load_dwordx4 v[40:43], v5, s[32:35], s71 offen sc1\n"
    "v_mov_b64 v[12:13], 0\n"
    "v_mov_b64 v[14:15], 0\n"
    "v_mov_b64 v[16:17], 0\n"
    "v_mov_b64 v[18:19], 0\n"
    "v_mov_b64 v[20:21], 0\n"
    "v_mov_b64 v[22:23], 0\n"
    "v_mov_b64 v[24:25], 0\n"
    "v_mov_b64 v[26:27], 0\n"
    "s_waitcnt vmcnt(0)\n"
    "v_max3_u32 v7, v28, v29, v30\n"
    "v_max_u32 v7, v7, v31\n"
    "v_max3_u32 v7, v32, v33, v7\n"
    "v_max3_u32 v7, v34, v35, v7\n"
    "v_max3_u32 v7, v36, v37, v7\n"
    "v_max3_u32 v7, v38, v39, v7\n"
    "v_max3_u32 v7, v40, v41, v7\n"
    "v_max3_u32 v7, v42, v43, v7\n"
    "v_cmp_eq_u32 vcc, -1, v7\n"
    "v_add_u32 v7, s63, v3\n"
    "s_cbranch_vccz .Lqk_mgot_%=\n"
    "s_sub_u32 s26, s26, 1\n"
    "s_cbranch_scc0 .Lqk_mspin_%=\n"
    "s_endpgm\n"
    ".Lqk_mgot_%=:\n"
    "ds_write_b128 v7, v[28:31] offset:0\n"
    "ds_write_b128 v7, v[32:35] offset:4096\n"
    "ds_write_b128 v7, v[36:39] offset:8192\n"
    "ds_write_b128 v7, v[40:43] offset:12288\n"
    "s_waitcnt lgkmcnt(0)\n"
    "s_branch .Lqk_mjoin_%=\n"
    ".Lqk_last_%=:\n"
    "ds_read_b64_tr_b16 v[20:21], v9 offset:18432\n"
    "ds_read_b64_tr_b16 v[22:23], v9 offset:18944\n"
    "ds_read_b64_tr_b16 v[24:25], v9 offset:19456\n"
    "ds_read_b64_tr_b16 v[26:27], v9 offset:19968\n"
    ".Lqk_lastw_%=:\n"
    "s_waitcnt vmcnt(0) lgkmcnt(0)\n"
    "v_max3_u32 v7, v72, v73, v74\n"
    "v_max3_u32 v7, v7, v75, v76\n"
    "v_max3_u32 v7, v7, v77, v78\n"
    "v_max_u32 v7, v7, v79\n"
    "v_cmp_eq_u32 vcc, -1, v7\n"
    "s_cbranch_vccz .Lqk_lastg_%=\n"
    "buffer_load_dwordx4 v[72:75], v8, s[32:35], s60 offen sc1\n"
    "buffer_load_dwordx4 v[76:79], v8, s[32:35], s61 offen sc1\n"
    "s_sub_u32 s26, s26, 1\n"
    "s_cbranch_scc0 .Lqk_lastw_%=\n"
    "s_endpgm\n"
    ".Lqk_lastg_%=:\n"
    "v_add_u32 v10, s65, v3\n"
    "v_add_u32 v9, s66, v3\n"
    "v_mov_b32 v7, 63\n"
    "ds_read_b128 v[28:31], v10 offset:0\n"
    "ds_read_b128 v[32:35], v9 offset:0\n"
    "ds_read_b128 v[36:39], v9 offset:4096\n"
    "ds_read_b128 v[44:47], v10 offset:1024\n"
    "ds_read_b128 v[48:51], v9 offset:1024\n"
    "ds_read_b128 v[52:55], v9 offset:5120\n"
    "s_mov_b64 exec, 1\n"
    "buffer_store_dword v7, off, s[48:51], s99 sc1\n"
    "s_mov_b64 exec, -1\n"
    "s_branch .Lqk_h1_%=\n"
    ".Lqk_h0_%=:\n"
    "v_lshlrev_b32 v11, 3, v3\n"
    "s_mov_b32 exec_hi, 0\n"
    "buffer_load_dwordx2 v[0:1], v11, s[48:51], s96 offen sc1\n"
    "s_mov_b64 exec, -1\n"
    "s_memtime s[16:17]\n"
    "s_memrealtime s[0:1]\n"
    "s_nop 2\n"
    "s_memrealtime s[2:3]\n"
    "s_nop 2\n"
    "s_memrealtime s[4:5]\n"
    "s_nop 2\n"
    "s_memrealtime s[6:7]\n"
    "s_nop 2\n"
    "s_memrealtime s[8:9]\n"
    "s_nop 2\n"
    "s_memrealtime s[10:11]\n"
    "s_nop 2\n"
    "s_memrealtime s[12:13]\n"
    "s_nop 2\n"
    "s_memrealtime s[14:15]\n"
    "s_mov_b32 s18, 0\n"
    "s_waitcnt lgkmcnt(0)\n"
    "s_cmp_eq_u32 s2, s0\n"
    "s_addc_u32 s18, s18, 0\n"
    "s_cmp_eq_u32 s4, s0\n"
    "s_addc_u32 s18, s18, 0\n"
    "s_cmp_eq_u32 s6, s0\n"
    "s_addc_u32 s18, s18, 0\n"
    "s_cmp_eq_u32 s8, s0\n"
    "s_addc_u32 s18, s18, 0\n"
    "s_cmp_eq_u32 s10, s0\n"
    "s_addc_u32 s18, s18, 0\n"
    "s_cmp_eq_u32 s12, s0\n"
    "s_addc_u32 s18, s18, 0\n"
    "s_cmp_eq_u32 s14, s0\n"
    "s_addc_u32 s18, s18, 0\n"
    "s_lshl_b32 s18, s18, 4\n"
    "s_add_u32 s16, s16, s18\n"
    "s_add_u32 s0, s0, 4\n"
    "s_lshl_b32 s0, s0, 4\n"
    "s_sub_u32 s18, s0, s38\n"
    "s_sub_u32 s19, s16, s39\n"
    "s_mov_b32 s38, s0\n"
    "s_mov_b32 s39, s16\n"
    "s_branch .Lqk_h1_%=\n"
    ".Lqk_vmretry_%=:\n"
    "v_and_b32 v7, 0x1ff, v3\n"
    "buffer_load_dwordx4 v[72:75], v7, s[32:35], s60 offen sc1\n"
    "buffer_load_dwordx4 v[76:79], v7, s[32:35], s61 offen sc1\n"
    "s_waitcnt vmcnt(0)\n"
    "s_nop 3\n"
    "v_mfma_f32_16x16x32_bf16 v[36:39], v[44:47], v[56:59], 0\n"
    "v_mfma_f32_16x16x32_bf16 v[40:43], v[48:51], v[56:59], 0\n"
    "s_sub_u32 s26, s26, 1\n"
    "s_cbranch_scc0 .Lqk_vmuse_%=\n"
    "s_endpgm\n"
    ".Lqk_f0poll_%=:\n"
    "s_sleep 1\n"
    "s_mov_b64 exec, 0xfff\n"
    "buffer_load_dword v11, v0, s[48:51], s70 offen sc1\n"
    "s_mov_b64 exec, -1\n"
    "s_waitcnt vmcnt(0)\n"
    "v_cmp_ge_u32 s[36:37], s54, v11\n"
    "s_nop 3\n"
    "s_sub_u32 s26, s26, 1\n"
    "s_cbranch_scc0 .Lqk_f0chk_%=\n"
    "s_endpgm\n"
    ".Lqk_f1poll_%=:\n"
    "s_sleep 1\n"
    "s_mov_b64 exec, 0xfff\n"
    "buffer_load_dword v11, v0, s[48:51], s70 offen sc1\n"
    "s_mov_b64 exec, -1\n"
    "s_waitcnt vmcnt(0)\n"
    "v_cmp_ge_u32 s[36:37], s54, v11\n"
    "s_nop 3\n"
    "s_sub_u32 s26, s26, 1\n"
    "s_cbranch_scc0 .Lqk_f1chk_%=\n"
    "s_endpgm\n"
    ".Lqk_hsp_%=:\n"
    "s_sleep 2\n"
    "s_add_u32 s10, s19, 69632\n"
    "s_mov_b64 exec, 1\n"
    "buffer_load_dword v0, off, s[12:15], s10 sc1\n"
    "s_mov_b64 exec, -1\n"
    "s_waitcnt vmcnt(0)\n"
    "s_sub_u32 s26, s26, 1\n"
    "s_cbranch_scc0 .Lqk_hsc_%=\n"
    "s_endpgm\n"
    ".Lqk_hsr_%=:\n"
    "s_mov_b32 s17, s11\n"
    "s_waitcnt vmcnt(0) lgkmcnt(0)\n"
    "s_barrier\n"
    "s_nop 15\n"
    "s_branch .Lk_dispatch_%=\n"
    ".Lqk_near_%=:\n"
    "s_and_b32 s0, s96, 8192\n"
    "s_add_u32 s0, s0, 39936\n"
    "buffer_load_dword v8, off, s[48:51], s0 sc1\n"
    "s_waitcnt vmcnt(0)\n"
    "v_readfirstlane_b32 s0, v8\n"
    "s_nop 1\n"
    "s_bitcmp1_b32 s0, 2\n"
    "s_cbranch_scc0 .Lqk_nearb_%=\n"
    "s_xor_b32 s0, s0, s25\n"
    "s_and_b32 s1, s0, 1\n"
    "s_lshl_b32 s1, s1, 14\n"
    "s_bfe_u32 s2, s0, 65537\n"
    "s_lshl_b32 s2, s2, 12\n"
    "s_add_u32 s1, s1, s2\n"
    "v_xor_b32 v5, s1, v5\n"
    "s_branch .Lqk_nearb_%=\n"
    ".Lvo_entry_%=:\n"
    "s_load_dwordx4 s[52:55], s[28:29], 72\n"
    "s_load_dwordx2 s[56:57], s[28:29], 48\n"
    "s_waitcnt lgkmcnt(0)\n"
    "v_lshlrev_b32 v11, 2, v1\n"
    "s_bfe_u32 s43, s24, 65540\n"
    "s_lshr_b32 s42, s24, 5\n"
    "s_lshl_b32 s42, s42, 4\n"
    "s_and_b32 s50, s24, 15\n"
    "s_add_u32 s42, s42, s50\n"
    "s_lshl_b32 s93, s43, 5\n"
    "s_add_u32 s93, s93, s42\n"
    "s_and_b32 s50, s42, 7\n"
    "s_lshl_b32 s50, s50, 1\n"
    "s_add_u32 s50, s50, s43\n"
    "s_lshl_b32 s50, s50, 8\n"
    "s_lshr_b32 s51, s42, 3\n"
    "s_lshl_b32 s51, s51, 15\n"
    "s_add_u32 s50, s50, s51\n"
    "s_and_b32 s51, s25, 1\n"
    "s_lshl_b32 s51, s51, 14\n"
    "s_add_u32 s50, s50, s51\n"
    "s_lshr_b32 s51, s25, 1\n"
    "s_lshl_b32 s51, s51, 12\n"
    "s_add_u32 s50, s50, s51\n"
    "s_add_u32 s50, s50, 1048576\n"
    "v_add_u32 v11, s50, v11\n"
    "s_mov_b32 s36, s56\n"
    "s_and_b32 s37, s57, 0xffff\n"
    "s_mov_b32 s38, 16384\n"
    "s_mov_b32 s39, 0x20000\n"
    "s_lshl_b32 s42, s42, 6\n"
    "s_lshl_b32 s43, s43, 13\n"
    "s_add_u32 s42, s42, s43\n"
    "v_bfe_u32 v2, v1, 4, 2\n"
    "v_lshlrev_b32 v2, 11, v2\n"
    "v_bfe_u32 v3, v1, 0, 4\n"
    "v_lshl_add_u32 v2, v3, 1, v2\n"
    "buffer_load_ushort v64, v2, s[36:39], s42 offen\n"
    "buffer_load_ushort v65, v2, s[36:39], s42 offen offset:32\n"
    "s_mul_i32 s41, s25, 40960\n"
    "s_mov_b32 s36, s52\n"
    "s_and_b32 s37, s53, 0xffff\n"
    "s_mov_b32 s38, 8388608\n"
    "s_mov_b32 s39, 0x20000\n"
    "s_lshl_b32 s40, s25, 21\n"
    "s_lshl_b32 s42, s24, 7\n"
    "s_add_u32 s40, s40, s42\n"
    "s_and_b32 s70, s24, 0\n"
    "s_and_b32 s71, s24, 0\n"
    "s_mov_b32 s88, s54\n"
    "s_and_b32 s89, s55, 0xffff\n"
    "s_mov_b32 s90, 8388608\n"
    "s_mov_b32 s91, 0x20000\n"
    "s_lshl_b32 s92, s25, 21\n"
    "s_and_b32 s42, s93, 31\n"
    "s_lshl_b32 s42, s42, 6\n"
    "s_add_u32 s92, s92, s42\n"
    "v_bfe_u32 v2, v1, 3, 1\n"
    "v_lshlrev_b32 v2, 14, v2\n"
    "v_bfe_u32 v3, v1, 4, 1\n"
    "v_lshl_add_u32 v2, v3, 15, v2\n"
    "v_bfe_u32 v3, v1, 5, 1\n"
    "v_lshl_add_u32 v2, v3, 17, v2\n"
    "v_bfe_u32 v3, v1, 0, 3\n"
    "v_lshl_add_u32 v2, v3, 4, v2\n"
    "v_bfe_u32 v4, v1, 4, 2\n"
    "v_lshlrev_b32 v4, 5, v4\n"
    "v_xor_b32 v2, v2, v4\n"
    "v_bfe_u32 v4, v1, 5, 1\n"
    "v_lshlrev_b32 v4, 11, v4\n"
    "v_bfe_u32 v3, v1, 4, 1\n"
    "v_lshl_add_u32 v4, v3, 7, v4\n"
    "v_bfe_u32 v3, v1, 3, 1\n"
    "v_lshl_add_u32 v4, v3, 8, v4\n"
    "v_bfe_u32 v3, v1, 2, 1\n"
    "v_lshl_add_u32 v4, v3, 9, v4\n"
    "v_bfe_u32 v3, v1, 3, 1\n"
    "v_lshl_add_u32 v4, v3, 5, v4\n"
    "v_bfe_u32 v3, v1, 2, 1\n"
    "v_lshl_add_u32 v4, v3, 6, v4\n"
    "v_bfe_u32 v3, v1, 0, 2\n"
    "v_lshl_add_u32 v4, v3, 3, v4\n"
    "v_add_u32 v4, s41, v4\n"
    "v_xor_b32 v5, 32, v4\n"
    "v_xor_b32 v6, 64, v4\n"
    "v_xor_b32 v7, 96, v4\n"
    "v_bfe_u32 v16, v1, 2, 1\n"
    "v_lshlrev_b32 v16, 11, v16\n"
    "v_bfe_u32 v17, v1, 3, 1\n"
    "v_lshl_add_u32 v16, v17, 13, v16\n"
    "v_bfe_u32 v17, v1, 4, 1\n"
    "v_lshl_add_u32 v16, v17, 15, v16\n"
    "v_bfe_u32 v17, v1, 5, 1\n"
    "v_lshl_add_u32 v16, v17, 16, v16\n"
    "v_bfe_u32 v17, v1, 0, 2\n"
    "v_lshl_add_u32 v16, v17, 4, v16\n"
    "v_bfe_u32 v18, v1, 4, 1\n"
    "v_lshlrev_b32 v18, 5, v18\n"
    "v_xor_b32 v16, v16, v18\n"
    "v_bfe_u32 v18, v1, 2, 4\n"
    "v_lshlrev_b32 v18, 6, v18\n"
    "v_bfe_u32 v17, v1, 4, 1\n"
    "v_lshl_add_u32 v18, v17, 5, v18\n"
    "v_bfe_u32 v17, v1, 0, 2\n"
    "v_lshl_add_u32 v18, v17, 3, v18\n"
    "v_add_u32 v18, s41, v18\n"
    "v_xor_b32 v19, 32, v18\n"
    "s_xor_b32 s42, s70, 0\n"
    "s_lshl_b32 s42, s42, 19\n"
    "s_add_u32 s42, s42, s40\n"
    "s_add_u32 m0, s41, 0\n"
    "s_add_u32 s43, s42, 0\n"
    "buffer_load_dwordx4 v2, s[36:39], s43 offen lds\n"
    "s_add_u32 m0, s41, 1024\n"
    "s_add_u32 s43, s42, 8192\n"
    "buffer_load_dwordx4 v2, s[36:39], s43 offen lds\n"
    "s_add_u32 m0, s41, 2048\n"
    "s_add_u32 s43, s42, 65536\n"
    "buffer_load_dwordx4 v2, s[36:39], s43 offen lds\n"
    "s_add_u32 m0, s41, 3072\n"
    "s_add_u32 s43, s42, 73728\n"
    "buffer_load_dwordx4 v2, s[36:39], s43 offen lds\n"
    "s_add_u32 m0, s41, 4096\n"
    "s_add_u32 s43, s42, 262144\n"
    "buffer_load_dwordx4 v2, s[36:39], s43 offen lds\n"
    "s_add_u32 m0, s41, 5120\n"
    "s_add_u32 s43, s42, 270336\n"
    "buffer_load_dwordx4 v2, s[36:39], s43 offen lds\n"
    "s_add_u32 m0, s41, 6144\n"
    "s_add_u32 s43, s42, 327680\n"
    "buffer_load_dwordx4 v2, s[36:39], s43 offen lds\n"
    "s_add_u32 m0, s41, 7168\n"
    "s_add_u32 s43, s42, 335872\n"
    "buffer_load_dwordx4 v2, s[36:39], s43 offen lds\n"
    "s_xor_b32 s42, s70, 1\n"
    "s_lshl_b32 s42, s42, 19\n"
    "s_add_u32 s42, s42, s40\n"
    "s_add_u32 m0, s41, 8192\n"
    "s_add_u32 s43, s42, 0\n"
    "buffer_load_dwordx4 v2, s[36:39], s43 offen lds\n"
    "s_add_u32 m0, s41, 9216\n"
    "s_add_u32 s43, s42, 8192\n"
    "buffer_load_dwordx4 v2, s[36:39], s43 offen lds\n"
    "s_add_u32 m0, s41, 10240\n"
    "s_add_u32 s43, s42, 65536\n"
    "buffer_load_dwordx4 v2, s[36:39], s43 offen lds\n"
    "s_add_u32 m0, s41, 11264\n"
    "s_add_u32 s43, s42, 73728\n"
    "buffer_load_dwordx4 v2, s[36:39], s43 offen lds\n"
    "s_add_u32 m0, s41, 12288\n"
    "s_add_u32 s43, s42, 262144\n"
    "buffer_load_dwordx4 v2, s[36:39], s43 offen lds\n"
    "s_add_u32 m0, s41, 13312\n"
    "s_add_u32 s43, s42, 270336\n"
    "buffer_load_dwordx4 v2, s[36:39], s43 offen lds\n"
    "s_add_u32 m0, s41, 14336\n"
    "s_add_u32 s43, s42, 327680\n"
    "buffer_load_dwordx4 v2, s[36:39], s43 offen lds\n"
    "s_add_u32 m0, s41, 15360\n"
    "s_add_u32 s43, s42, 335872\n"
    "buffer_load_dwordx4 v2, s[36:39], s43 offen lds\n"
    "s_xor_b32 s42, s70, 2\n"
    "s_lshl_b32 s42, s42, 19\n"
    "s_add_u32 s42, s42, s40\n"
    "s_add_u32 m0, s41, 16384\n"
    "s_add_u32 s43, s42, 0\n"
    "buffer_load_dwordx4 v2, s[36:39], s43 offen lds\n"
    "s_add_u32 m0, s41, 17408\n"
    "s_add_u32 s43, s42, 8192\n"
    "buffer_load_dwordx4 v2, s[36:39], s43 offen lds\n"
    "s_add_u32 m0, s41, 18432\n"
    "s_add_u32 s43, s42, 65536\n"
    "buffer_load_dwordx4 v2, s[36:39], s43 offen lds\n"
    "s_add_u32 m0, s41, 19456\n"
    "s_add_u32 s43, s42, 73728\n"
    "buffer_load_dwordx4 v2, s[36:39], s43 offen lds\n"
    "s_add_u32 m0, s41, 20480\n"
    "s_add_u32 s43, s42, 262144\n"
    "buffer_load_dwordx4 v2, s[36:39], s43 offen lds\n"
    "s_add_u32 m0, s41, 21504\n"
    "s_add_u32 s43, s42, 270336\n"
    "buffer_load_dwordx4 v2, s[36:39], s43 offen lds\n"
    "s_add_u32 m0, s41, 22528\n"
    "s_add_u32 s43, s42, 327680\n"
    "buffer_load_dwordx4 v2, s[36:39], s43 offen lds\n"
    "s_add_u32 m0, s41, 23552\n"
    "s_add_u32 s43, s42, 335872\n"
    "buffer_load_dwordx4 v2, s[36:39], s43 offen lds\n"
    "s_xor_b32 s42, s70, 3\n"
    "s_lshl_b32 s42, s42, 19\n"
    "s_add_u32 s42, s42, s40\n"
    "s_add_u32 m0, s41, 24576\n"
    "s_add_u32 s43, s42, 0\n"
    "buffer_load_dwordx4 v2, s[36:39], s43 offen lds\n"
    "s_add_u32 m0, s41, 25600\n"
    "s_add_u32 s43, s42, 8192\n"
    "buffer_load_dwordx4 v2, s[36:39], s43 offen lds\n"
    "s_add_u32 m0, s41, 26624\n"
    "s_add_u32 s43, s42, 65536\n"
    "buffer_load_dwordx4 v2, s[36:39], s43 offen lds\n"
    "s_add_u32 m0, s41, 27648\n"
    "s_add_u32 s43, s42, 73728\n"
    "buffer_load_dwordx4 v2, s[36:39], s43 offen lds\n"
    "s_add_u32 m0, s41, 28672\n"
    "s_add_u32 s43, s42, 262144\n"
    "buffer_load_dwordx4 v2, s[36:39], s43 offen lds\n"
    "s_add_u32 m0, s41, 29696\n"
    "s_add_u32 s43, s42, 270336\n"
    "buffer_load_dwordx4 v2, s[36:39], s43 offen lds\n"
    "s_add_u32 m0, s41, 30720\n"
    "s_add_u32 s43, s42, 327680\n"
    "buffer_load_dwordx4 v2, s[36:39], s43 offen lds\n"
    "s_add_u32 m0, s41, 31744\n"
    "s_add_u32 s43, s42, 335872\n"
    "buffer_load_dwordx4 v2, s[36:39], s43 offen lds\n"
    "s_xor_b32 s42, s71, 0\n"
    "s_lshl_b32 s42, s42, 18\n"
    "s_add_u32 s42, s42, 0\n"
    "s_add_u32 s42, s42, s92\n"
    "s_add_u32 m0, s41, 32768\n"
    "s_add_u32 s43, s42, 0\n"
    "buffer_load_dwordx4 v16, s[88:91], s43 offen lds\n"
    "s_add_u32 m0, s41, 33792\n"
    "s_add_u32 s43, s42, 4096\n"
    "buffer_load_dwordx4 v16, s[88:91], s43 offen lds\n"
    "s_add_u32 m0, s41, 34816\n"
    "s_add_u32 s43, s42, 131072\n"
    "buffer_load_dwordx4 v16, s[88:91], s43 offen lds\n"
    "s_add_u32 m0, s41, 35840\n"
    "s_add_u32 s43, s42, 135168\n"
    "buffer_load_dwordx4 v16, s[88:91], s43 offen lds\n"
    "s_xor_b32 s42, s71, 0\n"
    "s_lshl_b32 s42, s42, 18\n"
    "s_add_u32 s42, s42, 16384\n"
    "s_add_u32 s42, s42, s92\n"
    "s_add_u32 m0, s41, 36864\n"
    "s_add_u32 s43, s42, 0\n"
    "buffer_load_dwordx4 v16, s[88:91], s43 offen lds\n"
    "s_add_u32 m0, s41, 37888\n"
    "s_add_u32 s43, s42, 4096\n"
    "buffer_load_dwordx4 v16, s[88:91], s43 offen lds\n"
    "s_add_u32 m0, s41, 38912\n"
    "s_add_u32 s43, s42, 131072\n"
    "buffer_load_dwordx4 v16, s[88:91], s43 offen lds\n"
    "s_add_u32 m0, s41, 39936\n"
    "s_add_u32 s43, s42, 135168\n"
    "buffer_load_dwordx4 v16, s[88:91], s43 offen lds\n"
    "s_waitcnt vmcnt(42)\n"
    "v_readfirstlane_b32 s18, v0\n"
    "s_add_u32 s10, s19, 65536\n"
    "s_or_b32 s18, s18, 0xff\n"
    "s_add_u32 s18, s18, 1\n"
    "v_mov_b32 v0, s18\n"
    "s_mov_b64 exec, 1\n"
    "buffer_store_dword v0, off, s[12:15], s10 sc1\n"
    "s_mov_b64 exec, -1\n"
    "s_waitcnt vmcnt(33)\n"
    "ds_read_b64_tr_b16 v[128:129], v4 offset:0\n"
    "ds_read_b64_tr_b16 v[130:131], v4 offset:1024\n"
    "ds_read_b64_tr_b16 v[132:133], v4 offset:4096\n"
    "ds_read_b64_tr_b16 v[134:135], v4 offset:5120\n"
    "ds_read_b64_tr_b16 v[136:137], v5 offset:0\n"
    "ds_read_b64_tr_b16 v[138:139], v5 offset:1024\n"
    "ds_read_b64_tr_b16 v[140:141], v5 offset:4096\n"
    "ds_read_b64_tr_b16 v[142:143], v5 offset:5120\n"
    "ds_read_b64_tr_b16 v[144:145], v6 offset:0\n"
    "ds_read_b64_tr_b16 v[146:147], v6 offset:1024\n"
    "ds_read_b64_tr_b16 v[148:149], v6 offset:4096\n"
    "ds_read_b64_tr_b16 v[150:151], v6 offset:5120\n"
    "ds_read_b64_tr_b16 v[152:153], v7 offset:0\n"
    "ds_read_b64_tr_b16 v[154:155], v7 offset:1024\n"
    "ds_read_b64_tr_b16 v[156:157], v7 offset:4096\n"
    "ds_read_b64_tr_b16 v[158:159], v7 offset:5120\n"
    "s_waitcnt lgkmcnt(0)\n"
    "s_xor_b32 s42, s71, 1\n"
    "s_lshl_b32 s42, s42, 18\n"
    "s_add_u32 s42, s42, 0\n"
    "s_add_u32 s42, s42, s92\n"
    "s_add_u32 m0, s41, 0\n"
    "s_add_u32 s43, s42, 0\n"
    "buffer_load_dwordx4 v16, s[88:91], s43 offen lds\n"
    "s_add_u32 m0, s41, 1024\n"
    "s_add_u32 s43, s42, 4096\n"
    "buffer_load_dwordx4 v16, s[88:91], s43 offen lds\n"
    "s_add_u32 m0, s41, 2048\n"
    "s_add_u32 s43, s42, 131072\n"
    "buffer_load_dwordx4 v16, s[88:91], s43 offen lds\n"
    "s_add_u32 m0, s41, 3072\n"
    "s_add_u32 s43, s42, 135168\n"
    "buffer_load_dwordx4 v16, s[88:91], s43 offen lds\n"
    "s_xor_b32 s42, s71, 1\n"
    "s_lshl_b32 s42, s42, 18\n"
    "s_add_u32 s42, s42, 16384\n"
    "s_add_u32 s42, s42, s92\n"
    "s_add_u32 m0, s41, 4096\n"
    "s_add_u32 s43, s42, 0\n"
    "buffer_load_dwordx4 v16, s[88:91], s43 offen lds\n"
    "s_add_u32 m0, s41, 5120\n"
    "s_add_u32 s43, s42, 4096\n"
    "buffer_load_dwordx4 v16, s[88:91], s43 offen lds\n"
    "s_add_u32 m0, s41, 6144\n"
    "s_add_u32 s43, s42, 131072\n"
    "buffer_load_dwordx4 v16, s[88:91], s43 offen lds\n"
    "s_add_u32 m0, s41, 7168\n"
    "s_add_u32 s43, s42, 135168\n"
    "buffer_load_dwordx4 v16, s[88:91], s43 offen lds\n"
    "s_waitcnt vmcnt(33)\n"
    "ds_read_b64_tr_b16 v[160:161], v4 offset:8192\n"
    "ds_read_b64_tr_b16 v[162:163], v4 offset:9216\n"
    "ds_read_b64_tr_b16 v[164:165], v4 offset:12288\n"
    "ds_read_b64_tr_b16 v[166:167], v4 offset:13312\n"
    "ds_read_b64_tr_b16 v[168:169], v5 offset:8192\n"
    "ds_read_b64_tr_b16 v[170:171], v5 offset:9216\n"
    "ds_read_b64_tr_b16 v[172:173], v5 offset:12288\n"
    "ds_read_b64_tr_b16 v[174:175], v5 offset:13312\n"
    "ds_read_b64_tr_b16 v[176:177], v6 offset:8192\n"
    "ds_read_b64_tr_b16 v[178:179], v6 offset:9216\n"
    "ds_read_b64_tr_b16 v[180:181], v6 offset:12288\n"
    "ds_read_b64_tr_b16 v[182:183], v6 offset:13312\n"
    "ds_read_b64_tr_b16 v[184:185], v7 offset:8192\n"
    "ds_read_b64_tr_b16 v[186:187], v7 offset:9216\n"
    "ds_read_b64_tr_b16 v[188:189], v7 offset:12288\n"
    "ds_read_b64_tr_b16 v[190:191], v7 offset:13312\n"
    "s_waitcnt lgkmcnt(0)\n"
    "s_xor_b32 s42, s71, 2\n"
    "s_lshl_b32 s42, s42, 18\n"
    "s_add_u32 s42, s42, 0\n"
    "s_add_u32 s42, s42, s92\n"
    "s_add_u32 m0, s41, 8192\n"
    "s_add_u32 s43, s42, 0\n"
    "buffer_load_dwordx4 v16, s[88:91], s43 offen lds\n"
    "s_add_u32 m0, s41, 9216\n"
    "s_add_u32 s43, s42, 4096\n"
    "buffer_load_dwordx4 v16, s[88:91], s43 offen lds\n"
    "s_add_u32 m0, s41, 10240\n"
    "s_add_u32 s43, s42, 131072\n"
    "buffer_load_dwordx4 v16, s[88:91], s43 offen lds\n"
    "s_add_u32 m0, s41, 11264\n"
    "s_add_u32 s43, s42, 135168\n"
    "buffer_load_dwordx4 v16, s[88:91], s43 offen lds\n"
    "s_xor_b32 s42, s71, 2\n"
    "s_lshl_b32 s42, s42, 18\n"
    "s_add_u32 s42, s42, 16384\n"
    "s_add_u32 s42, s42, s92\n"
    "s_add_u32 m0, s41, 12288\n"
    "s_add_u32 s43, s42, 0\n"
    "buffer_load_dwordx4 v16, s[88:91], s43 offen lds\n"
    "s_add_u32 m0, s41, 13312\n"
    "s_add_u32 s43, s42, 4096\n"
    "buffer_load_dwordx4 v16, s[88:91], s43 offen lds\n"
    "s_add_u32 m0, s41, 14336\n"
    "s_add_u32 s43, s42, 131072\n"
    "buffer_load_dwordx4 v16, s[88:91], s43 offen lds\n"
    "s_add_u32 m0, s41, 15360\n"
    "s_add_u32 s43, s42, 135168\n"
    "buffer_load_dwordx4 v16, s[88:91], s43 offen lds\n"
    "s_waitcnt vmcnt(33)\n"
    "ds_read_b64_tr_b16 v[192:193], v4 offset:16384\n"
    "ds_read_b64_tr_b16 v[194:195], v4 offset:17408\n"
    "ds_read_b64_tr_b16 v[196:197], v4 offset:20480\n"
    "ds_read_b64_tr_b16 v[198:199], v4 offset:21504\n"
    "ds_read_b64_tr_b16 v[200:201], v5 offset:16384\n"
    "ds_read_b64_tr_b16 v[202:203], v5 offset:17408\n"
    "ds_read_b64_tr_b16 v[204:205], v5 offset:20480\n"
    "ds_read_b64_tr_b16 v[206:207], v5 offset:21504\n"
    "ds_read_b64_tr_b16 v[208:209], v6 offset:16384\n"
    "ds_read_b64_tr_b16 v[210:211], v6 offset:17408\n"
    "ds_read_b64_tr_b16 v[212:213], v6 offset:20480\n"
    "ds_read_b64_tr_b16 v[214:215], v6 offset:21504\n"
    "ds_read_b64_tr_b16 v[216:217], v7 offset:16384\n"
    "ds_read_b64_tr_b16 v[218:219], v7 offset:17408\n"
    "ds_read_b64_tr_b16 v[220:221], v7 offset:20480\n"
    "ds_read_b64_tr_b16 v[222:223], v7 offset:21504\n"
    "s_waitcnt lgkmcnt(0)\n"
    "s_xor_b32 s42, s71, 3\n"
    "s_lshl_b32 s42, s42, 18\n"
    "s_add_u32 s42, s42, 0\n"
    "s_add_u32 s42, s42, s92\n"
    "s_add_u32 m0, s41, 16384\n"
    "s_add_u32 s43, s42, 0\n"
    "buffer_load_dwordx4 v16, s[88:91], s43 offen lds\n"
    "s_add_u32 m0, s41, 17408\n"
    "s_add_u32 s43, s42, 4096\n"
    "buffer_load_dwordx4 v16, s[88:91], s43 offen lds\n"
    "s_add_u32 m0, s41, 18432\n"
    "s_add_u32 s43, s42, 131072\n"
    "buffer_load_dwordx4 v16, s[88:91], s43 offen lds\n"
    "s_add_u32 m0, s41, 19456\n"
    "s_add_u32 s43, s42, 135168\n"
    "buffer_load_dwordx4 v16, s[88:91], s43 offen lds\n"
    "s_xor_b32 s42, s71, 3\n"
    "s_lshl_b32 s42, s42, 18\n"
    "s_add_u32 s42, s42, 16384\n"
    "s_add_u32 s42, s42, s92\n"
    "s_add_u32 m0, s41, 20480\n"
    "s_add_u32 s43, s42, 0\n"
    "buffer_load_dwordx4 v16, s[88:91], s43 offen lds\n"
    "s_add_u32 m0, s41, 21504\n"
    "s_add_u32 s43, s42, 4096\n"
    "buffer_load_dwordx4 v16, s[88:91], s43 offen lds\n"
    "s_add_u32 m0, s41, 22528\n"
    "s_add_u32 s43, s42, 131072\n"
    "buffer_load_dwordx4 v16, s[88:91], s43 offen lds\n"
    "s_add_u32 m0, s41, 23552\n"
    "s_add_u32 s43, s42, 135168\n"
    "buffer_load_dwordx4 v16, s[88:91], s43 offen lds\n"
    "s_waitcnt vmcnt(33)\n"
    "ds_read_b64_tr_b16 v[224:225], v4 offset:24576\n"
    "ds_read_b64_tr_b16 v[226:227], v4 offset:25600\n"
    "ds_read_b64_tr_b16 v[228:229], v4 offset:28672\n"
    "ds_read_b64_tr_b16 v[230:231], v4 offset:29696\n"
    "ds_read_b64_tr_b16 v[232:233], v5 offset:24576\n"
    "ds_read_b64_tr_b16 v[234:235], v5 offset:25600\n"
    "ds_read_b64_tr_b16 v[236:237], v5 offset:28672\n"
    "ds_read_b64_tr_b16 v[238:239], v5 offset:29696\n"
    "ds_read_b64_tr_b16 v[240:241], v6 offset:24576\n"
    "ds_read_b64_tr_b16 v[242:243], v6 offset:25600\n"
    "ds_read_b64_tr_b16 v[244:245], v6 offset:28672\n"
    "ds_read_b64_tr_b16 v[246:247], v6 offset:29696\n"
    "ds_read_b64_tr_b16 v[248:249], v7 offset:24576\n"
    "ds_read_b64_tr_b16 v[250:251], v7 offset:25600\n"
    "ds_read_b64_tr_b16 v[252:253], v7 offset:28672\n"
    "ds_read_b64_tr_b16 v[254:255], v7 offset:29696\n"
    "s_waitcnt lgkmcnt(0)\n"
    "s_xor_b32 s42, s71, 4\n"
    "s_lshl_b32 s42, s42, 18\n"
    "s_add_u32 s42, s42, 0\n"
    "s_add_u32 s42, s42, s92\n"
    "s_add_u32 m0, s41, 24576\n"
    "s_add_u32 s43, s42, 0\n"
    "buffer_load_dwordx4 v16, s[88:91], s43 offen lds\n"
    "s_add_u32 m0, s41, 25600\n"
    "s_add_u32 s43, s42, 4096\n"
    "buffer_load_dwordx4 v16, s[88:91], s43 offen lds\n"
    "s_add_u32 m0, s41, 26624\n"
    "s_add_u32 s43, s42, 131072\n"
    "buffer_load_dwordx4 v16, s[88:91], s43 offen lds\n"
    "s_add_u32 m0, s41, 27648\n"
    "s_add_u32 s43, s42, 135168\n"
    "buffer_load_dwordx4 v16, s[88:91], s43 offen lds\n"
    "s_xor_b32 s42, s71, 4\n"
    "s_lshl_b32 s42, s42, 18\n"
    "s_add_u32 s42, s42, 16384\n"
    "s_add_u32 s42, s42, s92\n"
    "s_add_u32 m0, s41, 28672\n"
    "s_add_u32 s43, s42, 0\n"
    "buffer_load_dwordx4 v16, s[88:91], s43 offen lds\n"
    "s_add_u32 m0, s41, 29696\n"
    "s_add_u32 s43, s42, 4096\n"
    "buffer_load_dwordx4 v16, s[88:91], s43 offen lds\n"
    "s_add_u32 m0, s41, 30720\n"
    "s_add_u32 s43, s42, 131072\n"
    "buffer_load_dwordx4 v16, s[88:91], s43 offen lds\n"
    "s_add_u32 m0, s41, 31744\n"
    "s_add_u32 s43, s42, 135168\n"
    "buffer_load_dwordx4 v16, s[88:91], s43 offen lds\n"
    "s_waitcnt vmcnt(37)\n"
    "ds_read_b64_tr_b16 a[0:1], v18 offset:32768\n"
    "ds_read_b64_tr_b16 a[2:3], v18 offset:33792\n"
    "ds_read_b64_tr_b16 a[4:5], v18 offset:34816\n"
    "ds_read_b64_tr_b16 a[6:7], v18 offset:35840\n"
    "ds_read_b64_tr_b16 a[8:9], v19 offset:32768\n"
    "ds_read_b64_tr_b16 a[10:11], v19 offset:33792\n"
    "ds_read_b64_tr_b16 a[12:13], v19 offset:34816\n"
    "ds_read_b64_tr_b16 a[14:15], v19 offset:35840\n"
    "s_waitcnt lgkmcnt(0)\n"
    "s_xor_b32 s42, s71, 5\n"
    "s_lshl_b32 s42, s42, 18\n"
    "s_add_u32 s42, s42, 0\n"
    "s_add_u32 s42, s42, s92\n"
    "s_add_u32 m0, s41, 32768\n"
    "s_add_u32 s43, s42, 0\n"
    "buffer_load_dwordx4 v16, s[88:91], s43 offen lds\n"
    "s_add_u32 m0, s41, 33792\n"
    "s_add_u32 s43, s42, 4096\n"
    "buffer_load_dwordx4 v16, s[88:91], s43 offen lds\n"
    "s_add_u32 m0, s41, 34816\n"
    "s_add_u32 s43, s42, 131072\n"
    "buffer_load_dwordx4 v16, s[88:91], s43 offen lds\n"
    "s_add_u32 m0, s41, 35840\n"
    "s_add_u32 s43, s42, 135168\n"
    "buffer_load_dwordx4 v16, s[88:91], s43 offen lds\n"
    "s_waitcnt vmcnt(37)\n"
    "ds_read_b64_tr_b16 a[16:17], v18 offset:36864\n"
    "ds_read_b64_tr_b16 a[18:19], v18 offset:37888\n"
    "ds_read_b64_tr_b16 a[20:21], v18 offset:38912\n"
    "ds_read_b64_tr_b16 a[22:23], v18 offset:39936\n"
    "ds_read_b64_tr_b16 a[24:25], v19 offset:36864\n"
    "ds_read_b64_tr_b16 a[26:27], v19 offset:37888\n"
    "ds_read_b64_tr_b16 a[28:29], v19 offset:38912\n"
    "ds_read_b64_tr_b16 a[30:31], v19 offset:39936\n"
    "s_waitcnt lgkmcnt(0)\n"
    "s_xor_b32 s42, s71, 5\n"
    "s_lshl_b32 s42, s42, 18\n"
    "s_add_u32 s42, s42, 16384\n"
    "s_add_u32 s42, s42, s92\n"
    "s_add_u32 m0, s41, 36864\n"
    "s_add_u32 s43, s42, 0\n"
    "buffer_load_dwordx4 v16, s[88:91], s43 offen lds\n"
    "s_add_u32 m0, s41, 37888\n"
    "s_add_u32 s43, s42, 4096\n"
    "buffer_load_dwordx4 v16, s[88:91], s43 offen lds\n"
    "s_add_u32 m0, s41, 38912\n"
    "s_add_u32 s43, s42, 131072\n"
    "buffer_load_dwordx4 v16, s[88:91], s43 offen lds\n"
    "s_add_u32 m0, s41, 39936\n"
    "s_add_u32 s43, s42, 135168\n"
    "buffer_load_dwordx4 v16, s[88:91], s43 offen lds\n"
    "s_waitcnt vmcnt(36)\n"
    "ds_read_b64_tr_b16 a[32:33], v18 offset:0\n"
    "ds_read_b64_tr_b16 a[34:35], v18 offset:1024\n"
    "ds_read_b64_tr_b16 a[36:37], v18 offset:2048\n"
    "ds_read_b64_tr_b16 a[38:39], v18 offset:3072\n"
    "ds_read_b64_tr_b16 a[40:41], v19 offset:0\n"
    "ds_read_b64_tr_b16 a[42:43], v19 offset:1024\n"
    "ds_read_b64_tr_b16 a[44:45], v19 offset:2048\n"
    "ds_read_b64_tr_b16 a[46:47], v19 offset:3072\n"
    "s_waitcnt lgkmcnt(0)\n"
    "s_xor_b32 s42, s71, 6\n"
    "s_lshl_b32 s42, s42, 18\n"
    "s_add_u32 s42, s42, 0\n"
    "s_add_u32 s42, s42, s92\n"
    "s_add_u32 m0, s41, 0\n"
    "s_add_u32 s43, s42, 0\n"
    "buffer_load_dwordx4 v16, s[88:91], s43 offen lds\n"
    "s_add_u32 m0, s41, 1024\n"
    "s_add_u32 s43, s42, 4096\n"
    "buffer_load_dwordx4 v16, s[88:91], s43 offen lds\n"
    "s_add_u32 m0, s41, 2048\n"
    "s_add_u32 s43, s42, 131072\n"
    "buffer_load_dwordx4 v16, s[88:91], s43 offen lds\n"
    "s_add_u32 m0, s41, 3072\n"
    "s_add_u32 s43, s42, 135168\n"
    "buffer_load_dwordx4 v16, s[88:91], s43 offen lds\n"
    "s_waitcnt vmcnt(36)\n"
    "ds_read_b64_tr_b16 a[48:49], v18 offset:4096\n"
    "ds_read_b64_tr_b16 a[50:51], v18 offset:5120\n"
    "ds_read_b64_tr_b16 a[52:53], v18 offset:6144\n"
    "ds_read_b64_tr_b16 a[54:55], v18 offset:7168\n"
    "ds_read_b64_tr_b16 a[56:57], v19 offset:4096\n"
    "ds_read_b64_tr_b16 a[58:59], v19 offset:5120\n"
    "ds_read_b64_tr_b16 a[60:61], v19 offset:6144\n"
    "ds_read_b64_tr_b16 a[62:63], v19 offset:7168\n"
    "s_waitcnt lgkmcnt(0)\n"
    "s_xor_b32 s42, s71, 6\n"
    "s_lshl_b32 s42, s42, 18\n"
    "s_add_u32 s42, s42, 16384\n"
    "s_add_u32 s42, s42, s92\n"
    "s_add_u32 m0, s41, 4096\n"
    "s_add_u32 s43, s42, 0\n"
    "buffer_load_dwordx4 v16, s[88:91], s43 offen lds\n"
    "s_add_u32 m0, s41, 5120\n"
    "s_add_u32 s43, s42, 4096\n"
    "buffer_load_dwordx4 v16, s[88:91], s43 offen lds\n"
    "s_add_u32 m0, s41, 6144\n"
    "s_add_u32 s43, s42, 131072\n"
    "buffer_load_dwordx4 v16, s[88:91], s43 offen lds\n"
    "s_add_u32 m0, s41, 7168\n"
    "s_add_u32 s43, s42, 135168\n"
    "buffer_load_dwordx4 v16, s[88:91], s43 offen lds\n"
    "s_add_u32 s10, s19, 69632\n"
    "s_mov_b64 exec, 1\n"
    "buffer_load_dword v0, off, s[12:15], s10 sc1\n"
    "s_mov_b64 exec, -1\n"
    "s_waitcnt vmcnt(37)\n"
    "ds_read_b64_tr_b16 a[64:65], v18 offset:8192\n"
    "ds_read_b64_tr_b16 a[66:67], v18 offset:9216\n"
    "ds_read_b64_tr_b16 a[68:69], v18 offset:10240\n"
    "ds_read_b64_tr_b16 a[70:71], v18 offset:11264\n"
    "ds_read_b64_tr_b16 a[72:73], v19 offset:8192\n"
    "ds_read_b64_tr_b16 a[74:75], v19 offset:9216\n"
    "ds_read_b64_tr_b16 a[76:77], v19 offset:10240\n"
    "ds_read_b64_tr_b16 a[78:79], v19 offset:11264\n"
    "s_waitcnt lgkmcnt(0)\n"
    "s_xor_b32 s42, s71, 7\n"
    "s_lshl_b32 s42, s42, 18\n"
    "s_add_u32 s42, s42, 0\n"
    "s_add_u32 s42, s42, s92\n"
    "s_add_u32 m0, s41, 8192\n"
    "s_add_u32 s43, s42, 0\n"
    "buffer_load_dwordx4 v16, s[88:91], s43 offen lds\n"
    "s_add_u32 m0, s41, 9216\n"
    "s_add_u32 s43, s42, 4096\n"
    "buffer_load_dwordx4 v16, s[88:91], s43 offen lds\n"
    "s_add_u32 m0, s41, 10240\n"
    "s_add_u32 s43, s42, 131072\n"
    "buffer_load_dwordx4 v16, s[88:91], s43 offen lds\n"
    "s_add_u32 m0, s41, 11264\n"
    "s_add_u32 s43, s42, 135168\n"
    "buffer_load_dwordx4 v16, s[88:91], s43 offen lds\n"
    "s_getpc_b64 s[0:1]\n"
    ".Lvo_pc2_%=:\n"
    "s_and_b32 s1, s1, 0xffff\n"
    "s_mov_b32 s2, .Lk_end_%=-.Lvo_pc2_%=\n"
    "s_mov_b32 s3, 0x20000\n"
    "s_lshl_b32 s4, s25, 13\n"
    "v_lshlrev_b32 v72, 7, v1\n"
    "buffer_load_dword v72, v72, s[0:3], s4 offen\n"
    "s_waitcnt vmcnt(38)\n"
    "ds_read_b64_tr_b16 a[80:81], v18 offset:12288\n"
    "ds_read_b64_tr_b16 a[82:83], v18 offset:13312\n"
    "ds_read_b64_tr_b16 a[84:85], v18 offset:14336\n"
    "ds_read_b64_tr_b16 a[86:87], v18 offset:15360\n"
    "ds_read_b64_tr_b16 a[88:89], v19 offset:12288\n"
    "ds_read_b64_tr_b16 a[90:91], v19 offset:13312\n"
    "ds_read_b64_tr_b16 a[92:93], v19 offset:14336\n"
    "ds_read_b64_tr_b16 a[94:95], v19 offset:15360\n"
    "s_waitcnt lgkmcnt(0)\n"
    "s_xor_b32 s42, s71, 7\n"
    "s_lshl_b32 s42, s42, 18\n"
    "s_add_u32 s42, s42, 16384\n"
    "s_add_u32 s42, s42, s92\n"
    "s_add_u32 m0, s41, 12288\n"
    "s_add_u32 s43, s42, 0\n"
    "buffer_load_dwordx4 v16, s[88:91], s43 offen lds\n"
    "s_add_u32 m0, s41, 13312\n"
    "s_add_u32 s43, s42, 4096\n"
    "buffer_load_dwordx4 v16, s[88:91], s43 offen lds\n"
    "s_add_u32 m0, s41, 14336\n"
    "s_add_u32 s43, s42, 131072\n"
    "buffer_load_dwordx4 v16, s[88:91], s43 offen lds\n"
    "s_add_u32 m0, s41, 15360\n"
    "s_add_u32 s43, s42, 135168\n"
    "buffer_load_dwordx4 v16, s[88:91], s43 offen lds\n"
    "s_waitcnt vmcnt(38)\n"
    "ds_read_b64_tr_b16 a[96:97], v18 offset:16384\n"
    "ds_read_b64_tr_b16 a[98:99], v18 offset:17408\n"
    "ds_read_b64_tr_b16 a[100:101], v18 offset:18432\n"
    "ds_read_b64_tr_b16 a[102:103], v18 offset:19456\n"
    "ds_read_b64_tr_b16 a[104:105], v19 offset:16384\n"
    "ds_read_b64_tr_b16 a[106:107], v19 offset:17408\n"
    "ds_read_b64_tr_b16 a[108:109], v19 offset:18432\n"
    "ds_read_b64_tr_b16 a[110:111], v19 offset:19456\n"
    "s_waitcnt lgkmcnt(0)\n"
    "s_waitcnt vmcnt(34)\n"
    "ds_read_b64_tr_b16 a[112:113], v18 offset:20480\n"
    "ds_read_b64_tr_b16 a[114:115], v18 offset:21504\n"
    "ds_read_b64_tr_b16 a[116:117], v18 offset:22528\n"
    "ds_read_b64_tr_b16 a[118:119], v18 offset:23552\n"
    "ds_read_b64_tr_b16 a[120:121], v19 offset:20480\n"
    "ds_read_b64_tr_b16 a[122:123], v19 offset:21504\n"
    "ds_read_b64_tr_b16 a[124:125], v19 offset:22528\n"
    "ds_read_b64_tr_b16 a[126:127], v19 offset:23552\n"
    "s_waitcnt lgkmcnt(0)\n"
    "s_waitcnt vmcnt(30)\n"
    "ds_read_b64_tr_b16 a[128:129], v18 offset:24576\n"
    "ds_read_b64_tr_b16 a[130:131], v18 offset:25600\n"
    "ds_read_b64_tr_b16 a[132:133], v18 offset:26624\n"
    "ds_read_b64_tr_b16 a[134:135], v18 offset:27648\n"
    "ds_read_b64_tr_b16 a[136:137], v19 offset:24576\n"
    "ds_read_b64_tr_b16 a[138:139], v19 offset:25600\n"
    "ds_read_b64_tr_b16 a[140:141], v19 offset:26624\n"
    "ds_read_b64_tr_b16 a[142:143], v19 offset:27648\n"
    "s_waitcnt lgkmcnt(0)\n"
    "s_waitcnt vmcnt(26)\n"
    "ds_read_b64_tr_b16 a[144:145], v18 offset:28672\n"
    "ds_read_b64_tr_b16 a[146:147], v18 offset:29696\n"
    "ds_read_b64_tr_b16 a[148:149], v18 offset:30720\n"
    "ds_read_b64_tr_b16 a[150:151], v18 offset:31744\n"
    "ds_read_b64_tr_b16 a[152:153], v19 offset:28672\n"
    "ds_read_b64_tr_b16 a[154:155], v19 offset:29696\n"
    "ds_read_b64_tr_b16 a[156:157], v19 offset:30720\n"
    "ds_read_b64_tr_b16 a[158:159], v19 offset:31744\n"
    "s_waitcnt lgkmcnt(0)\n"
    "s_waitcnt vmcnt(22)\n"
    "ds_read_b64_tr_b16 a[160:161], v18 offset:32768\n"
    "ds_read_b64_tr_b16 a[162:163], v18 offset:33792\n"
    "ds_read_b64_tr_b16 a[164:165], v18 offset:34816\n"
    "ds_read_b64_tr_b16 a[166:167], v18 offset:35840\n"
    "ds_read_b64_tr_b16 a[168:169], v19 offset:32768\n"
    "ds_read_b64_tr_b16 a[170:171], v19 offset:33792\n"
    "ds_read_b64_tr_b16 a[172:173], v19 offset:34816\n"
    "ds_read_b64_tr_b16 a[174:175], v19 offset:35840\n"
    "s_waitcnt lgkmcnt(0)\n"
    "s_waitcnt vmcnt(18)\n"
    "ds_read_b64_tr_b16 a[176:177], v18 offset:36864\n"
    "ds_read_b64_tr_b16 a[178:179], v18 offset:37888\n"
    "ds_read_b64_tr_b16 a[180:181], v18 offset:38912\n"
    "ds_read_b64_tr_b16 a[182:183], v18 offset:39936\n"
    "ds_read_b64_tr_b16 a[184:185], v19 offset:36864\n"
    "ds_read_b64_tr_b16 a[186:187], v19 offset:37888\n"
    "ds_read_b64_tr_b16 a[188:189], v19 offset:38912\n"
    "ds_read_b64_tr_b16 a[190:191], v19 offset:39936\n"
    "s_waitcnt lgkmcnt(0)\n"
    "s_waitcnt vmcnt(9)\n"
    ".Lvo_hsc_%=:\n"
    "v_readfirstlane_b32 s10, v0\n"
    "s_nop 1\n"
    "s_andn2_b32 s11, s10, 0xff\n"
    "s_cmp_lg_u32 s11, s18\n"
    "s_cbranch_scc1 .Lvo_hsp_%=\n"
    "s_and_b32 s11, s10, 7\n"
    "s_cmp_lg_u32 s11, s17\n"
    "s_cbranch_scc1 .Lvo_hsr_%=\n"
    "s_and_b32 s10, s10, 8\n"
    "s_lshl_b32 s27, s10, 10\n"
    "s_add_u32 s10, s19, 12288\n"
    "v_mov_b32 v0, 1\n"
    "s_mov_b64 exec, 1\n"
    "buffer_store_dword v0, off, s[12:15], s10 sc1\n"
    "s_mov_b64 exec, -1\n"
    "s_add_u32 s42, s27, 0\n"
    "s_add_u32 s32, s30, s42\n"
    "s_addc_u32 s33, s31, 0\n"
    "s_and_b32 s33, s33, 0xffff\n"
    "s_mov_b32 s34, 1572864\n"
    "s_mov_b32 s35, 0x20000\n"
    "s_add_u32 s60, s30, 0\n"
    "s_addc_u32 s61, s31, 0\n"
    "s_and_b32 s61, s61, 0xffff\n"
    "s_mov_b32 s62, 65536\n"
    "s_mov_b32 s63, 0x20000\n"
    "v_lshl_or_b32 v64, v65, 16, v64\n"
    "v_min_u32 v64, 0xfffffffe, v64\n"
    "v_mov_b64 v[96:97], -1\n"
    "v_mov_b64 v[98:99], -1\n"
    "v_lshlrev_b32 v66, 4, v1\n"
    "s_cmp_gt_u32 s25, 0\n"
    "s_cselect_b32 s50, 0x1000000, 0\n"
    "s_mov_b32 s43, 0\n"
    "buffer_store_dword v64, v11, s[32:35], s43 offen sc1\n"
    "s_mov_b32 s43, 131072\n"
    "buffer_store_dword v96, v11, s[32:35], s43 offen sc1\n"
    "s_mov_b32 s43, 262144\n"
    "buffer_store_dword v96, v11, s[32:35], s43 offen sc1\n"
    "s_mov_b32 s43, 393216\n"
    "buffer_store_dword v96, v11, s[32:35], s43 offen sc1\n"
    "s_lshr_b32 s42, s24, 3\n"
    "s_lshl_b32 s42, s42, 14\n"
    "s_and_b32 s43, s24, 7\n"
    "s_lshl_b32 s43, s43, 10\n"
    "s_add_u32 s42, s42, s43\n"
    "s_add_u32 s42, s42, s50\n"
    "s_add_u32 s43, s42, 262144\n"
    "buffer_store_dwordx4 v[96:99], v66, s[32:35], s43 offen sc1\n"
    "s_add_u32 s43, s42, 393216\n"
    "buffer_store_dwordx4 v[96:99], v66, s[32:35], s43 offen sc1\n"
    "s_add_u32 s64, s27, 16384\n"
    "s_add_u32 s65, s27, 49152\n"
    "s_lshl_b32 s42, s93, 7\n"
    "s_add_u32 s66, s64, s42\n"
    "s_add_u32 s66, s66, s50\n"
    "ds_read_b64_tr_b16 a[192:193], v18 offset:0\n"
    "ds_read_b64_tr_b16 a[194:195], v18 offset:1024\n"
    "ds_read_b64_tr_b16 a[196:197], v18 offset:2048\n"
    "ds_read_b64_tr_b16 a[198:199], v18 offset:3072\n"
    "ds_read_b64_tr_b16 a[200:201], v19 offset:0\n"
    "ds_read_b64_tr_b16 a[202:203], v19 offset:1024\n"
    "ds_read_b64_tr_b16 a[204:205], v19 offset:2048\n"
    "ds_read_b64_tr_b16 a[206:207], v19 offset:3072\n"
    "s_waitcnt lgkmcnt(0)\n"
    "ds_read_b64_tr_b16 a[208:209], v18 offset:4096\n"
    "ds_read_b64_tr_b16 a[210:211], v18 offset:5120\n"
    "ds_read_b64_tr_b16 a[212:213], v18 offset:6144\n"
    "ds_read_b64_tr_b16 a[214:215], v18 offset:7168\n"
    "ds_read_b64_tr_b16 a[216:217], v19 offset:4096\n"
    "ds_read_b64_tr_b16 a[218:219], v19 offset:5120\n"
    "ds_read_b64_tr_b16 a[220:221], v19 offset:6144\n"
    "ds_read_b64_tr_b16 a[222:223], v19 offset:7168\n"
    "s_waitcnt lgkmcnt(0)\n"
    "s_waitcnt vmcnt(12)\n"
    "ds_read_b64_tr_b16 a[224:225], v18 offset:8192\n"
    "ds_read_b64_tr_b16 a[226:227], v18 offset:9216\n"
    "ds_read_b64_tr_b16 a[228:229], v18 offset:10240\n"
    "ds_read_b64_tr_b16 a[230:231], v18 offset:11264\n"
    "ds_read_b64_tr_b16 a[232:233], v19 offset:8192\n"
    "ds_read_b64_tr_b16 a[234:235], v19 offset:9216\n"
    "ds_read_b64_tr_b16 a[236:237], v19 offset:10240\n"
    "ds_read_b64_tr_b16 a[238:239], v19 offset:11264\n"
    "s_waitcnt lgkmcnt(0)\n"
    "s_waitcnt vmcnt(7)\n"
    "ds_read_b64_tr_b16 a[240:241], v18 offset:12288\n"
    "ds_read_b64_tr_b16 a[242:243], v18 offset:13312\n"
    "ds_read_b64_tr_b16 a[244:245], v18 offset:14336\n"
    "ds_read_b64_tr_b16 a[246:247], v18 offset:15360\n"
    "ds_read_b64_tr_b16 a[248:249], v19 offset:12288\n"
    "ds_read_b64_tr_b16 a[250:251], v19 offset:13312\n"
    "ds_read_b64_tr_b16 a[252:253], v19 offset:14336\n"
    "ds_read_b64_tr_b16 a[254:255], v19 offset:15360\n"
    "s_waitcnt lgkmcnt(0)\n"
    "s_waitcnt vmcnt(0)\n"
    "v_and_b32 v2, 1, v1\n"
    "v_mul_u32_u24 v2, 0xaaaa, v2\n"
    "v_add_u32 v8, 0x4444, v2\n"
    "s_lshr_b32 s50, s24, 4\n"
    "s_xor_b32 s50, s50, s25\n"
    "s_and_b32 s42, s50, 1\n"
    "s_lshl_b32 s42, s42, 14\n"
    "s_bfe_u32 s43, s50, 65537\n"
    "s_lshl_b32 s43, s43, 12\n"
    "s_add_u32 s42, s42, s43\n"
    "s_lshl_b32 s43, s25, 15\n"
    "s_add_u32 s42, s42, s43\n"
    "s_and_b32 s50, s42, 114688\n"
    "s_add_u32 s42, s42, 1048576\n"
    "v_bfe_u32 v9, v1, 5, 1\n"
    "v_lshlrev_b32 v9, 9, v9\n"
    "v_bfe_u32 v2, v1, 3, 1\n"
    "v_lshl_add_u32 v9, v2, 8, v9\n"
    "v_bfe_u32 v2, v1, 1, 2\n"
    "v_lshl_add_u32 v9, v2, 6, v9\n"
    "v_bfe_u32 v2, v1, 4, 1\n"
    "v_lshl_add_u32 v9, v2, 5, v9\n"
    "v_bfe_u32 v2, v1, 0, 1\n"
    "v_lshl_add_u32 v9, v2, 4, v9\n"
    "v_add_u32 v9, s42, v9\n"
    "v_bfe_u32 v10, v1, 3, 3\n"
    "v_lshlrev_b32 v10, 7, v10\n"
    "v_bfe_u32 v2, v1, 0, 2\n"
    "v_lshl_add_u32 v10, v2, 5, v10\n"
    "v_bfe_u32 v2, v1, 2, 1\n"
    "v_lshl_add_u32 v10, v2, 4, v10\n"
    "v_add_u32 v10, s50, v10\n"
    "s_lshr_b32 s42, s24, 3\n"
    "s_lshl_b32 s42, s42, 14\n"
    "s_and_b32 s43, s24, 7\n"
    "s_lshl_b32 s43, s43, 10\n"
    "s_add_u32 s42, s42, s43\n"
    "s_and_b32 s43, s25, 1\n"
    "s_lshl_b32 s43, s43, 9\n"
    "s_add_u32 s42, s42, s43\n"
    "v_bfe_u32 v12, v1, 5, 1\n"
    "v_lshlrev_b32 v12, 8, v12\n"
    "v_bfe_u32 v2, v1, 0, 4\n"
    "v_lshl_add_u32 v12, v2, 4, v12\n"
    "v_bfe_u32 v2, v1, 4, 1\n"
    "v_lshl_add_u32 v12, v2, 3, v12\n"
    "v_add_u32 v12, s42, v12\n"
    "s_lshl_b32 s42, s25, 11\n"
    "v_lshlrev_b32 v2, 4, v1\n"
    "v_add_u32 v13, s42, v2\n"
    "s_and_b32 s42, s25, 1\n"
    "s_lshl_b32 s42, s42, 10\n"
    "v_add_u32 v14, s42, v2\n"
    "s_lshl_b32 s42, s25, 9\n"
    "v_lshlrev_b32 v2, 3, v1\n"
    "v_add_u32 v15, s42, v2\n"
    "v_xor_b32 v5, 512, v15\n"
    "v_xor_b32 v6, 1024, v15\n"
    "v_xor_b32 v7, 1536, v15\n"
    "s_mov_b32 s44, 0\n"
    "s_lshl_b32 s42, s25, 10\n"
    "v_bfe_u32 v100, v1, 0, 3\n"
    "v_lshlrev_b32 v100, 7, v100\n"
    "v_bfe_u32 v2, v1, 3, 1\n"
    "v_lshl_add_u32 v100, v2, 12, v100\n"
    "v_add_u32 v100, s42, v100\n"
    "s_barrier\n"
    "v_mov_b32 v67, 1\n"
    "s_mov_b64 exec, 1\n"
    "buffer_store_dword v67, off, s[60:63], s66 sc1\n"
    "s_mov_b64 exec, -1\n"
    "s_mov_b32 s67, 1\n"
    "s_mov_b64 exec, 65535\n"
    "buffer_load_dword v104, v100, s[60:63], s64 offen sc1\n"
    ".Lvo_ivbl_%=:\n"
    "s_waitcnt vmcnt(0)\n"
    "v_cmp_gt_u32 vcc, s67, v104\n"
    "s_cbranch_vccz .Lvo_ivbok_%=\n"
    "buffer_load_dword v104, v100, s[60:63], s64 offen sc1\n"
    "s_sub_u32 s26, s26, 1\n"
    "s_cbranch_scc0 .Lvo_ivbl_%=\n"
    "s_endpgm\n"
    ".Lvo_ivbok_%=:\n"
    "s_mov_b64 exec, -1\n"
    "v_lshlrev_b32 v100, 4, v1\n"
    "s_memtime s[16:17]\n"
    "s_memrealtime s[0:1]\n"
    "s_nop 2\n"
    "s_memrealtime s[2:3]\n"
    "s_nop 2\n"
    "s_memrealtime s[4:5]\n"
    "s_nop 2\n"
    "s_memrealtime s[6:7]\n"
    "s_nop 2\n"
    "s_memrealtime s[8:9]\n"
    "s_nop 2\n"
    "s_memrealtime s[10:11]\n"
    "s_nop 2\n"
    "s_memrealtime s[12:13]\n"
    "s_nop 2\n"
    "s_memrealtime s[14:15]\n"
    "s_mov_b32 s18, 0\n"
    "s_waitcnt lgkmcnt(0)\n"
    "s_cmp_eq_u32 s2, s0\n"
    "s_addc_u32 s18, s18, 0\n"
    "s_cmp_eq_u32 s4, s0\n"
    "s_addc_u32 s18, s18, 0\n"
    "s_cmp_eq_u32 s6, s0\n"
    "s_addc_u32 s18, s18, 0\n"
    "s_cmp_eq_u32 s8, s0\n"
    "s_addc_u32 s18, s18, 0\n"
    "s_cmp_eq_u32 s10, s0\n"
    "s_addc_u32 s18, s18, 0\n"
    "s_cmp_eq_u32 s12, s0\n"
    "s_addc_u32 s18, s18, 0\n"
    "s_cmp_eq_u32 s14, s0\n"
    "s_addc_u32 s18, s18, 0\n"
    "s_lshl_b32 s18, s18, 4\n"
    "s_add_u32 s16, s16, s18\n"
    "s_add_u32 s0, s0, 4\n"
    "s_lshl_b32 s0, s0, 4\n"
    "s_sub_u32 s18, s0, s84\n"
    "s_sub_u32 s19, s16, s85\n"
    "s_mov_b32 s84, s0\n"
    "s_mov_b32 s85, s16\n"
    "s_mov_b32 s86, 45590\n"
    "s_mov_b32 s87, 94207\n"
    "s_mov_b32 s18, 1600\n"
    "s_mov_b32 s19, 2300\n"
    ".Lvo_step_%=:\n"
    "s_and_b32 s42, s44, 3\n"
    "s_lshl_b32 s45, s42, 17\n"
    "s_add_u32 s67, s44, 1\n"
    "s_and_b32 s42, s67, 3\n"
    "s_lshl_b32 s46, s42, 17\n"
    "s_and_b32 s42, s44, 1\n"
    "s_lshl_b32 s42, s42, 17\n"
    "s_add_u32 s48, s42, 262144\n"
    "s_lshl_b32 s47, s42, 1\n"
    "s_add_u32 s47, s47, 524288\n"
    "s_lshl_b32 s43, s70, 10\n"
    "s_add_u32 s72, s45, s43\n"
    "s_xor_b32 s73, s72, 1024\n"
    "s_xor_b32 s74, s72, 2048\n"
    "s_xor_b32 s75, s72, 3072\n"
    "s_xor_b32 s69, s42, 131072\n"
    "s_add_u32 s69, s69, 262144\n"
    "s_add_u32 s42, s44, 3\n"
    "s_and_b32 s42, s42, 3\n"
    "s_lshl_b32 s68, s42, 17\n"
    ".Lvo_mpoll_%=:\n"
    "buffer_load_dwordx4 v[32:35], v9, s[32:35], s72 offen sc1\n"
    "v_mov_b64 v[16:17], 0\n"
    "v_mov_b64 v[18:19], 0\n"
    "s_nop 11\n"
    "buffer_load_dwordx4 v[36:39], v9, s[32:35], s73 offen sc1\n"
    "v_mov_b64 v[20:21], 0\n"
    "v_mov_b64 v[22:23], 0\n"
    "s_nop 11\n"
    "buffer_load_dwordx4 v[40:43], v9, s[32:35], s74 offen sc1\n"
    "v_mov_b64 v[24:25], 0\n"
    "v_mov_b64 v[26:27], 0\n"
    "s_nop 11\n"
    "buffer_load_dwordx4 v[44:47], v9, s[32:35], s75 offen sc1\n"
    "v_mov_b64 v[28:29], 0\n"
    "v_mov_b64 v[30:31], 0\n"
    ".Lvo_muse_%=:\n"
    "s_waitcnt vmcnt(3)\n"
    "v_smfmac_f32_16x16x64_bf16 v[16:19], v[32:35], v[128:135], v8\n"
    "v_max3_u32 v94, v32, v33, v34\n"
    "v_smfmac_f32_16x16x64_bf16 v[20:23], v[32:35], v[136:143], v8\n"
    "v_max_u32 v94, v94, v35\n"
    "v_smfmac_f32_16x16x64_bf16 v[24:27], v[32:35], v[144:151], v8\n"
    "v_smfmac_f32_16x16x64_bf16 v[28:31], v[32:35], v[152:159], v8\n"
    "s_waitcnt vmcnt(2)\n"
    "v_smfmac_f32_16x16x64_bf16 v[16:19], v[36:39], v[160:167], v8\n"
    "v_max3_u32 v94, v36, v37, v94\n"
    "v_smfmac_f32_16x16x64_bf16 v[20:23], v[36:39], v[168:175], v8\n"
    "v_max3_u32 v94, v38, v39, v94\n"
    "v_smfmac_f32_16x16x64_bf16 v[24:27], v[36:39], v[176:183], v8\n"
    "v_smfmac_f32_16x16x64_bf16 v[28:31], v[36:39], v[184:191], v8\n"
    "s_waitcnt vmcnt(1)\n"
    "v_smfmac_f32_16x16x64_bf16 v[16:19], v[40:43], v[192:199], v8\n"
    "v_max3_u32 v94, v40, v41, v94\n"
    "v_smfmac_f32_16x16x64_bf16 v[20:23], v[40:43], v[200:207], v8\n"
    "v_max3_u32 v94, v42, v43, v94\n"
    "v_smfmac_f32_16x16x64_bf16 v[24:27], v[40:43], v[208:215], v8\n"
    "v_smfmac_f32_16x16x64_bf16 v[28:31], v[40:43], v[216:223], v8\n"
    "s_waitcnt vmcnt(0)\n"
    "v_smfmac_f32_16x16x64_bf16 v[16:19], v[44:47], v[224:231], v8\n"
    "v_max3_u32 v94, v44, v45, v94\n"
    "v_smfmac_f32_16x16x64_bf16 v[20:23], v[44:47], v[232:239], v8\n"
    "v_max3_u32 v94, v46, v47, v94\n"
    "v_smfmac_f32_16x16x64_bf16 v[24:27], v[44:47], v[240:247], v8\n"
    "v_cmp_eq_u32 vcc, -1, v94\n"
    "v_smfmac_f32_16x16x64_bf16 v[28:31], v[44:47], v[248:255], v8\n"
    "s_cbranch_vccnz .Lvo_mretry_%=\n"
    "s_nop 0\n"
    "v_add_f32 v64, v16, v17\n"
    "v_add_f32 v65, v18, v19\n"
    "v_add_f32 v66, v20, v21\n"
    "v_add_f32 v67, v22, v23\n"
    "ds_write_b128 v13, v[64:67] offset:0\n"
    "v_add_f32 v68, v24, v25\n"
    "v_add_f32 v69, v26, v27\n"
    "v_add_f32 v70, v28, v29\n"
    "v_add_f32 v71, v30, v31\n"
    "ds_write_b128 v13, v[68:71] offset:1024\n"
    "s_cmp_gt_u32 s25, 1\n"
    "s_waitcnt lgkmcnt(0)\n"
    "s_barrier\n"
    "s_cbranch_scc1 .Lvo_novm_%=\n"
    "ds_read_b128 v[64:67], v14 offset:0\n"
    "ds_read_b128 v[68:71], v14 offset:2048\n"
    "ds_read_b128 v[72:75], v14 offset:4096\n"
    "ds_read_b128 v[76:79], v14 offset:6144\n"
    "s_waitcnt lgkmcnt(2)\n"
    "v_pk_add_f32 v[64:65], v[64:65], v[68:69]\n"
    "v_pk_add_f32 v[66:67], v[66:67], v[70:71]\n"
    "s_waitcnt lgkmcnt(0)\n"
    "v_pk_add_f32 v[72:73], v[72:73], v[76:77]\n"
    "v_pk_add_f32 v[74:75], v[74:75], v[78:79]\n"
    "v_pk_add_f32 v[64:65], v[64:65], v[72:73]\n"
    "v_pk_add_f32 v[66:67], v[66:67], v[74:75]\n"
    "v_cvt_pk_bf16_f32 v80, v64, v65\n"
    "v_cvt_pk_bf16_f32 v81, v66, v67\n"
    "buffer_store_dwordx2 v[80:81], v12, s[32:35], s48 offen sc1\n"
    "buffer_store_dwordx2 v[96:97], v12, s[32:35], s69 offen sc1\n"
    ".Lvo_novm_%=:\n"
    "buffer_store_dword v96, v11, s[32:35], s68 offen sc1\n"
    "s_cmp_eq_u32 s44, 63\n"
    "s_cbranch_scc1 .Lvo_done_%=\n"
    "s_lshl_b32 s43, s71, 10\n"
    "s_add_u32 s76, s47, s43\n"
    "s_xor_b32 s77, s76, 1024\n"
    "s_xor_b32 s78, s76, 2048\n"
    "s_xor_b32 s79, s76, 3072\n"
    "s_xor_b32 s80, s76, 4096\n"
    "s_xor_b32 s81, s76, 5120\n"
    "s_xor_b32 s82, s76, 6144\n"
    "s_xor_b32 s83, s76, 7168\n"
    "s_cmp_eq_u32 s44, 3\n"
    "s_cbranch_scc1 .Lvo_near_%=\n"
    ".Lvo_nearb_%=:\n"
    "s_cmp_lg_u32 s25, 2\n"
    "s_cbranch_scc1 .Lvo_oae_%=\n"
    "s_memtime s[16:17]\n"
    "s_memrealtime s[0:1]\n"
    "s_nop 2\n"
    "s_memrealtime s[2:3]\n"
    "s_nop 2\n"
    "s_memrealtime s[4:5]\n"
    "s_nop 2\n"
    "s_memrealtime s[6:7]\n"
    "s_nop 2\n"
    "s_memrealtime s[8:9]\n"
    "s_nop 2\n"
    "s_memrealtime s[10:11]\n"
    "s_nop 2\n"
    "s_memrealtime s[12:13]\n"
    "s_nop 2\n"
    "s_memrealtime s[14:15]\n"
    "s_mov_b32 s18, 0\n"
    "s_waitcnt lgkmcnt(0)\n"
    "s_cmp_eq_u32 s2, s0\n"
    "s_addc_u32 s18, s18, 0\n"
    "s_cmp_eq_u32 s4, s0\n"
    "s_addc_u32 s18, s18, 0\n"
    "s_cmp_eq_u32 s6, s0\n"
    "s_addc_u32 s18, s18, 0\n"
    "s_cmp_eq_u32 s8, s0\n"
    "s_addc_u32 s18, s18, 0\n"
    "s_cmp_eq_u32 s10, s0\n"
    "s_addc_u32 s18, s18, 0\n"
    "s_cmp_eq_u32 s12, s0\n"
    "s_addc_u32 s18, s18, 0\n"
    "s_cmp_eq_u32 s14, s0\n"
    "s_addc_u32 s18, s18, 0\n"
    "s_lshl_b32 s18, s18, 4\n"
    "s_add_u32 s16, s16, s18\n"
    "s_add_u32 s0, s0, 4\n"
    "s_lshl_b32 s0, s0, 4\n"
    "s_sub_u32 s18, s0, s84\n"
    "s_sub_u32 s19, s16, s85\n"
    "s_mov_b32 s84, s0\n"
    "s_mov_b32 s85, s16\n"
    "v_cvt_f32_u32 v107, s19\n"
    "v_cvt_f32_u32 v106, s18\n"
    "v_rcp_f32 v107, v107\n"
    "s_nop 0\n"
    "v_mul_f32 v106, v106, v107\n"
    "v_med3_f32 v106, v106, 0.5, 1.0\n"
    "v_rcp_f32 v107, v106\n"
    "v_mul_f32 v106, 0x47800000, v106\n"
    "v_mul_f32 v107, 0x47800000, v107\n"
    "v_cvt_u32_f32 v106, v106\n"
    "v_cvt_u32_f32 v107, v107\n"
    "v_readfirstlane_b32 s86, v106\n"
    "v_readfirstlane_b32 s87, v107\n"
    "s_nop 15\n"
    "s_nop 8\n"
    "v_lshlrev_b32 v101, 3, v100\n"
    "s_mov_b32 exec_hi, 0\n"
    "buffer_load_dwordx2 v[104:105], v101, s[60:63], s65 offen sc1\n"
    "s_mov_b64 exec, -1\n"
    "s_lshl_b32 s6, s67, 20\n"
    ".Lvo_oac_%=:\n"
    "s_waitcnt vmcnt(0)\n"
    "v_cmp_gt_u32 vcc, s6, v105\n"
    "v_subrev_u32 v104, s84, v104\n"
    "v_and_b32 v105, 0xfffff, v105\n"
    "s_cmp_lg_u32 vcc_lo, 0\n"
    "s_cbranch_scc1 .Lvo_oap_%=\n"
    "v_max_i32_dpp v104, v104, v104 row_ror:1 row_mask:0xf bank_mask:0xf\n"
    "s_nop 1\n"
    "v_max_i32_dpp v104, v104, v104 row_ror:2 row_mask:0xf bank_mask:0xf\n"
    "s_nop 1\n"
    "v_max_i32_dpp v104, v104, v104 row_ror:4 row_mask:0xf bank_mask:0xf\n"
    "s_nop 1\n"
    "v_max_i32_dpp v104, v104, v104 row_ror:8 row_mask:0xf bank_mask:0xf\n"
    "s_nop 0\n"
    "v_readlane_b32 s0, v104, 0\n"
    "v_readlane_b32 s1, v104, 16\n"
    "s_nop 1\n"
    "s_max_i32 s0, s0, s1\n"
    "s_max_i32 s0, s0, 0\n"
    "s_min_i32 s0, s0, 16000\n"
    "s_mul_i32 s0, s0, s87\n"
    "s_lshr_b32 s0, s0, 16\n"
    "s_add_u32 s0, s0, s85\n"
    "s_sub_u32 s0, s0, 100\n"
    "s_getpc_b64 s[4:5]\n"
    ".Lvo_oaw_%=:\n"
    "s_memtime s[2:3]\n"
    "s_waitcnt lgkmcnt(0)\n"
    "s_sub_u32 s2, s0, s2\n"
    "s_cmp_lt_i32 s2, 512\n"
    "s_cbranch_scc1 .Lvo_oaf_%=\n"
    "s_sleep 4\n"
    "s_sub_u32 s26, s26, 1\n"
    "s_cbranch_scc0 .Lvo_oaw_%=\n"
    "s_endpgm\n"
    ".Lvo_oap_%=:\n"
    "s_sleep 1\n"
    "v_lshlrev_b32 v101, 3, v100\n"
    "s_mov_b32 exec_hi, 0\n"
    "buffer_load_dwordx2 v[104:105], v101, s[60:63], s65 offen sc1\n"
    "s_mov_b64 exec, -1\n"
    "s_sub_u32 s26, s26, 1\n"
    "s_cbranch_scc0 .Lvo_oac_%=\n"
    "s_endpgm\n"
    ".Lvo_oaf_%=:\n"
    "s_max_i32 s2, s2, 0\n"
    "s_lshr_b32 s2, s2, 3\n"
    "s_lshl_b32 s2, s2, 2\n"
    "s_sub_u32 s2, .Lvo_oae_%=-.Lvo_oaw_%=, s2\n"
    "s_add_u32 s4, s4, s2\n"
    "s_addc_u32 s5, s5, 0\n"
    "s_setpc_b64 s[4:5]\n"
    "s_nop 1\n"
    "s_nop 1\n"
    "s_nop 1\n"
    "s_nop 1\n"
    "s_nop 1\n"
    "s_nop 1\n"
    "s_nop 1\n"
    "s_nop 1\n"
    "s_nop 1\n"
    "s_nop 1\n"
    "s_nop 1\n"
    "s_nop 1\n"
    "s_nop 1\n"
    "s_nop 1\n"
    "s_nop 1\n"
    "s_nop 1\n"
    "s_nop 1\n"
    "s_nop 1\n"
    "s_nop 1\n"
    "s_nop 1\n"
    "s_nop 1\n"
    "s_nop 1\n"
    "s_nop 1\n"
    "s_nop 1\n"
    "s_nop 1\n"
    "s_nop 1\n"
    "s_nop 1\n"
    "s_nop 1\n"
    "s_nop 1\n"
    "s_nop 1\n"
    "s_nop 1\n"
    "s_nop 1\n"
    "s_nop 1\n"
    "s_nop 1\n"
    "s_nop 1\n"
    "s_nop 1\n"
    "s_nop 1\n"
    "s_nop 1\n"
    "s_nop 1\n"
    "s_nop 1\n"
    "s_nop 1\n"
    "s_nop 1\n"
    "s_nop 1\n"
    "s_nop 1\n"
    "s_nop 1\n"
    "s_nop 1\n"
    "s_nop 1\n"
    "s_nop 1\n"
    "s_nop 1\n"
    "s_nop 1\n"
    "s_nop 1\n"
    "s_nop 1\n"
    "s_nop 1\n"
    "s_nop 1\n"
    "s_nop 1\n"
    "s_nop 1\n"
    "s_nop 1\n"
    "s_nop 1\n"
    "s_nop 1\n"
    "s_nop 1\n"
    "s_nop 1\n"
    "s_nop 1\n"
    "s_nop 1\n"
    "s_nop 1\n"
    ".Lvo_oae_%=:\n"
    "s_barrier\n"
    "buffer_load_dwordx4 v[32:35], v10, s[32:35], s76 offen sc1\n"
    "v_mov_b64 v[112:113], 0\n"
    "v_mov_b64 v[16:17], 0\n"
    "v_mov_b64 v[114:115], 0\n"
    "v_mov_b64 v[18:19], 0\n"
    "s_nop 8\n"
    "buffer_load_dwordx4 v[36:39], v10, s[32:35], s77 offen sc1\n"
    "v_mov_b64 v[116:117], 0\n"
    "v_mov_b64 v[20:21], 0\n"
    "v_mov_b64 v[118:119], 0\n"
    "v_mov_b64 v[22:23], 0\n"
    "s_nop 8\n"
    "buffer_load_dwordx4 v[40:43], v10, s[32:35], s78 offen sc1\n"
    "v_mov_b64 v[120:121], 0\n"
    "v_mov_b64 v[24:25], 0\n"
    "v_mov_b64 v[122:123], 0\n"
    "v_mov_b64 v[26:27], 0\n"
    "s_nop 8\n"
    "buffer_load_dwordx4 v[44:47], v10, s[32:35], s79 offen sc1\n"
    "v_mov_b64 v[124:125], 0\n"
    "v_mov_b64 v[28:29], 0\n"
    "v_mov_b64 v[126:127], 0\n"
    "v_mov_b64 v[30:31], 0\n"
    "s_nop 8\n"
    "buffer_load_dwordx4 v[48:51], v10, s[32:35], s80 offen sc1\n"
    "s_nop 12\n"
    "buffer_load_dwordx4 v[52:55], v10, s[32:35], s81 offen sc1\n"
    "s_nop 12\n"
    "buffer_load_dwordx4 v[56:59], v10, s[32:35], s82 offen sc1\n"
    ".Lvo_ouse_%=:\n"
    "s_waitcnt vmcnt(6)\n"
    "v_smfmac_f32_16x16x64_bf16 v[112:115], v[32:35], a[0:7], v8\n"
    "v_max3_u32 v94, v32, v33, v34\n"
    "v_smfmac_f32_16x16x64_bf16 v[116:119], v[32:35], a[8:15], v8\n"
    "v_max_u32 v94, v94, v35\n"
    "v_smfmac_f32_16x16x64_bf16 v[120:123], v[32:35], a[16:23], v8\n"
    "buffer_load_dwordx4 v[60:63], v10, s[32:35], s83 offen sc1\n"
    "v_smfmac_f32_16x16x64_bf16 v[124:127], v[32:35], a[24:31], v8\n"
    "s_waitcnt vmcnt(6)\n"
    "v_smfmac_f32_16x16x64_bf16 v[112:115], v[36:39], a[32:39], v8\n"
    "v_max3_u32 v94, v36, v37, v94\n"
    "v_smfmac_f32_16x16x64_bf16 v[116:119], v[36:39], a[40:47], v8\n"
    "v_max3_u32 v94, v38, v39, v94\n"
    "v_smfmac_f32_16x16x64_bf16 v[120:123], v[36:39], a[48:55], v8\n"
    "v_cmp_eq_u32 vcc, -1, v94\n"
    "v_smfmac_f32_16x16x64_bf16 v[124:127], v[36:39], a[56:63], v8\n"
    "s_waitcnt vmcnt(5)\n"
    "v_smfmac_f32_16x16x64_bf16 v[16:19], v[40:43], a[64:71], v8\n"
    "s_cbranch_vccnz .Lvo_oearly_%=\n"
    "v_max3_u32 v94, v40, v41, v42\n"
    "v_smfmac_f32_16x16x64_bf16 v[20:23], v[40:43], a[72:79], v8\n"
    "v_max_u32 v94, v94, v43\n"
    "v_smfmac_f32_16x16x64_bf16 v[24:27], v[40:43], a[80:87], v8\n"
    "v_smfmac_f32_16x16x64_bf16 v[28:31], v[40:43], a[88:95], v8\n"
    "s_waitcnt vmcnt(4)\n"
    "v_smfmac_f32_16x16x64_bf16 v[16:19], v[44:47], a[96:103], v8\n"
    "v_max3_u32 v94, v44, v45, v94\n"
    "v_smfmac_f32_16x16x64_bf16 v[20:23], v[44:47], a[104:111], v8\n"
    "v_max3_u32 v94, v46, v47, v94\n"
    "v_smfmac_f32_16x16x64_bf16 v[24:27], v[44:47], a[112:119], v8\n"
    "v_add_f32 v110, v112, v113\n"
    "v_smfmac_f32_16x16x64_bf16 v[28:31], v[44:47], a[120:127], v8\n"
    "s_waitcnt vmcnt(3)\n"
    "v_smfmac_f32_16x16x64_bf16 v[16:19], v[48:51], a[128:135], v8\n"
    "v_max3_u32 v94, v48, v49, v94\n"
    "v_smfmac_f32_16x16x64_bf16 v[20:23], v[48:51], a[136:143], v8\n"
    "v_max3_u32 v94, v50, v51, v94\n"
    "v_smfmac_f32_16x16x64_bf16 v[24:27], v[48:51], a[144:151], v8\n"
    "v_add_f32 v111, v116, v117\n"
    "v_smfmac_f32_16x16x64_bf16 v[28:31], v[48:51], a[152:159], v8\n"
    "s_waitcnt vmcnt(2)\n"
    "v_smfmac_f32_16x16x64_bf16 v[16:19], v[52:55], a[160:167], v8\n"
    "v_max3_u32 v94, v52, v53, v94\n"
    "v_smfmac_f32_16x16x64_bf16 v[20:23], v[52:55], a[168:175], v8\n"
    "v_max3_u32 v94, v54, v55, v94\n"
    "v_smfmac_f32_16x16x64_bf16 v[24:27], v[52:55], a[176:183], v8\n"
    "v_add_f32 v66, v122, v123\n"
    "v_smfmac_f32_16x16x64_bf16 v[28:31], v[52:55], a[184:191], v8\n"
    "s_waitcnt vmcnt(1)\n"
    "v_add_f32 v67, v126, v127\n"
    "v_smfmac_f32_16x16x64_bf16 v[16:19], v[56:59], a[192:199], v8\n"
    "v_max3_u32 v94, v56, v57, v94\n"
    "v_smfmac_f32_16x16x64_bf16 v[20:23], v[56:59], a[200:207], v8\n"
    "v_max3_u32 v94, v58, v59, v94\n"
    "v_smfmac_f32_16x16x64_bf16 v[24:27], v[56:59], a[208:215], v8\n"
    "v_add_f32 v110, v110, v66\n"
    "v_smfmac_f32_16x16x64_bf16 v[28:31], v[56:59], a[216:223], v8\n"
    "s_waitcnt vmcnt(0)\n"
    "v_add_f32 v111, v111, v67\n"
    "v_smfmac_f32_16x16x64_bf16 v[16:19], v[60:63], a[224:231], v8\n"
    "v_max3_u32 v94, v60, v61, v94\n"
    "v_smfmac_f32_16x16x64_bf16 v[20:23], v[60:63], a[232:239], v8\n"
    "v_max3_u32 v94, v62, v63, v94\n"
    "v_smfmac_f32_16x16x64_bf16 v[24:27], v[60:63], a[240:247], v8\n"
    "v_cmp_eq_u32 vcc, -1, v94\n"
    "v_smfmac_f32_16x16x64_bf16 v[28:31], v[60:63], a[248:255], v8\n"
    "s_nop 1\n"
    "v_add_f32 v64, v16, v17\n"
    "s_nop 0\n"
    "v_add_f32 v65, v20, v21\n"
    "s_cbranch_vccnz .Lvo_oretry_%=\n"
    "v_add_f32 v66, v26, v27\n"
    "s_nop 0\n"
    "v_add_f32 v67, v30, v31\n"
    "v_pk_add_f32 v[64:65], v[64:65], v[66:67]\n"
    ".Lvo_ojoin_%=:\n"
    "v_pk_add_f32 v[64:65], v[64:65], v[110:111]\n"
    "ds_write_b64 v15, v[64:65] offset:8192\n"
    "s_waitcnt lgkmcnt(0)\n"
    "s_barrier\n"
    "ds_read_b64 v[66:67], v5 offset:8192\n"
    "ds_read_b64 v[68:69], v6 offset:8192\n"
    "ds_read_b64 v[70:71], v7 offset:8192\n"
    "s_waitcnt lgkmcnt(2)\n"
    "v_pk_add_f32 v[64:65], v[64:65], v[66:67]\n"
    "s_waitcnt lgkmcnt(0)\n"
    "v_pk_add_f32 v[68:69], v[68:69], v[70:71]\n"
    "v_pk_add_f32 v[64:65], v[64:65], v[68:69]\n"
    "v_cvt_pk_bf16_f32 v80, v64, v65\n"
    "buffer_store_dword v80, v11, s[32:35], s46 offen sc1\n"
    "s_add_u32 s44, s44, 1\n"
    "s_nop 15\n"
    "s_nop 15\n"
    "s_nop 15\n"
    "s_nop 15\n"
    "s_nop 15\n"
    "s_nop 15\n"
    "s_nop 15\n"
    "s_nop 2\n"
    "s_branch .Lvo_step_%=\n"
    ".Lvo_mretry_%=:\n"
    ".Lvo_mspin_%=:\n"
    "buffer_load_dwordx4 v[32:35], v9, s[32:35], s72 offen sc1\n"
    "buffer_load_dwordx4 v[36:39], v9, s[32:35], s73 offen sc1\n"
    "buffer_load_dwordx4 v[40:43], v9, s[32:35], s74 offen sc1\n"
    "buffer_load_dwordx4 v[44:47], v9, s[32:35], s75 offen sc1\n"
    "s_waitcnt vmcnt(0)\n"
    "v_max3_u32 v94, v32, v33, v34\n"
    "v_max_u32 v94, v94, v35\n"
    "v_max3_u32 v94, v36, v37, v94\n"
    "v_max3_u32 v94, v38, v39, v94\n"
    "v_max3_u32 v94, v40, v41, v94\n"
    "v_max3_u32 v94, v42, v43, v94\n"
    "v_max3_u32 v94, v44, v45, v94\n"
    "v_max3_u32 v94, v46, v47, v94\n"
    "v_cmp_eq_u32 vcc, -1, v94\n"
    "v_mov_b64 v[16:17], 0\n"
    "v_mov_b64 v[18:19], 0\n"
    "v_mov_b64 v[20:21], 0\n"
    "v_mov_b64 v[22:23], 0\n"
    "v_mov_b64 v[24:25], 0\n"
    "v_mov_b64 v[26:27], 0\n"
    "v_mov_b64 v[28:29], 0\n"
    "v_mov_b64 v[30:31], 0\n"
    "s_cbranch_vccz .Lvo_muse_%=\n"
    "s_sub_u32 s26, s26, 1\n"
    "s_cbranch_scc0 .Lvo_mspin_%=\n"
    "s_endpgm\n"
    ".Lvo_oretry_%=:\n"
    "v_mov_b64 v[112:113], 0\n"
    "v_mov_b64 v[114:115], 0\n"
    "v_mov_b64 v[116:117], 0\n"
    "v_mov_b64 v[118:119], 0\n"
    "v_mov_b64 v[120:121], 0\n"
    "v_mov_b64 v[122:123], 0\n"
    "v_mov_b64 v[124:125], 0\n"
    "v_mov_b64 v[126:127], 0\n"
    ".Lvo_ospin_%=:\n"
    "buffer_load_dwordx4 v[32:35], v10, s[32:35], s76 offen sc1\n"
    "buffer_load_dwordx4 v[36:39], v10, s[32:35], s77 offen sc1\n"
    "buffer_load_dwordx4 v[40:43], v10, s[32:35], s78 offen sc1\n"
    "buffer_load_dwordx4 v[44:47], v10, s[32:35], s79 offen sc1\n"
    "buffer_load_dwordx4 v[48:51], v10, s[32:35], s80 offen sc1\n"
    "buffer_load_dwordx4 v[52:55], v10, s[32:35], s81 offen sc1\n"
    "buffer_load_dwordx4 v[56:59], v10, s[32:35], s82 offen sc1\n"
    "buffer_load_dwordx4 v[60:63], v10, s[32:35], s83 offen sc1\n"
    "s_waitcnt vmcnt(0)\n"
    "v_max3_u32 v94, v32, v33, v34\n"
    "v_max_u32 v94, v94, v35\n"
    "v_max3_u32 v94, v36, v37, v94\n"
    "v_max3_u32 v94, v38, v39, v94\n"
    "v_max3_u32 v94, v40, v41, v94\n"
    "v_max3_u32 v94, v42, v43, v94\n"
    "v_max3_u32 v94, v44, v45, v94\n"
    "v_max3_u32 v94, v46, v47, v94\n"
    "v_max3_u32 v94, v48, v49, v94\n"
    "v_max3_u32 v94, v50, v51, v94\n"
    "v_max3_u32 v94, v52, v53, v94\n"
    "v_max3_u32 v94, v54, v55, v94\n"
    "v_max3_u32 v94, v56, v57, v94\n"
    "v_max3_u32 v94, v58, v59, v94\n"
    "v_max3_u32 v94, v60, v61, v94\n"
    "v_max3_u32 v94, v62, v63, v94\n"
    "v_cmp_eq_u32 vcc, -1, v94\n"
    "v_mov_b64 v[16:17], 0\n"
    "v_mov_b64 v[18:19], 0\n"
    "v_mov_b64 v[20:21], 0\n"
    "v_mov_b64 v[22:23], 0\n"
    "v_mov_b64 v[24:25], 0\n"
    "v_mov_b64 v[26:27], 0\n"
    "v_mov_b64 v[28:29], 0\n"
    "v_mov_b64 v[30:31], 0\n"
    "s_cbranch_vccz .Lvo_ouse_%=\n"
    "s_sub_u32 s26, s26, 1\n"
    "s_cbranch_scc0 .Lvo_ospin_%=\n"
    "s_endpgm\n"
    ".Lvo_oearly_%=:\n"
    "buffer_load_dwordx4 v[32:35], v10, s[32:35], s76 offen sc1\n"
    "buffer_load_dwordx4 v[36:39], v10, s[32:35], s77 offen sc1\n"
    "v_max3_u32 v94, v40, v41, v42\n"
    "v_smfmac_f32_16x16x64_bf16 v[20:23], v[40:43], a[72:79], v8\n"
    "v_max_u32 v94, v94, v43\n"
    "v_smfmac_f32_16x16x64_bf16 v[24:27], v[40:43], a[80:87], v8\n"
    "v_smfmac_f32_16x16x64_bf16 v[28:31], v[40:43], a[88:95], v8\n"
    "s_waitcnt vmcnt(6)\n"
    "v_smfmac_f32_16x16x64_bf16 v[16:19], v[44:47], a[96:103], v8\n"
    "v_max3_u32 v94, v44, v45, v94\n"
    "v_smfmac_f32_16x16x64_bf16 v[20:23], v[44:47], a[104:111], v8\n"
    "v_max3_u32 v94, v46, v47, v94\n"
    "v_smfmac_f32_16x16x64_bf16 v[24:27], v[44:47], a[112:119], v8\n"
    "v_mov_b64 v[112:113], 0\n"
    "v_smfmac_f32_16x16x64_bf16 v[28:31], v[44:47], a[120:127], v8\n"
    "v_mov_b64 v[114:115], 0\n"
    "s_waitcnt vmcnt(5)\n"
    "v_smfmac_f32_16x16x64_bf16 v[16:19], v[48:51], a[128:135], v8\n"
    "v_max3_u32 v94, v48, v49, v94\n"
    "v_smfmac_f32_16x16x64_bf16 v[20:23], v[48:51], a[136:143], v8\n"
    "v_max3_u32 v94, v50, v51, v94\n"
    "v_smfmac_f32_16x16x64_bf16 v[24:27], v[48:51], a[144:151], v8\n"
    "v_mov_b64 v[116:117], 0\n"
    "v_smfmac_f32_16x16x64_bf16 v[28:31], v[48:51], a[152:159], v8\n"
    "v_mov_b64 v[118:119], 0\n"
    "s_waitcnt vmcnt(4)\n"
    "v_smfmac_f32_16x16x64_bf16 v[16:19], v[52:55], a[160:167], v8\n"
    "v_max3_u32 v94, v52, v53, v94\n"
    "v_smfmac_f32_16x16x64_bf16 v[20:23], v[52:55], a[168:175], v8\n"
    "v_max3_u32 v94, v54, v55, v94\n"
    "v_smfmac_f32_16x16x64_bf16 v[24:27], v[52:55], a[176:183], v8\n"
    "v_mov_b64 v[120:121], 0\n"
    "v_smfmac_f32_16x16x64_bf16 v[28:31], v[52:55], a[184:191], v8\n"
    "v_mov_b64 v[122:123], 0\n"
    "s_waitcnt vmcnt(3)\n"
    "v_smfmac_f32_16x16x64_bf16 v[16:19], v[56:59], a[192:199], v8\n"
    "v_max3_u32 v94, v56, v57, v94\n"
    "v_smfmac_f32_16x16x64_bf16 v[20:23], v[56:59], a[200:207], v8\n"
    "v_max3_u32 v94, v58, v59, v94\n"
    "v_smfmac_f32_16x16x64_bf16 v[24:27], v[56:59], a[208:215], v8\n"
    "v_mov_b64 v[124:125], 0\n"
    "v_smfmac_f32_16x16x64_bf16 v[28:31], v[56:59], a[216:223], v8\n"
    "v_mov_b64 v[126:127], 0\n"
    "s_waitcnt vmcnt(2)\n"
    "v_smfmac_f32_16x16x64_bf16 v[16:19], v[60:63], a[224:231], v8\n"
    "v_max3_u32 v94, v60, v61, v94\n"
    "v_smfmac_f32_16x16x64_bf16 v[20:23], v[60:63], a[232:239], v8\n"
    "v_max3_u32 v94, v62, v63, v94\n"
    "v_smfmac_f32_16x16x64_bf16 v[24:27], v[60:63], a[240:247], v8\n"
    "v_smfmac_f32_16x16x64_bf16 v[28:31], v[60:63], a[248:255], v8\n"
    "s_waitcnt vmcnt(1)\n"
    "v_smfmac_f32_16x16x64_bf16 v[112:115], v[32:35], a[0:7], v8\n"
    "v_max3_u32 v94, v32, v33, v94\n"
    "v_smfmac_f32_16x16x64_bf16 v[116:119], v[32:35], a[8:15], v8\n"
    "v_max3_u32 v94, v34, v35, v94\n"
    "v_smfmac_f32_16x16x64_bf16 v[120:123], v[32:35], a[16:23], v8\n"
    "v_smfmac_f32_16x16x64_bf16 v[124:127], v[32:35], a[24:31], v8\n"
    "s_waitcnt vmcnt(0)\n"
    "v_smfmac_f32_16x16x64_bf16 v[112:115], v[36:39], a[32:39], v8\n"
    "v_max3_u32 v94, v36, v37, v94\n"
    "v_smfmac_f32_16x16x64_bf16 v[116:119], v[36:39], a[40:47], v8\n"
    "v_max3_u32 v94, v38, v39, v94\n"
    "v_smfmac_f32_16x16x64_bf16 v[120:123], v[36:39], a[48:55], v8\n"
    "v_smfmac_f32_16x16x64_bf16 v[124:127], v[36:39], a[56:63], v8\n"
    "v_cmp_eq_u32 vcc, -1, v94\n"
    "v_add_f32 v64, v16, v17\n"
    "v_add_f32 v65, v20, v21\n"
    "v_add_f32 v66, v26, v27\n"
    "v_add_f32 v67, v30, v31\n"
    "v_add_f32 v64, v64, v66\n"
    "v_add_f32 v65, v65, v67\n"
    "s_cbranch_vccnz .Lvo_oretry_%=\n"
    "v_add_f32 v110, v112, v113\n"
    "v_add_f32 v111, v116, v117\n"
    "v_add_f32 v66, v122, v123\n"
    "v_add_f32 v67, v126, v127\n"
    "v_add_f32 v110, v110, v66\n"
    "v_add_f32 v111, v111, v67\n"
    "s_branch .Lvo_ojoin_%=\n"
    ".Lvo_done_%=:\n"
    "s_endpgm\n"
    ".Lvo_hsp_%=:\n"
    "s_sleep 2\n"
    "s_add_u32 s10, s19, 69632\n"
    "s_mov_b64 exec, 1\n"
    "buffer_load_dword v0, off, s[12:15], s10 sc1\n"
    "s_mov_b64 exec, -1\n"
    "s_waitcnt vmcnt(0)\n"
    "s_sub_u32 s26, s26, 1\n"
    "s_cbranch_scc0 .Lvo_hsc_%=\n"
    "s_endpgm\n"
    ".Lvo_hsr_%=:\n"
    "s_mov_b32 s17, s11\n"
    "s_waitcnt vmcnt(0) lgkmcnt(0)\n"
    "s_barrier\n"
    "s_nop 15\n"
    "s_branch .Lk_dispatch_%=\n"
    ".Lvo_near_%=:\n"
    "s_and_b32 s0, s65, 8192\n"
    "s_add_u32 s0, s0, 39936\n"
    "buffer_load_dword v88, off, s[60:63], s0 sc1\n"
    "s_waitcnt vmcnt(0)\n"
    "v_readfirstlane_b32 s0, v88\n"
    "s_nop 1\n"
    "s_bitcmp1_b32 s0, 2\n"
    "s_cbranch_scc0 .Lvo_nearb_%=\n"
    "s_xor_b32 s0, s0, s25\n"
    "s_and_b32 s1, s0, 1\n"
    "s_lshl_b32 s1, s1, 14\n"
    "s_bfe_u32 s2, s0, 65537\n"
    "s_lshl_b32 s2, s2, 12\n"
    "s_add_u32 s1, s1, s2\n"
    "v_xor_b32 v9, s1, v9\n"
    "s_branch .Lvo_nearb_%=\n"
    ".Lgx_entry_%=:\n"
    "s_lshr_b32 s65, s24, 5\n"
    "s_and_b32 s66, s24, 31\n"
    "s_lshl_b32 s58, s65, 3\n"
    "s_add_u32 s70, s28, s58\n"
    "s_addc_u32 s71, s29, 0\n"
    "s_load_dwordx2 s[70:71], s[70:71], 56\n"
    "s_load_dwordx2 s[72:73], s[28:29], 40\n"
    "s_load_dwordx4 s[76:79], s[28:29], 80\n"
    "s_waitcnt lgkmcnt(0)\n"
    "s_mov_b32 s32, s70\n"
    "s_and_b32 s33, s71, 0xffff\n"
    "s_mov_b32 s34, 8388608\n"
    "s_mov_b32 s35, 0x20000\n"
    "s_mov_b32 s36, s72\n"
    "s_and_b32 s37, s73, 0xffff\n"
    "s_mov_b32 s38, 4194304\n"
    "s_mov_b32 s39, 0x20000\n"
    "s_mov_b32 s52, s78\n"
    "s_and_b32 s53, s79, 0xffff\n"
    "s_mov_b32 s54, 8388608\n"
    "s_mov_b32 s55, 0x20000\n"
    "s_add_u32 s40, s30, 2097152\n"
    "s_addc_u32 s41, s31, 0\n"
    "s_and_b32 s41, s41, 0xffff\n"
    "s_mov_b32 s42, 50331648\n"
    "s_mov_b32 s43, 0x20000\n"
    "s_add_u32 s48, s30, 52428800\n"
    "s_addc_u32 s49, s31, 0\n"
    "s_and_b32 s49, s49, 0xffff\n"
    "s_mov_b32 s50, 16777216\n"
    "s_mov_b32 s51, 0x20000\n"
    "s_add_u32 s44, s30, 0\n"
    "s_addc_u32 s45, s31, 0\n"
    "s_and_b32 s45, s45, 0xffff\n"
    "s_mov_b32 s46, 65536\n"
    "s_mov_b32 s47, 0x20000\n"
    "v_lshlrev_b32 v11, 4, v1\n"
    "s_and_b32 s58, s25, 1\n"
    "s_lshl_b32 s58, s58, 6\n"
    "v_bfe_u32 v2, v1, 4, 2\n"
    "v_lshlrev_b32 v2, 4, v2\n"
    "v_add_u32 v2, s58, v2\n"
    "v_bfe_u32 v8, v1, 3, 3\n"
    "v_lshlrev_b32 v8, 11, v8\n"
    "v_bfe_u32 v3, v1, 0, 3\n"
    "v_lshl_add_u32 v8, v3, 4, v8\n"
    "v_xor_b32 v8, v8, v2\n"
    "s_lshl_b32 s58, s25, 14\n"
    "v_add_u32 v8, s58, v8\n"
    "s_lshl_b32 s62, s25, 10\n"
    "s_mov_b32 s61, 0\n"
    "s_add_u32 m0, s62, 0\n"
    "s_add_u32 s58, s61, 0\n"
    "buffer_load_dwordx4 v8, s[36:39], s58 offen lds\n"
    "s_add_u32 m0, s62, 4096\n"
    "s_add_u32 s58, s61, 128\n"
    "buffer_load_dwordx4 v8, s[36:39], s58 offen lds\n"
    "s_add_u32 m0, s62, 8192\n"
    "s_add_u32 s58, s61, 256\n"
    "buffer_load_dwordx4 v8, s[36:39], s58 offen lds\n"
    "s_add_u32 m0, s62, 12288\n"
    "s_add_u32 s58, s61, 384\n"
    "buffer_load_dwordx4 v8, s[36:39], s58 offen lds\n"
    "s_add_u32 m0, s62, 16384\n"
    "s_add_u32 s58, s61, 512\n"
    "buffer_load_dwordx4 v8, s[36:39], s58 offen lds\n"
    "s_add_u32 m0, s62, 20480\n"
    "s_add_u32 s58, s61, 640\n"
    "buffer_load_dwordx4 v8, s[36:39], s58 offen lds\n"
    "s_add_u32 m0, s62, 24576\n"
    "s_add_u32 s58, s61, 768\n"
    "buffer_load_dwordx4 v8, s[36:39], s58 offen lds\n"
    "s_add_u32 m0, s62, 28672\n"
    "s_add_u32 s58, s61, 896\n"
    "buffer_load_dwordx4 v8, s[36:39], s58 offen lds\n"
    "s_add_u32 m0, s62, 32768\n"
    "s_add_u32 s58, s61, 1024\n"
    "buffer_load_dwordx4 v8, s[36:39], s58 offen lds\n"
    "s_add_u32 m0, s62, 36864\n"
    "s_add_u32 s58, s61, 1152\n"
    "buffer_load_dwordx4 v8, s[36:39], s58 offen lds\n"
    "s_add_u32 m0, s62, 40960\n"
    "s_add_u32 s58, s61, 1280\n"
    "buffer_load_dwordx4 v8, s[36:39], s58 offen lds\n"
    "s_add_u32 m0, s62, 45056\n"
    "s_add_u32 s58, s61, 1408\n"
    "buffer_load_dwordx4 v8, s[36:39], s58 offen lds\n"
    "s_add_u32 m0, s62, 49152\n"
    "s_add_u32 s58, s61, 1536\n"
    "buffer_load_dwordx4 v8, s[36:39], s58 offen lds\n"
    "s_add_u32 m0, s62, 53248\n"
    "s_add_u32 s58, s61, 1664\n"
    "buffer_load_dwordx4 v8, s[36:39], s58 offen lds\n"
    "s_add_u32 m0, s62, 57344\n"
    "s_add_u32 s58, s61, 1792\n"
    "buffer_load_dwordx4 v8, s[36:39], s58 offen lds\n"
    "s_add_u32 m0, s62, 61440\n"
    "s_add_u32 s58, s61, 1920\n"
    "buffer_load_dwordx4 v8, s[36:39], s58 offen lds\n"
    "s_mul_i32 s57, s25, 24576\n"
    "s_add_u32 s57, s57, 65536\n"
    "s_lshl_b32 s56, s66, 8\n"
    "s_lshl_b32 s58, s25, 6\n"
    "s_add_u32 s56, s56, s58\n"
    "v_bfe_u32 v2, v1, 2, 1\n"
    "v_lshlrev_b32 v2, 13, v2\n"
    "v_bfe_u32 v3, v1, 3, 1\n"
    "v_lshl_add_u32 v2, v3, 14, v2\n"
    "v_bfe_u32 v3, v1, 4, 1\n"
    "v_lshl_add_u32 v2, v3, 16, v2\n"
    "v_bfe_u32 v3, v1, 5, 1\n"
    "v_lshl_add_u32 v2, v3, 17, v2\n"
    "v_bfe_u32 v3, v1, 0, 2\n"
    "v_lshl_add_u32 v2, v3, 4, v2\n"
    "v_bfe_u32 v4, v1, 4, 1\n"
    "v_lshlrev_b32 v4, 5, v4\n"
    "v_xor_b32 v2, v2, v4\n"
    "v_bfe_u32 v4, v1, 2, 4\n"
    "v_lshlrev_b32 v4, 6, v4\n"
    "v_bfe_u32 v3, v1, 4, 1\n"
    "v_lshl_add_u32 v4, v3, 5, v4\n"
    "v_bfe_u32 v3, v1, 0, 2\n"
    "v_lshl_add_u32 v4, v3, 3, v4\n"
    "v_add_u32 v4, s57, v4\n"
    "v_xor_b32 v5, 32, v4\n"
    "s_mov_b32 s58, 0\n"
    "s_add_u32 s58, s58, s56\n"
    "s_add_u32 m0, s57, 0\n"
    "s_add_u32 s59, s58, 0\n"
    "buffer_load_dwordx4 v2, s[32:35], s59 offen lds\n"
    "s_add_u32 m0, s57, 1024\n"
    "s_add_u32 s59, s58, 32768\n"
    "buffer_load_dwordx4 v2, s[32:35], s59 offen lds\n"
    "s_add_u32 m0, s57, 2048\n"
    "s_add_u32 s59, s58, 262144\n"
    "buffer_load_dwordx4 v2, s[32:35], s59 offen lds\n"
    "s_add_u32 m0, s57, 3072\n"
    "s_add_u32 s59, s58, 294912\n"
    "buffer_load_dwordx4 v2, s[32:35], s59 offen lds\n"
    "s_mov_b32 s58, 524288\n"
    "s_add_u32 s58, s58, s56\n"
    "s_add_u32 m0, s57, 4096\n"
    "s_add_u32 s59, s58, 0\n"
    "buffer_load_dwordx4 v2, s[32:35], s59 offen lds\n"
    "s_add_u32 m0, s57, 5120\n"
    "s_add_u32 s59, s58, 32768\n"
    "buffer_load_dwordx4 v2, s[32:35], s59 offen lds\n"
    "s_add_u32 m0, s57, 6144\n"
    "s_add_u32 s59, s58, 262144\n"
    "buffer_load_dwordx4 v2, s[32:35], s59 offen lds\n"
    "s_add_u32 m0, s57, 7168\n"
    "s_add_u32 s59, s58, 294912\n"
    "buffer_load_dwordx4 v2, s[32:35], s59 offen lds\n"
    "s_mov_b32 s58, 1048576\n"
    "s_add_u32 s58, s58, s56\n"
    "s_add_u32 m0, s57, 8192\n"
    "s_add_u32 s59, s58, 0\n"
    "buffer_load_dwordx4 v2, s[32:35], s59 offen lds\n"
    "s_add_u32 m0, s57, 9216\n"
    "s_add_u32 s59, s58, 32768\n"
    "buffer_load_dwordx4 v2, s[32:35], s59 offen lds\n"
    "s_add_u32 m0, s57, 10240\n"
    "s_add_u32 s59, s58, 262144\n"
    "buffer_load_dwordx4 v2, s[32:35], s59 offen lds\n"
    "s_add_u32 m0, s57, 11264\n"
    "s_add_u32 s59, s58, 294912\n"
    "buffer_load_dwordx4 v2, s[32:35], s59 offen lds\n"
    "s_mov_b32 s58, 1572864\n"
    "s_add_u32 s58, s58, s56\n"
    "s_add_u32 m0, s57, 12288\n"
    "s_add_u32 s59, s58, 0\n"
    "buffer_load_dwordx4 v2, s[32:35], s59 offen lds\n"
    "s_add_u32 m0, s57, 13312\n"
    "s_add_u32 s59, s58, 32768\n"
    "buffer_load_dwordx4 v2, s[32:35], s59 offen lds\n"
    "s_add_u32 m0, s57, 14336\n"
    "s_add_u32 s59, s58, 262144\n"
    "buffer_load_dwordx4 v2, s[32:35], s59 offen lds\n"
    "s_add_u32 m0, s57, 15360\n"
    "s_add_u32 s59, s58, 294912\n"
    "buffer_load_dwordx4 v2, s[32:35], s59 offen lds\n"
    "s_mov_b32 s58, 2097152\n"
    "s_add_u32 s58, s58, s56\n"
    "s_add_u32 m0, s57, 16384\n"
    "s_add_u32 s59, s58, 0\n"
    "buffer_load_dwordx4 v2, s[32:35], s59 offen lds\n"
    "s_add_u32 m0, s57, 17408\n"
    "s_add_u32 s59, s58, 32768\n"
    "buffer_load_dwordx4 v2, s[32:35], s59 offen lds\n"
    "s_add_u32 m0, s57, 18432\n"
    "s_add_u32 s59, s58, 262144\n"
    "buffer_load_dwordx4 v2, s[32:35], s59 offen lds\n"
    "s_add_u32 m0, s57, 19456\n"
    "s_add_u32 s59, s58, 294912\n"
    "buffer_load_dwordx4 v2, s[32:35], s59 offen lds\n"
    "s_mov_b32 s58, 2621440\n"
    "s_add_u32 s58, s58, s56\n"
    "s_add_u32 m0, s57, 20480\n"
    "s_add_u32 s59, s58, 0\n"
    "buffer_load_dwordx4 v2, s[32:35], s59 offen lds\n"
    "s_add_u32 m0, s57, 21504\n"
    "s_add_u32 s59, s58, 32768\n"
    "buffer_load_dwordx4 v2, s[32:35], s59 offen lds\n"
    "s_add_u32 m0, s57, 22528\n"
    "s_add_u32 s59, s58, 262144\n"
    "buffer_load_dwordx4 v2, s[32:35], s59 offen lds\n"
    "s_add_u32 m0, s57, 23552\n"
    "s_add_u32 s59, s58, 294912\n"
    "buffer_load_dwordx4 v2, s[32:35], s59 offen lds\n"
    "s_waitcnt vmcnt(40)\n"
    "v_readfirstlane_b32 s18, v0\n"
    "s_add_u32 s10, s19, 65536\n"
    "s_or_b32 s18, s18, 0xff\n"
    "s_add_u32 s18, s18, 1\n"
    "v_mov_b32 v0, s18\n"
    "s_mov_b64 exec, 1\n"
    "buffer_store_dword v0, off, s[12:15], s10 sc1\n"
    "s_mov_b64 exec, -1\n"
    "s_waitcnt vmcnt(21)\n"
    "ds_read_b64_tr_b16 a[0:1], v4 offset:0\n"
    "ds_read_b64_tr_b16 a[2:3], v4 offset:1024\n"
    "ds_read_b64_tr_b16 a[4:5], v4 offset:2048\n"
    "ds_read_b64_tr_b16 a[6:7], v4 offset:3072\n"
    "ds_read_b64_tr_b16 a[8:9], v5 offset:0\n"
    "ds_read_b64_tr_b16 a[10:11], v5 offset:1024\n"
    "ds_read_b64_tr_b16 a[12:13], v5 offset:2048\n"
    "ds_read_b64_tr_b16 a[14:15], v5 offset:3072\n"
    "s_waitcnt lgkmcnt(0)\n"
    "s_mov_b32 s58, 3145728\n"
    "s_add_u32 s58, s58, s56\n"
    "s_add_u32 m0, s57, 0\n"
    "s_add_u32 s59, s58, 0\n"
    "buffer_load_dwordx4 v2, s[32:35], s59 offen lds\n"
    "s_add_u32 m0, s57, 1024\n"
    "s_add_u32 s59, s58, 32768\n"
    "buffer_load_dwordx4 v2, s[32:35], s59 offen lds\n"
    "s_add_u32 m0, s57, 2048\n"
    "s_add_u32 s59, s58, 262144\n"
    "buffer_load_dwordx4 v2, s[32:35], s59 offen lds\n"
    "s_add_u32 m0, s57, 3072\n"
    "s_add_u32 s59, s58, 294912\n"
    "buffer_load_dwordx4 v2, s[32:35], s59 offen lds\n"
    "s_waitcnt vmcnt(21)\n"
    "ds_read_b64_tr_b16 a[16:17], v4 offset:4096\n"
    "ds_read_b64_tr_b16 a[18:19], v4 offset:5120\n"
    "ds_read_b64_tr_b16 a[20:21], v4 offset:6144\n"
    "ds_read_b64_tr_b16 a[22:23], v4 offset:7168\n"
    "ds_read_b64_tr_b16 a[24:25], v5 offset:4096\n"
    "ds_read_b64_tr_b16 a[26:27], v5 offset:5120\n"
    "ds_read_b64_tr_b16 a[28:29], v5 offset:6144\n"
    "ds_read_b64_tr_b16 a[30:31], v5 offset:7168\n"
    "s_waitcnt lgkmcnt(0)\n"
    "s_mov_b32 s58, 3670016\n"
    "s_add_u32 s58, s58, s56\n"
    "s_add_u32 m0, s57, 4096\n"
    "s_add_u32 s59, s58, 0\n"
    "buffer_load_dwordx4 v2, s[32:35], s59 offen lds\n"
    "s_add_u32 m0, s57, 5120\n"
    "s_add_u32 s59, s58, 32768\n"
    "buffer_load_dwordx4 v2, s[32:35], s59 offen lds\n"
    "s_add_u32 m0, s57, 6144\n"
    "s_add_u32 s59, s58, 262144\n"
    "buffer_load_dwordx4 v2, s[32:35], s59 offen lds\n"
    "s_add_u32 m0, s57, 7168\n"
    "s_add_u32 s59, s58, 294912\n"
    "buffer_load_dwordx4 v2, s[32:35], s59 offen lds\n"
    "s_waitcnt vmcnt(21)\n"
    "ds_read_b64_tr_b16 a[32:33], v4 offset:8192\n"
    "ds_read_b64_tr_b16 a[34:35], v4 offset:9216\n"
    "ds_read_b64_tr_b16 a[36:37], v4 offset:10240\n"
    "ds_read_b64_tr_b16 a[38:39], v4 offset:11264\n"
    "ds_read_b64_tr_b16 a[40:41], v5 offset:8192\n"
    "ds_read_b64_tr_b16 a[42:43], v5 offset:9216\n"
    "ds_read_b64_tr_b16 a[44:45], v5 offset:10240\n"
    "ds_read_b64_tr_b16 a[46:47], v5 offset:11264\n"
    "s_waitcnt lgkmcnt(0)\n"
    "s_mov_b32 s58, 4194304\n"
    "s_add_u32 s58, s58, s56\n"
    "s_add_u32 m0, s57, 8192\n"
    "s_add_u32 s59, s58, 0\n"
    "buffer_load_dwordx4 v2, s[32:35], s59 offen lds\n"
    "s_add_u32 m0, s57, 9216\n"
    "s_add_u32 s59, s58, 32768\n"
    "buffer_load_dwordx4 v2, s[32:35], s59 offen lds\n"
    "s_add_u32 m0, s57, 10240\n"
    "s_add_u32 s59, s58, 262144\n"
    "buffer_load_dwordx4 v2, s[32:35], s59 offen lds\n"
    "s_add_u32 m0, s57, 11264\n"
    "s_add_u32 s59, s58, 294912\n"
    "buffer_load_dwordx4 v2, s[32:35], s59 offen lds\n"
    "s_waitcnt vmcnt(21)\n"
    "ds_read_b64_tr_b16 a[48:49], v4 offset:12288\n"
    "ds_read_b64_tr_b16 a[50:51], v4 offset:13312\n"
    "ds_read_b64_tr_b16 a[52:53], v4 offset:14336\n"
    "ds_read_b64_tr_b16 a[54:55], v4 offset:15360\n"
    "ds_read_b64_tr_b16 a[56:57], v5 offset:12288\n"
    "ds_read_b64_tr_b16 a[58:59], v5 offset:13312\n"
    "ds_read_b64_tr_b16 a[60:61], v5 offset:14336\n"
    "ds_read_b64_tr_b16 a[62:63], v5 offset:15360\n"
    "s_waitcnt lgkmcnt(0)\n"
    "s_mov_b32 s58, 4718592\n"
    "s_add_u32 s58, s58, s56\n"
    "s_add_u32 m0, s57, 12288\n"
    "s_add_u32 s59, s58, 0\n"
    "buffer_load_dwordx4 v2, s[32:35], s59 offen lds\n"
    "s_add_u32 m0, s57, 13312\n"
    "s_add_u32 s59, s58, 32768\n"
    "buffer_load_dwordx4 v2, s[32:35], s59 offen lds\n"
    "s_add_u32 m0, s57, 14336\n"
    "s_add_u32 s59, s58, 262144\n"
    "buffer_load_dwordx4 v2, s[32:35], s59 offen lds\n"
    "s_add_u32 m0, s57, 15360\n"
    "s_add_u32 s59, s58, 294912\n"
    "buffer_load_dwordx4 v2, s[32:35], s59 offen lds\n"
    "s_waitcnt vmcnt(21)\n"
    "ds_read_b64_tr_b16 a[64:65], v4 offset:16384\n"
    "ds_read_b64_tr_b16 a[66:67], v4 offset:17408\n"
    "ds_read_b64_tr_b16 a[68:69], v4 offset:18432\n"
    "ds_read_b64_tr_b16 a[70:71], v4 offset:19456\n"
    "ds_read_b64_tr_b16 a[72:73], v5 offset:16384\n"
    "ds_read_b64_tr_b16 a[74:75], v5 offset:17408\n"
    "ds_read_b64_tr_b16 a[76:77], v5 offset:18432\n"
    "ds_read_b64_tr_b16 a[78:79], v5 offset:19456\n"
    "s_waitcnt lgkmcnt(0)\n"
    "s_mov_b32 s58, 5242880\n"
    "s_add_u32 s58, s58, s56\n"
    "s_add_u32 m0, s57, 16384\n"
    "s_add_u32 s59, s58, 0\n"
    "buffer_load_dwordx4 v2, s[32:35], s59 offen lds\n"
    "s_add_u32 m0, s57, 17408\n"
    "s_add_u32 s59, s58, 32768\n"
    "buffer_load_dwordx4 v2, s[32:35], s59 offen lds\n"
    "s_add_u32 m0, s57, 18432\n"
    "s_add_u32 s59, s58, 262144\n"
    "buffer_load_dwordx4 v2, s[32:35], s59 offen lds\n"
    "s_add_u32 m0, s57, 19456\n"
    "s_add_u32 s59, s58, 294912\n"
    "buffer_load_dwordx4 v2, s[32:35], s59 offen lds\n"
    "s_waitcnt vmcnt(21)\n"
    "ds_read_b64_tr_b16 a[80:81], v4 offset:20480\n"
    "ds_read_b64_tr_b16 a[82:83], v4 offset:21504\n"
    "ds_read_b64_tr_b16 a[84:85], v4 offset:22528\n"
    "ds_read_b64_tr_b16 a[86:87], v4 offset:23552\n"
    "ds_read_b64_tr_b16 a[88:89], v5 offset:20480\n"
    "ds_read_b64_tr_b16 a[90:91], v5 offset:21504\n"
    "ds_read_b64_tr_b16 a[92:93], v5 offset:22528\n"
    "ds_read_b64_tr_b16 a[94:95], v5 offset:23552\n"
    "s_waitcnt lgkmcnt(0)\n"
    "s_mov_b32 s58, 5767168\n"
    "s_add_u32 s58, s58, s56\n"
    "s_add_u32 m0, s57, 20480\n"
    "s_add_u32 s59, s58, 0\n"
    "buffer_load_dwordx4 v2, s[32:35], s59 offen lds\n"
    "s_add_u32 m0, s57, 21504\n"
    "s_add_u32 s59, s58, 32768\n"
    "buffer_load_dwordx4 v2, s[32:35], s59 offen lds\n"
    "s_add_u32 m0, s57, 22528\n"
    "s_add_u32 s59, s58, 262144\n"
    "buffer_load_dwordx4 v2, s[32:35], s59 offen lds\n"
    "s_add_u32 m0, s57, 23552\n"
    "s_add_u32 s59, s58, 294912\n"
    "buffer_load_dwordx4 v2, s[32:35], s59 offen lds\n"
    "s_waitcnt vmcnt(20)\n"
    "ds_read_b64_tr_b16 a[96:97], v4 offset:0\n"
    "ds_read_b64_tr_b16 a[98:99], v4 offset:1024\n"
    "ds_read_b64_tr_b16 a[100:101], v4 offset:2048\n"
    "ds_read_b64_tr_b16 a[102:103], v4 offset:3072\n"
    "ds_read_b64_tr_b16 a[104:105], v5 offset:0\n"
    "ds_read_b64_tr_b16 a[106:107], v5 offset:1024\n"
    "ds_read_b64_tr_b16 a[108:109], v5 offset:2048\n"
    "ds_read_b64_tr_b16 a[110:111], v5 offset:3072\n"
    "s_waitcnt lgkmcnt(0)\n"
    "s_mov_b32 s58, 6291456\n"
    "s_add_u32 s58, s58, s56\n"
    "s_add_u32 m0, s57, 0\n"
    "s_add_u32 s59, s58, 0\n"
    "buffer_load_dwordx4 v2, s[32:35], s59 offen lds\n"
    "s_add_u32 m0, s57, 1024\n"
    "s_add_u32 s59, s58, 32768\n"
    "buffer_load_dwordx4 v2, s[32:35], s59 offen lds\n"
    "s_add_u32 m0, s57, 2048\n"
    "s_add_u32 s59, s58, 262144\n"
    "buffer_load_dwordx4 v2, s[32:35], s59 offen lds\n"
    "s_add_u32 m0, s57, 3072\n"
    "s_add_u32 s59, s58, 294912\n"
    "buffer_load_dwordx4 v2, s[32:35], s59 offen lds\n"
    "s_waitcnt vmcnt(20)\n"
    "ds_read_b64_tr_b16 a[112:113], v4 offset:4096\n"
    "ds_read_b64_tr_b16 a[114:115], v4 offset:5120\n"
    "ds_read_b64_tr_b16 a[116:117], v4 offset:6144\n"
    "ds_read_b64_tr_b16 a[118:119], v4 offset:7168\n"
    "ds_read_b64_tr_b16 a[120:121], v5 offset:4096\n"
    "ds_read_b64_tr_b16 a[122:123], v5 offset:5120\n"
    "ds_read_b64_tr_b16 a[124:125], v5 offset:6144\n"
    "ds_read_b64_tr_b16 a[126:127], v5 offset:7168\n"
    "s_waitcnt lgkmcnt(0)\n"
    "s_mov_b32 s58, 6815744\n"
    "s_add_u32 s58, s58, s56\n"
    "s_add_u32 m0, s57, 4096\n"
    "s_add_u32 s59, s58, 0\n"
    "buffer_load_dwordx4 v2, s[32:35], s59 offen lds\n"
    "s_add_u32 m0, s57, 5120\n"
    "s_add_u32 s59, s58, 32768\n"
    "buffer_load_dwordx4 v2, s[32:35], s59 offen lds\n"
    "s_add_u32 m0, s57, 6144\n"
    "s_add_u32 s59, s58, 262144\n"
    "buffer_load_dwordx4 v2, s[32:35], s59 offen lds\n"
    "s_add_u32 m0, s57, 7168\n"
    "s_add_u32 s59, s58, 294912\n"
    "buffer_load_dwordx4 v2, s[32:35], s59 offen lds\n"
    "s_waitcnt vmcnt(20)\n"
    "ds_read_b64_tr_b16 a[128:129], v4 offset:8192\n"
    "ds_read_b64_tr_b16 a[130:131], v4 offset:9216\n"
    "ds_read_b64_tr_b16 a[132:133], v4 offset:10240\n"
    "ds_read_b64_tr_b16 a[134:135], v4 offset:11264\n"
    "ds_read_b64_tr_b16 a[136:137], v5 offset:8192\n"
    "ds_read_b64_tr_b16 a[138:139], v5 offset:9216\n"
    "ds_read_b64_tr_b16 a[140:141], v5 offset:10240\n"
    "ds_read_b64_tr_b16 a[142:143], v5 offset:11264\n"
    "s_waitcnt lgkmcnt(0)\n"
    "s_mov_b32 s58, 7340032\n"
    "s_add_u32 s58, s58, s56\n"
    "s_add_u32 m0, s57, 8192\n"
    "s_add_u32 s59, s58, 0\n"
    "buffer_load_dwordx4 v2, s[32:35], s59 offen lds\n"
    "s_add_u32 m0, s57, 9216\n"
    "s_add_u32 s59, s58, 32768\n"
    "buffer_load_dwordx4 v2, s[32:35], s59 offen lds\n"
    "s_add_u32 m0, s57, 10240\n"
    "s_add_u32 s59, s58, 262144\n"
    "buffer_load_dwordx4 v2, s[32:35], s59 offen lds\n"
    "s_add_u32 m0, s57, 11264\n"
    "s_add_u32 s59, s58, 294912\n"
    "buffer_load_dwordx4 v2, s[32:35], s59 offen lds\n"
    "s_waitcnt vmcnt(20)\n"
    "ds_read_b64_tr_b16 a[144:145], v4 offset:12288\n"
    "ds_read_b64_tr_b16 a[146:147], v4 offset:13312\n"
    "ds_read_b64_tr_b16 a[148:149], v4 offset:14336\n"
    "ds_read_b64_tr_b16 a[150:151], v4 offset:15360\n"
    "ds_read_b64_tr_b16 a[152:153], v5 offset:12288\n"
    "ds_read_b64_tr_b16 a[154:155], v5 offset:13312\n"
    "ds_read_b64_tr_b16 a[156:157], v5 offset:14336\n"
    "ds_read_b64_tr_b16 a[158:159], v5 offset:15360\n"
    "s_waitcnt lgkmcnt(0)\n"
    "s_mov_b32 s58, 7864320\n"
    "s_add_u32 s58, s58, s56\n"
    "s_add_u32 m0, s57, 12288\n"
    "s_add_u32 s59, s58, 0\n"
    "buffer_load_dwordx4 v2, s[32:35], s59 offen lds\n"
    "s_add_u32 m0, s57, 13312\n"
    "s_add_u32 s59, s58, 32768\n"
    "buffer_load_dwordx4 v2, s[32:35], s59 offen lds\n"
    "s_add_u32 m0, s57, 14336\n"
    "s_add_u32 s59, s58, 262144\n"
    "buffer_load_dwordx4 v2, s[32:35], s59 offen lds\n"
    "s_add_u32 m0, s57, 15360\n"
    "s_add_u32 s59, s58, 294912\n"
    "buffer_load_dwordx4 v2, s[32:35], s59 offen lds\n"
    "s_lshl_b32 s61, s24, 2\n"
    "s_add_u32 s61, s61, s25\n"
    "s_lshl_b32 s61, s61, 9\n"
    "v_bfe_u32 v13, v1, 4, 2\n"
    "v_lshlrev_b32 v13, 7, v13\n"
    "v_add_u32 v13, 65536, v13\n"
    "buffer_load_dword v14, v13, s[36:39], s61 offen\n"
    "s_mov_b32 s61, 0\n"
    "s_add_u32 s10, s19, 69632\n"
    "s_mov_b64 exec, 1\n"
    "buffer_load_dword v0, off, s[12:15], s10 sc1\n"
    "s_mov_b64 exec, -1\n"
    "s_waitcnt vmcnt(22)\n"
    "ds_read_b64_tr_b16 a[160:161], v4 offset:16384\n"
    "ds_read_b64_tr_b16 a[162:163], v4 offset:17408\n"
    "ds_read_b64_tr_b16 a[164:165], v4 offset:18432\n"
    "ds_read_b64_tr_b16 a[166:167], v4 offset:19456\n"
    "ds_read_b64_tr_b16 a[168:169], v5 offset:16384\n"
    "ds_read_b64_tr_b16 a[170:171], v5 offset:17408\n"
    "ds_read_b64_tr_b16 a[172:173], v5 offset:18432\n"
    "ds_read_b64_tr_b16 a[174:175], v5 offset:19456\n"
    "s_waitcnt lgkmcnt(0)\n"
    "s_waitcnt vmcnt(18)\n"
    "ds_read_b64_tr_b16 a[176:177], v4 offset:20480\n"
    "ds_read_b64_tr_b16 a[178:179], v4 offset:21504\n"
    "ds_read_b64_tr_b16 a[180:181], v4 offset:22528\n"
    "ds_read_b64_tr_b16 a[182:183], v4 offset:23552\n"
    "ds_read_b64_tr_b16 a[184:185], v5 offset:20480\n"
    "ds_read_b64_tr_b16 a[186:187], v5 offset:21504\n"
    "ds_read_b64_tr_b16 a[188:189], v5 offset:22528\n"
    "ds_read_b64_tr_b16 a[190:191], v5 offset:23552\n"
    "s_waitcnt lgkmcnt(0)\n"
    "s_waitcnt vmcnt(14)\n"
    "ds_read_b64_tr_b16 a[192:193], v4 offset:0\n"
    "ds_read_b64_tr_b16 a[194:195], v4 offset:1024\n"
    "ds_read_b64_tr_b16 a[196:197], v4 offset:2048\n"
    "ds_read_b64_tr_b16 a[198:199], v4 offset:3072\n"
    "ds_read_b64_tr_b16 a[200:201], v5 offset:0\n"
    "ds_read_b64_tr_b16 a[202:203], v5 offset:1024\n"
    "ds_read_b64_tr_b16 a[204:205], v5 offset:2048\n"
    "ds_read_b64_tr_b16 a[206:207], v5 offset:3072\n"
    "s_waitcnt lgkmcnt(0)\n"
    "s_waitcnt vmcnt(10)\n"
    "ds_read_b64_tr_b16 a[208:209], v4 offset:4096\n"
    "ds_read_b64_tr_b16 a[210:211], v4 offset:5120\n"
    "ds_read_b64_tr_b16 a[212:213], v4 offset:6144\n"
    "ds_read_b64_tr_b16 a[214:215], v4 offset:7168\n"
    "ds_read_b64_tr_b16 a[216:217], v5 offset:4096\n"
    "ds_read_b64_tr_b16 a[218:219], v5 offset:5120\n"
    "ds_read_b64_tr_b16 a[220:221], v5 offset:6144\n"
    "ds_read_b64_tr_b16 a[222:223], v5 offset:7168\n"
    "s_waitcnt lgkmcnt(0)\n"
    "s_waitcnt vmcnt(6)\n"
    "ds_read_b64_tr_b16 a[224:225], v4 offset:8192\n"
    "ds_read_b64_tr_b16 a[226:227], v4 offset:9216\n"
    "ds_read_b64_tr_b16 a[228:229], v4 offset:10240\n"
    "ds_read_b64_tr_b16 a[230:231], v4 offset:11264\n"
    "ds_read_b64_tr_b16 a[232:233], v5 offset:8192\n"
    "ds_read_b64_tr_b16 a[234:235], v5 offset:9216\n"
    "ds_read_b64_tr_b16 a[236:237], v5 offset:10240\n"
    "ds_read_b64_tr_b16 a[238:239], v5 offset:11264\n"
    "s_waitcnt lgkmcnt(0)\n"
    "s_waitcnt vmcnt(2)\n"
    "ds_read_b64_tr_b16 a[240:241], v4 offset:12288\n"
    "ds_read_b64_tr_b16 a[242:243], v4 offset:13312\n"
    "ds_read_b64_tr_b16 a[244:245], v4 offset:14336\n"
    "ds_read_b64_tr_b16 a[246:247], v4 offset:15360\n"
    "ds_read_b64_tr_b16 a[248:249], v5 offset:12288\n"
    "ds_read_b64_tr_b16 a[250:251], v5 offset:13312\n"
    "ds_read_b64_tr_b16 a[252:253], v5 offset:14336\n"
    "ds_read_b64_tr_b16 a[254:255], v5 offset:15360\n"
    "s_waitcnt lgkmcnt(0)\n"
    "s_waitcnt vmcnt(0)\n"
    ".Lgx_hsc_%=:\n"
    "v_readfirstlane_b32 s10, v0\n"
    "s_nop 1\n"
    "s_andn2_b32 s11, s10, 0xff\n"
    "s_cmp_lg_u32 s11, s18\n"
    "s_cbranch_scc1 .Lgx_hsp_%=\n"
    "s_and_b32 s11, s10, 7\n"
    "s_cmp_lg_u32 s11, s17\n"
    "s_cbranch_scc1 .Lgx_hsr_%=\n"
    "s_and_b32 s10, s10, 8\n"
    "s_lshl_b32 s27, s10, 10\n"
    "s_add_u32 s10, s19, 12288\n"
    "v_mov_b32 v0, 1\n"
    "s_mov_b64 exec, 1\n"
    "buffer_store_dword v0, off, s[12:15], s10 sc1\n"
    "s_mov_b64 exec, -1\n"
    "v_bfe_u32 v9, v1, 3, 1\n"
    "v_lshlrev_b32 v9, 10, v9\n"
    "v_bfe_u32 v3, v1, 0, 3\n"
    "v_lshl_add_u32 v9, v3, 7, v9\n"
    "v_bfe_u32 v3, v1, 4, 2\n"
    "v_lshl_add_u32 v9, v3, 4, v9\n"
    "v_bfe_u32 v2, v1, 1, 3\n"
    "v_lshlrev_b32 v2, 4, v2\n"
    "v_xor_b32 v9, v9, v2\n"
    "v_xor_b32 v10, 64, v9\n"
    "v_lshlrev_b32 v11, 4, v1\n"
    "s_lshl_b32 s58, s24, 2\n"
    "s_add_u32 s58, s58, s25\n"
    "s_and_b32 s58, s58, 255\n"
    "s_lshl_b32 s58, s58, 8\n"
    "s_add_u32 s58, s58, 196608\n"
    "v_bfe_u32 v13, v1, 5, 1\n"
    "v_lshlrev_b32 v13, 7, v13\n"
    "v_add_u32 v13, s58, v13\n"
    "s_lshl_b32 s64, s24, 6\n"
    "s_lshl_b32 s58, s25, 2\n"
    "s_add_u32 s64, s64, s58\n"
    "s_add_u32 s64, s64, s27\n"
    "s_add_u32 s64, s64, 32768\n"
    "s_mul_i32 s63, s66, 24576\n"
    "s_lshl_b32 s58, s65, 13\n"
    "s_add_u32 s63, s63, s58\n"
    "s_lshr_b32 s58, s65, 1\n"
    "s_add_u32 s58, s58, 10\n"
    "s_lshl_b32 s58, s25, s58\n"
    "s_add_u32 s63, s63, s58\n"
    "s_mov_b32 s60, 0\n"
    "s_barrier\n"
    "ds_read_b128 v[64:67], v9 offset:0\n"
    "ds_read_b128 v[68:71], v9 offset:2048\n"
    "ds_read_b128 v[72:75], v10 offset:0\n"
    "ds_read_b128 v[76:79], v10 offset:2048\n"
    "ds_read_b128 v[80:83], v9 offset:4096\n"
    "ds_read_b128 v[84:87], v9 offset:6144\n"
    "s_cmp_eq_u32 s65, 2\n"
    "s_cbranch_scc1 .Lgx_vloop_%=\n"
    ".Lgx_qloop_%=:\n"
    "s_waitcnt lgkmcnt(4)\n"
    "v_mfma_f32_16x16x32_bf16 v[16:19], a[0:3], v[64:67], 0\n"
    "ds_read_b128 v[88:91], v10 offset:4096\n"
    "ds_read_b128 v[92:95], v10 offset:6144\n"
    "v_mfma_f32_16x16x32_bf16 v[20:23], a[8:11], v[64:67], 0\n"
    "s_xor_b32 s62, s62, 65536\n"
    "s_add_u32 s61, s61, 65536\n"
    "s_mul_i32 s59, s60, 786432\n"
    "v_mfma_f32_16x16x32_bf16 v[24:27], a[0:3], v[68:71], 0\n"
    "ds_read_b128 v[96:99], v9 offset:8192\n"
    "ds_read_b128 v[100:103], v9 offset:10240\n"
    "v_mfma_f32_16x16x32_bf16 v[28:31], a[8:11], v[68:71], 0\n"
    "s_waitcnt lgkmcnt(6)\n"
    "s_add_u32 s59, s59, s63\n"
    "v_mov_b32 v12, s60\n"
    "v_mfma_f32_16x16x32_bf16 v[16:19], a[4:7], v[72:75], v[16:19]\n"
    "ds_read_b128 v[104:107], v10 offset:8192\n"
    "s_add_u32 m0, s62, 0\n"
    "s_add_u32 s58, s61, 0\n"
    "v_mfma_f32_16x16x32_bf16 v[20:23], a[12:15], v[72:75], v[20:23]\n"
    "buffer_load_dwordx4 v8, s[36:39], s58 offen lds\n"
    "v_mfma_f32_16x16x32_bf16 v[24:27], a[4:7], v[76:79], v[24:27]\n"
    "ds_read_b128 v[108:111], v10 offset:10240\n"
    "ds_read_b128 v[112:115], v9 offset:12288\n"
    "v_mfma_f32_16x16x32_bf16 v[28:31], a[12:15], v[76:79], v[28:31]\n"
    "s_waitcnt lgkmcnt(7)\n"
    "s_sub_u32 s67, s59, 786432\n"
    "s_cselect_b32 s67, 0x10000000, s67\n"
    "v_mfma_f32_16x16x32_bf16 v[16:19], a[16:19], v[80:83], v[16:19]\n"
    "ds_read_b128 v[116:119], v9 offset:14336\n"
    "s_add_u32 m0, s62, 4096\n"
    "s_add_u32 s58, s61, 128\n"
    "v_mfma_f32_16x16x32_bf16 v[20:23], a[24:27], v[80:83], v[20:23]\n"
    "buffer_load_dwordx4 v8, s[36:39], s58 offen lds\n"
    "v_mfma_f32_16x16x32_bf16 v[24:27], a[16:19], v[84:87], v[24:27]\n"
    "ds_read_b128 v[120:123], v10 offset:12288\n"
    "ds_read_b128 v[124:127], v10 offset:14336\n"
    "v_mfma_f32_16x16x32_bf16 v[28:31], a[24:27], v[84:87], v[28:31]\n"
    "s_waitcnt lgkmcnt(8)\n"
    "v_cvt_pk_bf16_f32 v48, v32, v33\n"
    "v_cvt_pk_bf16_f32 v49, v34, v35\n"
    "v_mfma_f32_16x16x32_bf16 v[16:19], a[20:23], v[88:91], v[16:19]\n"
    "ds_read_b128 v[64:67], v9 offset:16384\n"
    "s_add_u32 m0, s62, 8192\n"
    "s_add_u32 s58, s61, 256\n"
    "v_mfma_f32_16x16x32_bf16 v[20:23], a[28:31], v[88:91], v[20:23]\n"
    "buffer_load_dwordx4 v8, s[36:39], s58 offen lds\n"
    "v_mfma_f32_16x16x32_bf16 v[24:27], a[20:23], v[92:95], v[24:27]\n"
    "ds_read_b128 v[68:71], v9 offset:18432\n"
    "v_mfma_f32_16x16x32_bf16 v[28:31], a[28:31], v[92:95], v[28:31]\n"
    "s_waitcnt lgkmcnt(8)\n"
    "v_cvt_pk_bf16_f32 v50, v36, v37\n"
    "v_cvt_pk_bf16_f32 v51, v38, v39\n"
    "v_mfma_f32_16x16x32_bf16 v[16:19], a[32:35], v[96:99], v[16:19]\n"
    "ds_read_b128 v[72:75], v10 offset:16384\n"
    "s_add_u32 m0, s62, 12288\n"
    "s_add_u32 s58, s61, 384\n"
    "v_mfma_f32_16x16x32_bf16 v[20:23], a[40:43], v[96:99], v[20:23]\n"
    "buffer_load_dwordx4 v8, s[36:39], s58 offen lds\n"
    "v_mfma_f32_16x16x32_bf16 v[24:27], a[32:35], v[100:103], v[24:27]\n"
    "ds_read_b128 v[76:79], v10 offset:18432\n"
    "v_mfma_f32_16x16x32_bf16 v[28:31], a[40:43], v[100:103], v[28:31]\n"
    "s_waitcnt lgkmcnt(8)\n"
    "v_cvt_pk_bf16_f32 v52, v40, v41\n"
    "v_cvt_pk_bf16_f32 v53, v42, v43\n"
    "v_mfma_f32_16x16x32_bf16 v[16:19], a[36:39], v[104:107], v[16:19]\n"
    "ds_read_b128 v[80:83], v9 offset:20480\n"
    "s_add_u32 m0, s62, 16384\n"
    "s_add_u32 s58, s61, 512\n"
    "v_mfma_f32_16x16x32_bf16 v[20:23], a[44:47], v[104:107], v[20:23]\n"
    "buffer_load_dwordx4 v8, s[36:39], s58 offen lds\n"
    "v_mfma_f32_16x16x32_bf16 v[24:27], a[36:39], v[108:111], v[24:27]\n"
    "ds_read_b128 v[84:87], v9 offset:22528\n"
    "s_add_u32 s68, s67, 4096\n"
    "v_mfma_f32_16x16x32_bf16 v[28:31], a[44:47], v[108:111], v[28:31]\n"
    "s_waitcnt lgkmcnt(8)\n"
    "v_cvt_pk_bf16_f32 v54, v44, v45\n"
    "v_cvt_pk_bf16_f32 v55, v46, v47\n"
    "v_mfma_f32_16x16x32_bf16 v[16:19], a[48:51], v[112:115], v[16:19]\n"
    "ds_read_b128 v[88:91], v10 offset:20480\n"
    "s_add_u32 m0, s62, 20480\n"
    "s_add_u32 s58, s61, 640\n"
    "v_mfma_f32_16x16x32_bf16 v[20:23], a[56:59], v[112:115], v[20:23]\n"
    "buffer_load_dwordx4 v8, s[36:39], s58 offen lds\n"
    "v_mfma_f32_16x16x32_bf16 v[24:27], a[48:51], v[116:119], v[24:27]\n"
    "buffer_store_dwordx4 v[48:51], v11, s[40:43], s67 offen sc1\n"
    "v_mfma_f32_16x16x32_bf16 v[28:31], a[56:59], v[116:119], v[28:31]\n"
    "s_waitcnt lgkmcnt(7)\n"
    "v_mfma_f32_16x16x32_bf16 v[16:19], a[52:55], v[120:123], v[16:19]\n"
    "ds_read_b128 v[92:95], v10 offset:22528\n"
    "s_add_u32 m0, s62, 24576\n"
    "s_add_u32 s58, s61, 768\n"
    "v_mfma_f32_16x16x32_bf16 v[20:23], a[60:63], v[120:123], v[20:23]\n"
    "buffer_load_dwordx4 v8, s[36:39], s58 offen lds\n"
    "v_mfma_f32_16x16x32_bf16 v[24:27], a[52:55], v[124:127], v[24:27]\n"
    "buffer_store_dwordx4 v[52:55], v11, s[40:43], s68 offen sc1\n"
    "v_mfma_f32_16x16x32_bf16 v[28:31], a[60:63], v[124:127], v[28:31]\n"
    "s_waitcnt lgkmcnt(6)\n"
    "v_mfma_f32_16x16x32_bf16 v[16:19], a[64:67], v[64:67], v[16:19]\n"
    "ds_read_b128 v[96:99], v9 offset:24576\n"
    "s_add_u32 m0, s62, 28672\n"
    "s_add_u32 s58, s61, 896\n"
    "v_mfma_f32_16x16x32_bf16 v[20:23], a[72:75], v[64:67], v[20:23]\n"
    "buffer_load_dwordx4 v8, s[36:39], s58 offen lds\n"
    "v_mfma_f32_16x16x32_bf16 v[24:27], a[64:67], v[68:71], v[24:27]\n"
    "ds_read_b128 v[100:103], v9 offset:26624\n"
    "ds_read_b128 v[104:107], v10 offset:24576\n"
    "v_mfma_f32_16x16x32_bf16 v[28:31], a[72:75], v[68:71], v[28:31]\n"
    "s_waitcnt lgkmcnt(7)\n"
    "v_mfma_f32_16x16x32_bf16 v[16:19], a[68:71], v[72:75], v[16:19]\n"
    "ds_read_b128 v[108:111], v10 offset:26624\n"
    "s_add_u32 m0, s62, 32768\n"
    "s_add_u32 s58, s61, 1024\n"
    "v_mfma_f32_16x16x32_bf16 v[20:23], a[76:79], v[72:75], v[20:23]\n"
    "buffer_load_dwordx4 v8, s[36:39], s58 offen lds\n"
    "v_mfma_f32_16x16x32_bf16 v[24:27], a[68:71], v[76:79], v[24:27]\n"
    "ds_read_b128 v[112:115], v9 offset:28672\n"
    "ds_read_b128 v[116:119], v9 offset:30720\n"
    "v_mfma_f32_16x16x32_bf16 v[28:31], a[76:79], v[76:79], v[28:31]\n"
    "s_waitcnt lgkmcnt(8)\n"
    "v_mfma_f32_16x16x32_bf16 v[16:19], a[80:83], v[80:83], v[16:19]\n"
    "ds_read_b128 v[120:123], v10 offset:28672\n"
    "s_add_u32 m0, s62, 36864\n"
    "s_add_u32 s58, s61, 1152\n"
    "v_mfma_f32_16x16x32_bf16 v[20:23], a[88:91], v[80:83], v[20:23]\n"
    "buffer_load_dwordx4 v8, s[36:39], s58 offen lds\n"
    "v_mfma_f32_16x16x32_bf16 v[24:27], a[80:83], v[84:87], v[24:27]\n"
    "ds_read_b128 v[124:127], v10 offset:30720\n"
    "v_mfma_f32_16x16x32_bf16 v[28:31], a[88:91], v[84:87], v[28:31]\n"
    "s_waitcnt lgkmcnt(8)\n"
    "v_mfma_f32_16x16x32_bf16 v[16:19], a[84:87], v[88:91], v[16:19]\n"
    "ds_read_b128 v[64:67], v9 offset:32768\n"
    "s_add_u32 m0, s62, 40960\n"
    "s_add_u32 s58, s61, 1280\n"
    "v_mfma_f32_16x16x32_bf16 v[20:23], a[92:95], v[88:91], v[20:23]\n"
    "buffer_load_dwordx4 v8, s[36:39], s58 offen lds\n"
    "v_mfma_f32_16x16x32_bf16 v[24:27], a[84:87], v[92:95], v[24:27]\n"
    "ds_read_b128 v[68:71], v9 offset:34816\n"
    "v_mfma_f32_16x16x32_bf16 v[28:31], a[92:95], v[92:95], v[28:31]\n"
    "s_waitcnt lgkmcnt(8)\n"
    "v_mfma_f32_16x16x32_bf16 v[16:19], a[96:99], v[96:99], v[16:19]\n"
    "ds_read_b128 v[72:75], v10 offset:32768\n"
    "s_add_u32 m0, s62, 45056\n"
    "s_add_u32 s58, s61, 1408\n"
    "v_mfma_f32_16x16x32_bf16 v[20:23], a[104:107], v[96:99], v[20:23]\n"
    "buffer_load_dwordx4 v8, s[36:39], s58 offen lds\n"
    "v_mfma_f32_16x16x32_bf16 v[24:27], a[96:99], v[100:103], v[24:27]\n"
    "ds_read_b128 v[76:79], v10 offset:34816\n"
    "v_mfma_f32_16x16x32_bf16 v[28:31], a[104:107], v[100:103], v[28:31]\n"
    "s_waitcnt lgkmcnt(8)\n"
    "v_mfma_f32_16x16x32_bf16 v[16:19], a[100:103], v[104:107], v[16:19]\n"
    "ds_read_b128 v[80:83], v9 offset:36864\n"
    "s_add_u32 m0, s62, 49152\n"
    "s_add_u32 s58, s61, 1536\n"
    "v_mfma_f32_16x16x32_bf16 v[20:23], a[108:111], v[104:107], v[20:23]\n"
    "buffer_load_dwordx4 v8, s[36:39], s58 offen lds\n"
    "v_mfma_f32_16x16x32_bf16 v[24:27], a[100:103], v[108:111], v[24:27]\n"
    "ds_read_b128 v[84:87], v9 offset:38912\n"
    "v_mfma_f32_16x16x32_bf16 v[28:31], a[108:111], v[108:111], v[28:31]\n"
    "s_waitcnt lgkmcnt(8)\n"
    "v_mfma_f32_16x16x32_bf16 v[16:19], a[112:115], v[112:115], v[16:19]\n"
    "ds_read_b128 v[88:91], v10 offset:36864\n"
    "s_add_u32 m0, s62, 53248\n"
    "s_add_u32 s58, s61, 1664\n"
    "v_mfma_f32_16x16x32_bf16 v[20:23], a[120:123], v[112:115], v[20:23]\n"
    "buffer_load_dwordx4 v8, s[36:39], s58 offen lds\n"
    "v_mfma_f32_16x16x32_bf16 v[24:27], a[112:115], v[116:119], v[24:27]\n"
    "ds_read_b128 v[92:95], v10 offset:38912\n"
    "v_mfma_f32_16x16x32_bf16 v[28:31], a[120:123], v[116:119], v[28:31]\n"
    "s_waitcnt lgkmcnt(8)\n"
    "v_mfma_f32_16x16x32_bf16 v[16:19], a[116:119], v[120:123], v[16:19]\n"
    "ds_read_b128 v[96:99], v9 offset:40960\n"
    "s_add_u32 m0, s62, 57344\n"
    "s_add_u32 s58, s61, 1792\n"
    "v_mfma_f32_16x16x32_bf16 v[20:23], a[124:127], v[120:123], v[20:23]\n"
    "buffer_load_dwordx4 v8, s[36:39], s58 offen lds\n"
    "v_mfma_f32_16x16x32_bf16 v[24:27], a[116:119], v[124:127], v[24:27]\n"
    "ds_read_b128 v[100:103], v9 offset:43008\n"
    "v_mfma_f32_16x16x32_bf16 v[28:31], a[124:127], v[124:127], v[28:31]\n"
    "s_waitcnt lgkmcnt(8)\n"
    "v_mfma_f32_16x16x32_bf16 v[16:19], a[128:131], v[64:67], v[16:19]\n"
    "ds_read_b128 v[104:107], v10 offset:40960\n"
    "s_add_u32 m0, s62, 61440\n"
    "s_add_u32 s58, s61, 1920\n"
    "v_mfma_f32_16x16x32_bf16 v[20:23], a[136:139], v[64:67], v[20:23]\n"
    "buffer_load_dwordx4 v8, s[36:39], s58 offen lds\n"
    "v_mfma_f32_16x16x32_bf16 v[24:27], a[128:131], v[68:71], v[24:27]\n"
    "ds_read_b128 v[108:111], v10 offset:43008\n"
    "v_mfma_f32_16x16x32_bf16 v[28:31], a[136:139], v[68:71], v[28:31]\n"
    "s_waitcnt lgkmcnt(8)\n"
    "v_mfma_f32_16x16x32_bf16 v[16:19], a[132:135], v[72:75], v[16:19]\n"
    "ds_read_b128 v[112:115], v9 offset:45056\n"
    "v_mfma_f32_16x16x32_bf16 v[20:23], a[140:143], v[72:75], v[20:23]\n"
    "v_mfma_f32_16x16x32_bf16 v[24:27], a[132:135], v[76:79], v[24:27]\n"
    "ds_read_b128 v[116:119], v9 offset:47104\n"
    "v_mfma_f32_16x16x32_bf16 v[28:31], a[140:143], v[76:79], v[28:31]\n"
    "s_waitcnt lgkmcnt(8)\n"
    "v_mfma_f32_16x16x32_bf16 v[16:19], a[144:147], v[80:83], v[16:19]\n"
    "ds_read_b128 v[120:123], v10 offset:45056\n"
    "v_mfma_f32_16x16x32_bf16 v[20:23], a[152:155], v[80:83], v[20:23]\n"
    "v_mfma_f32_16x16x32_bf16 v[24:27], a[144:147], v[84:87], v[24:27]\n"
    "ds_read_b128 v[124:127], v10 offset:47104\n"
    "v_mfma_f32_16x16x32_bf16 v[28:31], a[152:155], v[84:87], v[28:31]\n"
    "s_waitcnt lgkmcnt(8)\n"
    "v_mfma_f32_16x16x32_bf16 v[16:19], a[148:151], v[88:91], v[16:19]\n"
    "ds_read_b128 v[64:67], v9 offset:49152\n"
    "v_mfma_f32_16x16x32_bf16 v[20:23], a[156:159], v[88:91], v[20:23]\n"
    "v_mfma_f32_16x16x32_bf16 v[24:27], a[148:151], v[92:95], v[24:27]\n"
    "ds_read_b128 v[68:71], v9 offset:51200\n"
    "v_mfma_f32_16x16x32_bf16 v[28:31], a[156:159], v[92:95], v[28:31]\n"
    "s_waitcnt lgkmcnt(8)\n"
    "v_mfma_f32_16x16x32_bf16 v[16:19], a[160:163], v[96:99], v[16:19]\n"
    "ds_read_b128 v[72:75], v10 offset:49152\n"
    "v_mfma_f32_16x16x32_bf16 v[20:23], a[168:171], v[96:99], v[20:23]\n"
    "v_mfma_f32_16x16x32_bf16 v[24:27], a[160:163], v[100:103], v[24:27]\n"
    "ds_read_b128 v[76:79], v10 offset:51200\n"
    "v_mfma_f32_16x16x32_bf16 v[28:31], a[168:171], v[100:103], v[28:31]\n"
    "s_waitcnt lgkmcnt(8)\n"
    "v_mfma_f32_16x16x32_bf16 v[16:19], a[164:167], v[104:107], v[16:19]\n"
    "ds_read_b128 v[80:83], v9 offset:53248\n"
    "v_mfma_f32_16x16x32_bf16 v[20:23], a[172:175], v[104:107], v[20:23]\n"
    "v_mfma_f32_16x16x32_bf16 v[24:27], a[164:167], v[108:111], v[24:27]\n"
    "ds_read_b128 v[84:87], v9 offset:55296\n"
    "v_mfma_f32_16x16x32_bf16 v[28:31], a[172:175], v[108:111], v[28:31]\n"
    "s_waitcnt lgkmcnt(8)\n"
    "v_mfma_f32_16x16x32_bf16 v[16:19], a[176:179], v[112:115], v[16:19]\n"
    "ds_read_b128 v[88:91], v10 offset:53248\n"
    "v_mfma_f32_16x16x32_bf16 v[20:23], a[184:187], v[112:115], v[20:23]\n"
    "s_waitcnt vmcnt(9)\n"
    "s_mov_b64 exec, 1\n"
    "buffer_store_dword v12, off, s[44:47], s64 sc1\n"
    "s_mov_b64 exec, -1\n"
    "v_mfma_f32_16x16x32_bf16 v[24:27], a[176:179], v[116:119], v[24:27]\n"
    "ds_read_b128 v[92:95], v10 offset:55296\n"
    "v_mfma_f32_16x16x32_bf16 v[28:31], a[184:187], v[116:119], v[28:31]\n"
    "s_waitcnt lgkmcnt(8)\n"
    "v_mfma_f32_16x16x32_bf16 v[16:19], a[180:183], v[120:123], v[16:19]\n"
    "ds_read_b128 v[96:99], v9 offset:57344\n"
    "v_mfma_f32_16x16x32_bf16 v[20:23], a[188:191], v[120:123], v[20:23]\n"
    "v_mfma_f32_16x16x32_bf16 v[24:27], a[180:183], v[124:127], v[24:27]\n"
    "ds_read_b128 v[100:103], v9 offset:59392\n"
    "v_mfma_f32_16x16x32_bf16 v[28:31], a[188:191], v[124:127], v[28:31]\n"
    "s_waitcnt lgkmcnt(8)\n"
    "v_mfma_f32_16x16x32_bf16 v[16:19], a[192:195], v[64:67], v[16:19]\n"
    "ds_read_b128 v[104:107], v10 offset:57344\n"
    "v_mfma_f32_16x16x32_bf16 v[20:23], a[200:203], v[64:67], v[20:23]\n"
    "v_mfma_f32_16x16x32_bf16 v[24:27], a[192:195], v[68:71], v[24:27]\n"
    "ds_read_b128 v[108:111], v10 offset:59392\n"
    "v_mfma_f32_16x16x32_bf16 v[28:31], a[200:203], v[68:71], v[28:31]\n"
    "s_waitcnt lgkmcnt(8)\n"
    "v_mfma_f32_16x16x32_bf16 v[16:19], a[196:199], v[72:75], v[16:19]\n"
    "ds_read_b128 v[112:115], v9 offset:61440\n"
    "v_mfma_f32_16x16x32_bf16 v[20:23], a[204:207], v[72:75], v[20:23]\n"
    "v_mfma_f32_16x16x32_bf16 v[24:27], a[196:199], v[76:79], v[24:27]\n"
    "ds_read_b128 v[116:119], v9 offset:63488\n"
    "v_mfma_f32_16x16x32_bf16 v[28:31], a[204:207], v[76:79], v[28:31]\n"
    "s_waitcnt lgkmcnt(8)\n"
    "v_mfma_f32_16x16x32_bf16 v[16:19], a[208:211], v[80:83], v[16:19]\n"
    "ds_read_b128 v[120:123], v10 offset:61440\n"
    "v_mfma_f32_16x16x32_bf16 v[20:23], a[216:219], v[80:83], v[20:23]\n"
    "v_mfma_f32_16x16x32_bf16 v[24:27], a[208:211], v[84:87], v[24:27]\n"
    "ds_read_b128 v[124:127], v10 offset:63488\n"
    "v_mfma_f32_16x16x32_bf16 v[28:31], a[216:219], v[84:87], v[28:31]\n"
    "s_waitcnt lgkmcnt(8)\n"
    "v_mfma_f32_16x16x32_bf16 v[16:19], a[212:215], v[88:91], v[16:19]\n"
    "v_mfma_f32_16x16x32_bf16 v[20:23], a[220:223], v[88:91], v[20:23]\n"
    "v_mfma_f32_16x16x32_bf16 v[24:27], a[212:215], v[92:95], v[24:27]\n"
    "v_mfma_f32_16x16x32_bf16 v[28:31], a[220:223], v[92:95], v[28:31]\n"
    "s_waitcnt lgkmcnt(6)\n"
    "v_mfma_f32_16x16x32_bf16 v[16:19], a[224:227], v[96:99], v[16:19]\n"
    "v_mfma_f32_16x16x32_bf16 v[20:23], a[232:235], v[96:99], v[20:23]\n"
    "v_mfma_f32_16x16x32_bf16 v[24:27], a[224:227], v[100:103], v[24:27]\n"
    "v_xor_b32 v9, 0x10000, v9\n"
    "v_xor_b32 v10, 0x10000, v10\n"
    "v_mfma_f32_16x16x32_bf16 v[28:31], a[232:235], v[100:103], v[28:31]\n"
    "s_waitcnt vmcnt(1)\n"
    "s_waitcnt lgkmcnt(0)\n"
    "s_barrier\n"
    "v_mfma_f32_16x16x32_bf16 v[16:19], a[228:231], v[104:107], v[16:19]\n"
    "ds_read_b128 v[64:67], v9 offset:0\n"
    "v_mfma_f32_16x16x32_bf16 v[20:23], a[236:239], v[104:107], v[20:23]\n"
    "buffer_load_dword v14, v13, s[36:39], s61 offen\n"
    "v_mfma_f32_16x16x32_bf16 v[24:27], a[228:231], v[108:111], v[24:27]\n"
    "ds_read_b128 v[68:71], v9 offset:2048\n"
    "v_mfma_f32_16x16x32_bf16 v[28:31], a[236:239], v[108:111], v[28:31]\n"
    "v_mfma_f32_16x16x32_bf16 v[16:19], a[240:243], v[112:115], v[16:19]\n"
    "ds_read_b128 v[72:75], v10 offset:0\n"
    "v_mfma_f32_16x16x32_bf16 v[20:23], a[248:251], v[112:115], v[20:23]\n"
    "v_mfma_f32_16x16x32_bf16 v[24:27], a[240:243], v[116:119], v[24:27]\n"
    "ds_read_b128 v[76:79], v10 offset:2048\n"
    "v_mfma_f32_16x16x32_bf16 v[28:31], a[248:251], v[116:119], v[28:31]\n"
    "v_mfma_f32_16x16x32_bf16 v[16:19], a[244:247], v[120:123], v[16:19]\n"
    "ds_read_b128 v[80:83], v9 offset:4096\n"
    "v_mfma_f32_16x16x32_bf16 v[20:23], a[252:255], v[120:123], v[20:23]\n"
    "s_add_u32 s60, s60, 1\n"
    "v_mfma_f32_16x16x32_bf16 v[24:27], a[244:247], v[124:127], v[24:27]\n"
    "ds_read_b128 v[84:87], v9 offset:6144\n"
    "v_mfma_f32_16x16x32_bf16 v[28:31], a[252:255], v[124:127], v[28:31]\n"
    "s_waitcnt lgkmcnt(4)\n"
    "v_mfma_f32_16x16x32_bf16 v[32:35], a[0:3], v[64:67], 0\n"
    "ds_read_b128 v[88:91], v10 offset:4096\n"
    "ds_read_b128 v[92:95], v10 offset:6144\n"
    "v_mfma_f32_16x16x32_bf16 v[36:39], a[8:11], v[64:67], 0\n"
    "s_xor_b32 s62, s62, 65536\n"
    "s_add_u32 s61, s61, 65536\n"
    "s_mul_i32 s59, s60, 786432\n"
    "v_mfma_f32_16x16x32_bf16 v[40:43], a[0:3], v[68:71], 0\n"
    "ds_read_b128 v[96:99], v9 offset:8192\n"
    "ds_read_b128 v[100:103], v9 offset:10240\n"
    "v_mfma_f32_16x16x32_bf16 v[44:47], a[8:11], v[68:71], 0\n"
    "s_waitcnt lgkmcnt(6)\n"
    "s_add_u32 s59, s59, s63\n"
    "v_mov_b32 v12, s60\n"
    "v_mfma_f32_16x16x32_bf16 v[32:35], a[4:7], v[72:75], v[32:35]\n"
    "ds_read_b128 v[104:107], v10 offset:8192\n"
    "s_add_u32 m0, s62, 0\n"
    "s_add_u32 s58, s61, 0\n"
    "v_mfma_f32_16x16x32_bf16 v[36:39], a[12:15], v[72:75], v[36:39]\n"
    "buffer_load_dwordx4 v8, s[36:39], s58 offen lds\n"
    "v_mfma_f32_16x16x32_bf16 v[40:43], a[4:7], v[76:79], v[40:43]\n"
    "ds_read_b128 v[108:111], v10 offset:10240\n"
    "ds_read_b128 v[112:115], v9 offset:12288\n"
    "v_mfma_f32_16x16x32_bf16 v[44:47], a[12:15], v[76:79], v[44:47]\n"
    "s_waitcnt lgkmcnt(7)\n"
    "s_sub_u32 s67, s59, 786432\n"
    "s_cselect_b32 s67, 0x10000000, s67\n"
    "v_mfma_f32_16x16x32_bf16 v[32:35], a[16:19], v[80:83], v[32:35]\n"
    "ds_read_b128 v[116:119], v9 offset:14336\n"
    "s_add_u32 m0, s62, 4096\n"
    "s_add_u32 s58, s61, 128\n"
    "v_mfma_f32_16x16x32_bf16 v[36:39], a[24:27], v[80:83], v[36:39]\n"
    "buffer_load_dwordx4 v8, s[36:39], s58 offen lds\n"
    "v_mfma_f32_16x16x32_bf16 v[40:43], a[16:19], v[84:87], v[40:43]\n"
    "ds_read_b128 v[120:123], v10 offset:12288\n"
    "ds_read_b128 v[124:127], v10 offset:14336\n"
    "v_mfma_f32_16x16x32_bf16 v[44:47], a[24:27], v[84:87], v[44:47]\n"
    "s_waitcnt lgkmcnt(8)\n"
    "v_cvt_pk_bf16_f32 v48, v16, v17\n"
    "v_cvt_pk_bf16_f32 v49, v18, v19\n"
    "v_mfma_f32_16x16x32_bf16 v[32:35], a[20:23], v[88:91], v[32:35]\n"
    "ds_read_b128 v[64:67], v9 offset:16384\n"
    "s_add_u32 m0, s62, 8192\n"
    "s_add_u32 s58, s61, 256\n"
    "v_mfma_f32_16x16x32_bf16 v[36:39], a[28:31], v[88:91], v[36:39]\n"
    "buffer_load_dwordx4 v8, s[36:39], s58 offen lds\n"
    "v_mfma_f32_16x16x32_bf16 v[40:43], a[20:23], v[92:95], v[40:43]\n"
    "ds_read_b128 v[68:71], v9 offset:18432\n"
    "v_mfma_f32_16x16x32_bf16 v[44:47], a[28:31], v[92:95], v[44:47]\n"
    "s_waitcnt lgkmcnt(8)\n"
    "v_cvt_pk_bf16_f32 v50, v20, v21\n"
    "v_cvt_pk_bf16_f32 v51, v22, v23\n"
    "v_mfma_f32_16x16x32_bf16 v[32:35], a[32:35], v[96:99], v[32:35]\n"
    "ds_read_b128 v[72:75], v10 offset:16384\n"
    "s_add_u32 m0, s62, 12288\n"
    "s_add_u32 s58, s61, 384\n"
    "v_mfma_f32_16x16x32_bf16 v[36:39], a[40:43], v[96:99], v[36:39]\n"
    "buffer_load_dwordx4 v8, s[36:39], s58 offen lds\n"
    "v_mfma_f32_16x16x32_bf16 v[40:43], a[32:35], v[100:103], v[40:43]\n"
    "ds_read_b128 v[76:79], v10 offset:18432\n"
    "v_mfma_f32_16x16x32_bf16 v[44:47], a[40:43], v[100:103], v[44:47]\n"
    "s_waitcnt lgkmcnt(8)\n"
    "v_cvt_pk_bf16_f32 v52, v24, v25\n"
    "v_cvt_pk_bf16_f32 v53, v26, v27\n"
    "v_mfma_f32_16x16x32_bf16 v[32:35], a[36:39], v[104:107], v[32:35]\n"
    "ds_read_b128 v[80:83], v9 offset:20480\n"
    "s_add_u32 m0, s62, 16384\n"
    "s_add_u32 s58, s61, 512\n"
    "v_mfma_f32_16x16x32_bf16 v[36:39], a[44:47], v[104:107], v[36:39]\n"
    "buffer_load_dwordx4 v8, s[36:39], s58 offen lds\n"
    "v_mfma_f32_16x16x32_bf16 v[40:43], a[36:39], v[108:111], v[40:43]\n"
    "ds_read_b128 v[84:87], v9 offset:22528\n"
    "s_add_u32 s68, s67, 4096\n"
    "v_mfma_f32_16x16x32_bf16 v[44:47], a[44:47], v[108:111], v[44:47]\n"
    "s_waitcnt lgkmcnt(8)\n"
    "v_cvt_pk_bf16_f32 v54, v28, v29\n"
    "v_cvt_pk_bf16_f32 v55, v30, v31\n"
    "v_mfma_f32_16x16x32_bf16 v[32:35], a[48:51], v[112:115], v[32:35]\n"
    "ds_read_b128 v[88:91], v10 offset:20480\n"
    "s_add_u32 m0, s62, 20480\n"
    "s_add_u32 s58, s61, 640\n"
    "v_mfma_f32_16x16x32_bf16 v[36:39], a[56:59], v[112:115], v[36:39]\n"
    "buffer_load_dwordx4 v8, s[36:39], s58 offen lds\n"
    "v_mfma_f32_16x16x32_bf16 v[40:43], a[48:51], v[116:119], v[40:43]\n"
    "buffer_store_dwordx4 v[48:51], v11, s[40:43], s67 offen sc1\n"
    "v_mfma_f32_16x16x32_bf16 v[44:47], a[56:59], v[116:119], v[44:47]\n"
    "s_waitcnt lgkmcnt(7)\n"
    "v_mfma_f32_16x16x32_bf16 v[32:35], a[52:55], v[120:123], v[32:35]\n"
    "ds_read_b128 v[92:95], v10 offset:22528\n"
    "s_add_u32 m0, s62, 24576\n"
    "s_add_u32 s58, s61, 768\n"
    "v_mfma_f32_16x16x32_bf16 v[36:39], a[60:63], v[120:123], v[36:39]\n"
    "buffer_load_dwordx4 v8, s[36:39], s58 offen lds\n"
    "v_mfma_f32_16x16x32_bf16 v[40:43], a[52:55], v[124:127], v[40:43]\n"
    "buffer_store_dwordx4 v[52:55], v11, s[40:43], s68 offen sc1\n"
    "v_mfma_f32_16x16x32_bf16 v[44:47], a[60:63], v[124:127], v[44:47]\n"
    "s_waitcnt lgkmcnt(6)\n"
    "v_mfma_f32_16x16x32_bf16 v[32:35], a[64:67], v[64:67], v[32:35]\n"
    "ds_read_b128 v[96:99], v9 offset:24576\n"
    "s_add_u32 m0, s62, 28672\n"
    "s_add_u32 s58, s61, 896\n"
    "v_mfma_f32_16x16x32_bf16 v[36:39], a[72:75], v[64:67], v[36:39]\n"
    "buffer_load_dwordx4 v8, s[36:39], s58 offen lds\n"
    "v_mfma_f32_16x16x32_bf16 v[40:43], a[64:67], v[68:71], v[40:43]\n"
    "ds_read_b128 v[100:103], v9 offset:26624\n"
    "ds_read_b128 v[104:107], v10 offset:24576\n"
    "v_mfma_f32_16x16x32_bf16 v[44:47], a[72:75], v[68:71], v[44:47]\n"
    "s_waitcnt lgkmcnt(7)\n"
    "v_mfma_f32_16x16x32_bf16 v[32:35], a[68:71], v[72:75], v[32:35]\n"
    "ds_read_b128 v[108:111], v10 offset:26624\n"
    "s_add_u32 m0, s62, 32768\n"
    "s_add_u32 s58, s61, 1024\n"
    "v_mfma_f32_16x16x32_bf16 v[36:39], a[76:79], v[72:75], v[36:39]\n"
    "buffer_load_dwordx4 v8, s[36:39], s58 offen lds\n"
    "v_mfma_f32_16x16x32_bf16 v[40:43], a[68:71], v[76:79], v[40:43]\n"
    "ds_read_b128 v[112:115], v9 offset:28672\n"
    "ds_read_b128 v[116:119], v9 offset:30720\n"
    "v_mfma_f32_16x16x32_bf16 v[44:47], a[76:79], v[76:79], v[44:47]\n"
    "s_waitcnt lgkmcnt(8)\n"
    "v_mfma_f32_16x16x32_bf16 v[32:35], a[80:83], v[80:83], v[32:35]\n"
    "ds_read_b128 v[120:123], v10 offset:28672\n"
    "s_add_u32 m0, s62, 36864\n"
    "s_add_u32 s58, s61, 1152\n"
    "v_mfma_f32_16x16x32_bf16 v[36:39], a[88:91], v[80:83], v[36:39]\n"
    "buffer_load_dwordx4 v8, s[36:39], s58 offen lds\n"
    "v_mfma_f32_16x16x32_bf16 v[40:43], a[80:83], v[84:87], v[40:43]\n"
    "ds_read_b128 v[124:127], v10 offset:30720\n"
    "v_mfma_f32_16x16x32_bf16 v[44:47], a[88:91], v[84:87], v[44:47]\n"
    "s_waitcnt lgkmcnt(8)\n"
    "v_mfma_f32_16x16x32_bf16 v[32:35], a[84:87], v[88:91], v[32:35]\n"
    "ds_read_b128 v[64:67], v9 offset:32768\n"
    "s_add_u32 m0, s62, 40960\n"
    "s_add_u32 s58, s61, 1280\n"
    "v_mfma_f32_16x16x32_bf16 v[36:39], a[92:95], v[88:91], v[36:39]\n"
    "buffer_load_dwordx4 v8, s[36:39], s58 offen lds\n"
    "v_mfma_f32_16x16x32_bf16 v[40:43], a[84:87], v[92:95], v[40:43]\n"
    "ds_read_b128 v[68:71], v9 offset:34816\n"
    "v_mfma_f32_16x16x32_bf16 v[44:47], a[92:95], v[92:95], v[44:47]\n"
    "s_waitcnt lgkmcnt(8)\n"
    "v_mfma_f32_16x16x32_bf16 v[32:35], a[96:99], v[96:99], v[32:35]\n"
    "ds_read_b128 v[72:75], v10 offset:32768\n"
    "s_add_u32 m0, s62, 45056\n"
    "s_add_u32 s58, s61, 1408\n"
    "v_mfma_f32_16x16x32_bf16 v[36:39], a[104:107], v[96:99], v[36:39]\n"
    "buffer_load_dwordx4 v8, s[36:39], s58 offen lds\n"
    "v_mfma_f32_16x16x32_bf16 v[40:43], a[96:99], v[100:103], v[40:43]\n"
    "ds_read_b128 v[76:79], v10 offset:34816\n"
    "v_mfma_f32_16x16x32_bf16 v[44:47], a[104:107], v[100:103], v[44:47]\n"
    "s_waitcnt lgkmcnt(8)\n"
    "v_mfma_f32_16x16x32_bf16 v[32:35], a[100:103], v[104:107], v[32:35]\n"
    "ds_read_b128 v[80:83], v9 offset:36864\n"
    "s_add_u32 m0, s62, 49152\n"
    "s_add_u32 s58, s61, 1536\n"
    "v_mfma_f32_16x16x32_bf16 v[36:39], a[108:111], v[104:107], v[36:39]\n"
    "buffer_load_dwordx4 v8, s[36:39], s58 offen lds\n"
    "v_mfma_f32_16x16x32_bf16 v[40:43], a[100:103], v[108:111], v[40:43]\n"
    "ds_read_b128 v[84:87], v9 offset:38912\n"
    "v_mfma_f32_16x16x32_bf16 v[44:47], a[108:111], v[108:111], v[44:47]\n"
    "s_waitcnt lgkmcnt(8)\n"
    "v_mfma_f32_16x16x32_bf16 v[32:35], a[112:115], v[112:115], v[32:35]\n"
    "ds_read_b128 v[88:91], v10 offset:36864\n"
    "s_add_u32 m0, s62, 53248\n"
    "s_add_u32 s58, s61, 1664\n"
    "v_mfma_f32_16x16x32_bf16 v[36:39], a[120:123], v[112:115], v[36:39]\n"
    "buffer_load_dwordx4 v8, s[36:39], s58 offen lds\n"
    "v_mfma_f32_16x16x32_bf16 v[40:43], a[112:115], v[116:119], v[40:43]\n"
    "ds_read_b128 v[92:95], v10 offset:38912\n"
    "v_mfma_f32_16x16x32_bf16 v[44:47], a[120:123], v[116:119], v[44:47]\n"
    "s_waitcnt lgkmcnt(8)\n"
    "v_mfma_f32_16x16x32_bf16 v[32:35], a[116:119], v[120:123], v[32:35]\n"
    "ds_read_b128 v[96:99], v9 offset:40960\n"
    "s_add_u32 m0, s62, 57344\n"
    "s_add_u32 s58, s61, 1792\n"
    "v_mfma_f32_16x16x32_bf16 v[36:39], a[124:127], v[120:123], v[36:39]\n"
    "buffer_load_dwordx4 v8, s[36:39], s58 offen lds\n"
    "v_mfma_f32_16x16x32_bf16 v[40:43], a[116:119], v[124:127], v[40:43]\n"
    "ds_read_b128 v[100:103], v9 offset:43008\n"
    "v_mfma_f32_16x16x32_bf16 v[44:47], a[124:127], v[124:127], v[44:47]\n"
    "s_waitcnt lgkmcnt(8)\n"
    "v_mfma_f32_16x16x32_bf16 v[32:35], a[128:131], v[64:67], v[32:35]\n"
    "ds_read_b128 v[104:107], v10 offset:40960\n"
    "s_add_u32 m0, s62, 61440\n"
    "s_add_u32 s58, s61, 1920\n"
    "v_mfma_f32_16x16x32_bf16 v[36:39], a[136:139], v[64:67], v[36:39]\n"
    "buffer_load_dwordx4 v8, s[36:39], s58 offen lds\n"
    "v_mfma_f32_16x16x32_bf16 v[40:43], a[128:131], v[68:71], v[40:43]\n"
    "ds_read_b128 v[108:111], v10 offset:43008\n"
    "v_mfma_f32_16x16x32_bf16 v[44:47], a[136:139], v[68:71], v[44:47]\n"
    "s_waitcnt lgkmcnt(8)\n"
    "v_mfma_f32_16x16x32_bf16 v[32:35], a[132:135], v[72:75], v[32:35]\n"
    "ds_read_b128 v[112:115], v9 offset:45056\n"
    "v_mfma_f32_16x16x32_bf16 v[36:39], a[140:143], v[72:75], v[36:39]\n"
    "v_mfma_f32_16x16x32_bf16 v[40:43], a[132:135], v[76:79], v[40:43]\n"
    "ds_read_b128 v[116:119], v9 offset:47104\n"
    "v_mfma_f32_16x16x32_bf16 v[44:47], a[140:143], v[76:79], v[44:47]\n"
    "s_waitcnt lgkmcnt(8)\n"
    "v_mfma_f32_16x16x32_bf16 v[32:35], a[144:147], v[80:83], v[32:35]\n"
    "ds_read_b128 v[120:123], v10 offset:45056\n"
    "v_mfma_f32_16x16x32_bf16 v[36:39], a[152:155], v[80:83], v[36:39]\n"
    "v_mfma_f32_16x16x32_bf16 v[40:43], a[144:147], v[84:87], v[40:43]\n"
    "ds_read_b128 v[124:127], v10 offset:47104\n"
    "v_mfma_f32_16x16x32_bf16 v[44:47], a[152:155], v[84:87], v[44:47]\n"
    "s_waitcnt lgkmcnt(8)\n"
    "v_mfma_f32_16x16x32_bf16 v[32:35], a[148:151], v[88:91], v[32:35]\n"
    "ds_read_b128 v[64:67], v9 offset:49152\n"
    "v_mfma_f32_16x16x32_bf16 v[36:39], a[156:159], v[88:91], v[36:39]\n"
    "v_mfma_f32_16x16x32_bf16 v[40:43], a[148:151], v[92:95], v[40:43]\n"
    "ds_read_b128 v[68:71], v9 offset:51200\n"
    "v_mfma_f32_16x16x32_bf16 v[44:47], a[156:159], v[92:95], v[44:47]\n"
    "s_waitcnt lgkmcnt(8)\n"
    "v_mfma_f32_16x16x32_bf16 v[32:35], a[160:163], v[96:99], v[32:35]\n"
    "ds_read_b128 v[72:75], v10 offset:49152\n"
    "v_mfma_f32_16x16x32_bf16 v[36:39], a[168:171], v[96:99], v[36:39]\n"
    "v_mfma_f32_16x16x32_bf16 v[40:43], a[160:163], v[100:103], v[40:43]\n"
    "ds_read_b128 v[76:79], v10 offset:51200\n"
    "v_mfma_f32_16x16x32_bf16 v[44:47], a[168:171], v[100:103], v[44:47]\n"
    "s_waitcnt lgkmcnt(8)\n"
    "v_mfma_f32_16x16x32_bf16 v[32:35], a[164:167], v[104:107], v[32:35]\n"
    "ds_read_b128 v[80:83], v9 offset:53248\n"
    "v_mfma_f32_16x16x32_bf16 v[36:39], a[172:175], v[104:107], v[36:39]\n"
    "v_mfma_f32_16x16x32_bf16 v[40:43], a[164:167], v[108:111], v[40:43]\n"
    "ds_read_b128 v[84:87], v9 offset:55296\n"
    "v_mfma_f32_16x16x32_bf16 v[44:47], a[172:175], v[108:111], v[44:47]\n"
    "s_waitcnt lgkmcnt(8)\n"
    "v_mfma_f32_16x16x32_bf16 v[32:35], a[176:179], v[112:115], v[32:35]\n"
    "ds_read_b128 v[88:91], v10 offset:53248\n"
    "v_mfma_f32_16x16x32_bf16 v[36:39], a[184:187], v[112:115], v[36:39]\n"
    "s_waitcnt vmcnt(9)\n"
    "s_mov_b64 exec, 1\n"
    "buffer_store_dword v12, off, s[44:47], s64 sc1\n"
    "s_mov_b64 exec, -1\n"
    "v_mfma_f32_16x16x32_bf16 v[40:43], a[176:179], v[116:119], v[40:43]\n"
    "ds_read_b128 v[92:95], v10 offset:55296\n"
    "v_mfma_f32_16x16x32_bf16 v[44:47], a[184:187], v[116:119], v[44:47]\n"
    "s_waitcnt lgkmcnt(8)\n"
    "v_mfma_f32_16x16x32_bf16 v[32:35], a[180:183], v[120:123], v[32:35]\n"
    "ds_read_b128 v[96:99], v9 offset:57344\n"
    "v_mfma_f32_16x16x32_bf16 v[36:39], a[188:191], v[120:123], v[36:39]\n"
    "v_mfma_f32_16x16x32_bf16 v[40:43], a[180:183], v[124:127], v[40:43]\n"
    "ds_read_b128 v[100:103], v9 offset:59392\n"
    "v_mfma_f32_16x16x32_bf16 v[44:47], a[188:191], v[124:127], v[44:47]\n"
    "s_waitcnt lgkmcnt(8)\n"
    "v_mfma_f32_16x16x32_bf16 v[32:35], a[192:195], v[64:67], v[32:35]\n"
    "ds_read_b128 v[104:107], v10 offset:57344\n"
    "v_mfma_f32_16x16x32_bf16 v[36:39], a[200:203], v[64:67], v[36:39]\n"
    "v_mfma_f32_16x16x32_bf16 v[40:43], a[192:195], v[68:71], v[40:43]\n"
    "ds_read_b128 v[108:111], v10 offset:59392\n"
    "v_mfma_f32_16x16x32_bf16 v[44:47], a[200:203], v[68:71], v[44:47]\n"
    "s_waitcnt lgkmcnt(8)\n"
    "v_mfma_f32_16x16x32_bf16 v[32:35], a[196:199], v[72:75], v[32:35]\n"
    "ds_read_b128 v[112:115], v9 offset:61440\n"
    "v_mfma_f32_16x16x32_bf16 v[36:39], a[204:207], v[72:75], v[36:39]\n"
    "v_mfma_f32_16x16x32_bf16 v[40:43], a[196:199], v[76:79], v[40:43]\n"
    "ds_read_b128 v[116:119], v9 offset:63488\n"
    "v_mfma_f32_16x16x32_bf16 v[44:47], a[204:207], v[76:79], v[44:47]\n"
    "s_waitcnt lgkmcnt(8)\n"
    "v_mfma_f32_16x16x32_bf16 v[32:35], a[208:211], v[80:83], v[32:35]\n"
    "ds_read_b128 v[120:123], v10 offset:61440\n"
    "v_mfma_f32_16x16x32_bf16 v[36:39], a[216:219], v[80:83], v[36:39]\n"
    "v_mfma_f32_16x16x32_bf16 v[40:43], a[208:211], v[84:87], v[40:43]\n"
    "ds_read_b128 v[124:127], v10 offset:63488\n"
    "v_mfma_f32_16x16x32_bf16 v[44:47], a[216:219], v[84:87], v[44:47]\n"
    "s_waitcnt lgkmcnt(8)\n"
    "v_mfma_f32_16x16x32_bf16 v[32:35], a[212:215], v[88:91], v[32:35]\n"
    "v_mfma_f32_16x16x32_bf16 v[36:39], a[220:223], v[88:91], v[36:39]\n"
    "v_mfma_f32_16x16x32_bf16 v[40:43], a[212:215], v[92:95], v[40:43]\n"
    "v_mfma_f32_16x16x32_bf16 v[44:47], a[220:223], v[92:95], v[44:47]\n"
    "s_waitcnt lgkmcnt(6)\n"
    "v_mfma_f32_16x16x32_bf16 v[32:35], a[224:227], v[96:99], v[32:35]\n"
    "v_mfma_f32_16x16x32_bf16 v[36:39], a[232:235], v[96:99], v[36:39]\n"
    "v_mfma_f32_16x16x32_bf16 v[40:43], a[224:227], v[100:103], v[40:43]\n"
    "v_xor_b32 v9, 0x10000, v9\n"
    "v_xor_b32 v10, 0x10000, v10\n"
    "v_mfma_f32_16x16x32_bf16 v[44:47], a[232:235], v[100:103], v[44:47]\n"
    "s_waitcnt vmcnt(1)\n"
    "s_waitcnt lgkmcnt(0)\n"
    "s_barrier\n"
    "v_mfma_f32_16x16x32_bf16 v[32:35], a[228:231], v[104:107], v[32:35]\n"
    "ds_read_b128 v[64:67], v9 offset:0\n"
    "v_mfma_f32_16x16x32_bf16 v[36:39], a[236:239], v[104:107], v[36:39]\n"
    "buffer_load_dword v14, v13, s[36:39], s61 offen\n"
    "v_mfma_f32_16x16x32_bf16 v[40:43], a[228:231], v[108:111], v[40:43]\n"
    "ds_read_b128 v[68:71], v9 offset:2048\n"
    "v_mfma_f32_16x16x32_bf16 v[44:47], a[236:239], v[108:111], v[44:47]\n"
    "v_mfma_f32_16x16x32_bf16 v[32:35], a[240:243], v[112:115], v[32:35]\n"
    "ds_read_b128 v[72:75], v10 offset:0\n"
    "v_mfma_f32_16x16x32_bf16 v[36:39], a[248:251], v[112:115], v[36:39]\n"
    "v_mfma_f32_16x16x32_bf16 v[40:43], a[240:243], v[116:119], v[40:43]\n"
    "ds_read_b128 v[76:79], v10 offset:2048\n"
    "v_mfma_f32_16x16x32_bf16 v[44:47], a[248:251], v[116:119], v[44:47]\n"
    "v_mfma_f32_16x16x32_bf16 v[32:35], a[244:247], v[120:123], v[32:35]\n"
    "ds_read_b128 v[80:83], v9 offset:4096\n"
    "v_mfma_f32_16x16x32_bf16 v[36:39], a[252:255], v[120:123], v[36:39]\n"
    "s_add_u32 s60, s60, 1\n"
    "v_mfma_f32_16x16x32_bf16 v[40:43], a[244:247], v[124:127], v[40:43]\n"
    "ds_read_b128 v[84:87], v9 offset:6144\n"
    "s_cmp_lt_u32 s60, 64\n"
    "v_mfma_f32_16x16x32_bf16 v[44:47], a[252:255], v[124:127], v[44:47]\n"
    "s_cbranch_scc1 .Lgx_qloop_%=\n"
    "s_mul_i32 s59, s60, 786432\n"
    "s_add_u32 s59, s59, s63\n"
    "s_sub_u32 s67, s59, 786432\n"
    "s_cselect_b32 s67, 0x10000000, s67\n"
    "s_add_u32 s68, s67, 4096\n"
    "s_nop 3\n"
    "v_cvt_pk_bf16_f32 v48, v32, v33\n"
    "v_cvt_pk_bf16_f32 v49, v34, v35\n"
    "v_cvt_pk_bf16_f32 v50, v36, v37\n"
    "v_cvt_pk_bf16_f32 v51, v38, v39\n"
    "buffer_store_dwordx4 v[48:51], v11, s[40:43], s67 offen sc1\n"
    "v_cvt_pk_bf16_f32 v52, v40, v41\n"
    "v_cvt_pk_bf16_f32 v53, v42, v43\n"
    "v_cvt_pk_bf16_f32 v54, v44, v45\n"
    "v_cvt_pk_bf16_f32 v55, v46, v47\n"
    "buffer_store_dwordx4 v[52:55], v11, s[40:43], s68 offen sc1\n"
    "s_branch .Lgx_qkvdone_%=\n"
    ".Lgx_vloop_%=:\n"
    "s_waitcnt lgkmcnt(4)\n"
    "v_mfma_f32_16x16x32_bf16 v[16:19], v[64:67], a[0:3], 0\n"
    "ds_read_b128 v[88:91], v10 offset:4096\n"
    "ds_read_b128 v[92:95], v10 offset:6144\n"
    "v_mfma_f32_16x16x32_bf16 v[20:23], v[64:67], a[8:11], 0\n"
    "s_xor_b32 s62, s62, 65536\n"
    "s_add_u32 s61, s61, 65536\n"
    "s_mul_i32 s59, s60, 786432\n"
    "v_mfma_f32_16x16x32_bf16 v[24:27], v[68:71], a[0:3], 0\n"
    "ds_read_b128 v[96:99], v9 offset:8192\n"
    "ds_read_b128 v[100:103], v9 offset:10240\n"
    "v_mfma_f32_16x16x32_bf16 v[28:31], v[68:71], a[8:11], 0\n"
    "s_waitcnt lgkmcnt(6)\n"
    "s_add_u32 s59, s59, s63\n"
    "v_mov_b32 v12, s60\n"
    "v_mfma_f32_16x16x32_bf16 v[16:19], v[72:75], a[4:7], v[16:19]\n"
    "ds_read_b128 v[104:107], v10 offset:8192\n"
    "s_add_u32 m0, s62, 0\n"
    "s_add_u32 s58, s61, 0\n"
    "v_mfma_f32_16x16x32_bf16 v[20:23], v[72:75], a[12:15], v[20:23]\n"
    "buffer_load_dwordx4 v8, s[36:39], s58 offen lds\n"
    "v_mfma_f32_16x16x32_bf16 v[24:27], v[76:79], a[4:7], v[24:27]\n"
    "ds_read_b128 v[108:111], v10 offset:10240\n"
    "ds_read_b128 v[112:115], v9 offset:12288\n"
    "v_mfma_f32_16x16x32_bf16 v[28:31], v[76:79], a[12:15], v[28:31]\n"
    "s_waitcnt lgkmcnt(7)\n"
    "s_sub_u32 s67, s59, 786432\n"
    "s_cselect_b32 s67, 0x10000000, s67\n"
    "v_mfma_f32_16x16x32_bf16 v[16:19], v[80:83], a[16:19], v[16:19]\n"
    "ds_read_b128 v[116:119], v9 offset:14336\n"
    "s_add_u32 m0, s62, 4096\n"
    "s_add_u32 s58, s61, 128\n"
    "v_mfma_f32_16x16x32_bf16 v[20:23], v[80:83], a[24:27], v[20:23]\n"
    "buffer_load_dwordx4 v8, s[36:39], s58 offen lds\n"
    "v_mfma_f32_16x16x32_bf16 v[24:27], v[84:87], a[16:19], v[24:27]\n"
    "ds_read_b128 v[120:123], v10 offset:12288\n"
    "ds_read_b128 v[124:127], v10 offset:14336\n"
    "v_mfma_f32_16x16x32_bf16 v[28:31], v[84:87], a[24:27], v[28:31]\n"
    "s_waitcnt lgkmcnt(8)\n"
    "v_cvt_pk_bf16_f32 v48, v32, v33\n"
    "v_cvt_pk_bf16_f32 v49, v34, v35\n"
    "v_mfma_f32_16x16x32_bf16 v[16:19], v[88:91], a[20:23], v[16:19]\n"
    "ds_read_b128 v[64:67], v9 offset:16384\n"
    "s_add_u32 m0, s62, 8192\n"
    "s_add_u32 s58, s61, 256\n"
    "v_mfma_f32_16x16x32_bf16 v[20:23], v[88:91], a[28:31], v[20:23]\n"
    "buffer_load_dwordx4 v8, s[36:39], s58 offen lds\n"
    "v_mfma_f32_16x16x32_bf16 v[24:27], v[92:95], a[20:23], v[24:27]\n"
    "ds_read_b128 v[68:71], v9 offset:18432\n"
    "v_mfma_f32_16x16x32_bf16 v[28:31], v[92:95], a[28:31], v[28:31]\n"
    "s_waitcnt lgkmcnt(8)\n"
    "v_cvt_pk_bf16_f32 v50, v40, v41\n"
    "v_cvt_pk_bf16_f32 v51, v42, v43\n"
    "v_mfma_f32_16x16x32_bf16 v[16:19], v[96:99], a[32:35], v[16:19]\n"
    "ds_read_b128 v[72:75], v10 offset:16384\n"
    "s_add_u32 m0, s62, 12288\n"
    "s_add_u32 s58, s61, 384\n"
    "v_mfma_f32_16x16x32_bf16 v[20:23], v[96:99], a[40:43], v[20:23]\n"
    "buffer_load_dwordx4 v8, s[36:39], s58 offen lds\n"
    "v_mfma_f32_16x16x32_bf16 v[24:27], v[100:103], a[32:35], v[24:27]\n"
    "ds_read_b128 v[76:79], v10 offset:18432\n"
    "v_mfma_f32_16x16x32_bf16 v[28:31], v[100:103], a[40:43], v[28:31]\n"
    "s_waitcnt lgkmcnt(8)\n"
    "v_cvt_pk_bf16_f32 v52, v36, v37\n"
    "v_cvt_pk_bf16_f32 v53, v38, v39\n"
    "v_mfma_f32_16x16x32_bf16 v[16:19], v[104:107], a[36:39], v[16:19]\n"
    "ds_read_b128 v[80:83], v9 offset:20480\n"
    "s_add_u32 m0, s62, 16384\n"
    "s_add_u32 s58, s61, 512\n"
    "v_mfma_f32_16x16x32_bf16 v[20:23], v[104:107], a[44:47], v[20:23]\n"
    "buffer_load_dwordx4 v8, s[36:39], s58 offen lds\n"
    "v_mfma_f32_16x16x32_bf16 v[24:27], v[108:111], a[36:39], v[24:27]\n"
    "ds_read_b128 v[84:87], v9 offset:22528\n"
    "s_add_u32 s68, s67, 4096\n"
    "v_mfma_f32_16x16x32_bf16 v[28:31], v[108:111], a[44:47], v[28:31]\n"
    "s_waitcnt lgkmcnt(8)\n"
    "v_cvt_pk_bf16_f32 v54, v44, v45\n"
    "v_cvt_pk_bf16_f32 v55, v46, v47\n"
    "v_mfma_f32_16x16x32_bf16 v[16:19], v[112:115], a[48:51], v[16:19]\n"
    "ds_read_b128 v[88:91], v10 offset:20480\n"
    "s_add_u32 m0, s62, 20480\n"
    "s_add_u32 s58, s61, 640\n"
    "v_mfma_f32_16x16x32_bf16 v[20:23], v[112:115], a[56:59], v[20:23]\n"
    "buffer_load_dwordx4 v8, s[36:39], s58 offen lds\n"
    "v_mfma_f32_16x16x32_bf16 v[24:27], v[116:119], a[48:51], v[24:27]\n"
    "buffer_store_dwordx4 v[48:51], v11, s[40:43], s67 offen offset:0 sc1\n"
    "v_mfma_f32_16x16x32_bf16 v[28:31], v[116:119], a[56:59], v[28:31]\n"
    "s_waitcnt lgkmcnt(7)\n"
    "v_mfma_f32_16x16x32_bf16 v[16:19], v[120:123], a[52:55], v[16:19]\n"
    "ds_read_b128 v[92:95], v10 offset:22528\n"
    "s_add_u32 m0, s62, 24576\n"
    "s_add_u32 s58, s61, 768\n"
    "v_mfma_f32_16x16x32_bf16 v[20:23], v[120:123], a[60:63], v[20:23]\n"
    "buffer_load_dwordx4 v8, s[36:39], s58 offen lds\n"
    "v_mfma_f32_16x16x32_bf16 v[24:27], v[124:127], a[52:55], v[24:27]\n"
    "buffer_store_dwordx4 v[52:55], v11, s[40:43], s67 offen offset:1024 sc1\n"
    "v_mfma_f32_16x16x32_bf16 v[28:31], v[124:127], a[60:63], v[28:31]\n"
    "s_waitcnt lgkmcnt(6)\n"
    "v_mfma_f32_16x16x32_bf16 v[16:19], v[64:67], a[64:67], v[16:19]\n"
    "ds_read_b128 v[96:99], v9 offset:24576\n"
    "s_add_u32 m0, s62, 28672\n"
    "s_add_u32 s58, s61, 896\n"
    "v_mfma_f32_16x16x32_bf16 v[20:23], v[64:67], a[72:75], v[20:23]\n"
    "buffer_load_dwordx4 v8, s[36:39], s58 offen lds\n"
    "v_mfma_f32_16x16x32_bf16 v[24:27], v[68:71], a[64:67], v[24:27]\n"
    "ds_read_b128 v[100:103], v9 offset:26624\n"
    "ds_read_b128 v[104:107], v10 offset:24576\n"
    "v_mfma_f32_16x16x32_bf16 v[28:31], v[68:71], a[72:75], v[28:31]\n"
    "s_waitcnt lgkmcnt(7)\n"
    "v_mfma_f32_16x16x32_bf16 v[16:19], v[72:75], a[68:71], v[16:19]\n"
    "ds_read_b128 v[108:111], v10 offset:26624\n"
    "s_add_u32 m0, s62, 32768\n"
    "s_add_u32 s58, s61, 1024\n"
    "v_mfma_f32_16x16x32_bf16 v[20:23], v[72:75], a[76:79], v[20:23]\n"
    "buffer_load_dwordx4 v8, s[36:39], s58 offen lds\n"
    "v_mfma_f32_16x16x32_bf16 v[24:27], v[76:79], a[68:71], v[24:27]\n"
    "ds_read_b128 v[112:115], v9 offset:28672\n"
    "ds_read_b128 v[116:119], v9 offset:30720\n"
    "v_mfma_f32_16x16x32_bf16 v[28:31], v[76:79], a[76:79], v[28:31]\n"
    "s_waitcnt lgkmcnt(8)\n"
    "v_mfma_f32_16x16x32_bf16 v[16:19], v[80:83], a[80:83], v[16:19]\n"
    "ds_read_b128 v[120:123], v10 offset:28672\n"
    "s_add_u32 m0, s62, 36864\n"
    "s_add_u32 s58, s61, 1152\n"
    "v_mfma_f32_16x16x32_bf16 v[20:23], v[80:83], a[88:91], v[20:23]\n"
    "buffer_load_dwordx4 v8, s[36:39], s58 offen lds\n"
    "v_mfma_f32_16x16x32_bf16 v[24:27], v[84:87], a[80:83], v[24:27]\n"
    "ds_read_b128 v[124:127], v10 offset:30720\n"
    "v_mfma_f32_16x16x32_bf16 v[28:31], v[84:87], a[88:91], v[28:31]\n"
    "s_waitcnt lgkmcnt(8)\n"
    "v_mfma_f32_16x16x32_bf16 v[16:19], v[88:91], a[84:87], v[16:19]\n"
    "ds_read_b128 v[64:67], v9 offset:32768\n"
    "s_add_u32 m0, s62, 40960\n"
    "s_add_u32 s58, s61, 1280\n"
    "v_mfma_f32_16x16x32_bf16 v[20:23], v[88:91], a[92:95], v[20:23]\n"
    "buffer_load_dwordx4 v8, s[36:39], s58 offen lds\n"
    "v_mfma_f32_16x16x32_bf16 v[24:27], v[92:95], a[84:87], v[24:27]\n"
    "ds_read_b128 v[68:71], v9 offset:34816\n"
    "v_mfma_f32_16x16x32_bf16 v[28:31], v[92:95], a[92:95], v[28:31]\n"
    "s_waitcnt lgkmcnt(8)\n"
    "v_mfma_f32_16x16x32_bf16 v[16:19], v[96:99], a[96:99], v[16:19]\n"
    "ds_read_b128 v[72:75], v10 offset:32768\n"
    "s_add_u32 m0, s62, 45056\n"
    "s_add_u32 s58, s61, 1408\n"
    "v_mfma_f32_16x16x32_bf16 v[20:23], v[96:99], a[104:107], v[20:23]\n"
    "buffer_load_dwordx4 v8, s[36:39], s58 offen lds\n"
    "v_mfma_f32_16x16x32_bf16 v[24:27], v[100:103], a[96:99], v[24:27]\n"
    "ds_read_b128 v[76:79], v10 offset:34816\n"
    "v_mfma_f32_16x16x32_bf16 v[28:31], v[100:103], a[104:107], v[28:31]\n"
    "s_waitcnt lgkmcnt(8)\n"
    "v_mfma_f32_16x16x32_bf16 v[16:19], v[104:107], a[100:103], v[16:19]\n"
    "ds_read_b128 v[80:83], v9 offset:36864\n"
    "s_add_u32 m0, s62, 49152\n"
    "s_add_u32 s58, s61, 1536\n"
    "v_mfma_f32_16x16x32_bf16 v[20:23], v[104:107], a[108:111], v[20:23]\n"
    "buffer_load_dwordx4 v8, s[36:39], s58 offen lds\n"
    "v_mfma_f32_16x16x32_bf16 v[24:27], v[108:111], a[100:103], v[24:27]\n"
    "ds_read_b128 v[84:87], v9 offset:38912\n"
    "v_mfma_f32_16x16x32_bf16 v[28:31], v[108:111], a[108:111], v[28:31]\n"
    "s_waitcnt lgkmcnt(8)\n"
    "v_mfma_f32_16x16x32_bf16 v[16:19], v[112:115], a[112:115], v[16:19]\n"
    "ds_read_b128 v[88:91], v10 offset:36864\n"
    "s_add_u32 m0, s62, 53248\n"
    "s_add_u32 s58, s61, 1664\n"
    "v_mfma_f32_16x16x32_bf16 v[20:23], v[112:115], a[120:123], v[20:23]\n"
    "buffer_load_dwordx4 v8, s[36:39], s58 offen lds\n"
    "v_mfma_f32_16x16x32_bf16 v[24:27], v[116:119], a[112:115], v[24:27]\n"
    "ds_read_b128 v[92:95], v10 offset:38912\n"
    "v_mfma_f32_16x16x32_bf16 v[28:31], v[116:119], a[120:123], v[28:31]\n"
    "s_waitcnt lgkmcnt(8)\n"
    "v_mfma_f32_16x16x32_bf16 v[16:19], v[120:123], a[116:119], v[16:19]\n"
    "ds_read_b128 v[96:99], v9 offset:40960\n"
    "s_add_u32 m0, s62, 57344\n"
    "s_add_u32 s58, s61, 1792\n"
    "v_mfma_f32_16x16x32_bf16 v[20:23], v[120:123], a[124:127], v[20:23]\n"
    "buffer_load_dwordx4 v8, s[36:39], s58 offen lds\n"
    "v_mfma_f32_16x16x32_bf16 v[24:27], v[124:127], a[116:119], v[24:27]\n"
    "ds_read_b128 v[100:103], v9 offset:43008\n"
    "v_mfma_f32_16x16x32_bf16 v[28:31], v[124:127], a[124:127], v[28:31]\n"
    "s_waitcnt lgkmcnt(8)\n"
    "v_mfma_f32_16x16x32_bf16 v[16:19], v[64:67], a[128:131], v[16:19]\n"
    "ds_read_b128 v[104:107], v10 offset:40960\n"
    "s_add_u32 m0, s62, 61440\n"
    "s_add_u32 s58, s61, 1920\n"
    "v_mfma_f32_16x16x32_bf16 v[20:23], v[64:67], a[136:139], v[20:23]\n"
    "buffer_load_dwordx4 v8, s[36:39], s58 offen lds\n"
    "v_mfma_f32_16x16x32_bf16 v[24:27], v[68:71], a[128:131], v[24:27]\n"
    "ds_read_b128 v[108:111], v10 offset:43008\n"
    "v_mfma_f32_16x16x32_bf16 v[28:31], v[68:71], a[136:139], v[28:31]\n"
    "s_waitcnt lgkmcnt(8)\n"
    "v_mfma_f32_16x16x32_bf16 v[16:19], v[72:75], a[132:135], v[16:19]\n"
    "ds_read_b128 v[112:115], v9 offset:45056\n"
    "v_mfma_f32_16x16x32_bf16 v[20:23], v[72:75], a[140:143], v[20:23]\n"
    "v_mfma_f32_16x16x32_bf16 v[24:27], v[76:79], a[132:135], v[24:27]\n"
    "ds_read_b128 v[116:119], v9 offset:47104\n"
    "v_mfma_f32_16x16x32_bf16 v[28:31], v[76:79], a[140:143], v[28:31]\n"
    "s_waitcnt lgkmcnt(8)\n"
    "v_mfma_f32_16x16x32_bf16 v[16:19], v[80:83], a[144:147], v[16:19]\n"
    "ds_read_b128 v[120:123], v10 offset:45056\n"
    "v_mfma_f32_16x16x32_bf16 v[20:23], v[80:83], a[152:155], v[20:23]\n"
    "v_mfma_f32_16x16x32_bf16 v[24:27], v[84:87], a[144:147], v[24:27]\n"
    "ds_read_b128 v[124:127], v10 offset:47104\n"
    "v_mfma_f32_16x16x32_bf16 v[28:31], v[84:87], a[152:155], v[28:31]\n"
    "s_waitcnt lgkmcnt(8)\n"
    "v_mfma_f32_16x16x32_bf16 v[16:19], v[88:91], a[148:151], v[16:19]\n"
    "ds_read_b128 v[64:67], v9 offset:49152\n"
    "v_mfma_f32_16x16x32_bf16 v[20:23], v[88:91], a[156:159], v[20:23]\n"
    "v_mfma_f32_16x16x32_bf16 v[24:27], v[92:95], a[148:151], v[24:27]\n"
    "ds_read_b128 v[68:71], v9 offset:51200\n"
    "v_mfma_f32_16x16x32_bf16 v[28:31], v[92:95], a[156:159], v[28:31]\n"
    "s_waitcnt lgkmcnt(8)\n"
    "v_mfma_f32_16x16x32_bf16 v[16:19], v[96:99], a[160:163], v[16:19]\n"
    "ds_read_b128 v[72:75], v10 offset:49152\n"
    "v_mfma_f32_16x16x32_bf16 v[20:23], v[96:99], a[168:171], v[20:23]\n"
    "v_mfma_f32_16x16x32_bf16 v[24:27], v[100:103], a[160:163], v[24:27]\n"
    "ds_read_b128 v[76:79], v10 offset:51200\n"
    "v_mfma_f32_16x16x32_bf16 v[28:31], v[100:103], a[168:171], v[28:31]\n"
    "s_waitcnt lgkmcnt(8)\n"
    "v_mfma_f32_16x16x32_bf16 v[16:19], v[104:107], a[164:167], v[16:19]\n"
    "ds_read_b128 v[80:83], v9 offset:53248\n"
    "v_mfma_f32_16x16x32_bf16 v[20:23], v[104:107], a[172:175], v[20:23]\n"
    "v_mfma_f32_16x16x32_bf16 v[24:27], v[108:111], a[164:167], v[24:27]\n"
    "ds_read_b128 v[84:87], v9 offset:55296\n"
    "v_mfma_f32_16x16x32_bf16 v[28:31], v[108:111], a[172:175], v[28:31]\n"
    "s_waitcnt lgkmcnt(8)\n"
    "v_mfma_f32_16x16x32_bf16 v[16:19], v[112:115], a[176:179], v[16:19]\n"
    "ds_read_b128 v[88:91], v10 offset:53248\n"
    "v_mfma_f32_16x16x32_bf16 v[20:23], v[112:115], a[184:187], v[20:23]\n"
    "s_waitcnt vmcnt(9)\n"
    "s_mov_b64 exec, 1\n"
    "buffer_store_dword v12, off, s[44:47], s64 sc1\n"
    "s_mov_b64 exec, -1\n"
    "v_mfma_f32_16x16x32_bf16 v[24:27], v[116:119], a[176:179], v[24:27]\n"
    "ds_read_b128 v[92:95], v10 offset:55296\n"
    "v_mfma_f32_16x16x32_bf16 v[28:31], v[116:119], a[184:187], v[28:31]\n"
    "s_waitcnt lgkmcnt(8)\n"
    "v_mfma_f32_16x16x32_bf16 v[16:19], v[120:123], a[180:183], v[16:19]\n"
    "ds_read_b128 v[96:99], v9 offset:57344\n"
    "v_mfma_f32_16x16x32_bf16 v[20:23], v[120:123], a[188:191], v[20:23]\n"
    "v_mfma_f32_16x16x32_bf16 v[24:27], v[124:127], a[180:183], v[24:27]\n"
    "ds_read_b128 v[100:103], v9 offset:59392\n"
    "v_mfma_f32_16x16x32_bf16 v[28:31], v[124:127], a[188:191], v[28:31]\n"
    "s_waitcnt lgkmcnt(8)\n"
    "v_mfma_f32_16x16x32_bf16 v[16:19], v[64:67], a[192:195], v[16:19]\n"
    "ds_read_b128 v[104:107], v10 offset:57344\n"
    "v_mfma_f32_16x16x32_bf16 v[20:23], v[64:67], a[200:203], v[20:23]\n"
    "v_mfma_f32_16x16x32_bf16 v[24:27], v[68:71], a[192:195], v[24:27]\n"
    "ds_read_b128 v[108:111], v10 offset:59392\n"
    "v_mfma_f32_16x16x32_bf16 v[28:31], v[68:71], a[200:203], v[28:31]\n"
    "s_waitcnt lgkmcnt(8)\n"
    "v_mfma_f32_16x16x32_bf16 v[16:19], v[72:75], a[196:199], v[16:19]\n"
    "ds_read_b128 v[112:115], v9 offset:61440\n"
    "v_mfma_f32_16x16x32_bf16 v[20:23], v[72:75], a[204:207], v[20:23]\n"
    "v_mfma_f32_16x16x32_bf16 v[24:27], v[76:79], a[196:199], v[24:27]\n"
    "ds_read_b128 v[116:119], v9 offset:63488\n"
    "v_mfma_f32_16x16x32_bf16 v[28:31], v[76:79], a[204:207], v[28:31]\n"
    "s_waitcnt lgkmcnt(8)\n"
    "v_mfma_f32_16x16x32_bf16 v[16:19], v[80:83], a[208:211], v[16:19]\n"
    "ds_read_b128 v[120:123], v10 offset:61440\n"
    "v_mfma_f32_16x16x32_bf16 v[20:23], v[80:83], a[216:219], v[20:23]\n"
    "v_mfma_f32_16x16x32_bf16 v[24:27], v[84:87], a[208:211], v[24:27]\n"
    "ds_read_b128 v[124:127], v10 offset:63488\n"
    "v_mfma_f32_16x16x32_bf16 v[28:31], v[84:87], a[216:219], v[28:31]\n"
    "s_waitcnt lgkmcnt(8)\n"
    "v_mfma_f32_16x16x32_bf16 v[16:19], v[88:91], a[212:215], v[16:19]\n"
    "v_mfma_f32_16x16x32_bf16 v[20:23], v[88:91], a[220:223], v[20:23]\n"
    "v_mfma_f32_16x16x32_bf16 v[24:27], v[92:95], a[212:215], v[24:27]\n"
    "v_mfma_f32_16x16x32_bf16 v[28:31], v[92:95], a[220:223], v[28:31]\n"
    "s_waitcnt lgkmcnt(6)\n"
    "v_mfma_f32_16x16x32_bf16 v[16:19], v[96:99], a[224:227], v[16:19]\n"
    "v_mfma_f32_16x16x32_bf16 v[20:23], v[96:99], a[232:235], v[20:23]\n"
    "v_mfma_f32_16x16x32_bf16 v[24:27], v[100:103], a[224:227], v[24:27]\n"
    "v_xor_b32 v9, 0x10000, v9\n"
    "v_xor_b32 v10, 0x10000, v10\n"
    "v_mfma_f32_16x16x32_bf16 v[28:31], v[100:103], a[232:235], v[28:31]\n"
    "s_waitcnt vmcnt(1)\n"
    "s_waitcnt lgkmcnt(0)\n"
    "s_barrier\n"
    "v_mfma_f32_16x16x32_bf16 v[16:19], v[104:107], a[228:231], v[16:19]\n"
    "ds_read_b128 v[64:67], v9 offset:0\n"
    "v_mfma_f32_16x16x32_bf16 v[20:23], v[104:107], a[236:239], v[20:23]\n"
    "buffer_load_dword v14, v13, s[36:39], s61 offen\n"
    "v_mfma_f32_16x16x32_bf16 v[24:27], v[108:111], a[228:231], v[24:27]\n"
    "ds_read_b128 v[68:71], v9 offset:2048\n"
    "v_mfma_f32_16x16x32_bf16 v[28:31], v[108:111], a[236:239], v[28:31]\n"
    "v_mfma_f32_16x16x32_bf16 v[16:19], v[112:115], a[240:243], v[16:19]\n"
    "ds_read_b128 v[72:75], v10 offset:0\n"
    "v_mfma_f32_16x16x32_bf16 v[20:23], v[112:115], a[248:251], v[20:23]\n"
    "v_mfma_f32_16x16x32_bf16 v[24:27], v[116:119], a[240:243], v[24:27]\n"
    "ds_read_b128 v[76:79], v10 offset:2048\n"
    "v_mfma_f32_16x16x32_bf16 v[28:31], v[116:119], a[248:251], v[28:31]\n"
    "v_mfma_f32_16x16x32_bf16 v[16:19], v[120:123], a[244:247], v[16:19]\n"
    "ds_read_b128 v[80:83], v9 offset:4096\n"
    "v_mfma_f32_16x16x32_bf16 v[20:23], v[120:123], a[252:255], v[20:23]\n"
    "s_add_u32 s60, s60, 1\n"
    "v_mfma_f32_16x16x32_bf16 v[24:27], v[124:127], a[244:247], v[24:27]\n"
    "ds_read_b128 v[84:87], v9 offset:6144\n"
    "v_mfma_f32_16x16x32_bf16 v[28:31], v[124:127], a[252:255], v[28:31]\n"
    "s_waitcnt lgkmcnt(4)\n"
    "v_mfma_f32_16x16x32_bf16 v[32:35], v[64:67], a[0:3], 0\n"
    "ds_read_b128 v[88:91], v10 offset:4096\n"
    "ds_read_b128 v[92:95], v10 offset:6144\n"
    "v_mfma_f32_16x16x32_bf16 v[36:39], v[64:67], a[8:11], 0\n"
    "s_xor_b32 s62, s62, 65536\n"
    "s_add_u32 s61, s61, 65536\n"
    "s_mul_i32 s59, s60, 786432\n"
    "v_mfma_f32_16x16x32_bf16 v[40:43], v[68:71], a[0:3], 0\n"
    "ds_read_b128 v[96:99], v9 offset:8192\n"
    "ds_read_b128 v[100:103], v9 offset:10240\n"
    "v_mfma_f32_16x16x32_bf16 v[44:47], v[68:71], a[8:11], 0\n"
    "s_waitcnt lgkmcnt(6)\n"
    "s_add_u32 s59, s59, s63\n"
    "v_mov_b32 v12, s60\n"
    "v_mfma_f32_16x16x32_bf16 v[32:35], v[72:75], a[4:7], v[32:35]\n"
    "ds_read_b128 v[104:107], v10 offset:8192\n"
    "s_add_u32 m0, s62, 0\n"
    "s_add_u32 s58, s61, 0\n"
    "v_mfma_f32_16x16x32_bf16 v[36:39], v[72:75], a[12:15], v[36:39]\n"
    "buffer_load_dwordx4 v8, s[36:39], s58 offen lds\n"
    "v_mfma_f32_16x16x32_bf16 v[40:43], v[76:79], a[4:7], v[40:43]\n"
    "ds_read_b128 v[108:111], v10 offset:10240\n"
    "ds_read_b128 v[112:115], v9 offset:12288\n"
    "v_mfma_f32_16x16x32_bf16 v[44:47], v[76:79], a[12:15], v[44:47]\n"
    "s_waitcnt lgkmcnt(7)\n"
    "s_sub_u32 s67, s59, 786432\n"
    "s_cselect_b32 s67, 0x10000000, s67\n"
    "v_mfma_f32_16x16x32_bf16 v[32:35], v[80:83], a[16:19], v[32:35]\n"
    "ds_read_b128 v[116:119], v9 offset:14336\n"
    "s_add_u32 m0, s62, 4096\n"
    "s_add_u32 s58, s61, 128\n"
    "v_mfma_f32_16x16x32_bf16 v[36:39], v[80:83], a[24:27], v[36:39]\n"
    "buffer_load_dwordx4 v8, s[36:39], s58 offen lds\n"
    "v_mfma_f32_16x16x32_bf16 v[40:43], v[84:87], a[16:19], v[40:43]\n"
    "ds_read_b128 v[120:123], v10 offset:12288\n"
    "ds_read_b128 v[124:127], v10 offset:14336\n"
    "v_mfma_f32_16x16x32_bf16 v[44:47], v[84:87], a[24:27], v[44:47]\n"
    "s_waitcnt lgkmcnt(8)\n"
    "v_cvt_pk_bf16_f32 v48, v16, v17\n"
    "v_cvt_pk_bf16_f32 v49, v18, v19\n"
    "v_mfma_f32_16x16x32_bf16 v[32:35], v[88:91], a[20:23], v[32:35]\n"
    "ds_read_b128 v[64:67], v9 offset:16384\n"
    "s_add_u32 m0, s62, 8192\n"
    "s_add_u32 s58, s61, 256\n"
    "v_mfma_f32_16x16x32_bf16 v[36:39], v[88:91], a[28:31], v[36:39]\n"
    "buffer_load_dwordx4 v8, s[36:39], s58 offen lds\n"
    "v_mfma_f32_16x16x32_bf16 v[40:43], v[92:95], a[20:23], v[40:43]\n"
    "ds_read_b128 v[68:71], v9 offset:18432\n"
    "v_mfma_f32_16x16x32_bf16 v[44:47], v[92:95], a[28:31], v[44:47]\n"
    "s_waitcnt lgkmcnt(8)\n"
    "v_cvt_pk_bf16_f32 v50, v24, v25\n"
    "v_cvt_pk_bf16_f32 v51, v26, v27\n"
    "v_mfma_f32_16x16x32_bf16 v[32:35], v[96:99], a[32:35], v[32:35]\n"
    "ds_read_b128 v[72:75], v10 offset:16384\n"
    "s_add_u32 m0, s62, 12288\n"
    "s_add_u32 s58, s61, 384\n"
    "v_mfma_f32_16x16x32_bf16 v[36:39], v[96:99], a[40:43], v[36:39]\n"
    "buffer_load_dwordx4 v8, s[36:39], s58 offen lds\n"
    "v_mfma_f32_16x16x32_bf16 v[40:43], v[100:103], a[32:35], v[40:43]\n"
    "ds_read_b128 v[76:79], v10 offset:18432\n"
    "v_mfma_f32_16x16x32_bf16 v[44:47], v[100:103], a[40:43], v[44:47]\n"
    "s_waitcnt lgkmcnt(8)\n"
    "v_cvt_pk_bf16_f32 v52, v20, v21\n"
    "v_cvt_pk_bf16_f32 v53, v22, v23\n"
    "v_mfma_f32_16x16x32_bf16 v[32:35], v[104:107], a[36:39], v[32:35]\n"
    "ds_read_b128 v[80:83], v9 offset:20480\n"
    "s_add_u32 m0, s62, 16384\n"
    "s_add_u32 s58, s61, 512\n"
    "v_mfma_f32_16x16x32_bf16 v[36:39], v[104:107], a[44:47], v[36:39]\n"
    "buffer_load_dwordx4 v8, s[36:39], s58 offen lds\n"
    "v_mfma_f32_16x16x32_bf16 v[40:43], v[108:111], a[36:39], v[40:43]\n"
    "ds_read_b128 v[84:87], v9 offset:22528\n"
    "s_add_u32 s68, s67, 4096\n"
    "v_mfma_f32_16x16x32_bf16 v[44:47], v[108:111], a[44:47], v[44:47]\n"
    "s_waitcnt lgkmcnt(8)\n"
    "v_cvt_pk_bf16_f32 v54, v28, v29\n"
    "v_cvt_pk_bf16_f32 v55, v30, v31\n"
    "v_mfma_f32_16x16x32_bf16 v[32:35], v[112:115], a[48:51], v[32:35]\n"
    "ds_read_b128 v[88:91], v10 offset:20480\n"
    "s_add_u32 m0, s62, 20480\n"
    "s_add_u32 s58, s61, 640\n"
    "v_mfma_f32_16x16x32_bf16 v[36:39], v[112:115], a[56:59], v[36:39]\n"
    "buffer_load_dwordx4 v8, s[36:39], s58 offen lds\n"
    "v_mfma_f32_16x16x32_bf16 v[40:43], v[116:119], a[48:51], v[40:43]\n"
    "buffer_store_dwordx4 v[48:51], v11, s[40:43], s67 offen offset:0 sc1\n"
    "v_mfma_f32_16x16x32_bf16 v[44:47], v[116:119], a[56:59], v[44:47]\n"
    "s_waitcnt lgkmcnt(7)\n"
    "v_mfma_f32_16x16x32_bf16 v[32:35], v[120:123], a[52:55], v[32:35]\n"
    "ds_read_b128 v[92:95], v10 offset:22528\n"
    "s_add_u32 m0, s62, 24576\n"
    "s_add_u32 s58, s61, 768\n"
    "v_mfma_f32_16x16x32_bf16 v[36:39], v[120:123], a[60:63], v[36:39]\n"
    "buffer_load_dwordx4 v8, s[36:39], s58 offen lds\n"
    "v_mfma_f32_16x16x32_bf16 v[40:43], v[124:127], a[52:55], v[40:43]\n"
    "buffer_store_dwordx4 v[52:55], v11, s[40:43], s67 offen offset:1024 sc1\n"
    "v_mfma_f32_16x16x32_bf16 v[44:47], v[124:127], a[60:63], v[44:47]\n"
    "s_waitcnt lgkmcnt(6)\n"
    "v_mfma_f32_16x16x32_bf16 v[32:35], v[64:67], a[64:67], v[32:35]\n"
    "ds_read_b128 v[96:99], v9 offset:24576\n"
    "s_add_u32 m0, s62, 28672\n"
    "s_add_u32 s58, s61, 896\n"
    "v_mfma_f32_16x16x32_bf16 v[36:39], v[64:67], a[72:75], v[36:39]\n"
    "buffer_load_dwordx4 v8, s[36:39], s58 offen lds\n"
    "v_mfma_f32_16x16x32_bf16 v[40:43], v[68:71], a[64:67], v[40:43]\n"
    "ds_read_b128 v[100:103], v9 offset:26624\n"
    "ds_read_b128 v[104:107], v10 offset:24576\n"
    "v_mfma_f32_16x16x32_bf16 v[44:47], v[68:71], a[72:75], v[44:47]\n"
    "s_waitcnt lgkmcnt(7)\n"
    "v_mfma_f32_16x16x32_bf16 v[32:35], v[72:75], a[68:71], v[32:35]\n"
    "ds_read_b128 v[108:111], v10 offset:26624\n"
    "s_add_u32 m0, s62, 32768\n"
    "s_add_u32 s58, s61, 1024\n"
    "v_mfma_f32_16x16x32_bf16 v[36:39], v[72:75], a[76:79], v[36:39]\n"
    "buffer_load_dwordx4 v8, s[36:39], s58 offen lds\n"
    "v_mfma_f32_16x16x32_bf16 v[40:43], v[76:79], a[68:71], v[40:43]\n"
    "ds_read_b128 v[112:115], v9 offset:28672\n"
    "ds_read_b128 v[116:119], v9 offset:30720\n"
    "v_mfma_f32_16x16x32_bf16 v[44:47], v[76:79], a[76:79], v[44:47]\n"
    "s_waitcnt lgkmcnt(8)\n"
    "v_mfma_f32_16x16x32_bf16 v[32:35], v[80:83], a[80:83], v[32:35]\n"
    "ds_read_b128 v[120:123], v10 offset:28672\n"
    "s_add_u32 m0, s62, 36864\n"
    "s_add_u32 s58, s61, 1152\n"
    "v_mfma_f32_16x16x32_bf16 v[36:39], v[80:83], a[88:91], v[36:39]\n"
    "buffer_load_dwordx4 v8, s[36:39], s58 offen lds\n"
    "v_mfma_f32_16x16x32_bf16 v[40:43], v[84:87], a[80:83], v[40:43]\n"
    "ds_read_b128 v[124:127], v10 offset:30720\n"
    "v_mfma_f32_16x16x32_bf16 v[44:47], v[84:87], a[88:91], v[44:47]\n"
    "s_waitcnt lgkmcnt(8)\n"
    "v_mfma_f32_16x16x32_bf16 v[32:35], v[88:91], a[84:87], v[32:35]\n"
    "ds_read_b128 v[64:67], v9 offset:32768\n"
    "s_add_u32 m0, s62, 40960\n"
    "s_add_u32 s58, s61, 1280\n"
    "v_mfma_f32_16x16x32_bf16 v[36:39], v[88:91], a[92:95], v[36:39]\n"
    "buffer_load_dwordx4 v8, s[36:39], s58 offen lds\n"
    "v_mfma_f32_16x16x32_bf16 v[40:43], v[92:95], a[84:87], v[40:43]\n"
    "ds_read_b128 v[68:71], v9 offset:34816\n"
    "v_mfma_f32_16x16x32_bf16 v[44:47], v[92:95], a[92:95], v[44:47]\n"
    "s_waitcnt lgkmcnt(8)\n"
    "v_mfma_f32_16x16x32_bf16 v[32:35], v[96:99], a[96:99], v[32:35]\n"
    "ds_read_b128 v[72:75], v10 offset:32768\n"
    "s_add_u32 m0, s62, 45056\n"
    "s_add_u32 s58, s61, 1408\n"
    "v_mfma_f32_16x16x32_bf16 v[36:39], v[96:99], a[104:107], v[36:39]\n"
    "buffer_load_dwordx4 v8, s[36:39], s58 offen lds\n"
    "v_mfma_f32_16x16x32_bf16 v[40:43], v[100:103], a[96:99], v[40:43]\n"
    "ds_read_b128 v[76:79], v10 offset:34816\n"
    "v_mfma_f32_16x16x32_bf16 v[44:47], v[100:103], a[104:107], v[44:47]\n"
    "s_waitcnt lgkmcnt(8)\n"
    "v_mfma_f32_16x16x32_bf16 v[32:35], v[104:107], a[100:103], v[32:35]\n"
    "ds_read_b128 v[80:83], v9 offset:36864\n"
    "s_add_u32 m0, s62, 49152\n"
    "s_add_u32 s58, s61, 1536\n"
    "v_mfma_f32_16x16x32_bf16 v[36:39], v[104:107], a[108:111], v[36:39]\n"
    "buffer_load_dwordx4 v8, s[36:39], s58 offen lds\n"
    "v_mfma_f32_16x16x32_bf16 v[40:43], v[108:111], a[100:103], v[40:43]\n"
    "ds_read_b128 v[84:87], v9 offset:38912\n"
    "v_mfma_f32_16x16x32_bf16 v[44:47], v[108:111], a[108:111], v[44:47]\n"
    "s_waitcnt lgkmcnt(8)\n"
    "v_mfma_f32_16x16x32_bf16 v[32:35], v[112:115], a[112:115], v[32:35]\n"
    "ds_read_b128 v[88:91], v10 offset:36864\n"
    "s_add_u32 m0, s62, 53248\n"
    "s_add_u32 s58, s61, 1664\n"
    "v_mfma_f32_16x16x32_bf16 v[36:39], v[112:115], a[120:123], v[36:39]\n"
    "buffer_load_dwordx4 v8, s[36:39], s58 offen lds\n"
    "v_mfma_f32_16x16x32_bf16 v[40:43], v[116:119], a[112:115], v[40:43]\n"
    "ds_read_b128 v[92:95], v10 offset:38912\n"
    "v_mfma_f32_16x16x32_bf16 v[44:47], v[116:119], a[120:123], v[44:47]\n"
    "s_waitcnt lgkmcnt(8)\n"
    "v_mfma_f32_16x16x32_bf16 v[32:35], v[120:123], a[116:119], v[32:35]\n"
    "ds_read_b128 v[96:99], v9 offset:40960\n"
    "s_add_u32 m0, s62, 57344\n"
    "s_add_u32 s58, s61, 1792\n"
    "v_mfma_f32_16x16x32_bf16 v[36:39], v[120:123], a[124:127], v[36:39]\n"
    "buffer_load_dwordx4 v8, s[36:39], s58 offen lds\n"
    "v_mfma_f32_16x16x32_bf16 v[40:43], v[124:127], a[116:119], v[40:43]\n"
    "ds_read_b128 v[100:103], v9 offset:43008\n"
    "v_mfma_f32_16x16x32_bf16 v[44:47], v[124:127], a[124:127], v[44:47]\n"
    "s_waitcnt lgkmcnt(8)\n"
    "v_mfma_f32_16x16x32_bf16 v[32:35], v[64:67], a[128:131], v[32:35]\n"
    "ds_read_b128 v[104:107], v10 offset:40960\n"
    "s_add_u32 m0, s62, 61440\n"
    "s_add_u32 s58, s61, 1920\n"
    "v_mfma_f32_16x16x32_bf16 v[36:39], v[64:67], a[136:139], v[36:39]\n"
    "buffer_load_dwordx4 v8, s[36:39], s58 offen lds\n"
    "v_mfma_f32_16x16x32_bf16 v[40:43], v[68:71], a[128:131], v[40:43]\n"
    "ds_read_b128 v[108:111], v10 offset:43008\n"
    "v_mfma_f32_16x16x32_bf16 v[44:47], v[68:71], a[136:139], v[44:47]\n"
    "s_waitcnt lgkmcnt(8)\n"
    "v_mfma_f32_16x16x32_bf16 v[32:35], v[72:75], a[132:135], v[32:35]\n"
    "ds_read_b128 v[112:115], v9 offset:45056\n"
    "v_mfma_f32_16x16x32_bf16 v[36:39], v[72:75], a[140:143], v[36:39]\n"
    "v_mfma_f32_16x16x32_bf16 v[40:43], v[76:79], a[132:135], v[40:43]\n"
    "ds_read_b128 v[116:119], v9 offset:47104\n"
    "v_mfma_f32_16x16x32_bf16 v[44:47], v[76:79], a[140:143], v[44:47]\n"
    "s_waitcnt lgkmcnt(8)\n"
    "v_mfma_f32_16x16x32_bf16 v[32:35], v[80:83], a[144:147], v[32:35]\n"
    "ds_read_b128 v[120:123], v10 offset:45056\n"
    "v_mfma_f32_16x16x32_bf16 v[36:39], v[80:83], a[152:155], v[36:39]\n"
    "v_mfma_f32_16x16x32_bf16 v[40:43], v[84:87], a[144:147], v[40:43]\n"
    "ds_read_b128 v[124:127], v10 offset:47104\n"
    "v_mfma_f32_16x16x32_bf16 v[44:47], v[84:87], a[152:155], v[44:47]\n"
    "s_waitcnt lgkmcnt(8)\n"
    "v_mfma_f32_16x16x32_bf16 v[32:35], v[88:91], a[148:151], v[32:35]\n"
    "ds_read_b128 v[64:67], v9 offset:49152\n"
    "v_mfma_f32_16x16x32_bf16 v[36:39], v[88:91], a[156:159], v[36:39]\n"
    "v_mfma_f32_16x16x32_bf16 v[40:43], v[92:95], a[148:151], v[40:43]\n"
    "ds_read_b128 v[68:71], v9 offset:51200\n"
    "v_mfma_f32_16x16x32_bf16 v[44:47], v[92:95], a[156:159], v[44:47]\n"
    "s_waitcnt lgkmcnt(8)\n"
    "v_mfma_f32_16x16x32_bf16 v[32:35], v[96:99], a[160:163], v[32:35]\n"
    "ds_read_b128 v[72:75], v10 offset:49152\n"
    "v_mfma_f32_16x16x32_bf16 v[36:39], v[96:99], a[168:171], v[36:39]\n"
    "v_mfma_f32_16x16x32_bf16 v[40:43], v[100:103], a[160:163], v[40:43]\n"
    "ds_read_b128 v[76:79], v10 offset:51200\n"
    "v_mfma_f32_16x16x32_bf16 v[44:47], v[100:103], a[168:171], v[44:47]\n"
    "s_waitcnt lgkmcnt(8)\n"
    "v_mfma_f32_16x16x32_bf16 v[32:35], v[104:107], a[164:167], v[32:35]\n"
    "ds_read_b128 v[80:83], v9 offset:53248\n"
    "v_mfma_f32_16x16x32_bf16 v[36:39], v[104:107], a[172:175], v[36:39]\n"
    "v_mfma_f32_16x16x32_bf16 v[40:43], v[108:111], a[164:167], v[40:43]\n"
    "ds_read_b128 v[84:87], v9 offset:55296\n"
    "v_mfma_f32_16x16x32_bf16 v[44:47], v[108:111], a[172:175], v[44:47]\n"
    "s_waitcnt lgkmcnt(8)\n"
    "v_mfma_f32_16x16x32_bf16 v[32:35], v[112:115], a[176:179], v[32:35]\n"
    "ds_read_b128 v[88:91], v10 offset:53248\n"
    "v_mfma_f32_16x16x32_bf16 v[36:39], v[112:115], a[184:187], v[36:39]\n"
    "s_waitcnt vmcnt(9)\n"
    "s_mov_b64 exec, 1\n"
    "buffer_store_dword v12, off, s[44:47], s64 sc1\n"
    "s_mov_b64 exec, -1\n"
    "v_mfma_f32_16x16x32_bf16 v[40:43], v[116:119], a[176:179], v[40:43]\n"
    "ds_read_b128 v[92:95], v10 offset:55296\n"
    "v_mfma_f32_16x16x32_bf16 v[44:47], v[116:119], a[184:187], v[44:47]\n"
    "s_waitcnt lgkmcnt(8)\n"
    "v_mfma_f32_16x16x32_bf16 v[32:35], v[120:123], a[180:183], v[32:35]\n"
    "ds_read_b128 v[96:99], v9 offset:57344\n"
    "v_mfma_f32_16x16x32_bf16 v[36:39], v[120:123], a[188:191], v[36:39]\n"
    "v_mfma_f32_16x16x32_bf16 v[40:43], v[124:127], a[180:183], v[40:43]\n"
    "ds_read_b128 v[100:103], v9 offset:59392\n"
    "v_mfma_f32_16x16x32_bf16 v[44:47], v[124:127], a[188:191], v[44:47]\n"
    "s_waitcnt lgkmcnt(8)\n"
    "v_mfma_f32_16x16x32_bf16 v[32:35], v[64:67], a[192:195], v[32:35]\n"
    "ds_read_b128 v[104:107], v10 offset:57344\n"
    "v_mfma_f32_16x16x32_bf16 v[36:39], v[64:67], a[200:203], v[36:39]\n"
    "v_mfma_f32_16x16x32_bf16 v[40:43], v[68:71], a[192:195], v[40:43]\n"
    "ds_read_b128 v[108:111], v10 offset:59392\n"
    "v_mfma_f32_16x16x32_bf16 v[44:47], v[68:71], a[200:203], v[44:47]\n"
    "s_waitcnt lgkmcnt(8)\n"
    "v_mfma_f32_16x16x32_bf16 v[32:35], v[72:75], a[196:199], v[32:35]\n"
    "ds_read_b128 v[112:115], v9 offset:61440\n"
    "v_mfma_f32_16x16x32_bf16 v[36:39], v[72:75], a[204:207], v[36:39]\n"
    "v_mfma_f32_16x16x32_bf16 v[40:43], v[76:79], a[196:199], v[40:43]\n"
    "ds_read_b128 v[116:119], v9 offset:63488\n"
    "v_mfma_f32_16x16x32_bf16 v[44:47], v[76:79], a[204:207], v[44:47]\n"
    "s_waitcnt lgkmcnt(8)\n"
    "v_mfma_f32_16x16x32_bf16 v[32:35], v[80:83], a[208:211], v[32:35]\n"
    "ds_read_b128 v[120:123], v10 offset:61440\n"
    "v_mfma_f32_16x16x32_bf16 v[36:39], v[80:83], a[216:219], v[36:39]\n"
    "v_mfma_f32_16x16x32_bf16 v[40:43], v[84:87], a[208:211], v[40:43]\n"
    "ds_read_b128 v[124:127], v10 offset:63488\n"
    "v_mfma_f32_16x16x32_bf16 v[44:47], v[84:87], a[216:219], v[44:47]\n"
    "s_waitcnt lgkmcnt(8)\n"
    "v_mfma_f32_16x16x32_bf16 v[32:35], v[88:91], a[212:215], v[32:35]\n"
    "v_mfma_f32_16x16x32_bf16 v[36:39], v[88:91], a[220:223], v[36:39]\n"
    "v_mfma_f32_16x16x32_bf16 v[40:43], v[92:95], a[212:215], v[40:43]\n"
    "v_mfma_f32_16x16x32_bf16 v[44:47], v[92:95], a[220:223], v[44:47]\n"
    "s_waitcnt lgkmcnt(6)\n"
    "v_mfma_f32_16x16x32_bf16 v[32:35], v[96:99], a[224:227], v[32:35]\n"
    "v_mfma_f32_16x16x32_bf16 v[36:39], v[96:99], a[232:235], v[36:39]\n"
    "v_mfma_f32_16x16x32_bf16 v[40:43], v[100:103], a[224:227], v[40:43]\n"
    "v_xor_b32 v9, 0x10000, v9\n"
    "v_xor_b32 v10, 0x10000, v10\n"
    "v_mfma_f32_16x16x32_bf16 v[44:47], v[100:103], a[232:235], v[44:47]\n"
    "s_waitcnt vmcnt(1)\n"
    "s_waitcnt lgkmcnt(0)\n"
    "s_barrier\n"
    "v_mfma_f32_16x16x32_bf16 v[32:35], v[104:107], a[228:231], v[32:35]\n"
    "ds_read_b128 v[64:67], v9 offset:0\n"
    "v_mfma_f32_16x16x32_bf16 v[36:39], v[104:107], a[236:239], v[36:39]\n"
    "buffer_load_dword v14, v13, s[36:39], s61 offen\n"
    "v_mfma_f32_16x16x32_bf16 v[40:43], v[108:111], a[228:231], v[40:43]\n"
    "ds_read_b128 v[68:71], v9 offset:2048\n"
    "v_mfma_f32_16x16x32_bf16 v[44:47], v[108:111], a[236:239], v[44:47]\n"
    "v_mfma_f32_16x16x32_bf16 v[32:35], v[112:115], a[240:243], v[32:35]\n"
    "ds_read_b128 v[72:75], v10 offset:0\n"
    "v_mfma_f32_16x16x32_bf16 v[36:39], v[112:115], a[248:251], v[36:39]\n"
    "v_mfma_f32_16x16x32_bf16 v[40:43], v[116:119], a[240:243], v[40:43]\n"
    "ds_read_b128 v[76:79], v10 offset:2048\n"
    "v_mfma_f32_16x16x32_bf16 v[44:47], v[116:119], a[248:251], v[44:47]\n"
    "v_mfma_f32_16x16x32_bf16 v[32:35], v[120:123], a[244:247], v[32:35]\n"
    "ds_read_b128 v[80:83], v9 offset:4096\n"
    "v_mfma_f32_16x16x32_bf16 v[36:39], v[120:123], a[252:255], v[36:39]\n"
    "s_add_u32 s60, s60, 1\n"
    "v_mfma_f32_16x16x32_bf16 v[40:43], v[124:127], a[244:247], v[40:43]\n"
    "ds_read_b128 v[84:87], v9 offset:6144\n"
    "s_cmp_lt_u32 s60, 64\n"
    "v_mfma_f32_16x16x32_bf16 v[44:47], v[124:127], a[252:255], v[44:47]\n"
    "s_cbranch_scc1 .Lgx_vloop_%=\n"
    "s_mul_i32 s59, s60, 786432\n"
    "s_add_u32 s59, s59, s63\n"
    "s_sub_u32 s67, s59, 786432\n"
    "s_cselect_b32 s67, 0x10000000, s67\n"
    "s_add_u32 s68, s67, 4096\n"
    "s_nop 3\n"
    "v_cvt_pk_bf16_f32 v48, v32, v33\n"
    "v_cvt_pk_bf16_f32 v49, v34, v35\n"
    "v_cvt_pk_bf16_f32 v50, v40, v41\n"
    "v_cvt_pk_bf16_f32 v51, v42, v43\n"
    "buffer_store_dwordx4 v[48:51], v11, s[40:43], s67 offen offset:0 sc1\n"
    "v_cvt_pk_bf16_f32 v52, v36, v37\n"
    "v_cvt_pk_bf16_f32 v53, v38, v39\n"
    "v_cvt_pk_bf16_f32 v54, v44, v45\n"
    "v_cvt_pk_bf16_f32 v55, v46, v47\n"
    "buffer_store_dwordx4 v[52:55], v11, s[40:43], s67 offen offset:1024 sc1\n"
    ".Lgx_qkvdone_%=:\n"
    "v_mov_b32 v12, 64\n"
    "s_waitcnt vmcnt(0) lgkmcnt(0)\n"
    "s_mov_b64 exec, 1\n"
    "buffer_store_dword v12, off, s[44:47], s64 sc1\n"
    "s_mov_b64 exec, -1\n"
    "s_barrier\n"
    "s_lshr_b32 s86, s66, 4\n"
    "s_mul_i32 s86, s86, 3\n"
    "s_add_u32 s86, s86, s65\n"
    "s_and_b32 s87, s66, 15\n"
    "s_load_dwordx4 s[76:79], s[28:29], 80\n"
    "s_waitcnt lgkmcnt(0)\n"
    "s_mov_b32 s32, s76\n"
    "s_and_b32 s33, s77, 0xffff\n"
    "s_mov_b32 s34, 8388608\n"
    "s_mov_b32 s35, 0x20000\n"
    "s_mov_b32 s52, s78\n"
    "s_and_b32 s53, s79, 0xffff\n"
    "s_mov_b32 s54, 8388608\n"
    "s_mov_b32 s55, 0x20000\n"
    "s_add_u32 s48, s30, 52428800\n"
    "s_addc_u32 s49, s31, 0\n"
    "s_and_b32 s49, s49, 0xffff\n"
    "s_mov_b32 s50, 16777216\n"
    "s_mov_b32 s51, 0x20000\n"
    "s_add_u32 s44, s30, 0\n"
    "s_addc_u32 s45, s31, 0\n"
    "s_and_b32 s45, s45, 0xffff\n"
    "s_mov_b32 s46, 65536\n"
    "s_mov_b32 s47, 0x20000\n"
    "s_mul_i32 s57, s25, 40960\n"
    "s_lshl_b32 s56, s25, 21\n"
    "s_lshl_b32 s58, s87, 7\n"
    "s_add_u32 s56, s56, s58\n"
    "s_and_b32 s62, s87, 7\n"
    "v_lshlrev_b32 v8, 4, v1\n"
    "v_add_u32 v9, s57, v8\n"
    "v_bfe_u32 v2, v1, 3, 1\n"
    "v_lshlrev_b32 v2, 13, v2\n"
    "v_bfe_u32 v3, v1, 4, 1\n"
    "v_lshl_add_u32 v2, v3, 12, v2\n"
    "v_bfe_u32 v3, v1, 5, 1\n"
    "v_lshl_add_u32 v2, v3, 11, v2\n"
    "v_bfe_u32 v3, v1, 0, 3\n"
    "v_lshl_add_u32 v2, v3, 4, v2\n"
    "v_bfe_u32 v4, v1, 4, 2\n"
    "v_lshlrev_b32 v4, 5, v4\n"
    "v_xor_b32 v2, v2, v4\n"
    "v_bfe_u32 v4, v1, 5, 1\n"
    "v_lshlrev_b32 v4, 11, v4\n"
    "v_bfe_u32 v3, v1, 4, 1\n"
    "v_lshl_add_u32 v4, v3, 7, v4\n"
    "v_bfe_u32 v3, v1, 3, 1\n"
    "v_lshl_add_u32 v4, v3, 8, v4\n"
    "v_bfe_u32 v3, v1, 2, 1\n"
    "v_lshl_add_u32 v4, v3, 9, v4\n"
    "v_bfe_u32 v3, v1, 3, 1\n"
    "v_lshl_add_u32 v4, v3, 5, v4\n"
    "v_bfe_u32 v3, v1, 2, 1\n"
    "v_lshl_add_u32 v4, v3, 6, v4\n"
    "v_bfe_u32 v3, v1, 0, 2\n"
    "v_lshl_add_u32 v4, v3, 3, v4\n"
    "v_add_u32 v4, s57, v4\n"
    "v_xor_b32 v5, 32, v4\n"
    "v_xor_b32 v6, 64, v4\n"
    "v_xor_b32 v7, 96, v4\n"
    "s_xor_b32 s58, s62, 0\n"
    "s_lshl_b32 s58, s58, 18\n"
    "s_add_u32 s58, s58, 0\n"
    "s_add_u32 s58, s58, s56\n"
    "s_add_u32 m0, s57, 32768\n"
    "s_add_u32 s59, s58, 0\n"
    "buffer_load_dwordx4 v2, s[32:35], s59 offen lds\n"
    "s_add_u32 m0, s57, 33792\n"
    "s_add_u32 s59, s58, 32768\n"
    "buffer_load_dwordx4 v2, s[32:35], s59 offen lds\n"
    "s_add_u32 m0, s57, 34816\n"
    "s_add_u32 s59, s58, 16384\n"
    "buffer_load_dwordx4 v2, s[32:35], s59 offen lds\n"
    "s_add_u32 m0, s57, 35840\n"
    "s_add_u32 s59, s58, 49152\n"
    "buffer_load_dwordx4 v2, s[32:35], s59 offen lds\n"
    "s_add_u32 m0, s57, 36864\n"
    "s_add_u32 s59, s58, 65536\n"
    "buffer_load_dwordx4 v2, s[32:35], s59 offen lds\n"
    "s_add_u32 m0, s57, 37888\n"
    "s_add_u32 s59, s58, 98304\n"
    "buffer_load_dwordx4 v2, s[32:35], s59 offen lds\n"
    "s_add_u32 m0, s57, 38912\n"
    "s_add_u32 s59, s58, 81920\n"
    "buffer_load_dwordx4 v2, s[32:35], s59 offen lds\n"
    "s_add_u32 m0, s57, 39936\n"
    "s_add_u32 s59, s58, 114688\n"
    "buffer_load_dwordx4 v2, s[32:35], s59 offen lds\n"
    "s_xor_b32 s58, s62, 0\n"
    "s_lshl_b32 s58, s58, 18\n"
    "s_add_u32 s58, s58, 131072\n"
    "s_add_u32 s58, s58, s56\n"
    "s_add_u32 m0, s57, 0\n"
    "s_add_u32 s59, s58, 0\n"
    "buffer_load_dwordx4 v2, s[32:35], s59 offen lds\n"
    "s_add_u32 m0, s57, 1024\n"
    "s_add_u32 s59, s58, 32768\n"
    "buffer_load_dwordx4 v2, s[32:35], s59 offen lds\n"
    "s_add_u32 m0, s57, 2048\n"
    "s_add_u32 s59, s58, 16384\n"
    "buffer_load_dwordx4 v2, s[32:35], s59 offen lds\n"
    "s_add_u32 m0, s57, 3072\n"
    "s_add_u32 s59, s58, 49152\n"
    "buffer_load_dwordx4 v2, s[32:35], s59 offen lds\n"
    "s_add_u32 m0, s57, 4096\n"
    "s_add_u32 s59, s58, 65536\n"
    "buffer_load_dwordx4 v2, s[32:35], s59 offen lds\n"
    "s_add_u32 m0, s57, 5120\n"
    "s_add_u32 s59, s58, 98304\n"
    "buffer_load_dwordx4 v2, s[32:35], s59 offen lds\n"
    "s_add_u32 m0, s57, 6144\n"
    "s_add_u32 s59, s58, 81920\n"
    "buffer_load_dwordx4 v2, s[32:35], s59 offen lds\n"
    "s_add_u32 m0, s57, 7168\n"
    "s_add_u32 s59, s58, 114688\n"
    "buffer_load_dwordx4 v2, s[32:35], s59 offen lds\n"
    "s_xor_b32 s58, s62, 1\n"
    "s_lshl_b32 s58, s58, 18\n"
    "s_add_u32 s58, s58, 0\n"
    "s_add_u32 s58, s58, s56\n"
    "s_add_u32 m0, s57, 8192\n"
    "s_add_u32 s59, s58, 0\n"
    "buffer_load_dwordx4 v2, s[32:35], s59 offen lds\n"
    "s_add_u32 m0, s57, 9216\n"
    "s_add_u32 s59, s58, 32768\n"
    "buffer_load_dwordx4 v2, s[32:35], s59 offen lds\n"
    "s_add_u32 m0, s57, 10240\n"
    "s_add_u32 s59, s58, 16384\n"
    "buffer_load_dwordx4 v2, s[32:35], s59 offen lds\n"
    "s_add_u32 m0, s57, 11264\n"
    "s_add_u32 s59, s58, 49152\n"
    "buffer_load_dwordx4 v2, s[32:35], s59 offen lds\n"
    "s_add_u32 m0, s57, 12288\n"
    "s_add_u32 s59, s58, 65536\n"
    "buffer_load_dwordx4 v2, s[32:35], s59 offen lds\n"
    "s_add_u32 m0, s57, 13312\n"
    "s_add_u32 s59, s58, 98304\n"
    "buffer_load_dwordx4 v2, s[32:35], s59 offen lds\n"
    "s_add_u32 m0, s57, 14336\n"
    "s_add_u32 s59, s58, 81920\n"
    "buffer_load_dwordx4 v2, s[32:35], s59 offen lds\n"
    "s_add_u32 m0, s57, 15360\n"
    "s_add_u32 s59, s58, 114688\n"
    "buffer_load_dwordx4 v2, s[32:35], s59 offen lds\n"
    "s_xor_b32 s58, s62, 1\n"
    "s_lshl_b32 s58, s58, 18\n"
    "s_add_u32 s58, s58, 131072\n"
    "s_add_u32 s58, s58, s56\n"
    "s_add_u32 m0, s57, 16384\n"
    "s_add_u32 s59, s58, 0\n"
    "buffer_load_dwordx4 v2, s[32:35], s59 offen lds\n"
    "s_add_u32 m0, s57, 17408\n"
    "s_add_u32 s59, s58, 32768\n"
    "buffer_load_dwordx4 v2, s[32:35], s59 offen lds\n"
    "s_add_u32 m0, s57, 18432\n"
    "s_add_u32 s59, s58, 16384\n"
    "buffer_load_dwordx4 v2, s[32:35], s59 offen lds\n"
    "s_add_u32 m0, s57, 19456\n"
    "s_add_u32 s59, s58, 49152\n"
    "buffer_load_dwordx4 v2, s[32:35], s59 offen lds\n"
    "s_add_u32 m0, s57, 20480\n"
    "s_add_u32 s59, s58, 65536\n"
    "buffer_load_dwordx4 v2, s[32:35], s59 offen lds\n"
    "s_add_u32 m0, s57, 21504\n"
    "s_add_u32 s59, s58, 98304\n"
    "buffer_load_dwordx4 v2, s[32:35], s59 offen lds\n"
    "s_add_u32 m0, s57, 22528\n"
    "s_add_u32 s59, s58, 81920\n"
    "buffer_load_dwordx4 v2, s[32:35], s59 offen lds\n"
    "s_add_u32 m0, s57, 23552\n"
    "s_add_u32 s59, s58, 114688\n"
    "buffer_load_dwordx4 v2, s[32:35], s59 offen lds\n"
    "s_xor_b32 s58, s62, 2\n"
    "s_lshl_b32 s58, s58, 18\n"
    "s_add_u32 s58, s58, 0\n"
    "s_add_u32 s58, s58, s56\n"
    "s_add_u32 m0, s57, 24576\n"
    "s_add_u32 s59, s58, 0\n"
    "buffer_load_dwordx4 v2, s[32:35], s59 offen lds\n"
    "s_add_u32 m0, s57, 25600\n"
    "s_add_u32 s59, s58, 32768\n"
    "buffer_load_dwordx4 v2, s[32:35], s59 offen lds\n"
    "s_add_u32 m0, s57, 26624\n"
    "s_add_u32 s59, s58, 16384\n"
    "buffer_load_dwordx4 v2, s[32:35], s59 offen lds\n"
    "s_add_u32 m0, s57, 27648\n"
    "s_add_u32 s59, s58, 49152\n"
    "buffer_load_dwordx4 v2, s[32:35], s59 offen lds\n"
    "s_add_u32 m0, s57, 28672\n"
    "s_add_u32 s59, s58, 65536\n"
    "buffer_load_dwordx4 v2, s[32:35], s59 offen lds\n"
    "s_add_u32 m0, s57, 29696\n"
    "s_add_u32 s59, s58, 98304\n"
    "buffer_load_dwordx4 v2, s[32:35], s59 offen lds\n"
    "s_add_u32 m0, s57, 30720\n"
    "s_add_u32 s59, s58, 81920\n"
    "buffer_load_dwordx4 v2, s[32:35], s59 offen lds\n"
    "s_add_u32 m0, s57, 31744\n"
    "s_add_u32 s59, s58, 114688\n"
    "buffer_load_dwordx4 v2, s[32:35], s59 offen lds\n"
    "s_waitcnt vmcnt(32)\n"
    "ds_read_b64_tr_b16 a[0:1], v4 offset:32768\n"
    "ds_read_b64_tr_b16 a[2:3], v4 offset:33792\n"
    "ds_read_b64_tr_b16 a[4:5], v4 offset:36864\n"
    "ds_read_b64_tr_b16 a[6:7], v4 offset:37888\n"
    "ds_read_b64_tr_b16 a[8:9], v5 offset:32768\n"
    "ds_read_b64_tr_b16 a[10:11], v5 offset:33792\n"
    "ds_read_b64_tr_b16 a[12:13], v5 offset:36864\n"
    "ds_read_b64_tr_b16 a[14:15], v5 offset:37888\n"
    "ds_read_b64_tr_b16 v[128:129], v6 offset:32768\n"
    "ds_read_b64_tr_b16 v[130:131], v6 offset:33792\n"
    "ds_read_b64_tr_b16 v[132:133], v6 offset:36864\n"
    "ds_read_b64_tr_b16 v[134:135], v6 offset:37888\n"
    "ds_read_b64_tr_b16 v[16:17], v7 offset:32768\n"
    "ds_read_b64_tr_b16 v[18:19], v7 offset:33792\n"
    "ds_read_b64_tr_b16 v[20:21], v7 offset:36864\n"
    "ds_read_b64_tr_b16 v[22:23], v7 offset:37888\n"
    "s_waitcnt lgkmcnt(0)\n"
    "s_xor_b32 s58, s62, 2\n"
    "s_lshl_b32 s58, s58, 18\n"
    "s_add_u32 s58, s58, 131072\n"
    "s_add_u32 s58, s58, s56\n"
    "s_add_u32 m0, s57, 32768\n"
    "s_add_u32 s59, s58, 0\n"
    "buffer_load_dwordx4 v2, s[32:35], s59 offen lds\n"
    "s_add_u32 m0, s57, 33792\n"
    "s_add_u32 s59, s58, 32768\n"
    "buffer_load_dwordx4 v2, s[32:35], s59 offen lds\n"
    "s_add_u32 m0, s57, 34816\n"
    "s_add_u32 s59, s58, 16384\n"
    "buffer_load_dwordx4 v2, s[32:35], s59 offen lds\n"
    "s_add_u32 m0, s57, 35840\n"
    "s_add_u32 s59, s58, 49152\n"
    "buffer_load_dwordx4 v2, s[32:35], s59 offen lds\n"
    "s_add_u32 m0, s57, 36864\n"
    "s_add_u32 s59, s58, 65536\n"
    "buffer_load_dwordx4 v2, s[32:35], s59 offen lds\n"
    "s_add_u32 m0, s57, 37888\n"
    "s_add_u32 s59, s58, 98304\n"
    "buffer_load_dwordx4 v2, s[32:35], s59 offen lds\n"
    "s_add_u32 m0, s57, 38912\n"
    "s_add_u32 s59, s58, 81920\n"
    "buffer_load_dwordx4 v2, s[32:35], s59 offen lds\n"
    "s_add_u32 m0, s57, 39936\n"
    "s_add_u32 s59, s58, 114688\n"
    "buffer_load_dwordx4 v2, s[32:35], s59 offen lds\n"
    "s_waitcnt vmcnt(32)\n"
    "ds_read_b64_tr_b16 a[16:17], v4 offset:0\n"
    "ds_read_b64_tr_b16 a[18:19], v4 offset:1024\n"
    "ds_read_b64_tr_b16 a[20:21], v4 offset:4096\n"
    "ds_read_b64_tr_b16 a[22:23], v4 offset:5120\n"
    "ds_read_b64_tr_b16 a[24:25], v5 offset:0\n"
    "ds_read_b64_tr_b16 a[26:27], v5 offset:1024\n"
    "ds_read_b64_tr_b16 a[28:29], v5 offset:4096\n"
    "ds_read_b64_tr_b16 a[30:31], v5 offset:5120\n"
    "ds_read_b64_tr_b16 v[136:137], v6 offset:0\n"
    "ds_read_b64_tr_b16 v[138:139], v6 offset:1024\n"
    "ds_read_b64_tr_b16 v[140:141], v6 offset:4096\n"
    "ds_read_b64_tr_b16 v[142:143], v6 offset:5120\n"
    "ds_read_b64_tr_b16 v[24:25], v7 offset:0\n"
    "ds_read_b64_tr_b16 v[26:27], v7 offset:1024\n"
    "ds_read_b64_tr_b16 v[28:29], v7 offset:4096\n"
    "ds_read_b64_tr_b16 v[30:31], v7 offset:5120\n"
    "s_waitcnt lgkmcnt(0)\n"
    "s_xor_b32 s58, s62, 3\n"
    "s_lshl_b32 s58, s58, 18\n"
    "s_add_u32 s58, s58, 0\n"
    "s_add_u32 s58, s58, s56\n"
    "s_add_u32 m0, s57, 0\n"
    "s_add_u32 s59, s58, 0\n"
    "buffer_load_dwordx4 v2, s[32:35], s59 offen lds\n"
    "s_add_u32 m0, s57, 1024\n"
    "s_add_u32 s59, s58, 32768\n"
    "buffer_load_dwordx4 v2, s[32:35], s59 offen lds\n"
    "s_add_u32 m0, s57, 2048\n"
    "s_add_u32 s59, s58, 16384\n"
    "buffer_load_dwordx4 v2, s[32:35], s59 offen lds\n"
    "s_add_u32 m0, s57, 3072\n"
    "s_add_u32 s59, s58, 49152\n"
    "buffer_load_dwordx4 v2, s[32:35], s59 offen lds\n"
    "s_add_u32 m0, s57, 4096\n"
    "s_add_u32 s59, s58, 65536\n"
    "buffer_load_dwordx4 v2, s[32:35], s59 offen lds\n"
    "s_add_u32 m0, s57, 5120\n"
    "s_add_u32 s59, s58, 98304\n"
    "buffer_load_dwordx4 v2, s[32:35], s59 offen lds\n"
    "s_add_u32 m0, s57, 6144\n"
    "s_add_u32 s59, s58, 81920\n"
    "buffer_load_dwordx4 v2, s[32:35], s59 offen lds\n"
    "s_add_u32 m0, s57, 7168\n"
    "s_add_u32 s59, s58, 114688\n"
    "buffer_load_dwordx4 v2, s[32:35], s59 offen lds\n"
    "s_waitcnt vmcnt(32)\n"
    "ds_read_b64_tr_b16 a[32:33], v4 offset:8192\n"
    "ds_read_b64_tr_b16 a[34:35], v4 offset:9216\n"
    "ds_read_b64_tr_b16 a[36:37], v4 offset:12288\n"
    "ds_read_b64_tr_b16 a[38:39], v4 offset:13312\n"
    "ds_read_b64_tr_b16 a[40:41], v5 offset:8192\n"
    "ds_read_b64_tr_b16 a[42:43], v5 offset:9216\n"
    "ds_read_b64_tr_b16 a[44:45], v5 offset:12288\n"
    "ds_read_b64_tr_b16 a[46:47], v5 offset:13312\n"
    "ds_read_b64_tr_b16 v[144:145], v6 offset:8192\n"
    "ds_read_b64_tr_b16 v[146:147], v6 offset:9216\n"
    "ds_read_b64_tr_b16 v[148:149], v6 offset:12288\n"
    "ds_read_b64_tr_b16 v[150:151], v6 offset:13312\n"
    "ds_read_b64_tr_b16 v[32:33], v7 offset:8192\n"
    "ds_read_b64_tr_b16 v[34:35], v7 offset:9216\n"
    "ds_read_b64_tr_b16 v[36:37], v7 offset:12288\n"
    "ds_read_b64_tr_b16 v[38:39], v7 offset:13312\n"
    "s_waitcnt lgkmcnt(0)\n"
    "s_xor_b32 s58, s62, 3\n"
    "s_lshl_b32 s58, s58, 18\n"
    "s_add_u32 s58, s58, 131072\n"
    "s_add_u32 s58, s58, s56\n"
    "s_add_u32 m0, s57, 8192\n"
    "s_add_u32 s59, s58, 0\n"
    "buffer_load_dwordx4 v2, s[32:35], s59 offen lds\n"
    "s_add_u32 m0, s57, 9216\n"
    "s_add_u32 s59, s58, 32768\n"
    "buffer_load_dwordx4 v2, s[32:35], s59 offen lds\n"
    "s_add_u32 m0, s57, 10240\n"
    "s_add_u32 s59, s58, 16384\n"
    "buffer_load_dwordx4 v2, s[32:35], s59 offen lds\n"
    "s_add_u32 m0, s57, 11264\n"
    "s_add_u32 s59, s58, 49152\n"
    "buffer_load_dwordx4 v2, s[32:35], s59 offen lds\n"
    "s_add_u32 m0, s57, 12288\n"
    "s_add_u32 s59, s58, 65536\n"
    "buffer_load_dwordx4 v2, s[32:35], s59 offen lds\n"
    "s_add_u32 m0, s57, 13312\n"
    "s_add_u32 s59, s58, 98304\n"
    "buffer_load_dwordx4 v2, s[32:35], s59 offen lds\n"
    "s_add_u32 m0, s57, 14336\n"
    "s_add_u32 s59, s58, 81920\n"
    "buffer_load_dwordx4 v2, s[32:35], s59 offen lds\n"
    "s_add_u32 m0, s57, 15360\n"
    "s_add_u32 s59, s58, 114688\n"
    "buffer_load_dwordx4 v2, s[32:35], s59 offen lds\n"
    "s_waitcnt vmcnt(32)\n"
    "ds_read_b64_tr_b16 a[48:49], v4 offset:16384\n"
    "ds_read_b64_tr_b16 a[50:51], v4 offset:17408\n"
    "ds_read_b64_tr_b16 a[52:53], v4 offset:20480\n"
    "ds_read_b64_tr_b16 a[54:55], v4 offset:21504\n"
    "ds_read_b64_tr_b16 a[56:57], v5 offset:16384\n"
    "ds_read_b64_tr_b16 a[58:59], v5 offset:17408\n"
    "ds_read_b64_tr_b16 a[60:61], v5 offset:20480\n"
    "ds_read_b64_tr_b16 a[62:63], v5 offset:21504\n"
    "ds_read_b64_tr_b16 v[152:153], v6 offset:16384\n"
    "ds_read_b64_tr_b16 v[154:155], v6 offset:17408\n"
    "ds_read_b64_tr_b16 v[156:157], v6 offset:20480\n"
    "ds_read_b64_tr_b16 v[158:159], v6 offset:21504\n"
    "ds_read_b64_tr_b16 v[40:41], v7 offset:16384\n"
    "ds_read_b64_tr_b16 v[42:43], v7 offset:17408\n"
    "ds_read_b64_tr_b16 v[44:45], v7 offset:20480\n"
    "ds_read_b64_tr_b16 v[46:47], v7 offset:21504\n"
    "s_waitcnt lgkmcnt(0)\n"
    "s_xor_b32 s58, s62, 4\n"
    "s_lshl_b32 s58, s58, 18\n"
    "s_add_u32 s58, s58, 0\n"
    "s_add_u32 s58, s58, s56\n"
    "s_add_u32 m0, s57, 16384\n"
    "s_add_u32 s59, s58, 0\n"
    "buffer_load_dwordx4 v2, s[32:35], s59 offen lds\n"
    "s_add_u32 m0, s57, 17408\n"
    "s_add_u32 s59, s58, 32768\n"
    "buffer_load_dwordx4 v2, s[32:35], s59 offen lds\n"
    "s_add_u32 m0, s57, 18432\n"
    "s_add_u32 s59, s58, 16384\n"
    "buffer_load_dwordx4 v2, s[32:35], s59 offen lds\n"
    "s_add_u32 m0, s57, 19456\n"
    "s_add_u32 s59, s58, 49152\n"
    "buffer_load_dwordx4 v2, s[32:35], s59 offen lds\n"
    "s_add_u32 m0, s57, 20480\n"
    "s_add_u32 s59, s58, 65536\n"
    "buffer_load_dwordx4 v2, s[32:35], s59 offen lds\n"
    "s_add_u32 m0, s57, 21504\n"
    "s_add_u32 s59, s58, 98304\n"
    "buffer_load_dwordx4 v2, s[32:35], s59 offen lds\n"
    "s_add_u32 m0, s57, 22528\n"
    "s_add_u32 s59, s58, 81920\n"
    "buffer_load_dwordx4 v2, s[32:35], s59 offen lds\n"
    "s_add_u32 m0, s57, 23552\n"
    "s_add_u32 s59, s58, 114688\n"
    "buffer_load_dwordx4 v2, s[32:35], s59 offen lds\n"
    "s_waitcnt vmcnt(32)\n"
    "ds_read_b64_tr_b16 a[64:65], v4 offset:24576\n"
    "ds_read_b64_tr_b16 a[66:67], v4 offset:25600\n"
    "ds_read_b64_tr_b16 a[68:69], v4 offset:28672\n"
    "ds_read_b64_tr_b16 a[70:71], v4 offset:29696\n"
    "ds_read_b64_tr_b16 a[72:73], v5 offset:24576\n"
    "ds_read_b64_tr_b16 a[74:75], v5 offset:25600\n"
    "ds_read_b64_tr_b16 a[76:77], v5 offset:28672\n"
    "ds_read_b64_tr_b16 a[78:79], v5 offset:29696\n"
    "ds_read_b64_tr_b16 v[160:161], v6 offset:24576\n"
    "ds_read_b64_tr_b16 v[162:163], v6 offset:25600\n"
    "ds_read_b64_tr_b16 v[164:165], v6 offset:28672\n"
    "ds_read_b64_tr_b16 v[166:167], v6 offset:29696\n"
    "ds_read_b64_tr_b16 v[48:49], v7 offset:24576\n"
    "ds_read_b64_tr_b16 v[50:51], v7 offset:25600\n"
    "ds_read_b64_tr_b16 v[52:53], v7 offset:28672\n"
    "ds_read_b64_tr_b16 v[54:55], v7 offset:29696\n"
    "s_waitcnt lgkmcnt(0)\n"
    "s_xor_b32 s58, s62, 4\n"
    "s_lshl_b32 s58, s58, 18\n"
    "s_add_u32 s58, s58, 131072\n"
    "s_add_u32 s58, s58, s56\n"
    "s_add_u32 m0, s57, 24576\n"
    "s_add_u32 s59, s58, 0\n"
    "buffer_load_dwordx4 v2, s[32:35], s59 offen lds\n"
    "s_add_u32 m0, s57, 25600\n"
    "s_add_u32 s59, s58, 32768\n"
    "buffer_load_dwordx4 v2, s[32:35], s59 offen lds\n"
    "s_add_u32 m0, s57, 26624\n"
    "s_add_u32 s59, s58, 16384\n"
    "buffer_load_dwordx4 v2, s[32:35], s59 offen lds\n"
    "s_add_u32 m0, s57, 27648\n"
    "s_add_u32 s59, s58, 49152\n"
    "buffer_load_dwordx4 v2, s[32:35], s59 offen lds\n"
    "s_add_u32 m0, s57, 28672\n"
    "s_add_u32 s59, s58, 65536\n"
    "buffer_load_dwordx4 v2, s[32:35], s59 offen lds\n"
    "s_add_u32 m0, s57, 29696\n"
    "s_add_u32 s59, s58, 98304\n"
    "buffer_load_dwordx4 v2, s[32:35], s59 offen lds\n"
    "s_add_u32 m0, s57, 30720\n"
    "s_add_u32 s59, s58, 81920\n"
    "buffer_load_dwordx4 v2, s[32:35], s59 offen lds\n"
    "s_add_u32 m0, s57, 31744\n"
    "s_add_u32 s59, s58, 114688\n"
    "buffer_load_dwordx4 v2, s[32:35], s59 offen lds\n"
    "s_waitcnt vmcnt(32)\n"
    "ds_read_b64_tr_b16 a[80:81], v4 offset:32768\n"
    "ds_read_b64_tr_b16 a[82:83], v4 offset:33792\n"
    "ds_read_b64_tr_b16 a[84:85], v4 offset:36864\n"
    "ds_read_b64_tr_b16 a[86:87], v4 offset:37888\n"
    "ds_read_b64_tr_b16 a[88:89], v5 offset:32768\n"
    "ds_read_b64_tr_b16 a[90:91], v5 offset:33792\n"
    "ds_read_b64_tr_b16 a[92:93], v5 offset:36864\n"
    "ds_read_b64_tr_b16 a[94:95], v5 offset:37888\n"
    "ds_read_b64_tr_b16 v[168:169], v6 offset:32768\n"
    "ds_read_b64_tr_b16 v[170:171], v6 offset:33792\n"
    "ds_read_b64_tr_b16 v[172:173], v6 offset:36864\n"
    "ds_read_b64_tr_b16 v[174:175], v6 offset:37888\n"
    "ds_read_b64_tr_b16 v[56:57], v7 offset:32768\n"
    "ds_read_b64_tr_b16 v[58:59], v7 offset:33792\n"
    "ds_read_b64_tr_b16 v[60:61], v7 offset:36864\n"
    "ds_read_b64_tr_b16 v[62:63], v7 offset:37888\n"
    "s_waitcnt lgkmcnt(0)\n"
    "s_xor_b32 s58, s62, 5\n"
    "s_lshl_b32 s58, s58, 18\n"
    "s_add_u32 s58, s58, 0\n"
    "s_add_u32 s58, s58, s56\n"
    "s_add_u32 m0, s57, 32768\n"
    "s_add_u32 s59, s58, 0\n"
    "buffer_load_dwordx4 v2, s[32:35], s59 offen lds\n"
    "s_add_u32 m0, s57, 33792\n"
    "s_add_u32 s59, s58, 32768\n"
    "buffer_load_dwordx4 v2, s[32:35], s59 offen lds\n"
    "s_add_u32 m0, s57, 34816\n"
    "s_add_u32 s59, s58, 16384\n"
    "buffer_load_dwordx4 v2, s[32:35], s59 offen lds\n"
    "s_add_u32 m0, s57, 35840\n"
    "s_add_u32 s59, s58, 49152\n"
    "buffer_load_dwordx4 v2, s[32:35], s59 offen lds\n"
    "s_add_u32 m0, s57, 36864\n"
    "s_add_u32 s59, s58, 65536\n"
    "buffer_load_dwordx4 v2, s[32:35], s59 offen lds\n"
    "s_add_u32 m0, s57, 37888\n"
    "s_add_u32 s59, s58, 98304\n"
    "buffer_load_dwordx4 v2, s[32:35], s59 offen lds\n"
    "s_add_u32 m0, s57, 38912\n"
    "s_add_u32 s59, s58, 81920\n"
    "buffer_load_dwordx4 v2, s[32:35], s59 offen lds\n"
    "s_add_u32 m0, s57, 39936\n"
    "s_add_u32 s59, s58, 114688\n"
    "buffer_load_dwordx4 v2, s[32:35], s59 offen lds\n"
    "s_waitcnt vmcnt(32)\n"
    "ds_read_b64_tr_b16 a[96:97], v4 offset:0\n"
    "ds_read_b64_tr_b16 a[98:99], v4 offset:1024\n"
    "ds_read_b64_tr_b16 a[100:101], v4 offset:4096\n"
    "ds_read_b64_tr_b16 a[102:103], v4 offset:5120\n"
    "ds_read_b64_tr_b16 a[104:105], v5 offset:0\n"
    "ds_read_b64_tr_b16 a[106:107], v5 offset:1024\n"
    "ds_read_b64_tr_b16 a[108:109], v5 offset:4096\n"
    "ds_read_b64_tr_b16 a[110:111], v5 offset:5120\n"
    "ds_read_b64_tr_b16 v[176:177], v6 offset:0\n"
    "ds_read_b64_tr_b16 v[178:179], v6 offset:1024\n"
    "ds_read_b64_tr_b16 v[180:181], v6 offset:4096\n"
    "ds_read_b64_tr_b16 v[182:183], v6 offset:5120\n"
    "ds_read_b64_tr_b16 v[64:65], v7 offset:0\n"
    "ds_read_b64_tr_b16 v[66:67], v7 offset:1024\n"
    "ds_read_b64_tr_b16 v[68:69], v7 offset:4096\n"
    "ds_read_b64_tr_b16 v[70:71], v7 offset:5120\n"
    "s_waitcnt lgkmcnt(0)\n"
    "s_xor_b32 s58, s62, 5\n"
    "s_lshl_b32 s58, s58, 18\n"
    "s_add_u32 s58, s58, 131072\n"
    "s_add_u32 s58, s58, s56\n"
    "s_add_u32 m0, s57, 0\n"
    "s_add_u32 s59, s58, 0\n"
    "buffer_load_dwordx4 v2, s[32:35], s59 offen lds\n"
    "s_add_u32 m0, s57, 1024\n"
    "s_add_u32 s59, s58, 32768\n"
    "buffer_load_dwordx4 v2, s[32:35], s59 offen lds\n"
    "s_add_u32 m0, s57, 2048\n"
    "s_add_u32 s59, s58, 16384\n"
    "buffer_load_dwordx4 v2, s[32:35], s59 offen lds\n"
    "s_add_u32 m0, s57, 3072\n"
    "s_add_u32 s59, s58, 49152\n"
    "buffer_load_dwordx4 v2, s[32:35], s59 offen lds\n"
    "s_add_u32 m0, s57, 4096\n"
    "s_add_u32 s59, s58, 65536\n"
    "buffer_load_dwordx4 v2, s[32:35], s59 offen lds\n"
    "s_add_u32 m0, s57, 5120\n"
    "s_add_u32 s59, s58, 98304\n"
    "buffer_load_dwordx4 v2, s[32:35], s59 offen lds\n"
    "s_add_u32 m0, s57, 6144\n"
    "s_add_u32 s59, s58, 81920\n"
    "buffer_load_dwordx4 v2, s[32:35], s59 offen lds\n"
    "s_add_u32 m0, s57, 7168\n"
    "s_add_u32 s59, s58, 114688\n"
    "buffer_load_dwordx4 v2, s[32:35], s59 offen lds\n"
    "s_waitcnt vmcnt(32)\n"
    "ds_read_b64_tr_b16 a[112:113], v4 offset:8192\n"
    "ds_read_b64_tr_b16 a[114:115], v4 offset:9216\n"
    "ds_read_b64_tr_b16 a[116:117], v4 offset:12288\n"
    "ds_read_b64_tr_b16 a[118:119], v4 offset:13312\n"
    "ds_read_b64_tr_b16 a[120:121], v5 offset:8192\n"
    "ds_read_b64_tr_b16 a[122:123], v5 offset:9216\n"
    "ds_read_b64_tr_b16 a[124:125], v5 offset:12288\n"
    "ds_read_b64_tr_b16 a[126:127], v5 offset:13312\n"
    "ds_read_b64_tr_b16 v[184:185], v6 offset:8192\n"
    "ds_read_b64_tr_b16 v[186:187], v6 offset:9216\n"
    "ds_read_b64_tr_b16 v[188:189], v6 offset:12288\n"
    "ds_read_b64_tr_b16 v[190:191], v6 offset:13312\n"
    "ds_read_b64_tr_b16 v[72:73], v7 offset:8192\n"
    "ds_read_b64_tr_b16 v[74:75], v7 offset:9216\n"
    "ds_read_b64_tr_b16 v[76:77], v7 offset:12288\n"
    "ds_read_b64_tr_b16 v[78:79], v7 offset:13312\n"
    "s_waitcnt lgkmcnt(0)\n"
    "s_xor_b32 s58, s62, 6\n"
    "s_lshl_b32 s58, s58, 18\n"
    "s_add_u32 s58, s58, 0\n"
    "s_add_u32 s58, s58, s56\n"
    "s_add_u32 m0, s57, 8192\n"
    "s_add_u32 s59, s58, 0\n"
    "buffer_load_dwordx4 v2, s[32:35], s59 offen lds\n"
    "s_add_u32 m0, s57, 9216\n"
    "s_add_u32 s59, s58, 32768\n"
    "buffer_load_dwordx4 v2, s[32:35], s59 offen lds\n"
    "s_add_u32 m0, s57, 10240\n"
    "s_add_u32 s59, s58, 16384\n"
    "buffer_load_dwordx4 v2, s[32:35], s59 offen lds\n"
    "s_add_u32 m0, s57, 11264\n"
    "s_add_u32 s59, s58, 49152\n"
    "buffer_load_dwordx4 v2, s[32:35], s59 offen lds\n"
    "s_add_u32 m0, s57, 12288\n"
    "s_add_u32 s59, s58, 65536\n"
    "buffer_load_dwordx4 v2, s[32:35], s59 offen lds\n"
    "s_add_u32 m0, s57, 13312\n"
    "s_add_u32 s59, s58, 98304\n"
    "buffer_load_dwordx4 v2, s[32:35], s59 offen lds\n"
    "s_add_u32 m0, s57, 14336\n"
    "s_add_u32 s59, s58, 81920\n"
    "buffer_load_dwordx4 v2, s[32:35], s59 offen lds\n"
    "s_add_u32 m0, s57, 15360\n"
    "s_add_u32 s59, s58, 114688\n"
    "buffer_load_dwordx4 v2, s[32:35], s59 offen lds\n"
    "s_waitcnt vmcnt(32)\n"
    "ds_read_b64_tr_b16 a[128:129], v4 offset:16384\n"
    "ds_read_b64_tr_b16 a[130:131], v4 offset:17408\n"
    "ds_read_b64_tr_b16 a[132:133], v4 offset:20480\n"
    "ds_read_b64_tr_b16 a[134:135], v4 offset:21504\n"
    "ds_read_b64_tr_b16 a[136:137], v5 offset:16384\n"
    "ds_read_b64_tr_b16 a[138:139], v5 offset:17408\n"
    "ds_read_b64_tr_b16 a[140:141], v5 offset:20480\n"
    "ds_read_b64_tr_b16 a[142:143], v5 offset:21504\n"
    "ds_read_b64_tr_b16 v[192:193], v6 offset:16384\n"
    "ds_read_b64_tr_b16 v[194:195], v6 offset:17408\n"
    "ds_read_b64_tr_b16 v[196:197], v6 offset:20480\n"
    "ds_read_b64_tr_b16 v[198:199], v6 offset:21504\n"
    "ds_read_b64_tr_b16 v[80:81], v7 offset:16384\n"
    "ds_read_b64_tr_b16 v[82:83], v7 offset:17408\n"
    "ds_read_b64_tr_b16 v[84:85], v7 offset:20480\n"
    "ds_read_b64_tr_b16 v[86:87], v7 offset:21504\n"
    "s_waitcnt lgkmcnt(0)\n"
    "s_xor_b32 s58, s62, 6\n"
    "s_lshl_b32 s58, s58, 18\n"
    "s_add_u32 s58, s58, 131072\n"
    "s_add_u32 s58, s58, s56\n"
    "s_add_u32 m0, s57, 16384\n"
    "s_add_u32 s59, s58, 0\n"
    "buffer_load_dwordx4 v2, s[32:35], s59 offen lds\n"
    "s_add_u32 m0, s57, 17408\n"
    "s_add_u32 s59, s58, 32768\n"
    "buffer_load_dwordx4 v2, s[32:35], s59 offen lds\n"
    "s_add_u32 m0, s57, 18432\n"
    "s_add_u32 s59, s58, 16384\n"
    "buffer_load_dwordx4 v2, s[32:35], s59 offen lds\n"
    "s_add_u32 m0, s57, 19456\n"
    "s_add_u32 s59, s58, 49152\n"
    "buffer_load_dwordx4 v2, s[32:35], s59 offen lds\n"
    "s_add_u32 m0, s57, 20480\n"
    "s_add_u32 s59, s58, 65536\n"
    "buffer_load_dwordx4 v2, s[32:35], s59 offen lds\n"
    "s_add_u32 m0, s57, 21504\n"
    "s_add_u32 s59, s58, 98304\n"
    "buffer_load_dwordx4 v2, s[32:35], s59 offen lds\n"
    "s_add_u32 m0, s57, 22528\n"
    "s_add_u32 s59, s58, 81920\n"
    "buffer_load_dwordx4 v2, s[32:35], s59 offen lds\n"
    "s_add_u32 m0, s57, 23552\n"
    "s_add_u32 s59, s58, 114688\n"
    "buffer_load_dwordx4 v2, s[32:35], s59 offen lds\n"
    "s_waitcnt vmcnt(32)\n"
    "ds_read_b64_tr_b16 a[144:145], v4 offset:24576\n"
    "ds_read_b64_tr_b16 a[146:147], v4 offset:25600\n"
    "ds_read_b64_tr_b16 a[148:149], v4 offset:28672\n"
    "ds_read_b64_tr_b16 a[150:151], v4 offset:29696\n"
    "ds_read_b64_tr_b16 a[152:153], v5 offset:24576\n"
    "ds_read_b64_tr_b16 a[154:155], v5 offset:25600\n"
    "ds_read_b64_tr_b16 a[156:157], v5 offset:28672\n"
    "ds_read_b64_tr_b16 a[158:159], v5 offset:29696\n"
    "ds_read_b64_tr_b16 v[200:201], v6 offset:24576\n"
    "ds_read_b64_tr_b16 v[202:203], v6 offset:25600\n"
    "ds_read_b64_tr_b16 v[204:205], v6 offset:28672\n"
    "ds_read_b64_tr_b16 v[206:207], v6 offset:29696\n"
    "ds_read_b64_tr_b16 v[88:89], v7 offset:24576\n"
    "ds_read_b64_tr_b16 v[90:91], v7 offset:25600\n"
    "ds_read_b64_tr_b16 v[92:93], v7 offset:28672\n"
    "ds_read_b64_tr_b16 v[94:95], v7 offset:29696\n"
    "s_waitcnt lgkmcnt(0)\n"
    "s_xor_b32 s58, s62, 7\n"
    "s_lshl_b32 s58, s58, 18\n"
    "s_add_u32 s58, s58, 0\n"
    "s_add_u32 s58, s58, s56\n"
    "s_add_u32 m0, s57, 24576\n"
    "s_add_u32 s59, s58, 0\n"
    "buffer_load_dwordx4 v2, s[32:35], s59 offen lds\n"
    "s_add_u32 m0, s57, 25600\n"
    "s_add_u32 s59, s58, 32768\n"
    "buffer_load_dwordx4 v2, s[32:35], s59 offen lds\n"
    "s_add_u32 m0, s57, 26624\n"
    "s_add_u32 s59, s58, 16384\n"
    "buffer_load_dwordx4 v2, s[32:35], s59 offen lds\n"
    "s_add_u32 m0, s57, 27648\n"
    "s_add_u32 s59, s58, 49152\n"
    "buffer_load_dwordx4 v2, s[32:35], s59 offen lds\n"
    "s_add_u32 m0, s57, 28672\n"
    "s_add_u32 s59, s58, 65536\n"
    "buffer_load_dwordx4 v2, s[32:35], s59 offen lds\n"
    "s_add_u32 m0, s57, 29696\n"
    "s_add_u32 s59, s58, 98304\n"
    "buffer_load_dwordx4 v2, s[32:35], s59 offen lds\n"
    "s_add_u32 m0, s57, 30720\n"
    "s_add_u32 s59, s58, 81920\n"
    "buffer_load_dwordx4 v2, s[32:35], s59 offen lds\n"
    "s_add_u32 m0, s57, 31744\n"
    "s_add_u32 s59, s58, 114688\n"
    "buffer_load_dwordx4 v2, s[32:35], s59 offen lds\n"
    "s_waitcnt vmcnt(32)\n"
    "ds_read_b64_tr_b16 a[160:161], v4 offset:32768\n"
    "ds_read_b64_tr_b16 a[162:163], v4 offset:33792\n"
    "ds_read_b64_tr_b16 a[164:165], v4 offset:36864\n"
    "ds_read_b64_tr_b16 a[166:167], v4 offset:37888\n"
    "ds_read_b64_tr_b16 a[168:169], v5 offset:32768\n"
    "ds_read_b64_tr_b16 a[170:171], v5 offset:33792\n"
    "ds_read_b64_tr_b16 a[172:173], v5 offset:36864\n"
    "ds_read_b64_tr_b16 a[174:175], v5 offset:37888\n"
    "ds_read_b64_tr_b16 v[208:209], v6 offset:32768\n"
    "ds_read_b64_tr_b16 v[210:211], v6 offset:33792\n"
    "ds_read_b64_tr_b16 v[212:213], v6 offset:36864\n"
    "ds_read_b64_tr_b16 v[214:215], v6 offset:37888\n"
    "ds_read_b64_tr_b16 v[96:97], v7 offset:32768\n"
    "ds_read_b64_tr_b16 v[98:99], v7 offset:33792\n"
    "ds_read_b64_tr_b16 v[100:101], v7 offset:36864\n"
    "ds_read_b64_tr_b16 v[102:103], v7 offset:37888\n"
    "s_waitcnt lgkmcnt(0)\n"
    "s_xor_b32 s58, s62, 7\n"
    "s_lshl_b32 s58, s58, 18\n"
    "s_add_u32 s58, s58, 131072\n"
    "s_add_u32 s58, s58, s56\n"
    "s_add_u32 m0, s57, 32768\n"
    "s_add_u32 s59, s58, 0\n"
    "buffer_load_dwordx4 v2, s[32:35], s59 offen lds\n"
    "s_add_u32 m0, s57, 33792\n"
    "s_add_u32 s59, s58, 32768\n"
    "buffer_load_dwordx4 v2, s[32:35], s59 offen lds\n"
    "s_add_u32 m0, s57, 34816\n"
    "s_add_u32 s59, s58, 16384\n"
    "buffer_load_dwordx4 v2, s[32:35], s59 offen lds\n"
    "s_add_u32 m0, s57, 35840\n"
    "s_add_u32 s59, s58, 49152\n"
    "buffer_load_dwordx4 v2, s[32:35], s59 offen lds\n"
    "s_add_u32 m0, s57, 36864\n"
    "s_add_u32 s59, s58, 65536\n"
    "buffer_load_dwordx4 v2, s[32:35], s59 offen lds\n"
    "s_add_u32 m0, s57, 37888\n"
    "s_add_u32 s59, s58, 98304\n"
    "buffer_load_dwordx4 v2, s[32:35], s59 offen lds\n"
    "s_add_u32 m0, s57, 38912\n"
    "s_add_u32 s59, s58, 81920\n"
    "buffer_load_dwordx4 v2, s[32:35], s59 offen lds\n"
    "s_add_u32 m0, s57, 39936\n"
    "s_add_u32 s59, s58, 114688\n"
    "buffer_load_dwordx4 v2, s[32:35], s59 offen lds\n"
    "s_waitcnt vmcnt(32)\n"
    "ds_read_b64_tr_b16 a[176:177], v4 offset:0\n"
    "ds_read_b64_tr_b16 a[178:179], v4 offset:1024\n"
    "ds_read_b64_tr_b16 a[180:181], v4 offset:4096\n"
    "ds_read_b64_tr_b16 a[182:183], v4 offset:5120\n"
    "ds_read_b64_tr_b16 a[184:185], v5 offset:0\n"
    "ds_read_b64_tr_b16 a[186:187], v5 offset:1024\n"
    "ds_read_b64_tr_b16 a[188:189], v5 offset:4096\n"
    "ds_read_b64_tr_b16 a[190:191], v5 offset:5120\n"
    "ds_read_b64_tr_b16 v[216:217], v6 offset:0\n"
    "ds_read_b64_tr_b16 v[218:219], v6 offset:1024\n"
    "ds_read_b64_tr_b16 v[220:221], v6 offset:4096\n"
    "ds_read_b64_tr_b16 v[222:223], v6 offset:5120\n"
    "ds_read_b64_tr_b16 v[104:105], v7 offset:0\n"
    "ds_read_b64_tr_b16 v[106:107], v7 offset:1024\n"
    "ds_read_b64_tr_b16 v[108:109], v7 offset:4096\n"
    "ds_read_b64_tr_b16 v[110:111], v7 offset:5120\n"
    "s_waitcnt lgkmcnt(0)\n"
    "ds_write_b128 v9, v[16:19] offset:0\n"
    "ds_write_b128 v9, v[20:23] offset:1024\n"
    "ds_write_b128 v9, v[24:27] offset:2048\n"
    "ds_write_b128 v9, v[28:31] offset:3072\n"
    "ds_write_b128 v9, v[32:35] offset:4096\n"
    "ds_write_b128 v9, v[36:39] offset:5120\n"
    "ds_write_b128 v9, v[40:43] offset:6144\n"
    "ds_write_b128 v9, v[44:47] offset:7168\n"
    "s_waitcnt lgkmcnt(0)\n"
    "s_waitcnt vmcnt(24)\n"
    "ds_read_b64_tr_b16 a[192:193], v4 offset:8192\n"
    "ds_read_b64_tr_b16 a[194:195], v4 offset:9216\n"
    "ds_read_b64_tr_b16 a[196:197], v4 offset:12288\n"
    "ds_read_b64_tr_b16 a[198:199], v4 offset:13312\n"
    "ds_read_b64_tr_b16 a[200:201], v5 offset:8192\n"
    "ds_read_b64_tr_b16 a[202:203], v5 offset:9216\n"
    "ds_read_b64_tr_b16 a[204:205], v5 offset:12288\n"
    "ds_read_b64_tr_b16 a[206:207], v5 offset:13312\n"
    "ds_read_b64_tr_b16 v[224:225], v6 offset:8192\n"
    "ds_read_b64_tr_b16 v[226:227], v6 offset:9216\n"
    "ds_read_b64_tr_b16 v[228:229], v6 offset:12288\n"
    "ds_read_b64_tr_b16 v[230:231], v6 offset:13312\n"
    "ds_read_b64_tr_b16 v[16:17], v7 offset:8192\n"
    "ds_read_b64_tr_b16 v[18:19], v7 offset:9216\n"
    "ds_read_b64_tr_b16 v[20:21], v7 offset:12288\n"
    "ds_read_b64_tr_b16 v[22:23], v7 offset:13312\n"
    "s_waitcnt lgkmcnt(0)\n"
    "ds_write_b128 v9, v[48:51] offset:8192\n"
    "ds_write_b128 v9, v[52:55] offset:9216\n"
    "ds_write_b128 v9, v[56:59] offset:10240\n"
    "ds_write_b128 v9, v[60:63] offset:11264\n"
    "ds_write_b128 v9, v[64:67] offset:12288\n"
    "ds_write_b128 v9, v[68:71] offset:13312\n"
    "ds_write_b128 v9, v[72:75] offset:14336\n"
    "ds_write_b128 v9, v[76:79] offset:15360\n"
    "s_waitcnt lgkmcnt(0)\n"
    "s_waitcnt vmcnt(16)\n"
    "ds_read_b64_tr_b16 a[208:209], v4 offset:16384\n"
    "ds_read_b64_tr_b16 a[210:211], v4 offset:17408\n"
    "ds_read_b64_tr_b16 a[212:213], v4 offset:20480\n"
    "ds_read_b64_tr_b16 a[214:215], v4 offset:21504\n"
    "ds_read_b64_tr_b16 a[216:217], v5 offset:16384\n"
    "ds_read_b64_tr_b16 a[218:219], v5 offset:17408\n"
    "ds_read_b64_tr_b16 a[220:221], v5 offset:20480\n"
    "ds_read_b64_tr_b16 a[222:223], v5 offset:21504\n"
    "ds_read_b64_tr_b16 v[232:233], v6 offset:16384\n"
    "ds_read_b64_tr_b16 v[234:235], v6 offset:17408\n"
    "ds_read_b64_tr_b16 v[236:237], v6 offset:20480\n"
    "ds_read_b64_tr_b16 v[238:239], v6 offset:21504\n"
    "ds_read_b64_tr_b16 v[24:25], v7 offset:16384\n"
    "ds_read_b64_tr_b16 v[26:27], v7 offset:17408\n"
    "ds_read_b64_tr_b16 v[28:29], v7 offset:20480\n"
    "ds_read_b64_tr_b16 v[30:31], v7 offset:21504\n"
    "s_waitcnt lgkmcnt(0)\n"
    "ds_write_b128 v9, v[80:83] offset:16384\n"
    "ds_write_b128 v9, v[84:87] offset:17408\n"
    "ds_write_b128 v9, v[88:91] offset:18432\n"
    "ds_write_b128 v9, v[92:95] offset:19456\n"
    "ds_write_b128 v9, v[96:99] offset:20480\n"
    "ds_write_b128 v9, v[100:103] offset:21504\n"
    "ds_write_b128 v9, v[104:107] offset:22528\n"
    "ds_write_b128 v9, v[108:111] offset:23552\n"
    "s_waitcnt lgkmcnt(0)\n"
    "s_waitcnt vmcnt(8)\n"
    "ds_read_b64_tr_b16 a[224:225], v4 offset:24576\n"
    "ds_read_b64_tr_b16 a[226:227], v4 offset:25600\n"
    "ds_read_b64_tr_b16 a[228:229], v4 offset:28672\n"
    "ds_read_b64_tr_b16 a[230:231], v4 offset:29696\n"
    "ds_read_b64_tr_b16 a[232:233], v5 offset:24576\n"
    "ds_read_b64_tr_b16 a[234:235], v5 offset:25600\n"
    "ds_read_b64_tr_b16 a[236:237], v5 offset:28672\n"
    "ds_read_b64_tr_b16 a[238:239], v5 offset:29696\n"
    "ds_read_b64_tr_b16 v[240:241], v6 offset:24576\n"
    "ds_read_b64_tr_b16 v[242:243], v6 offset:25600\n"
    "ds_read_b64_tr_b16 v[244:245], v6 offset:28672\n"
    "ds_read_b64_tr_b16 v[246:247], v6 offset:29696\n"
    "ds_read_b64_tr_b16 v[32:33], v7 offset:24576\n"
    "ds_read_b64_tr_b16 v[34:35], v7 offset:25600\n"
    "ds_read_b64_tr_b16 v[36:37], v7 offset:28672\n"
    "ds_read_b64_tr_b16 v[38:39], v7 offset:29696\n"
    "s_waitcnt lgkmcnt(0)\n"
    "ds_write_b128 v9, v[16:19] offset:24576\n"
    "ds_write_b128 v9, v[20:23] offset:25600\n"
    "ds_write_b128 v9, v[24:27] offset:26624\n"
    "ds_write_b128 v9, v[28:31] offset:27648\n"
    "ds_write_b128 v9, v[32:35] offset:28672\n"
    "ds_write_b128 v9, v[36:39] offset:29696\n"
    "s_waitcnt lgkmcnt(0)\n"
    "s_waitcnt vmcnt(0)\n"
    "ds_read_b64_tr_b16 a[240:241], v4 offset:32768\n"
    "ds_read_b64_tr_b16 a[242:243], v4 offset:33792\n"
    "ds_read_b64_tr_b16 a[244:245], v4 offset:36864\n"
    "ds_read_b64_tr_b16 a[246:247], v4 offset:37888\n"
    "ds_read_b64_tr_b16 a[248:249], v5 offset:32768\n"
    "ds_read_b64_tr_b16 a[250:251], v5 offset:33792\n"
    "ds_read_b64_tr_b16 a[252:253], v5 offset:36864\n"
    "ds_read_b64_tr_b16 a[254:255], v5 offset:37888\n"
    "ds_read_b64_tr_b16 v[248:249], v6 offset:32768\n"
    "ds_read_b64_tr_b16 v[250:251], v6 offset:33792\n"
    "ds_read_b64_tr_b16 v[252:253], v6 offset:36864\n"
    "ds_read_b64_tr_b16 v[254:255], v6 offset:37888\n"
    "ds_read_b64_tr_b16 v[40:41], v7 offset:32768\n"
    "ds_read_b64_tr_b16 v[42:43], v7 offset:33792\n"
    "ds_read_b64_tr_b16 v[44:45], v7 offset:36864\n"
    "ds_read_b64_tr_b16 v[46:47], v7 offset:37888\n"
    "s_waitcnt lgkmcnt(0)\n"
    "ds_write_b128 v9, v[40:43] offset:30720\n"
    "ds_write_b128 v9, v[44:47] offset:31744\n"
    "s_waitcnt lgkmcnt(0)\n"
    "s_lshr_b32 s58, s25, 1\n"
    "s_lshl_b32 s58, s58, 12\n"
    "s_and_b32 s59, s25, 1\n"
    "s_lshl_b32 s59, s59, 7\n"
    "s_add_u32 s58, s58, s59\n"
    "s_add_u32 s58, s58, 32768\n"
    "v_bfe_u32 v10, v1, 1, 2\n"
    "v_lshlrev_b32 v10, 10, v10\n"
    "v_bfe_u32 v3, v1, 0, 1\n"
    "v_lshl_add_u32 v10, v3, 9, v10\n"
    "v_bfe_u32 v3, v1, 3, 3\n"
    "v_lshl_add_u32 v10, v3, 4, v10\n"
    "v_add_u32 v10, s58, v10\n"
    "v_bfe_u32 v3, v1, 0, 3\n"
    "v_lshlrev_b32 v3, 4, v3\n"
    "v_xor_b32 v10, v10, v3\n"
    "v_add_u32 v11, 81920, v10\n"
    "v_bfe_u32 v3, v1, 5, 1\n"
    "v_lshlrev_b32 v3, 4, v3\n"
    "v_xor_b32 v15, v9, v3\n"
    "s_lshl_b32 s58, s25, 15\n"
    "s_lshl_b32 s59, s87, 7\n"
    "s_add_u32 s58, s58, s59\n"
    "s_add_u32 s58, s58, 2048\n"
    "v_bfe_u32 v12, v1, 3, 3\n"
    "v_lshlrev_b32 v12, 12, v12\n"
    "v_bfe_u32 v3, v1, 0, 3\n"
    "v_lshl_add_u32 v12, v3, 4, v12\n"
    "v_add_u32 v12, s58, v12\n"
    "s_lshl_b32 s58, s25, 10\n"
    "s_add_u32 s58, s58, 4096\n"
    "v_bfe_u32 v13, v1, 2, 3\n"
    "v_lshlrev_b32 v13, 7, v13\n"
    "v_bfe_u32 v3, v1, 0, 2\n"
    "v_lshl_add_u32 v13, v3, 2, v13\n"
    "v_add_u32 v13, s58, v13\n"
    "s_mov_b32 s60, s86\n"
    "s_lshl_b32 s62, s62, 13\n"
    "s_lshl_b32 s58, s25, 16\n"
    "s_add_u32 s62, s62, s58\n"
    ".Lgx_oseg_%=:\n"
    "s_mov_b32 exec_hi, 0\n"
    ".Lgx_opoll2_%=:\n"
    "buffer_load_dword v14, v13, s[44:47], 0 offen sc1\n"
    "s_waitcnt vmcnt(0)\n"
    "v_cmp_ge_u32 vcc, s60, v14\n"
    "s_cbranch_vccz .Lgx_ook2_%=\n"
    "s_sleep 4\n"
    "s_sub_u32 s26, s26, 1\n"
    "s_cbranch_scc0 .Lgx_opoll2_%=\n"
    "s_endpgm\n"
    ".Lgx_ook2_%=:\n"
    "s_mov_b64 exec, -1\n"
    "s_lshl_b32 s64, s60, 18\n"
    "s_add_u32 s64, s64, s62\n"
    "s_xor_b32 s58, s64, 0\n"
    "s_add_u32 s59, s58, 4096\n"
    "buffer_load_dwordx4 v[64:67], v8, s[48:51], s58 offen offset:0 sc1\n"
    "buffer_load_dwordx4 v[68:71], v8, s[48:51], s59 offen offset:0 sc1\n"
    "s_nop 15\n"
    "buffer_load_dwordx4 v[72:75], v8, s[48:51], s58 offen offset:1024 sc1\n"
    "buffer_load_dwordx4 v[76:79], v8, s[48:51], s59 offen offset:1024 sc1\n"
    "s_nop 15\n"
    "buffer_load_dwordx4 v[80:83], v8, s[48:51], s58 offen offset:2048 sc1\n"
    "buffer_load_dwordx4 v[84:87], v8, s[48:51], s59 offen offset:2048 sc1\n"
    "s_nop 15\n"
    "buffer_load_dwordx4 v[88:91], v8, s[48:51], s58 offen offset:3072 sc1\n"
    "buffer_load_dwordx4 v[92:95], v8, s[48:51], s59 offen offset:3072 sc1\n"
    "s_nop 15\n"
    "s_xor_b32 s58, s64, 8192\n"
    "s_add_u32 s59, s58, 4096\n"
    "buffer_load_dwordx4 v[96:99], v8, s[48:51], s58 offen offset:0 sc1\n"
    "buffer_load_dwordx4 v[100:103], v8, s[48:51], s59 offen offset:0 sc1\n"
    "s_nop 15\n"
    "buffer_load_dwordx4 v[104:107], v8, s[48:51], s58 offen offset:1024 sc1\n"
    "buffer_load_dwordx4 v[108:111], v8, s[48:51], s59 offen offset:1024 sc1\n"
    "s_nop 15\n"
    "buffer_load_dwordx4 v[112:115], v8, s[48:51], s58 offen offset:2048 sc1\n"
    "buffer_load_dwordx4 v[116:119], v8, s[48:51], s59 offen offset:2048 sc1\n"
    "s_nop 15\n"
    "buffer_load_dwordx4 v[120:123], v8, s[48:51], s58 offen offset:3072 sc1\n"
    "buffer_load_dwordx4 v[124:127], v8, s[48:51], s59 offen offset:3072 sc1\n"
    "s_nop 15\n"
    ".Lgx_ogo_%=:\n"
    "ds_read_b128 v[48:51], v9 offset:0\n"
    "ds_read_b128 v[52:55], v9 offset:1024\n"
    "ds_read_b128 v[56:59], v9 offset:2048\n"
    "s_mov_b32 s61, s64\n"
    "s_add_u32 s63, s60, 6\n"
    "s_waitcnt vmcnt(15)\n"
    "v_mfma_f32_16x16x32_bf16 v[16:19], a[0:3], v[64:67], 0\n"
    "v_mfma_f32_16x16x32_bf16 v[20:23], a[8:11], v[64:67], 0\n"
    "s_xor_b32 s58, s61, 16384\n"
    "s_add_u32 s59, s58, 4096\n"
    "ds_read_b128 v[60:63], v9 offset:3072\n"
    "v_mfma_f32_16x16x32_bf16 v[24:27], v[128:131], v[64:67], 0\n"
    "s_waitcnt lgkmcnt(3)\n"
    "v_mfma_f32_16x16x32_bf16 v[28:31], v[48:51], v[64:67], 0\n"
    "s_waitcnt vmcnt(14)\n"
    "v_mfma_f32_16x16x32_bf16 v[32:35], a[0:3], v[68:71], 0\n"
    "buffer_load_dwordx4 v[64:67], v8, s[48:51], s58 offen offset:0 sc1\n"
    "v_mfma_f32_16x16x32_bf16 v[36:39], a[8:11], v[68:71], 0\n"
    "v_mfma_f32_16x16x32_bf16 v[40:43], v[128:131], v[68:71], 0\n"
    "v_mfma_f32_16x16x32_bf16 v[44:47], v[48:51], v[68:71], 0\n"
    "s_waitcnt vmcnt(14)\n"
    "v_mfma_f32_16x16x32_bf16 v[16:19], a[4:7], v[72:75], v[16:19]\n"
    "buffer_load_dwordx4 v[68:71], v8, s[48:51], s59 offen offset:0 sc1\n"
    "v_mfma_f32_16x16x32_bf16 v[20:23], a[12:15], v[72:75], v[20:23]\n"
    "ds_read_b128 v[48:51], v9 offset:4096\n"
    "v_mfma_f32_16x16x32_bf16 v[24:27], v[132:135], v[72:75], v[24:27]\n"
    "s_waitcnt lgkmcnt(3)\n"
    "v_mfma_f32_16x16x32_bf16 v[28:31], v[52:55], v[72:75], v[28:31]\n"
    "s_waitcnt vmcnt(14)\n"
    "v_mfma_f32_16x16x32_bf16 v[32:35], a[4:7], v[76:79], v[32:35]\n"
    "buffer_load_dwordx4 v[72:75], v8, s[48:51], s58 offen offset:1024 sc1\n"
    "v_mfma_f32_16x16x32_bf16 v[36:39], a[12:15], v[76:79], v[36:39]\n"
    "v_mfma_f32_16x16x32_bf16 v[40:43], v[132:135], v[76:79], v[40:43]\n"
    "v_mfma_f32_16x16x32_bf16 v[44:47], v[52:55], v[76:79], v[44:47]\n"
    "s_waitcnt vmcnt(14)\n"
    "v_mfma_f32_16x16x32_bf16 v[16:19], a[16:19], v[80:83], v[16:19]\n"
    "buffer_load_dwordx4 v[76:79], v8, s[48:51], s59 offen offset:1024 sc1\n"
    "v_mfma_f32_16x16x32_bf16 v[20:23], a[24:27], v[80:83], v[20:23]\n"
    "ds_read_b128 v[52:55], v9 offset:5120\n"
    "v_mfma_f32_16x16x32_bf16 v[24:27], v[136:139], v[80:83], v[24:27]\n"
    "s_waitcnt lgkmcnt(3)\n"
    "v_mfma_f32_16x16x32_bf16 v[28:31], v[56:59], v[80:83], v[28:31]\n"
    "s_waitcnt vmcnt(14)\n"
    "v_mfma_f32_16x16x32_bf16 v[32:35], a[16:19], v[84:87], v[32:35]\n"
    "buffer_load_dwordx4 v[80:83], v8, s[48:51], s58 offen offset:2048 sc1\n"
    "v_mfma_f32_16x16x32_bf16 v[36:39], a[24:27], v[84:87], v[36:39]\n"
    "v_mfma_f32_16x16x32_bf16 v[40:43], v[136:139], v[84:87], v[40:43]\n"
    "v_mfma_f32_16x16x32_bf16 v[44:47], v[56:59], v[84:87], v[44:47]\n"
    "s_waitcnt vmcnt(14)\n"
    "v_mfma_f32_16x16x32_bf16 v[16:19], a[20:23], v[88:91], v[16:19]\n"
    "buffer_load_dwordx4 v[84:87], v8, s[48:51], s59 offen offset:2048 sc1\n"
    "v_mfma_f32_16x16x32_bf16 v[20:23], a[28:31], v[88:91], v[20:23]\n"
    "ds_read_b128 v[56:59], v9 offset:6144\n"
    "v_mfma_f32_16x16x32_bf16 v[24:27], v[140:143], v[88:91], v[24:27]\n"
    "s_waitcnt lgkmcnt(3)\n"
    "v_mfma_f32_16x16x32_bf16 v[28:31], v[60:63], v[88:91], v[28:31]\n"
    "s_waitcnt vmcnt(14)\n"
    "v_mfma_f32_16x16x32_bf16 v[32:35], a[20:23], v[92:95], v[32:35]\n"
    "buffer_load_dwordx4 v[88:91], v8, s[48:51], s58 offen offset:3072 sc1\n"
    "v_mfma_f32_16x16x32_bf16 v[36:39], a[28:31], v[92:95], v[36:39]\n"
    "v_mfma_f32_16x16x32_bf16 v[40:43], v[140:143], v[92:95], v[40:43]\n"
    "v_mfma_f32_16x16x32_bf16 v[44:47], v[60:63], v[92:95], v[44:47]\n"
    "s_waitcnt vmcnt(14)\n"
    "v_mfma_f32_16x16x32_bf16 v[16:19], a[32:35], v[96:99], v[16:19]\n"
    "buffer_load_dwordx4 v[92:95], v8, s[48:51], s59 offen offset:3072 sc1\n"
    "v_mfma_f32_16x16x32_bf16 v[20:23], a[40:43], v[96:99], v[20:23]\n"
    "s_xor_b32 s58, s61, 24576\n"
    "s_add_u32 s59, s58, 4096\n"
    "ds_read_b128 v[60:63], v9 offset:7168\n"
    "v_mfma_f32_16x16x32_bf16 v[24:27], v[144:147], v[96:99], v[24:27]\n"
    "s_waitcnt lgkmcnt(3)\n"
    "v_mfma_f32_16x16x32_bf16 v[28:31], v[48:51], v[96:99], v[28:31]\n"
    "s_waitcnt vmcnt(14)\n"
    "v_mfma_f32_16x16x32_bf16 v[32:35], a[32:35], v[100:103], v[32:35]\n"
    "buffer_load_dwordx4 v[96:99], v8, s[48:51], s58 offen offset:0 sc1\n"
    "v_mfma_f32_16x16x32_bf16 v[36:39], a[40:43], v[100:103], v[36:39]\n"
    "v_mfma_f32_16x16x32_bf16 v[40:43], v[144:147], v[100:103], v[40:43]\n"
    "v_mfma_f32_16x16x32_bf16 v[44:47], v[48:51], v[100:103], v[44:47]\n"
    "s_waitcnt vmcnt(14)\n"
    "v_mfma_f32_16x16x32_bf16 v[16:19], a[36:39], v[104:107], v[16:19]\n"
    "buffer_load_dwordx4 v[100:103], v8, s[48:51], s59 offen offset:0 sc1\n"
    "v_mfma_f32_16x16x32_bf16 v[20:23], a[44:47], v[104:107], v[20:23]\n"
    "ds_read_b128 v[48:51], v9 offset:8192\n"
    "v_mfma_f32_16x16x32_bf16 v[24:27], v[148:151], v[104:107], v[24:27]\n"
    "s_waitcnt lgkmcnt(3)\n"
    "v_mfma_f32_16x16x32_bf16 v[28:31], v[52:55], v[104:107], v[28:31]\n"
    "s_waitcnt vmcnt(14)\n"
    "v_mfma_f32_16x16x32_bf16 v[32:35], a[36:39], v[108:111], v[32:35]\n"
    "buffer_load_dwordx4 v[104:107], v8, s[48:51], s58 offen offset:1024 sc1\n"
    "v_mfma_f32_16x16x32_bf16 v[36:39], a[44:47], v[108:111], v[36:39]\n"
    "v_mfma_f32_16x16x32_bf16 v[40:43], v[148:151], v[108:111], v[40:43]\n"
    "v_mfma_f32_16x16x32_bf16 v[44:47], v[52:55], v[108:111], v[44:47]\n"
    "s_waitcnt vmcnt(14)\n"
    "v_mfma_f32_16x16x32_bf16 v[16:19], a[48:51], v[112:115], v[16:19]\n"
    "buffer_load_dwordx4 v[108:111], v8, s[48:51], s59 offen offset:1024 sc1\n"
    "v_mfma_f32_16x16x32_bf16 v[20:23], a[56:59], v[112:115], v[20:23]\n"
    "ds_read_b128 v[52:55], v9 offset:9216\n"
    "v_mfma_f32_16x16x32_bf16 v[24:27], v[152:155], v[112:115], v[24:27]\n"
    "s_waitcnt lgkmcnt(3)\n"
    "v_mfma_f32_16x16x32_bf16 v[28:31], v[56:59], v[112:115], v[28:31]\n"
    "s_waitcnt vmcnt(14)\n"
    "v_mfma_f32_16x16x32_bf16 v[32:35], a[48:51], v[116:119], v[32:35]\n"
    "buffer_load_dwordx4 v[112:115], v8, s[48:51], s58 offen offset:2048 sc1\n"
    "v_mfma_f32_16x16x32_bf16 v[36:39], a[56:59], v[116:119], v[36:39]\n"
    "v_mfma_f32_16x16x32_bf16 v[40:43], v[152:155], v[116:119], v[40:43]\n"
    "v_mfma_f32_16x16x32_bf16 v[44:47], v[56:59], v[116:119], v[44:47]\n"
    "s_waitcnt vmcnt(14)\n"
    "v_mfma_f32_16x16x32_bf16 v[16:19], a[52:55], v[120:123], v[16:19]\n"
    "buffer_load_dwordx4 v[116:119], v8, s[48:51], s59 offen offset:2048 sc1\n"
    "v_mfma_f32_16x16x32_bf16 v[20:23], a[60:63], v[120:123], v[20:23]\n"
    "ds_read_b128 v[56:59], v9 offset:10240\n"
    "v_mfma_f32_16x16x32_bf16 v[24:27], v[156:159], v[120:123], v[24:27]\n"
    "s_waitcnt lgkmcnt(3)\n"
    "v_mfma_f32_16x16x32_bf16 v[28:31], v[60:63], v[120:123], v[28:31]\n"
    "s_waitcnt vmcnt(14)\n"
    "v_mfma_f32_16x16x32_bf16 v[32:35], a[52:55], v[124:127], v[32:35]\n"
    "buffer_load_dwordx4 v[120:123], v8, s[48:51], s58 offen offset:3072 sc1\n"
    "v_mfma_f32_16x16x32_bf16 v[36:39], a[60:63], v[124:127], v[36:39]\n"
    "v_mfma_f32_16x16x32_bf16 v[40:43], v[156:159], v[124:127], v[40:43]\n"
    "v_mfma_f32_16x16x32_bf16 v[44:47], v[60:63], v[124:127], v[44:47]\n"
    "s_waitcnt vmcnt(14)\n"
    "v_mfma_f32_16x16x32_bf16 v[16:19], a[64:67], v[64:67], v[16:19]\n"
    "buffer_load_dwordx4 v[124:127], v8, s[48:51], s59 offen offset:3072 sc1\n"
    "v_mfma_f32_16x16x32_bf16 v[20:23], a[72:75], v[64:67], v[20:23]\n"
    "s_xor_b32 s58, s61, 32768\n"
    "s_add_u32 s59, s58, 4096\n"
    "ds_read_b128 v[60:63], v9 offset:11264\n"
    "v_mfma_f32_16x16x32_bf16 v[24:27], v[160:163], v[64:67], v[24:27]\n"
    "s_waitcnt lgkmcnt(3)\n"
    "v_mfma_f32_16x16x32_bf16 v[28:31], v[48:51], v[64:67], v[28:31]\n"
    "s_waitcnt vmcnt(14)\n"
    "v_mfma_f32_16x16x32_bf16 v[32:35], a[64:67], v[68:71], v[32:35]\n"
    "buffer_load_dwordx4 v[64:67], v8, s[48:51], s58 offen offset:0 sc1\n"
    "v_mfma_f32_16x16x32_bf16 v[36:39], a[72:75], v[68:71], v[36:39]\n"
    "v_mfma_f32_16x16x32_bf16 v[40:43], v[160:163], v[68:71], v[40:43]\n"
    "v_mfma_f32_16x16x32_bf16 v[44:47], v[48:51], v[68:71], v[44:47]\n"
    "s_waitcnt vmcnt(14)\n"
    "v_mfma_f32_16x16x32_bf16 v[16:19], a[68:71], v[72:75], v[16:19]\n"
    "buffer_load_dwordx4 v[68:71], v8, s[48:51], s59 offen offset:0 sc1\n"
    "v_mfma_f32_16x16x32_bf16 v[20:23], a[76:79], v[72:75], v[20:23]\n"
    "ds_read_b128 v[48:51], v9 offset:12288\n"
    "v_mfma_f32_16x16x32_bf16 v[24:27], v[164:167], v[72:75], v[24:27]\n"
    "s_waitcnt lgkmcnt(3)\n"
    "v_mfma_f32_16x16x32_bf16 v[28:31], v[52:55], v[72:75], v[28:31]\n"
    "s_waitcnt vmcnt(14)\n"
    "v_mfma_f32_16x16x32_bf16 v[32:35], a[68:71], v[76:79], v[32:35]\n"
    "buffer_load_dwordx4 v[72:75], v8, s[48:51], s58 offen offset:1024 sc1\n"
    "v_mfma_f32_16x16x32_bf16 v[36:39], a[76:79], v[76:79], v[36:39]\n"
    "v_mfma_f32_16x16x32_bf16 v[40:43], v[164:167], v[76:79], v[40:43]\n"
    "v_mfma_f32_16x16x32_bf16 v[44:47], v[52:55], v[76:79], v[44:47]\n"
    "s_waitcnt vmcnt(14)\n"
    "v_mfma_f32_16x16x32_bf16 v[16:19], a[80:83], v[80:83], v[16:19]\n"
    "buffer_load_dwordx4 v[76:79], v8, s[48:51], s59 offen offset:1024 sc1\n"
    "v_mfma_f32_16x16x32_bf16 v[20:23], a[88:91], v[80:83], v[20:23]\n"
    "ds_read_b128 v[52:55], v9 offset:13312\n"
    "v_mfma_f32_16x16x32_bf16 v[24:27], v[168:171], v[80:83], v[24:27]\n"
    "s_waitcnt lgkmcnt(3)\n"
    "v_mfma_f32_16x16x32_bf16 v[28:31], v[56:59], v[80:83], v[28:31]\n"
    "s_waitcnt vmcnt(14)\n"
    "v_mfma_f32_16x16x32_bf16 v[32:35], a[80:83], v[84:87], v[32:35]\n"
    "buffer_load_dwordx4 v[80:83], v8, s[48:51], s58 offen offset:2048 sc1\n"
    "v_mfma_f32_16x16x32_bf16 v[36:39], a[88:91], v[84:87], v[36:39]\n"
    "v_mfma_f32_16x16x32_bf16 v[40:43], v[168:171], v[84:87], v[40:43]\n"
    "v_mfma_f32_16x16x32_bf16 v[44:47], v[56:59], v[84:87], v[44:47]\n"
    "s_waitcnt vmcnt(14)\n"
    "v_mfma_f32_16x16x32_bf16 v[16:19], a[84:87], v[88:91], v[16:19]\n"
    "buffer_load_dwordx4 v[84:87], v8, s[48:51], s59 offen offset:2048 sc1\n"
    "v_mfma_f32_16x16x32_bf16 v[20:23], a[92:95], v[88:91], v[20:23]\n"
    "ds_read_b128 v[56:59], v9 offset:14336\n"
    "v_mfma_f32_16x16x32_bf16 v[24:27], v[172:175], v[88:91], v[24:27]\n"
    "s_waitcnt lgkmcnt(3)\n"
    "v_mfma_f32_16x16x32_bf16 v[28:31], v[60:63], v[88:91], v[28:31]\n"
    "s_waitcnt vmcnt(14)\n"
    "v_mfma_f32_16x16x32_bf16 v[32:35], a[84:87], v[92:95], v[32:35]\n"
    "buffer_load_dwordx4 v[88:91], v8, s[48:51], s58 offen offset:3072 sc1\n"
    "v_mfma_f32_16x16x32_bf16 v[36:39], a[92:95], v[92:95], v[36:39]\n"
    "v_mfma_f32_16x16x32_bf16 v[40:43], v[172:175], v[92:95], v[40:43]\n"
    "v_mfma_f32_16x16x32_bf16 v[44:47], v[60:63], v[92:95], v[44:47]\n"
    "s_waitcnt vmcnt(14)\n"
    "v_mfma_f32_16x16x32_bf16 v[16:19], a[96:99], v[96:99], v[16:19]\n"
    "buffer_load_dwordx4 v[92:95], v8, s[48:51], s59 offen offset:3072 sc1\n"
    "v_mfma_f32_16x16x32_bf16 v[20:23], a[104:107], v[96:99], v[20:23]\n"
    "s_xor_b32 s58, s61, 40960\n"
    "s_add_u32 s59, s58, 4096\n"
    "ds_read_b128 v[60:63], v9 offset:15360\n"
    "v_mfma_f32_16x16x32_bf16 v[24:27], v[176:179], v[96:99], v[24:27]\n"
    "s_waitcnt lgkmcnt(3)\n"
    "v_mfma_f32_16x16x32_bf16 v[28:31], v[48:51], v[96:99], v[28:31]\n"
    "s_waitcnt vmcnt(14)\n"
    "v_mfma_f32_16x16x32_bf16 v[32:35], a[96:99], v[100:103], v[32:35]\n"
    "buffer_load_dwordx4 v[96:99], v8, s[48:51], s58 offen offset:0 sc1\n"
    "v_mfma_f32_16x16x32_bf16 v[36:39], a[104:107], v[100:103], v[36:39]\n"
    "v_mfma_f32_16x16x32_bf16 v[40:43], v[176:179], v[100:103], v[40:43]\n"
    "v_mfma_f32_16x16x32_bf16 v[44:47], v[48:51], v[100:103], v[44:47]\n"
    "s_waitcnt vmcnt(14)\n"
    "v_mfma_f32_16x16x32_bf16 v[16:19], a[100:103], v[104:107], v[16:19]\n"
    "buffer_load_dwordx4 v[100:103], v8, s[48:51], s59 offen offset:0 sc1\n"
    "v_mfma_f32_16x16x32_bf16 v[20:23], a[108:111], v[104:107], v[20:23]\n"
    "ds_read_b128 v[48:51], v9 offset:16384\n"
    "v_mfma_f32_16x16x32_bf16 v[24:27], v[180:183], v[104:107], v[24:27]\n"
    "s_waitcnt lgkmcnt(3)\n"
    "v_mfma_f32_16x16x32_bf16 v[28:31], v[52:55], v[104:107], v[28:31]\n"
    "s_waitcnt vmcnt(14)\n"
    "v_mfma_f32_16x16x32_bf16 v[32:35], a[100:103], v[108:111], v[32:35]\n"
    "buffer_load_dwordx4 v[104:107], v8, s[48:51], s58 offen offset:1024 sc1\n"
    "v_mfma_f32_16x16x32_bf16 v[36:39], a[108:111], v[108:111], v[36:39]\n"
    "v_mfma_f32_16x16x32_bf16 v[40:43], v[180:183], v[108:111], v[40:43]\n"
    "buffer_load_dword v14, v13, s[44:47], 0 offen sc1\n"
    "v_mfma_f32_16x16x32_bf16 v[44:47], v[52:55], v[108:111], v[44:47]\n"
    "s_waitcnt vmcnt(15)\n"
    "v_mfma_f32_16x16x32_bf16 v[16:19], a[112:115], v[112:115], v[16:19]\n"
    "buffer_load_dwordx4 v[108:111], v8, s[48:51], s59 offen offset:1024 sc1\n"
    "v_mfma_f32_16x16x32_bf16 v[20:23], a[120:123], v[112:115], v[20:23]\n"
    "ds_read_b128 v[52:55], v9 offset:17408\n"
    "v_mfma_f32_16x16x32_bf16 v[24:27], v[184:187], v[112:115], v[24:27]\n"
    "s_waitcnt lgkmcnt(3)\n"
    "v_mfma_f32_16x16x32_bf16 v[28:31], v[56:59], v[112:115], v[28:31]\n"
    "s_waitcnt vmcnt(15)\n"
    "v_mfma_f32_16x16x32_bf16 v[32:35], a[112:115], v[116:119], v[32:35]\n"
    "buffer_load_dwordx4 v[112:115], v8, s[48:51], s58 offen offset:2048 sc1\n"
    "v_mfma_f32_16x16x32_bf16 v[36:39], a[120:123], v[116:119], v[36:39]\n"
    "v_mfma_f32_16x16x32_bf16 v[40:43], v[184:187], v[116:119], v[40:43]\n"
    "v_mfma_f32_16x16x32_bf16 v[44:47], v[56:59], v[116:119], v[44:47]\n"
    "s_waitcnt vmcnt(15)\n"
    "v_mfma_f32_16x16x32_bf16 v[16:19], a[116:119], v[120:123], v[16:19]\n"
    "buffer_load_dwordx4 v[116:119], v8, s[48:51], s59 offen offset:2048 sc1\n"
    "v_mfma_f32_16x16x32_bf16 v[20:23], a[124:127], v[120:123], v[20:23]\n"
    "ds_read_b128 v[56:59], v9 offset:18432\n"
    "v_mfma_f32_16x16x32_bf16 v[24:27], v[188:191], v[120:123], v[24:27]\n"
    "s_waitcnt lgkmcnt(3)\n"
    "v_mfma_f32_16x16x32_bf16 v[28:31], v[60:63], v[120:123], v[28:31]\n"
    "s_waitcnt vmcnt(15)\n"
    "v_mfma_f32_16x16x32_bf16 v[32:35], a[116:119], v[124:127], v[32:35]\n"
    "buffer_load_dwordx4 v[120:123], v8, s[48:51], s58 offen offset:3072 sc1\n"
    "v_mfma_f32_16x16x32_bf16 v[36:39], a[124:127], v[124:127], v[36:39]\n"
    "v_mfma_f32_16x16x32_bf16 v[40:43], v[188:191], v[124:127], v[40:43]\n"
    "v_mfma_f32_16x16x32_bf16 v[44:47], v[60:63], v[124:127], v[44:47]\n"
    "s_waitcnt vmcnt(15)\n"
    "v_mfma_f32_16x16x32_bf16 v[16:19], a[128:131], v[64:67], v[16:19]\n"
    "buffer_load_dwordx4 v[124:127], v8, s[48:51], s59 offen offset:3072 sc1\n"
    "v_mfma_f32_16x16x32_bf16 v[20:23], a[136:139], v[64:67], v[20:23]\n"
    "s_xor_b32 s58, s61, 49152\n"
    "s_add_u32 s59, s58, 4096\n"
    "ds_read_b128 v[60:63], v9 offset:19456\n"
    "v_mfma_f32_16x16x32_bf16 v[24:27], v[192:195], v[64:67], v[24:27]\n"
    "s_waitcnt lgkmcnt(3)\n"
    "v_mfma_f32_16x16x32_bf16 v[28:31], v[48:51], v[64:67], v[28:31]\n"
    "s_waitcnt vmcnt(15)\n"
    "v_mfma_f32_16x16x32_bf16 v[32:35], a[128:131], v[68:71], v[32:35]\n"
    "buffer_load_dwordx4 v[64:67], v8, s[48:51], s58 offen offset:0 sc1\n"
    "v_mfma_f32_16x16x32_bf16 v[36:39], a[136:139], v[68:71], v[36:39]\n"
    "v_mfma_f32_16x16x32_bf16 v[40:43], v[192:195], v[68:71], v[40:43]\n"
    "v_mfma_f32_16x16x32_bf16 v[44:47], v[48:51], v[68:71], v[44:47]\n"
    "s_waitcnt vmcnt(15)\n"
    "v_mfma_f32_16x16x32_bf16 v[16:19], a[132:135], v[72:75], v[16:19]\n"
    "buffer_load_dwordx4 v[68:71], v8, s[48:51], s59 offen offset:0 sc1\n"
    "v_mfma_f32_16x16x32_bf16 v[20:23], a[140:143], v[72:75], v[20:23]\n"
    "ds_read_b128 v[48:51], v9 offset:20480\n"
    "v_mfma_f32_16x16x32_bf16 v[24:27], v[196:199], v[72:75], v[24:27]\n"
    "s_waitcnt lgkmcnt(3)\n"
    "v_mfma_f32_16x16x32_bf16 v[28:31], v[52:55], v[72:75], v[28:31]\n"
    "s_waitcnt vmcnt(15)\n"
    "v_mfma_f32_16x16x32_bf16 v[32:35], a[132:135], v[76:79], v[32:35]\n"
    "buffer_load_dwordx4 v[72:75], v8, s[48:51], s58 offen offset:1024 sc1\n"
    "v_mfma_f32_16x16x32_bf16 v[36:39], a[140:143], v[76:79], v[36:39]\n"
    "v_mfma_f32_16x16x32_bf16 v[40:43], v[196:199], v[76:79], v[40:43]\n"
    "v_mfma_f32_16x16x32_bf16 v[44:47], v[52:55], v[76:79], v[44:47]\n"
    "s_waitcnt vmcnt(15)\n"
    "v_mfma_f32_16x16x32_bf16 v[16:19], a[144:147], v[80:83], v[16:19]\n"
    "buffer_load_dwordx4 v[76:79], v8, s[48:51], s59 offen offset:1024 sc1\n"
    "v_mfma_f32_16x16x32_bf16 v[20:23], a[152:155], v[80:83], v[20:23]\n"
    "ds_read_b128 v[52:55], v9 offset:21504\n"
    "v_mfma_f32_16x16x32_bf16 v[24:27], v[200:203], v[80:83], v[24:27]\n"
    "s_waitcnt lgkmcnt(3)\n"
    "v_mfma_f32_16x16x32_bf16 v[28:31], v[56:59], v[80:83], v[28:31]\n"
    "s_waitcnt vmcnt(15)\n"
    "v_mfma_f32_16x16x32_bf16 v[32:35], a[144:147], v[84:87], v[32:35]\n"
    "buffer_load_dwordx4 v[80:83], v8, s[48:51], s58 offen offset:2048 sc1\n"
    "v_mfma_f32_16x16x32_bf16 v[36:39], a[152:155], v[84:87], v[36:39]\n"
    "v_mfma_f32_16x16x32_bf16 v[40:43], v[200:203], v[84:87], v[40:43]\n"
    "v_mfma_f32_16x16x32_bf16 v[44:47], v[56:59], v[84:87], v[44:47]\n"
    "s_waitcnt vmcnt(15)\n"
    "v_mfma_f32_16x16x32_bf16 v[16:19], a[148:151], v[88:91], v[16:19]\n"
    "buffer_load_dwordx4 v[84:87], v8, s[48:51], s59 offen offset:2048 sc1\n"
    "v_mfma_f32_16x16x32_bf16 v[20:23], a[156:159], v[88:91], v[20:23]\n"
    "ds_read_b128 v[56:59], v9 offset:22528\n"
    "v_mfma_f32_16x16x32_bf16 v[24:27], v[204:207], v[88:91], v[24:27]\n"
    "s_waitcnt lgkmcnt(3)\n"
    "v_mfma_f32_16x16x32_bf16 v[28:31], v[60:63], v[88:91], v[28:31]\n"
    "s_waitcnt vmcnt(15)\n"
    "v_mfma_f32_16x16x32_bf16 v[32:35], a[148:151], v[92:95], v[32:35]\n"
    "buffer_load_dwordx4 v[88:91], v8, s[48:51], s58 offen offset:3072 sc1\n"
    "v_mfma_f32_16x16x32_bf16 v[36:39], a[156:159], v[92:95], v[36:39]\n"
    "v_mfma_f32_16x16x32_bf16 v[40:43], v[204:207], v[92:95], v[40:43]\n"
    "v_mfma_f32_16x16x32_bf16 v[44:47], v[60:63], v[92:95], v[44:47]\n"
    "s_waitcnt vmcnt(15)\n"
    "v_mfma_f32_16x16x32_bf16 v[16:19], a[160:163], v[96:99], v[16:19]\n"
    "buffer_load_dwordx4 v[92:95], v8, s[48:51], s59 offen offset:3072 sc1\n"
    "v_mfma_f32_16x16x32_bf16 v[20:23], a[168:171], v[96:99], v[20:23]\n"
    "s_xor_b32 s58, s61, 57344\n"
    "s_add_u32 s59, s58, 4096\n"
    "ds_read_b128 v[60:63], v9 offset:23552\n"
    "v_mfma_f32_16x16x32_bf16 v[24:27], v[208:211], v[96:99], v[24:27]\n"
    "s_waitcnt lgkmcnt(3)\n"
    "v_mfma_f32_16x16x32_bf16 v[28:31], v[48:51], v[96:99], v[28:31]\n"
    "s_waitcnt vmcnt(15)\n"
    "v_mfma_f32_16x16x32_bf16 v[32:35], a[160:163], v[100:103], v[32:35]\n"
    "buffer_load_dwordx4 v[96:99], v8, s[48:51], s58 offen offset:0 sc1\n"
    "v_mfma_f32_16x16x32_bf16 v[36:39], a[168:171], v[100:103], v[36:39]\n"
    "v_mfma_f32_16x16x32_bf16 v[40:43], v[208:211], v[100:103], v[40:43]\n"
    "v_mfma_f32_16x16x32_bf16 v[44:47], v[48:51], v[100:103], v[44:47]\n"
    "s_waitcnt vmcnt(15)\n"
    "v_mfma_f32_16x16x32_bf16 v[16:19], a[164:167], v[104:107], v[16:19]\n"
    "buffer_load_dwordx4 v[100:103], v8, s[48:51], s59 offen offset:0 sc1\n"
    "v_mfma_f32_16x16x32_bf16 v[20:23], a[172:175], v[104:107], v[20:23]\n"
    "ds_read_b128 v[48:51], v9 offset:24576\n"
    "v_mfma_f32_16x16x32_bf16 v[24:27], v[212:215], v[104:107], v[24:27]\n"
    "s_waitcnt lgkmcnt(3)\n"
    "v_mfma_f32_16x16x32_bf16 v[28:31], v[52:55], v[104:107], v[28:31]\n"
    "s_waitcnt vmcnt(14)\n"
    "v_mfma_f32_16x16x32_bf16 v[32:35], a[164:167], v[108:111], v[32:35]\n"
    "buffer_load_dwordx4 v[104:107], v8, s[48:51], s58 offen offset:1024 sc1\n"
    "v_mfma_f32_16x16x32_bf16 v[36:39], a[172:175], v[108:111], v[36:39]\n"
    "v_mfma_f32_16x16x32_bf16 v[40:43], v[212:215], v[108:111], v[40:43]\n"
    "v_mfma_f32_16x16x32_bf16 v[44:47], v[52:55], v[108:111], v[44:47]\n"
    "s_waitcnt vmcnt(14)\n"
    "v_mfma_f32_16x16x32_bf16 v[16:19], a[176:179], v[112:115], v[16:19]\n"
    "buffer_load_dwordx4 v[108:111], v8, s[48:51], s59 offen offset:1024 sc1\n"
    "v_mfma_f32_16x16x32_bf16 v[20:23], a[184:187], v[112:115], v[20:23]\n"
    "ds_read_b128 v[52:55], v9 offset:25600\n"
    "v_mfma_f32_16x16x32_bf16 v[24:27], v[216:219], v[112:115], v[24:27]\n"
    "s_waitcnt lgkmcnt(3)\n"
    "v_mfma_f32_16x16x32_bf16 v[28:31], v[56:59], v[112:115], v[28:31]\n"
    "s_waitcnt vmcnt(14)\n"
    "v_mfma_f32_16x16x32_bf16 v[32:35], a[176:179], v[116:119], v[32:35]\n"
    "buffer_load_dwordx4 v[112:115], v8, s[48:51], s58 offen offset:2048 sc1\n"
    "v_mfma_f32_16x16x32_bf16 v[36:39], a[184:187], v[116:119], v[36:39]\n"
    "v_min_u32 v14, 61, v14\n"
    "v_cmp_ge_u32 vcc, s63, v14\n"
    "v_mfma_f32_16x16x32_bf16 v[40:43], v[216:219], v[116:119], v[40:43]\n"
    "s_lshl_b32 s64, s63, 18\n"
    "s_add_u32 s64, s64, s62\n"
    "v_mfma_f32_16x16x32_bf16 v[44:47], v[56:59], v[116:119], v[44:47]\n"
    "s_waitcnt vmcnt(14)\n"
    "v_mfma_f32_16x16x32_bf16 v[16:19], a[180:183], v[120:123], v[16:19]\n"
    "buffer_load_dwordx4 v[116:119], v8, s[48:51], s59 offen offset:2048 sc1\n"
    "v_mfma_f32_16x16x32_bf16 v[20:23], a[188:191], v[120:123], v[20:23]\n"
    "ds_read_b128 v[56:59], v9 offset:26624\n"
    "v_mfma_f32_16x16x32_bf16 v[24:27], v[220:223], v[120:123], v[24:27]\n"
    "s_waitcnt lgkmcnt(3)\n"
    "v_mfma_f32_16x16x32_bf16 v[28:31], v[60:63], v[120:123], v[28:31]\n"
    "s_waitcnt vmcnt(14)\n"
    "v_mfma_f32_16x16x32_bf16 v[32:35], a[180:183], v[124:127], v[32:35]\n"
    "buffer_load_dwordx4 v[120:123], v8, s[48:51], s58 offen offset:3072 sc1\n"
    "v_mfma_f32_16x16x32_bf16 v[36:39], a[188:191], v[124:127], v[36:39]\n"
    "s_cmp_lg_u64 vcc, 0\n"
    "s_cselect_b32 s64, 0x10000000, s64\n"
    "v_mfma_f32_16x16x32_bf16 v[40:43], v[220:223], v[124:127], v[40:43]\n"
    "v_mfma_f32_16x16x32_bf16 v[44:47], v[60:63], v[124:127], v[44:47]\n"
    "s_waitcnt vmcnt(14)\n"
    "v_mfma_f32_16x16x32_bf16 v[16:19], a[192:195], v[64:67], v[16:19]\n"
    "buffer_load_dwordx4 v[124:127], v8, s[48:51], s59 offen offset:3072 sc1\n"
    "v_mfma_f32_16x16x32_bf16 v[20:23], a[200:203], v[64:67], v[20:23]\n"
    "s_xor_b32 s58, s64, 0\n"
    "s_add_u32 s59, s58, 4096\n"
    "ds_read_b128 v[60:63], v9 offset:27648\n"
    "v_mfma_f32_16x16x32_bf16 v[24:27], v[224:227], v[64:67], v[24:27]\n"
    "s_waitcnt lgkmcnt(3)\n"
    "v_mfma_f32_16x16x32_bf16 v[28:31], v[48:51], v[64:67], v[28:31]\n"
    "s_waitcnt vmcnt(14)\n"
    "v_mfma_f32_16x16x32_bf16 v[32:35], a[192:195], v[68:71], v[32:35]\n"
    "buffer_load_dwordx4 v[64:67], v8, s[48:51], s58 offen offset:0 sc1\n"
    "v_mfma_f32_16x16x32_bf16 v[36:39], a[200:203], v[68:71], v[36:39]\n"
    "v_mfma_f32_16x16x32_bf16 v[40:43], v[224:227], v[68:71], v[40:43]\n"
    "v_mfma_f32_16x16x32_bf16 v[44:47], v[48:51], v[68:71], v[44:47]\n"
    "s_waitcnt vmcnt(14)\n"
    "v_mfma_f32_16x16x32_bf16 v[16:19], a[196:199], v[72:75], v[16:19]\n"
    "buffer_load_dwordx4 v[68:71], v8, s[48:51], s59 offen offset:0 sc1\n"
    "v_mfma_f32_16x16x32_bf16 v[20:23], a[204:207], v[72:75], v[20:23]\n"
    "ds_read_b128 v[48:51], v9 offset:28672\n"
    "v_mfma_f32_16x16x32_bf16 v[24:27], v[228:231], v[72:75], v[24:27]\n"
    "s_waitcnt lgkmcnt(3)\n"
    "v_mfma_f32_16x16x32_bf16 v[28:31], v[52:55], v[72:75], v[28:31]\n"
    "s_waitcnt vmcnt(14)\n"
    "v_mfma_f32_16x16x32_bf16 v[32:35], a[196:199], v[76:79], v[32:35]\n"
    "buffer_load_dwordx4 v[72:75], v8, s[48:51], s58 offen offset:1024 sc1\n"
    "v_mfma_f32_16x16x32_bf16 v[36:39], a[204:207], v[76:79], v[36:39]\n"
    "v_mfma_f32_16x16x32_bf16 v[40:43], v[228:231], v[76:79], v[40:43]\n"
    "v_mfma_f32_16x16x32_bf16 v[44:47], v[52:55], v[76:79], v[44:47]\n"
    "s_waitcnt vmcnt(14)\n"
    "v_mfma_f32_16x16x32_bf16 v[16:19], a[208:211], v[80:83], v[16:19]\n"
    "buffer_load_dwordx4 v[76:79], v8, s[48:51], s59 offen offset:1024 sc1\n"
    "v_mfma_f32_16x16x32_bf16 v[20:23], a[216:219], v[80:83], v[20:23]\n"
    "ds_read_b128 v[52:55], v9 offset:29696\n"
    "v_mfma_f32_16x16x32_bf16 v[24:27], v[232:235], v[80:83], v[24:27]\n"
    "s_waitcnt lgkmcnt(3)\n"
    "v_mfma_f32_16x16x32_bf16 v[28:31], v[56:59], v[80:83], v[28:31]\n"
    "s_waitcnt vmcnt(14)\n"
    "v_mfma_f32_16x16x32_bf16 v[32:35], a[208:211], v[84:87], v[32:35]\n"
    "buffer_load_dwordx4 v[80:83], v8, s[48:51], s58 offen offset:2048 sc1\n"
    "v_mfma_f32_16x16x32_bf16 v[36:39], a[216:219], v[84:87], v[36:39]\n"
    "v_mfma_f32_16x16x32_bf16 v[40:43], v[232:235], v[84:87], v[40:43]\n"
    "v_mfma_f32_16x16x32_bf16 v[44:47], v[56:59], v[84:87], v[44:47]\n"
    "s_waitcnt vmcnt(14)\n"
    "v_mfma_f32_16x16x32_bf16 v[16:19], a[212:215], v[88:91], v[16:19]\n"
    "buffer_load_dwordx4 v[84:87], v8, s[48:51], s59 offen offset:2048 sc1\n"
    "v_mfma_f32_16x16x32_bf16 v[20:23], a[220:223], v[88:91], v[20:23]\n"
    "ds_read_b128 v[56:59], v9 offset:30720\n"
    "v_mfma_f32_16x16x32_bf16 v[24:27], v[236:239], v[88:91], v[24:27]\n"
    "s_waitcnt lgkmcnt(3)\n"
    "v_mfma_f32_16x16x32_bf16 v[28:31], v[60:63], v[88:91], v[28:31]\n"
    "s_waitcnt vmcnt(14)\n"
    "v_mfma_f32_16x16x32_bf16 v[32:35], a[212:215], v[92:95], v[32:35]\n"
    "buffer_load_dwordx4 v[88:91], v8, s[48:51], s58 offen offset:3072 sc1\n"
    "v_mfma_f32_16x16x32_bf16 v[36:39], a[220:223], v[92:95], v[36:39]\n"
    "v_mfma_f32_16x16x32_bf16 v[40:43], v[236:239], v[92:95], v[40:43]\n"
    "v_mfma_f32_16x16x32_bf16 v[44:47], v[60:63], v[92:95], v[44:47]\n"
    "s_waitcnt vmcnt(14)\n"
    "v_mfma_f32_16x16x32_bf16 v[16:19], a[224:227], v[96:99], v[16:19]\n"
    "buffer_load_dwordx4 v[92:95], v8, s[48:51], s59 offen offset:3072 sc1\n"
    "v_mfma_f32_16x16x32_bf16 v[20:23], a[232:235], v[96:99], v[20:23]\n"
    "s_xor_b32 s58, s64, 8192\n"
    "s_add_u32 s59, s58, 4096\n"
    "ds_read_b128 v[60:63], v9 offset:31744\n"
    "v_mfma_f32_16x16x32_bf16 v[24:27], v[240:243], v[96:99], v[24:27]\n"
    "s_waitcnt lgkmcnt(3)\n"
    "v_mfma_f32_16x16x32_bf16 v[28:31], v[48:51], v[96:99], v[28:31]\n"
    "s_waitcnt vmcnt(14)\n"
    "v_mfma_f32_16x16x32_bf16 v[32:35], a[224:227], v[100:103], v[32:35]\n"
    "buffer_load_dwordx4 v[96:99], v8, s[48:51], s58 offen offset:0 sc1\n"
    "v_mfma_f32_16x16x32_bf16 v[36:39], a[232:235], v[100:103], v[36:39]\n"
    "v_mfma_f32_16x16x32_bf16 v[40:43], v[240:243], v[100:103], v[40:43]\n"
    "v_mfma_f32_16x16x32_bf16 v[44:47], v[48:51], v[100:103], v[44:47]\n"
    "s_waitcnt vmcnt(14)\n"
    "v_mfma_f32_16x16x32_bf16 v[16:19], a[228:231], v[104:107], v[16:19]\n"
    "buffer_load_dwordx4 v[100:103], v8, s[48:51], s59 offen offset:0 sc1\n"
    "v_mfma_f32_16x16x32_bf16 v[20:23], a[236:239], v[104:107], v[20:23]\n"
    "v_mfma_f32_16x16x32_bf16 v[24:27], v[244:247], v[104:107], v[24:27]\n"
    "s_waitcnt lgkmcnt(2)\n"
    "v_mfma_f32_16x16x32_bf16 v[28:31], v[52:55], v[104:107], v[28:31]\n"
    "s_waitcnt vmcnt(14)\n"
    "v_mfma_f32_16x16x32_bf16 v[32:35], a[228:231], v[108:111], v[32:35]\n"
    "buffer_load_dwordx4 v[104:107], v8, s[48:51], s58 offen offset:1024 sc1\n"
    "v_mfma_f32_16x16x32_bf16 v[36:39], a[236:239], v[108:111], v[36:39]\n"
    "v_mfma_f32_16x16x32_bf16 v[40:43], v[244:247], v[108:111], v[40:43]\n"
    "v_mfma_f32_16x16x32_bf16 v[44:47], v[52:55], v[108:111], v[44:47]\n"
    "s_waitcnt vmcnt(14)\n"
    "v_mfma_f32_16x16x32_bf16 v[16:19], a[240:243], v[112:115], v[16:19]\n"
    "buffer_load_dwordx4 v[108:111], v8, s[48:51], s59 offen offset:1024 sc1\n"
    "v_mfma_f32_16x16x32_bf16 v[20:23], a[248:251], v[112:115], v[20:23]\n"
    "v_mfma_f32_16x16x32_bf16 v[24:27], v[248:251], v[112:115], v[24:27]\n"
    "s_waitcnt lgkmcnt(1)\n"
    "v_mfma_f32_16x16x32_bf16 v[28:31], v[56:59], v[112:115], v[28:31]\n"
    "s_waitcnt vmcnt(14)\n"
    "v_mfma_f32_16x16x32_bf16 v[32:35], a[240:243], v[116:119], v[32:35]\n"
    "buffer_load_dwordx4 v[112:115], v8, s[48:51], s58 offen offset:2048 sc1\n"
    "v_mfma_f32_16x16x32_bf16 v[36:39], a[248:251], v[116:119], v[36:39]\n"
    "v_mfma_f32_16x16x32_bf16 v[40:43], v[248:251], v[116:119], v[40:43]\n"
    "v_mfma_f32_16x16x32_bf16 v[44:47], v[56:59], v[116:119], v[44:47]\n"
    "s_waitcnt vmcnt(14)\n"
    "v_mfma_f32_16x16x32_bf16 v[16:19], a[244:247], v[120:123], v[16:19]\n"
    "buffer_load_dwordx4 v[116:119], v8, s[48:51], s59 offen offset:2048 sc1\n"
    "v_mfma_f32_16x16x32_bf16 v[20:23], a[252:255], v[120:123], v[20:23]\n"
    "v_mfma_f32_16x16x32_bf16 v[24:27], v[252:255], v[120:123], v[24:27]\n"
    "s_waitcnt lgkmcnt(0)\n"
    "v_mfma_f32_16x16x32_bf16 v[28:31], v[60:63], v[120:123], v[28:31]\n"
    "s_waitcnt vmcnt(14)\n"
    "v_mfma_f32_16x16x32_bf16 v[32:35], a[244:247], v[124:127], v[32:35]\n"
    "buffer_load_dwordx4 v[120:123], v8, s[48:51], s58 offen offset:3072 sc1\n"
    "v_mfma_f32_16x16x32_bf16 v[36:39], a[252:255], v[124:127], v[36:39]\n"
    "v_mfma_f32_16x16x32_bf16 v[40:43], v[252:255], v[124:127], v[40:43]\n"
    "v_mfma_f32_16x16x32_bf16 v[44:47], v[60:63], v[124:127], v[44:47]\n"
    "buffer_load_dwordx4 v[124:127], v8, s[48:51], s59 offen offset:3072 sc1\n"
    "v_xor_b32 v4, 32, v15\n"
    "v_xor_b32 v5, 64, v15\n"
    "v_xor_b32 v6, 96, v15\n"
    "ds_write_b128 v15, v[16:19] offset:32768\n"
    "ds_write_b128 v4, v[20:23] offset:33792\n"
    "ds_write_b128 v5, v[24:27] offset:34816\n"
    "ds_write_b128 v6, v[28:31] offset:35840\n"
    "ds_write_b128 v15, v[32:35] offset:36864\n"
    "ds_write_b128 v4, v[36:39] offset:37888\n"
    "ds_write_b128 v5, v[40:43] offset:38912\n"
    "ds_write_b128 v6, v[44:47] offset:39936\n"
    "s_waitcnt lgkmcnt(0)\n"
    "s_barrier\n"
    "ds_read_b128 v[16:19], v10 offset:0\n"
    "ds_read_b128 v[20:23], v10 offset:256\n"
    "ds_read_b128 v[24:27], v10 offset:40960\n"
    "ds_read_b128 v[28:31], v10 offset:41216\n"
    "ds_read_b128 v[32:35], v11 offset:0\n"
    "ds_read_b128 v[36:39], v11 offset:256\n"
    "ds_read_b128 v[40:43], v11 offset:40960\n"
    "ds_read_b128 v[44:47], v11 offset:41216\n"
    "s_lshl_b32 s58, s60, 17\n"
    "s_waitcnt lgkmcnt(0)\n"
    "s_barrier\n"
    "v_pk_add_f32 v[16:17], v[16:17], v[24:25]\n"
    "v_pk_add_f32 v[32:33], v[32:33], v[40:41]\n"
    "v_pk_add_f32 v[18:19], v[18:19], v[26:27]\n"
    "v_pk_add_f32 v[34:35], v[34:35], v[42:43]\n"
    "v_pk_add_f32 v[20:21], v[20:21], v[28:29]\n"
    "v_pk_add_f32 v[36:37], v[36:37], v[44:45]\n"
    "v_pk_add_f32 v[22:23], v[22:23], v[30:31]\n"
    "v_pk_add_f32 v[38:39], v[38:39], v[46:47]\n"
    "v_pk_add_f32 v[16:17], v[16:17], v[32:33]\n"
    "v_pk_add_f32 v[18:19], v[18:19], v[34:35]\n"
    "v_pk_add_f32 v[20:21], v[20:21], v[36:37]\n"
    "v_pk_add_f32 v[22:23], v[22:23], v[38:39]\n"
    "v_cvt_pk_bf16_f32 v4, v16, v17\n"
    "v_cvt_pk_bf16_f32 v5, v18, v19\n"
    "v_cvt_pk_bf16_f32 v6, v20, v21\n"
    "v_cvt_pk_bf16_f32 v7, v22, v23\n"
    "buffer_store_dwordx4 v[4:7], v12, s[52:55], s58 offen sc1\n"
    "s_mov_b32 s60, s63\n"
    "s_cbranch_vccz .Lgx_ogo_%=\n"
    "s_cmp_lt_u32 s60, 61\n"
    "s_cbranch_scc1 .Lgx_oseg_%=\n"
    "s_lshr_b32 s60, s86, 1\n"
    "s_add_u32 s60, s60, 61\n"
    "s_and_b32 s63, s86, 1\n"
    "s_lshl_b32 s61, s60, 18\n"
    "s_add_u32 s61, s61, s62\n"
    "s_lshl_b32 s58, s63, 12\n"
    "s_add_u32 s61, s61, s58\n"
    "v_add_u32 v15, s58, v15\n"
    "ds_read_b128 v[48:51], v9 offset:0\n"
    "ds_read_b128 v[52:55], v9 offset:1024\n"
    "ds_read_b128 v[56:59], v9 offset:2048\n"
    "s_mov_b32 exec_hi, 0\n"
    ".Lgx_hpoll_%=:\n"
    "buffer_load_dword v14, v13, s[44:47], 0 offen sc1\n"
    "s_waitcnt vmcnt(0)\n"
    "v_cmp_ge_u32 vcc, s60, v14\n"
    "s_cbranch_vccz .Lgx_hok_%=\n"
    "s_sub_u32 s26, s26, 1\n"
    "s_cbranch_scc0 .Lgx_hpoll_%=\n"
    "s_endpgm\n"
    ".Lgx_hok_%=:\n"
    "s_mov_b64 exec, -1\n"
    "s_xor_b32 s58, s61, 0\n"
    "buffer_load_dwordx4 v[64:67], v8, s[48:51], s58 offen offset:0 sc1\n"
    "s_nop 7\n"
    "buffer_load_dwordx4 v[68:71], v8, s[48:51], s58 offen offset:1024 sc1\n"
    "s_nop 7\n"
    "buffer_load_dwordx4 v[72:75], v8, s[48:51], s58 offen offset:2048 sc1\n"
    "s_nop 7\n"
    "buffer_load_dwordx4 v[76:79], v8, s[48:51], s58 offen offset:3072 sc1\n"
    "s_nop 7\n"
    "s_xor_b32 s58, s61, 8192\n"
    "buffer_load_dwordx4 v[80:83], v8, s[48:51], s58 offen offset:0 sc1\n"
    "s_nop 7\n"
    "buffer_load_dwordx4 v[84:87], v8, s[48:51], s58 offen offset:1024 sc1\n"
    "s_nop 7\n"
    "buffer_load_dwordx4 v[88:91], v8, s[48:51], s58 offen offset:2048 sc1\n"
    "s_nop 7\n"
    "buffer_load_dwordx4 v[92:95], v8, s[48:51], s58 offen offset:3072 sc1\n"
    "s_nop 7\n"
    "s_xor_b32 s58, s61, 16384\n"
    "buffer_load_dwordx4 v[96:99], v8, s[48:51], s58 offen offset:0 sc1\n"
    "s_nop 7\n"
    "buffer_load_dwordx4 v[100:103], v8, s[48:51], s58 offen offset:1024 sc1\n"
    "s_nop 7\n"
    "buffer_load_dwordx4 v[104:107], v8, s[48:51], s58 offen offset:2048 sc1\n"
    "s_nop 7\n"
    "buffer_load_dwordx4 v[108:111], v8, s[48:51], s58 offen offset:3072 sc1\n"
    "s_nop 7\n"
    "s_waitcnt vmcnt(11)\n"
    "v_mfma_f32_16x16x32_bf16 v[16:19], a[0:3], v[64:67], 0\n"
    "s_xor_b32 s58, s61, 24576\n"
    "ds_read_b128 v[60:63], v9 offset:3072\n"
    "v_mfma_f32_16x16x32_bf16 v[20:23], a[8:11], v[64:67], 0\n"
    "buffer_load_dwordx4 v[112:115], v8, s[48:51], s58 offen offset:0 sc1\n"
    "v_mfma_f32_16x16x32_bf16 v[24:27], v[128:131], v[64:67], 0\n"
    "s_waitcnt lgkmcnt(3)\n"
    "v_mfma_f32_16x16x32_bf16 v[28:31], v[48:51], v[64:67], 0\n"
    "s_waitcnt vmcnt(11)\n"
    "v_mfma_f32_16x16x32_bf16 v[16:19], a[4:7], v[68:71], v[16:19]\n"
    "ds_read_b128 v[48:51], v9 offset:4096\n"
    "v_mfma_f32_16x16x32_bf16 v[20:23], a[12:15], v[68:71], v[20:23]\n"
    "buffer_load_dwordx4 v[116:119], v8, s[48:51], s58 offen offset:1024 sc1\n"
    "v_mfma_f32_16x16x32_bf16 v[24:27], v[132:135], v[68:71], v[24:27]\n"
    "s_waitcnt lgkmcnt(3)\n"
    "v_mfma_f32_16x16x32_bf16 v[28:31], v[52:55], v[68:71], v[28:31]\n"
    "s_waitcnt vmcnt(11)\n"
    "v_mfma_f32_16x16x32_bf16 v[16:19], a[16:19], v[72:75], v[16:19]\n"
    "ds_read_b128 v[52:55], v9 offset:5120\n"
    "v_mfma_f32_16x16x32_bf16 v[20:23], a[24:27], v[72:75], v[20:23]\n"
    "buffer_load_dwordx4 v[120:123], v8, s[48:51], s58 offen offset:2048 sc1\n"
    "v_mfma_f32_16x16x32_bf16 v[24:27], v[136:139], v[72:75], v[24:27]\n"
    "s_waitcnt lgkmcnt(3)\n"
    "v_mfma_f32_16x16x32_bf16 v[28:31], v[56:59], v[72:75], v[28:31]\n"
    "s_waitcnt vmcnt(11)\n"
    "v_mfma_f32_16x16x32_bf16 v[16:19], a[20:23], v[76:79], v[16:19]\n"
    "ds_read_b128 v[56:59], v9 offset:6144\n"
    "v_mfma_f32_16x16x32_bf16 v[20:23], a[28:31], v[76:79], v[20:23]\n"
    "buffer_load_dwordx4 v[124:127], v8, s[48:51], s58 offen offset:3072 sc1\n"
    "v_mfma_f32_16x16x32_bf16 v[24:27], v[140:143], v[76:79], v[24:27]\n"
    "s_waitcnt lgkmcnt(3)\n"
    "v_mfma_f32_16x16x32_bf16 v[28:31], v[60:63], v[76:79], v[28:31]\n"
    "s_waitcnt vmcnt(11)\n"
    "v_mfma_f32_16x16x32_bf16 v[16:19], a[32:35], v[80:83], v[16:19]\n"
    "s_xor_b32 s58, s61, 32768\n"
    "ds_read_b128 v[60:63], v9 offset:7168\n"
    "v_mfma_f32_16x16x32_bf16 v[20:23], a[40:43], v[80:83], v[20:23]\n"
    "buffer_load_dwordx4 v[64:67], v8, s[48:51], s58 offen offset:0 sc1\n"
    "v_mfma_f32_16x16x32_bf16 v[24:27], v[144:147], v[80:83], v[24:27]\n"
    "s_waitcnt lgkmcnt(3)\n"
    "v_mfma_f32_16x16x32_bf16 v[28:31], v[48:51], v[80:83], v[28:31]\n"
    "s_waitcnt vmcnt(11)\n"
    "v_mfma_f32_16x16x32_bf16 v[16:19], a[36:39], v[84:87], v[16:19]\n"
    "ds_read_b128 v[48:51], v9 offset:8192\n"
    "v_mfma_f32_16x16x32_bf16 v[20:23], a[44:47], v[84:87], v[20:23]\n"
    "buffer_load_dwordx4 v[68:71], v8, s[48:51], s58 offen offset:1024 sc1\n"
    "v_mfma_f32_16x16x32_bf16 v[24:27], v[148:151], v[84:87], v[24:27]\n"
    "s_waitcnt lgkmcnt(3)\n"
    "v_mfma_f32_16x16x32_bf16 v[28:31], v[52:55], v[84:87], v[28:31]\n"
    "s_waitcnt vmcnt(11)\n"
    "v_mfma_f32_16x16x32_bf16 v[16:19], a[48:51], v[88:91], v[16:19]\n"
    "ds_read_b128 v[52:55], v9 offset:9216\n"
    "v_mfma_f32_16x16x32_bf16 v[20:23], a[56:59], v[88:91], v[20:23]\n"
    "buffer_load_dwordx4 v[72:75], v8, s[48:51], s58 offen offset:2048 sc1\n"
    "v_mfma_f32_16x16x32_bf16 v[24:27], v[152:155], v[88:91], v[24:27]\n"
    "s_waitcnt lgkmcnt(3)\n"
    "v_mfma_f32_16x16x32_bf16 v[28:31], v[56:59], v[88:91], v[28:31]\n"
    "s_waitcnt vmcnt(11)\n"
    "v_mfma_f32_16x16x32_bf16 v[16:19], a[52:55], v[92:95], v[16:19]\n"
    "ds_read_b128 v[56:59], v9 offset:10240\n"
    "v_mfma_f32_16x16x32_bf16 v[20:23], a[60:63], v[92:95], v[20:23]\n"
    "buffer_load_dwordx4 v[76:79], v8, s[48:51], s58 offen offset:3072 sc1\n"
    "v_mfma_f32_16x16x32_bf16 v[24:27], v[156:159], v[92:95], v[24:27]\n"
    "s_waitcnt lgkmcnt(3)\n"
    "v_mfma_f32_16x16x32_bf16 v[28:31], v[60:63], v[92:95], v[28:31]\n"
    "s_waitcnt vmcnt(11)\n"
    "v_mfma_f32_16x16x32_bf16 v[16:19], a[64:67], v[96:99], v[16:19]\n"
    "s_xor_b32 s58, s61, 40960\n"
    "ds_read_b128 v[60:63], v9 offset:11264\n"
    "v_mfma_f32_16x16x32_bf16 v[20:23], a[72:75], v[96:99], v[20:23]\n"
    "buffer_load_dwordx4 v[80:83], v8, s[48:51], s58 offen offset:0 sc1\n"
    "v_mfma_f32_16x16x32_bf16 v[24:27], v[160:163], v[96:99], v[24:27]\n"
    "s_waitcnt lgkmcnt(3)\n"
    "v_mfma_f32_16x16x32_bf16 v[28:31], v[48:51], v[96:99], v[28:31]\n"
    "s_waitcnt vmcnt(11)\n"
    "v_mfma_f32_16x16x32_bf16 v[16:19], a[68:71], v[100:103], v[16:19]\n"
    "ds_read_b128 v[48:51], v9 offset:12288\n"
    "v_mfma_f32_16x16x32_bf16 v[20:23], a[76:79], v[100:103], v[20:23]\n"
    "buffer_load_dwordx4 v[84:87], v8, s[48:51], s58 offen offset:1024 sc1\n"
    "v_mfma_f32_16x16x32_bf16 v[24:27], v[164:167], v[100:103], v[24:27]\n"
    "s_waitcnt lgkmcnt(3)\n"
    "v_mfma_f32_16x16x32_bf16 v[28:31], v[52:55], v[100:103], v[28:31]\n"
    "s_waitcnt vmcnt(11)\n"
    "v_mfma_f32_16x16x32_bf16 v[16:19], a[80:83], v[104:107], v[16:19]\n"
    "ds_read_b128 v[52:55], v9 offset:13312\n"
    "v_mfma_f32_16x16x32_bf16 v[20:23], a[88:91], v[104:107], v[20:23]\n"
    "buffer_load_dwordx4 v[88:91], v8, s[48:51], s58 offen offset:2048 sc1\n"
    "v_mfma_f32_16x16x32_bf16 v[24:27], v[168:171], v[104:107], v[24:27]\n"
    "s_waitcnt lgkmcnt(3)\n"
    "v_mfma_f32_16x16x32_bf16 v[28:31], v[56:59], v[104:107], v[28:31]\n"
    "s_waitcnt vmcnt(11)\n"
    "v_mfma_f32_16x16x32_bf16 v[16:19], a[84:87], v[108:111], v[16:19]\n"
    "ds_read_b128 v[56:59], v9 offset:14336\n"
    "v_mfma_f32_16x16x32_bf16 v[20:23], a[92:95], v[108:111], v[20:23]\n"
    "buffer_load_dwordx4 v[92:95], v8, s[48:51], s58 offen offset:3072 sc1\n"
    "v_mfma_f32_16x16x32_bf16 v[24:27], v[172:175], v[108:111], v[24:27]\n"
    "s_waitcnt lgkmcnt(3)\n"
    "v_mfma_f32_16x16x32_bf16 v[28:31], v[60:63], v[108:111], v[28:31]\n"
    "s_waitcnt vmcnt(11)\n"
    "v_mfma_f32_16x16x32_bf16 v[16:19], a[96:99], v[112:115], v[16:19]\n"
    "s_xor_b32 s58, s61, 49152\n"
    "ds_read_b128 v[60:63], v9 offset:15360\n"
    "v_mfma_f32_16x16x32_bf16 v[20:23], a[104:107], v[112:115], v[20:23]\n"
    "buffer_load_dwordx4 v[96:99], v8, s[48:51], s58 offen offset:0 sc1\n"
    "v_mfma_f32_16x16x32_bf16 v[24:27], v[176:179], v[112:115], v[24:27]\n"
    "s_waitcnt lgkmcnt(3)\n"
    "v_mfma_f32_16x16x32_bf16 v[28:31], v[48:51], v[112:115], v[28:31]\n"
    "s_waitcnt vmcnt(11)\n"
    "v_mfma_f32_16x16x32_bf16 v[16:19], a[100:103], v[116:119], v[16:19]\n"
    "ds_read_b128 v[48:51], v9 offset:16384\n"
    "v_mfma_f32_16x16x32_bf16 v[20:23], a[108:111], v[116:119], v[20:23]\n"
    "buffer_load_dwordx4 v[100:103], v8, s[48:51], s58 offen offset:1024 sc1\n"
    "v_mfma_f32_16x16x32_bf16 v[24:27], v[180:183], v[116:119], v[24:27]\n"
    "s_waitcnt lgkmcnt(3)\n"
    "v_mfma_f32_16x16x32_bf16 v[28:31], v[52:55], v[116:119], v[28:31]\n"
    "s_waitcnt vmcnt(11)\n"
    "v_mfma_f32_16x16x32_bf16 v[16:19], a[112:115], v[120:123], v[16:19]\n"
    "ds_read_b128 v[52:55], v9 offset:17408\n"
    "v_mfma_f32_16x16x32_bf16 v[20:23], a[120:123], v[120:123], v[20:23]\n"
    "buffer_load_dwordx4 v[104:107], v8, s[48:51], s58 offen offset:2048 sc1\n"
    "v_mfma_f32_16x16x32_bf16 v[24:27], v[184:187], v[120:123], v[24:27]\n"
    "s_waitcnt lgkmcnt(3)\n"
    "v_mfma_f32_16x16x32_bf16 v[28:31], v[56:59], v[120:123], v[28:31]\n"
    "s_waitcnt vmcnt(11)\n"
    "v_mfma_f32_16x16x32_bf16 v[16:19], a[116:119], v[124:127], v[16:19]\n"
    "ds_read_b128 v[56:59], v9 offset:18432\n"
    "v_mfma_f32_16x16x32_bf16 v[20:23], a[124:127], v[124:127], v[20:23]\n"
    "buffer_load_dwordx4 v[108:111], v8, s[48:51], s58 offen offset:3072 sc1\n"
    "v_mfma_f32_16x16x32_bf16 v[24:27], v[188:191], v[124:127], v[24:27]\n"
    "s_waitcnt lgkmcnt(3)\n"
    "v_mfma_f32_16x16x32_bf16 v[28:31], v[60:63], v[124:127], v[28:31]\n"
    "s_waitcnt vmcnt(11)\n"
    "v_mfma_f32_16x16x32_bf16 v[16:19], a[128:131], v[64:67], v[16:19]\n"
    "s_xor_b32 s58, s61, 57344\n"
    "ds_read_b128 v[60:63], v9 offset:19456\n"
    "v_mfma_f32_16x16x32_bf16 v[20:23], a[136:139], v[64:67], v[20:23]\n"
    "buffer_load_dwordx4 v[112:115], v8, s[48:51], s58 offen offset:0 sc1\n"
    "v_mfma_f32_16x16x32_bf16 v[24:27], v[192:195], v[64:67], v[24:27]\n"
    "s_waitcnt lgkmcnt(3)\n"
    "v_mfma_f32_16x16x32_bf16 v[28:31], v[48:51], v[64:67], v[28:31]\n"
    "s_waitcnt vmcnt(11)\n"
    "v_mfma_f32_16x16x32_bf16 v[16:19], a[132:135], v[68:71], v[16:19]\n"
    "ds_read_b128 v[48:51], v9 offset:20480\n"
    "v_mfma_f32_16x16x32_bf16 v[20:23], a[140:143], v[68:71], v[20:23]\n"
    "buffer_load_dwordx4 v[116:119], v8, s[48:51], s58 offen offset:1024 sc1\n"
    "v_mfma_f32_16x16x32_bf16 v[24:27], v[196:199], v[68:71], v[24:27]\n"
    "s_waitcnt lgkmcnt(3)\n"
    "v_mfma_f32_16x16x32_bf16 v[28:31], v[52:55], v[68:71], v[28:31]\n"
    "s_waitcnt vmcnt(11)\n"
    "v_mfma_f32_16x16x32_bf16 v[16:19], a[144:147], v[72:75], v[16:19]\n"
    "ds_read_b128 v[52:55], v9 offset:21504\n"
    "v_mfma_f32_16x16x32_bf16 v[20:23], a[152:155], v[72:75], v[20:23]\n"
    "buffer_load_dwordx4 v[120:123], v8, s[48:51], s58 offen offset:2048 sc1\n"
    "v_mfma_f32_16x16x32_bf16 v[24:27], v[200:203], v[72:75], v[24:27]\n"
    "s_waitcnt lgkmcnt(3)\n"
    "v_mfma_f32_16x16x32_bf16 v[28:31], v[56:59], v[72:75], v[28:31]\n"
    "s_waitcnt vmcnt(11)\n"
    "v_mfma_f32_16x16x32_bf16 v[16:19], a[148:151], v[76:79], v[16:19]\n"
    "ds_read_b128 v[56:59], v9 offset:22528\n"
    "v_mfma_f32_16x16x32_bf16 v[20:23], a[156:159], v[76:79], v[20:23]\n"
    "buffer_load_dwordx4 v[124:127], v8, s[48:51], s58 offen offset:3072 sc1\n"
    "v_mfma_f32_16x16x32_bf16 v[24:27], v[204:207], v[76:79], v[24:27]\n"
    "s_waitcnt lgkmcnt(3)\n"
    "v_mfma_f32_16x16x32_bf16 v[28:31], v[60:63], v[76:79], v[28:31]\n"
    "s_waitcnt vmcnt(11)\n"
    "v_mfma_f32_16x16x32_bf16 v[16:19], a[160:163], v[80:83], v[16:19]\n"
    "ds_read_b128 v[60:63], v9 offset:23552\n"
    "v_mfma_f32_16x16x32_bf16 v[20:23], a[168:171], v[80:83], v[20:23]\n"
    "v_mfma_f32_16x16x32_bf16 v[24:27], v[208:211], v[80:83], v[24:27]\n"
    "s_waitcnt lgkmcnt(3)\n"
    "v_mfma_f32_16x16x32_bf16 v[28:31], v[48:51], v[80:83], v[28:31]\n"
    "s_waitcnt vmcnt(10)\n"
    "v_mfma_f32_16x16x32_bf16 v[16:19], a[164:167], v[84:87], v[16:19]\n"
    "ds_read_b128 v[48:51], v9 offset:24576\n"
    "v_mfma_f32_16x16x32_bf16 v[20:23], a[172:175], v[84:87], v[20:23]\n"
    "v_mfma_f32_16x16x32_bf16 v[24:27], v[212:215], v[84:87], v[24:27]\n"
    "s_waitcnt lgkmcnt(3)\n"
    "v_mfma_f32_16x16x32_bf16 v[28:31], v[52:55], v[84:87], v[28:31]\n"
    "s_waitcnt vmcnt(9)\n"
    "v_mfma_f32_16x16x32_bf16 v[16:19], a[176:179], v[88:91], v[16:19]\n"
    "ds_read_b128 v[52:55], v9 offset:25600\n"
    "v_mfma_f32_16x16x32_bf16 v[20:23], a[184:187], v[88:91], v[20:23]\n"
    "v_mfma_f32_16x16x32_bf16 v[24:27], v[216:219], v[88:91], v[24:27]\n"
    "s_waitcnt lgkmcnt(3)\n"
    "v_mfma_f32_16x16x32_bf16 v[28:31], v[56:59], v[88:91], v[28:31]\n"
    "s_waitcnt vmcnt(8)\n"
    "v_mfma_f32_16x16x32_bf16 v[16:19], a[180:183], v[92:95], v[16:19]\n"
    "ds_read_b128 v[56:59], v9 offset:26624\n"
    "v_mfma_f32_16x16x32_bf16 v[20:23], a[188:191], v[92:95], v[20:23]\n"
    "v_mfma_f32_16x16x32_bf16 v[24:27], v[220:223], v[92:95], v[24:27]\n"
    "s_waitcnt lgkmcnt(3)\n"
    "v_mfma_f32_16x16x32_bf16 v[28:31], v[60:63], v[92:95], v[28:31]\n"
    "s_waitcnt vmcnt(7)\n"
    "v_mfma_f32_16x16x32_bf16 v[16:19], a[192:195], v[96:99], v[16:19]\n"
    "ds_read_b128 v[60:63], v9 offset:27648\n"
    "v_mfma_f32_16x16x32_bf16 v[20:23], a[200:203], v[96:99], v[20:23]\n"
    "v_mfma_f32_16x16x32_bf16 v[24:27], v[224:227], v[96:99], v[24:27]\n"
    "s_waitcnt lgkmcnt(3)\n"
    "v_mfma_f32_16x16x32_bf16 v[28:31], v[48:51], v[96:99], v[28:31]\n"
    "s_waitcnt vmcnt(6)\n"
    "v_mfma_f32_16x16x32_bf16 v[16:19], a[196:199], v[100:103], v[16:19]\n"
    "ds_read_b128 v[48:51], v9 offset:28672\n"
    "v_mfma_f32_16x16x32_bf16 v[20:23], a[204:207], v[100:103], v[20:23]\n"
    "v_mfma_f32_16x16x32_bf16 v[24:27], v[228:231], v[100:103], v[24:27]\n"
    "s_waitcnt lgkmcnt(3)\n"
    "v_mfma_f32_16x16x32_bf16 v[28:31], v[52:55], v[100:103], v[28:31]\n"
    "s_waitcnt vmcnt(5)\n"
    "v_mfma_f32_16x16x32_bf16 v[16:19], a[208:211], v[104:107], v[16:19]\n"
    "ds_read_b128 v[52:55], v9 offset:29696\n"
    "v_mfma_f32_16x16x32_bf16 v[20:23], a[216:219], v[104:107], v[20:23]\n"
    "v_mfma_f32_16x16x32_bf16 v[24:27], v[232:235], v[104:107], v[24:27]\n"
    "s_waitcnt lgkmcnt(3)\n"
    "v_mfma_f32_16x16x32_bf16 v[28:31], v[56:59], v[104:107], v[28:31]\n"
    "s_waitcnt vmcnt(4)\n"
    "v_mfma_f32_16x16x32_bf16 v[16:19], a[212:215], v[108:111], v[16:19]\n"
    "ds_read_b128 v[56:59], v9 offset:30720\n"
    "v_mfma_f32_16x16x32_bf16 v[20:23], a[220:223], v[108:111], v[20:23]\n"
    "v_mfma_f32_16x16x32_bf16 v[24:27], v[236:239], v[108:111], v[24:27]\n"
    "s_waitcnt lgkmcnt(3)\n"
    "v_mfma_f32_16x16x32_bf16 v[28:31], v[60:63], v[108:111], v[28:31]\n"
    "s_waitcnt vmcnt(3)\n"
    "v_mfma_f32_16x16x32_bf16 v[16:19], a[224:227], v[112:115], v[16:19]\n"
    "ds_read_b128 v[60:63], v9 offset:31744\n"
    "v_mfma_f32_16x16x32_bf16 v[20:23], a[232:235], v[112:115], v[20:23]\n"
    "v_mfma_f32_16x16x32_bf16 v[24:27], v[240:243], v[112:115], v[24:27]\n"
    "s_waitcnt lgkmcnt(3)\n"
    "v_mfma_f32_16x16x32_bf16 v[28:31], v[48:51], v[112:115], v[28:31]\n"
    "s_waitcnt vmcnt(2)\n"
    "v_mfma_f32_16x16x32_bf16 v[16:19], a[228:231], v[116:119], v[16:19]\n"
    "v_mfma_f32_16x16x32_bf16 v[20:23], a[236:239], v[116:119], v[20:23]\n"
    "v_mfma_f32_16x16x32_bf16 v[24:27], v[244:247], v[116:119], v[24:27]\n"
    "s_waitcnt lgkmcnt(2)\n"
    "v_mfma_f32_16x16x32_bf16 v[28:31], v[52:55], v[116:119], v[28:31]\n"
    "s_waitcnt vmcnt(1)\n"
    "v_mfma_f32_16x16x32_bf16 v[16:19], a[240:243], v[120:123], v[16:19]\n"
    "v_mfma_f32_16x16x32_bf16 v[20:23], a[248:251], v[120:123], v[20:23]\n"
    "v_mfma_f32_16x16x32_bf16 v[24:27], v[248:251], v[120:123], v[24:27]\n"
    "s_waitcnt lgkmcnt(1)\n"
    "v_mfma_f32_16x16x32_bf16 v[28:31], v[56:59], v[120:123], v[28:31]\n"
    "s_waitcnt vmcnt(0)\n"
    "v_mfma_f32_16x16x32_bf16 v[16:19], a[244:247], v[124:127], v[16:19]\n"
    "v_mfma_f32_16x16x32_bf16 v[20:23], a[252:255], v[124:127], v[20:23]\n"
    "v_mfma_f32_16x16x32_bf16 v[24:27], v[252:255], v[124:127], v[24:27]\n"
    "s_waitcnt lgkmcnt(0)\n"
    "v_mfma_f32_16x16x32_bf16 v[28:31], v[60:63], v[124:127], v[28:31]\n"
    "v_xor_b32 v4, 32, v15\n"
    "v_xor_b32 v5, 64, v15\n"
    "v_xor_b32 v6, 96, v15\n"
    "s_nop 0\n"
    "ds_write_b128 v15, v[16:19] offset:32768\n"
    "ds_write_b128 v4, v[20:23] offset:33792\n"
    "ds_write_b128 v5, v[24:27] offset:34816\n"
    "s_nop 0\n"
    "ds_write_b128 v6, v[28:31] offset:35840\n"
    "s_lshr_b32 s58, s25, 1\n"
    "s_cmp_lg_u32 s58, s63\n"
    "s_waitcnt lgkmcnt(0)\n"
    "s_barrier\n"
    "s_cbranch_scc1 .Lgx_hend_%=\n"
    "ds_read_b128 v[16:19], v10 offset:0\n"
    "ds_read_b128 v[20:23], v10 offset:256\n"
    "ds_read_b128 v[24:27], v10 offset:40960\n"
    "ds_read_b128 v[28:31], v10 offset:41216\n"
    "ds_read_b128 v[32:35], v11 offset:0\n"
    "ds_read_b128 v[36:39], v11 offset:256\n"
    "ds_read_b128 v[40:43], v11 offset:40960\n"
    "ds_read_b128 v[44:47], v11 offset:41216\n"
    "s_lshl_b32 s58, s60, 17\n"
    "s_waitcnt lgkmcnt(0)\n"
    "v_pk_add_f32 v[16:17], v[16:17], v[24:25]\n"
    "v_pk_add_f32 v[32:33], v[32:33], v[40:41]\n"
    "v_pk_add_f32 v[18:19], v[18:19], v[26:27]\n"
    "v_pk_add_f32 v[34:35], v[34:35], v[42:43]\n"
    "v_pk_add_f32 v[20:21], v[20:21], v[28:29]\n"
    "v_pk_add_f32 v[36:37], v[36:37], v[44:45]\n"
    "v_pk_add_f32 v[22:23], v[22:23], v[30:31]\n"
    "v_pk_add_f32 v[38:39], v[38:39], v[46:47]\n"
    "v_pk_add_f32 v[16:17], v[16:17], v[32:33]\n"
    "v_pk_add_f32 v[18:19], v[18:19], v[34:35]\n"
    "v_pk_add_f32 v[20:21], v[20:21], v[36:37]\n"
    "v_pk_add_f32 v[22:23], v[22:23], v[38:39]\n"
    "v_cvt_pk_bf16_f32 v4, v16, v17\n"
    "v_cvt_pk_bf16_f32 v5, v18, v19\n"
    "v_cvt_pk_bf16_f32 v6, v20, v21\n"
    "v_cvt_pk_bf16_f32 v7, v22, v23\n"
    "buffer_store_dwordx4 v[4:7], v12, s[52:55], s58 offen sc1\n"
    ".Lgx_hend_%=:\n"
    "s_endpgm\n"
    ".Lgx_hsp_%=:\n"
    "s_sleep 2\n"
    "s_add_u32 s10, s19, 69632\n"
    "s_mov_b64 exec, 1\n"
    "buffer_load_dword v0, off, s[12:15], s10 sc1\n"
    "s_mov_b64 exec, -1\n"
    "s_waitcnt vmcnt(0)\n"
    "s_sub_u32 s26, s26, 1\n"
    "s_cbranch_scc0 .Lgx_hsc_%=\n"
    "s_endpgm\n"
    ".Lgx_hsr_%=:\n"
    "s_mov_b32 s17, s11\n"
    "s_waitcnt vmcnt(0) lgkmcnt(0)\n"
    "s_barrier\n"
    "s_nop 15\n"
    "s_branch .Lk_dispatch_%=\n"
    ".Lat_entry_%=:\n"
    "s_load_dwordx8 s[72:79], s[28:29], 0\n"
    "s_load_dwordx2 s[80:81], s[28:29], 32\n"
    "s_load_dwordx2 s[82:83], s[28:29], 88\n"
    "s_and_b32 s59, s24, 3\n"
    "s_bfe_u32 s60, s24, 196610\n"
    "s_lshl_b32 s60, s60, 3\n"
    "s_add_u32 s60, s60, s25\n"
    "s_lshr_b32 s56, s59, 1\n"
    "s_lshr_b32 s57, s24, 5\n"
    "s_xor_b32 s56, s56, s57\n"
    "s_lshl_b32 s56, s56, 2\n"
    "s_add_u32 s60, s60, s56\n"
    "s_waitcnt lgkmcnt(0)\n"
    "s_mov_b32 s40, s72\n"
    "s_and_b32 s41, s73, 0xffff\n"
    "s_mov_b32 s42, 4194304\n"
    "s_mov_b32 s43, 0x20000\n"
    "s_mov_b32 s32, s74\n"
    "s_and_b32 s33, s75, 0xffff\n"
    "s_mov_b32 s34, 2097152\n"
    "s_mov_b32 s35, 0x20000\n"
    "s_bitcmp1_b32 s59, 0\n"
    "s_cselect_b32 s76, s78, s76\n"
    "s_cselect_b32 s77, s79, s77\n"
    "s_mov_b32 s36, s76\n"
    "s_and_b32 s37, s77, 0xffff\n"
    "s_mov_b32 s38, 65536\n"
    "s_mov_b32 s39, 0x20000\n"
    "s_mov_b32 s52, s82\n"
    "s_and_b32 s53, s83, 0xffff\n"
    "s_mov_b32 s54, 8388608\n"
    "s_mov_b32 s55, 0x20000\n"
    "s_add_u32 s44, s30, 69206016\n"
    "s_addc_u32 s45, s31, 0\n"
    "s_and_b32 s45, s45, 0xffff\n"
    "s_mov_b32 s46, 262144\n"
    "s_mov_b32 s47, 0x20000\n"
    "s_add_u32 s48, s30, 0\n"
    "s_addc_u32 s49, s31, 0\n"
    "s_and_b32 s49, s49, 0xffff\n"
    "s_mov_b32 s50, 65536\n"
    "s_mov_b32 s51, 0x20000\n"
    "v_lshlrev_b32 v2, 4, v1\n"
    "s_cmp_eq_u32 s16, 0\n"
    "s_cbranch_scc1 .Lat_noproj_%=\n"
    "s_load_dwordx8 s[88:95], s[28:29], 56\n"
    "s_and_b32 s56, s24, 7\n"
    "s_lshl_b32 s56, s56, 2\n"
    "s_add_u32 s56, s56, s25\n"
    "s_bfe_u32 s61, s24, 131075\n"
    "s_lshr_b32 s64, s56, 1\n"
    "s_lshl_b32 s64, s64, 19\n"
    "s_and_b32 s57, s56, 1\n"
    "s_lshl_b32 s57, s57, 10\n"
    "s_add_u32 s64, s64, s57\n"
    "s_lshl_b32 s57, s61, 11\n"
    "s_add_u32 s64, s64, s57\n"
    "s_lshl_b32 s65, s56, 18\n"
    "s_lshr_b32 s57, s61, 1\n"
    "s_lshl_b32 s57, s57, 10\n"
    "s_add_u32 s65, s65, s57\n"
    "s_cmp_lt_u32 s24, 32\n"
    "s_cselect_b32 s65, 0x10000000, s65\n"
    "s_add_u32 s66, s65, 131072\n"
    "v_lshlrev_b32 v19, 13, v1\n"
    "v_lshlrev_b32 v20, 11, v1\n"
    "s_waitcnt lgkmcnt(0)\n"
    "s_mov_b32 s96, s92\n"
    "s_and_b32 s97, s93, 0xffff\n"
    "s_mov_b32 s98, 8388608\n"
    "s_mov_b32 s99, 0x20000\n"
    "buffer_load_dword a242, v19, s[96:99], s64 offen offset:384\n"
    "buffer_load_dword a248, v19, s[96:99], s64 offen offset:256\n"
    "s_mov_b32 s96, s90\n"
    "s_and_b32 s97, s91, 0xffff\n"
    "s_mov_b32 s98, 8388608\n"
    "s_mov_b32 s99, 0x20000\n"
    "buffer_load_dword a241, v19, s[96:99], s64 offen offset:384\n"
    "buffer_load_dword a247, v19, s[96:99], s64 offen offset:256\n"
    "s_mov_b32 s96, s88\n"
    "s_and_b32 s97, s89, 0xffff\n"
    "s_mov_b32 s98, 8388608\n"
    "s_mov_b32 s99, 0x20000\n"
    "buffer_load_dword a240, v19, s[96:99], s64 offen offset:384\n"
    "buffer_load_dword a246, v19, s[96:99], s64 offen offset:256\n"
    "s_waitcnt vmcnt(6)\n"
    "v_readfirstlane_b32 s18, v0\n"
    "s_add_u32 s10, s19, 65536\n"
    "s_or_b32 s18, s18, 0xff\n"
    "s_add_u32 s18, s18, 1\n"
    "v_mov_b32 v0, s18\n"
    "s_mov_b64 exec, 1\n"
    "buffer_store_dword v0, off, s[12:15], s10 sc1\n"
    "s_mov_b64 exec, -1\n"
    "s_cmp_lt_u32 s24, 32\n"
    "s_cbranch_scc1 .Lat_noc3_%=\n"
    "s_sleep 36\n"
    "s_sleep 36\n"
    "s_mov_b32 s96, s94\n"
    "s_and_b32 s97, s95, 0xffff\n"
    "s_mov_b32 s98, 8388608\n"
    "s_mov_b32 s99, 0x20000\n"
    "buffer_load_dword a244, v20, s[96:99], s65 offen offset:384\n"
    "buffer_load_dword a245, v20, s[96:99], s66 offen offset:384\n"
    ".Lat_hsdo_%=:\n"
    "s_add_u32 s10, s19, 69632\n"
    "s_mov_b64 exec, 1\n"
    "buffer_load_dword v0, off, s[12:15], s10 sc1\n"
    "s_mov_b64 exec, -1\n"
    "s_waitcnt vmcnt(0)\n"
    ".Lat_hsc_%=:\n"
    "v_readfirstlane_b32 s10, v0\n"
    "s_nop 1\n"
    "s_andn2_b32 s11, s10, 0xff\n"
    "s_cmp_lg_u32 s11, s18\n"
    "s_cbranch_scc1 .Lat_hsp_%=\n"
    "s_and_b32 s11, s10, 7\n"
    "s_cmp_lg_u32 s11, s17\n"
    "s_cbranch_scc1 .Lat_hsr_%=\n"
    "s_and_b32 s10, s10, 8\n"
    "s_lshl_b32 s27, s10, 10\n"
    "s_add_u32 s10, s19, 12288\n"
    "v_mov_b32 v0, 1\n"
    "s_mov_b64 exec, 1\n"
    "buffer_store_dword v0, off, s[12:15], s10 sc1\n"
    "s_mov_b64 exec, -1\n"
    "s_cmp_ge_u32 s24, 32\n"
    "s_cbranch_scc1 .Lat_noproj_%=\n"
    "s_bitcmp1_b32 s59, 0\n"
    "s_cbranch_scc1 .Lat_pv_%=\n"
    "s_branch .Lat_pk_%=\n"
    ".Lat_noc3_%=:\n"
    "s_and_b32 s56, s25, 1\n"
    "s_lshl_b32 s56, s56, 7\n"
    "v_bfe_u32 v19, v1, 5, 1\n"
    "v_lshlrev_b32 v19, 6, v19\n"
    "v_add_u32 v19, s56, v19\n"
    "v_bfe_u32 v3, v1, 5, 1\n"
    "v_lshlrev_b32 v3, 11, v3\n"
    "v_bfe_u32 v20, v1, 0, 5\n"
    "v_lshl_add_u32 v3, v20, 4, v3\n"
    "v_xor_b32 v3, v3, v19\n"
    "s_lshl_b32 s56, s25, 12\n"
    "s_lshl_b32 s57, s59, 9\n"
    "s_add_u32 s56, s56, s57\n"
    "v_add_u32 v3, s56, v3\n"
    "v_bfe_u32 v4, v1, 3, 3\n"
    "v_lshlrev_b32 v4, 11, v4\n"
    "v_bfe_u32 v20, v1, 0, 3\n"
    "v_lshl_add_u32 v4, v20, 4, v4\n"
    "v_bfe_u32 v19, v1, 4, 2\n"
    "v_lshlrev_b32 v19, 4, v19\n"
    "v_xor_b32 v4, v4, v19\n"
    "v_xor_b32 v5, 64, v4\n"
    "v_bfe_u32 v6, v1, 5, 1\n"
    "v_lshlrev_b32 v6, 12, v6\n"
    "v_bfe_u32 v19, v1, 2, 2\n"
    "v_lshl_add_u32 v6, v19, 9, v6\n"
    "v_bfe_u32 v19, v1, 2, 2\n"
    "v_lshl_add_u32 v6, v19, 6, v6\n"
    "v_bfe_u32 v19, v1, 4, 1\n"
    "v_lshl_add_u32 v6, v19, 5, v6\n"
    "v_bfe_u32 v19, v1, 0, 2\n"
    "v_lshl_add_u32 v6, v19, 3, v6\n"
    "v_bfe_u32 v7, v1, 5, 1\n"
    "v_lshlrev_b32 v7, 9, v7\n"
    "v_bfe_u32 v19, v1, 2, 2\n"
    "v_lshl_add_u32 v7, v19, 6, v7\n"
    "v_bfe_u32 v19, v1, 4, 1\n"
    "v_lshl_add_u32 v7, v19, 5, v7\n"
    "v_bfe_u32 v19, v1, 0, 2\n"
    "v_lshl_add_u32 v7, v19, 3, v7\n"
    "s_lshl_b32 s56, s25, 13\n"
    "v_bfe_u32 v8, v1, 0, 5\n"
    "v_lshlrev_b32 v8, 7, v8\n"
    "v_bfe_u32 v19, v1, 5, 1\n"
    "v_lshl_add_u32 v8, v19, 4, v8\n"
    "v_add_u32 v8, s56, v8\n"
    "v_bfe_u32 v19, v1, 1, 3\n"
    "v_lshlrev_b32 v19, 4, v19\n"
    "v_xor_b32 v8, v8, v19\n"
    "s_bitcmp1_b32 s59, 0\n"
    "s_cbranch_scc1 .Lat_pv0_%=\n"
    "s_mov_b32 s64, 0\n"
    "s_lshl_b32 s65, s25, 10\n"
    "s_lshl_b32 s66, s60, 16\n"
    "s_xor_b32 s71, s66, 262144\n"
    "s_lshl_b32 s63, s25, 10\n"
    "s_lshl_b32 s70, s25, 13\n"
    "s_mov_b32 s58, 20480\n"
    "s_mov_b32 s62, 0\n"
    "s_mov_b32 s61, 0\n"
    "s_add_u32 m0, s70, 81920\n"
    "s_add_u32 s56, s66, 0\n"
    "buffer_load_dwordx4 v4, s[40:43], s56 offen lds\n"
    "s_add_u32 m0, s70, 82944\n"
    "s_add_u32 s56, s66, 16384\n"
    "buffer_load_dwordx4 v5, s[40:43], s56 offen lds\n"
    "s_add_u32 m0, s70, 83968\n"
    "s_add_u32 s56, s66, 32768\n"
    "buffer_load_dwordx4 v4, s[40:43], s56 offen lds\n"
    "s_add_u32 m0, s70, 84992\n"
    "s_add_u32 s56, s66, 49152\n"
    "buffer_load_dwordx4 v5, s[40:43], s56 offen lds\n"
    "s_add_u32 m0, s70, 86016\n"
    "s_add_u32 s56, s71, 0\n"
    "buffer_load_dwordx4 v4, s[40:43], s56 offen lds\n"
    "s_add_u32 m0, s70, 87040\n"
    "s_add_u32 s56, s71, 16384\n"
    "buffer_load_dwordx4 v5, s[40:43], s56 offen lds\n"
    "s_add_u32 m0, s70, 88064\n"
    "s_add_u32 s56, s71, 32768\n"
    "buffer_load_dwordx4 v4, s[40:43], s56 offen lds\n"
    "s_add_u32 m0, s70, 89088\n"
    "s_add_u32 s56, s71, 49152\n"
    "buffer_load_dwordx4 v5, s[40:43], s56 offen lds\n"
    "s_add_u32 s66, s66, 128\n"
    "s_add_u32 s71, s71, 128\n"
    "s_xor_b32 s70, s70, 32768\n"
    "s_add_u32 m0, s63, 0\n"
    "s_add_u32 s56, s64, 0\n"
    "buffer_load_dwordx4 v3, s[32:35], s56 offen lds\n"
    "s_add_u32 m0, s63, 4096\n"
    "s_add_u32 s56, s64, 16384\n"
    "buffer_load_dwordx4 v3, s[32:35], s56 offen lds\n"
    "s_add_u32 m0, s63, 8192\n"
    "s_add_u32 s56, s64, 32768\n"
    "buffer_load_dwordx4 v3, s[32:35], s56 offen lds\n"
    "s_add_u32 m0, s63, 12288\n"
    "s_add_u32 s56, s64, 49152\n"
    "buffer_load_dwordx4 v3, s[32:35], s56 offen lds\n"
    "s_add_u32 m0, s63, 16384\n"
    "s_nop 0\n"
    "buffer_load_dwordx4 v2, s[36:39], s65 offen lds\n"
    "s_add_u32 s64, s64, 65536\n"
    "s_add_u32 s65, s65, 2048\n"
    "s_cmp_ge_u32 s63, 61440\n"
    "s_cselect_b32 s56, 4294905856, s58\n"
    "s_add_u32 s63, s63, s56\n"
    "s_add_u32 m0, s63, 0\n"
    "s_add_u32 s56, s64, 0\n"
    "buffer_load_dwordx4 v3, s[32:35], s56 offen lds\n"
    "s_add_u32 m0, s63, 4096\n"
    "s_add_u32 s56, s64, 16384\n"
    "buffer_load_dwordx4 v3, s[32:35], s56 offen lds\n"
    "s_add_u32 m0, s63, 8192\n"
    "s_add_u32 s56, s64, 32768\n"
    "buffer_load_dwordx4 v3, s[32:35], s56 offen lds\n"
    "s_add_u32 m0, s63, 12288\n"
    "s_add_u32 s56, s64, 49152\n"
    "buffer_load_dwordx4 v3, s[32:35], s56 offen lds\n"
    "s_add_u32 m0, s63, 16384\n"
    "s_nop 0\n"
    "buffer_load_dwordx4 v2, s[36:39], s65 offen lds\n"
    "s_add_u32 s64, s64, 65536\n"
    "s_add_u32 s65, s65, 2048\n"
    "s_cmp_ge_u32 s63, 61440\n"
    "s_cselect_b32 s56, 4294905856, s58\n"
    "s_add_u32 s63, s63, s56\n"
    "s_add_u32 m0, s63, 0\n"
    "s_add_u32 s56, s64, 0\n"
    "buffer_load_dwordx4 v3, s[32:35], s56 offen lds\n"
    "s_add_u32 m0, s63, 4096\n"
    "s_add_u32 s56, s64, 16384\n"
    "buffer_load_dwordx4 v3, s[32:35], s56 offen lds\n"
    "s_add_u32 m0, s63, 8192\n"
    "s_add_u32 s56, s64, 32768\n"
    "buffer_load_dwordx4 v3, s[32:35], s56 offen lds\n"
    "s_add_u32 m0, s63, 12288\n"
    "s_add_u32 s56, s64, 49152\n"
    "buffer_load_dwordx4 v3, s[32:35], s56 offen lds\n"
    "s_add_u32 m0, s63, 16384\n"
    "s_nop 0\n"
    "buffer_load_dwordx4 v2, s[36:39], s65 offen lds\n"
    "s_add_u32 s64, s64, 65536\n"
    "s_add_u32 s65, s65, 2048\n"
    "s_cmp_ge_u32 s63, 61440\n"
    "s_cselect_b32 s56, 4294905856, s58\n"
    "s_add_u32 s63, s63, s56\n"
    "v_add_u32 v10, s62, v6\n"
    "v_add_u32 v14, s62, v7\n"
    "v_xor_b32 v11, 64, v10\n"
    "v_xor_b32 v12, 128, v10\n"
    "v_xor_b32 v13, 192, v10\n"
    "v_xor_b32 v15, 65536, v8\n"
    "v_xor_b32 v16, 65568, v8\n"
    "v_xor_b32 v17, 65600, v8\n"
    "v_xor_b32 v18, 65632, v8\n"
    "v_mov_b64 v[112:113], 0\n"
    "v_mov_b64 v[114:115], 0\n"
    "v_mov_b64 v[116:117], 0\n"
    "v_mov_b64 v[118:119], 0\n"
    "v_mov_b64 v[120:121], 0\n"
    "v_mov_b64 v[122:123], 0\n"
    "v_mov_b64 v[124:125], 0\n"
    "v_mov_b64 v[126:127], 0\n"
    "v_mov_b64 v[128:129], 0\n"
    "v_mov_b64 v[130:131], 0\n"
    "v_mov_b64 v[132:133], 0\n"
    "v_mov_b64 v[134:135], 0\n"
    "v_mov_b64 v[136:137], 0\n"
    "v_mov_b64 v[138:139], 0\n"
    "v_mov_b64 v[140:141], 0\n"
    "v_mov_b64 v[142:143], 0\n"
    "v_mov_b64 v[144:145], 0\n"
    "v_mov_b64 v[146:147], 0\n"
    "v_mov_b64 v[148:149], 0\n"
    "v_mov_b64 v[150:151], 0\n"
    "v_mov_b64 v[152:153], 0\n"
    "v_mov_b64 v[154:155], 0\n"
    "v_mov_b64 v[156:157], 0\n"
    "v_mov_b64 v[158:159], 0\n"
    "v_mov_b64 v[160:161], 0\n"
    "v_mov_b64 v[162:163], 0\n"
    "v_mov_b64 v[164:165], 0\n"
    "v_mov_b64 v[166:167], 0\n"
    "v_mov_b64 v[168:169], 0\n"
    "v_mov_b64 v[170:171], 0\n"
    "v_mov_b64 v[172:173], 0\n"
    "v_mov_b64 v[174:175], 0\n"
    "v_mov_b64 v[176:177], 0\n"
    "v_mov_b64 v[178:179], 0\n"
    "v_mov_b64 v[180:181], 0\n"
    "v_mov_b64 v[182:183], 0\n"
    "v_mov_b64 v[184:185], 0\n"
    "v_mov_b64 v[186:187], 0\n"
    "v_mov_b64 v[188:189], 0\n"
    "v_mov_b64 v[190:191], 0\n"
    "v_mov_b64 v[192:193], 0\n"
    "v_mov_b64 v[194:195], 0\n"
    "v_mov_b64 v[196:197], 0\n"
    "v_mov_b64 v[198:199], 0\n"
    "v_mov_b64 v[200:201], 0\n"
    "v_mov_b64 v[202:203], 0\n"
    "v_mov_b64 v[204:205], 0\n"
    "v_mov_b64 v[206:207], 0\n"
    "v_mov_b64 v[208:209], 0\n"
    "v_mov_b64 v[210:211], 0\n"
    "v_mov_b64 v[212:213], 0\n"
    "v_mov_b64 v[214:215], 0\n"
    "v_mov_b64 v[216:217], 0\n"
    "v_mov_b64 v[218:219], 0\n"
    "v_mov_b64 v[220:221], 0\n"
    "v_mov_b64 v[222:223], 0\n"
    "v_mov_b64 v[224:225], 0\n"
    "v_mov_b64 v[226:227], 0\n"
    "v_mov_b64 v[228:229], 0\n"
    "v_mov_b64 v[230:231], 0\n"
    "v_mov_b64 v[232:233], 0\n"
    "v_mov_b64 v[234:235], 0\n"
    "v_mov_b64 v[236:237], 0\n"
    "v_mov_b64 v[238:239], 0\n"
    "v_mov_b64 v[240:241], 0\n"
    "v_mov_b64 v[242:243], 0\n"
    "v_mov_b64 v[244:245], 0\n"
    "v_mov_b64 v[246:247], 0\n"
    "v_mov_b64 v[248:249], 0\n"
    "v_mov_b64 v[250:251], 0\n"
    "v_mov_b64 v[252:253], 0\n"
    "v_mov_b64 v[254:255], 0\n"
    "v_accvgpr_write_b32 a0, 0\n"
    "v_accvgpr_write_b32 a1, 0\n"
    "v_accvgpr_write_b32 a2, 0\n"
    "v_accvgpr_write_b32 a3, 0\n"
    "v_accvgpr_write_b32 a4, 0\n"
    "v_accvgpr_write_b32 a5, 0\n"
    "v_accvgpr_write_b32 a6, 0\n"
    "v_accvgpr_write_b32 a7, 0\n"
    "v_accvgpr_write_b32 a8, 0\n"
    "v_accvgpr_write_b32 a9, 0\n"
    "v_accvgpr_write_b32 a10, 0\n"
    "v_accvgpr_write_b32 a11, 0\n"
    "v_accvgpr_write_b32 a12, 0\n"
    "v_accvgpr_write_b32 a13, 0\n"
    "v_accvgpr_write_b32 a14, 0\n"
    "v_accvgpr_write_b32 a15, 0\n"
    "v_accvgpr_write_b32 a16, 0\n"
    "v_accvgpr_write_b32 a17, 0\n"
    "v_accvgpr_write_b32 a18, 0\n"
    "v_accvgpr_write_b32 a19, 0\n"
    "v_accvgpr_write_b32 a20, 0\n"
    "v_accvgpr_write_b32 a21, 0\n"
    "v_accvgpr_write_b32 a22, 0\n"
    "v_accvgpr_write_b32 a23, 0\n"
    "v_accvgpr_write_b32 a24, 0\n"
    "v_accvgpr_write_b32 a25, 0\n"
    "v_accvgpr_write_b32 a26, 0\n"
    "v_accvgpr_write_b32 a27, 0\n"
    "v_accvgpr_write_b32 a28, 0\n"
    "v_accvgpr_write_b32 a29, 0\n"
    "v_accvgpr_write_b32 a30, 0\n"
    "v_accvgpr_write_b32 a31, 0\n"
    "v_accvgpr_write_b32 a32, 0\n"
    "v_accvgpr_write_b32 a33, 0\n"
    "v_accvgpr_write_b32 a34, 0\n"
    "v_accvgpr_write_b32 a35, 0\n"
    "v_accvgpr_write_b32 a36, 0\n"
    "v_accvgpr_write_b32 a37, 0\n"
    "v_accvgpr_write_b32 a38, 0\n"
    "v_accvgpr_write_b32 a39, 0\n"
    "v_accvgpr_write_b32 a40, 0\n"
    "v_accvgpr_write_b32 a41, 0\n"
    "v_accvgpr_write_b32 a42, 0\n"
    "v_accvgpr_write_b32 a43, 0\n"
    "v_accvgpr_write_b32 a44, 0\n"
    "v_accvgpr_write_b32 a45, 0\n"
    "v_accvgpr_write_b32 a46, 0\n"
    "v_accvgpr_write_b32 a47, 0\n"
    "v_accvgpr_write_b32 a48, 0\n"
    "v_accvgpr_write_b32 a49, 0\n"
    "v_accvgpr_write_b32 a50, 0\n"
    "v_accvgpr_write_b32 a51, 0\n"
    "v_accvgpr_write_b32 a52, 0\n"
    "v_accvgpr_write_b32 a53, 0\n"
    "v_accvgpr_write_b32 a54, 0\n"
    "v_accvgpr_write_b32 a55, 0\n"
    "v_accvgpr_write_b32 a56, 0\n"
    "v_accvgpr_write_b32 a57, 0\n"
    "v_accvgpr_write_b32 a58, 0\n"
    "v_accvgpr_write_b32 a59, 0\n"
    "v_accvgpr_write_b32 a60, 0\n"
    "v_accvgpr_write_b32 a61, 0\n"
    "v_accvgpr_write_b32 a62, 0\n"
    "v_accvgpr_write_b32 a63, 0\n"
    "v_accvgpr_write_b32 a64, 0\n"
    "v_accvgpr_write_b32 a65, 0\n"
    "v_accvgpr_write_b32 a66, 0\n"
    "v_accvgpr_write_b32 a67, 0\n"
    "v_accvgpr_write_b32 a68, 0\n"
    "v_accvgpr_write_b32 a69, 0\n"
    "v_accvgpr_write_b32 a70, 0\n"
    "v_accvgpr_write_b32 a71, 0\n"
    "v_accvgpr_write_b32 a72, 0\n"
    "v_accvgpr_write_b32 a73, 0\n"
    "v_accvgpr_write_b32 a74, 0\n"
    "v_accvgpr_write_b32 a75, 0\n"
    "v_accvgpr_write_b32 a76, 0\n"
    "v_accvgpr_write_b32 a77, 0\n"
    "v_accvgpr_write_b32 a78, 0\n"
    "v_accvgpr_write_b32 a79, 0\n"
    "v_accvgpr_write_b32 a80, 0\n"
    "v_accvgpr_write_b32 a81, 0\n"
    "v_accvgpr_write_b32 a82, 0\n"
    "v_accvgpr_write_b32 a83, 0\n"
    "v_accvgpr_write_b32 a84, 0\n"
    "v_accvgpr_write_b32 a85, 0\n"
    "v_accvgpr_write_b32 a86, 0\n"
    "v_accvgpr_write_b32 a87, 0\n"
    "v_accvgpr_write_b32 a88, 0\n"
    "v_accvgpr_write_b32 a89, 0\n"
    "v_accvgpr_write_b32 a90, 0\n"
    "v_accvgpr_write_b32 a91, 0\n"
    "v_accvgpr_write_b32 a92, 0\n"
    "v_accvgpr_write_b32 a93, 0\n"
    "v_accvgpr_write_b32 a94, 0\n"
    "v_accvgpr_write_b32 a95, 0\n"
    "v_accvgpr_write_b32 a96, 0\n"
    "v_accvgpr_write_b32 a97, 0\n"
    "v_accvgpr_write_b32 a98, 0\n"
    "v_accvgpr_write_b32 a99, 0\n"
    "v_accvgpr_write_b32 a100, 0\n"
    "v_accvgpr_write_b32 a101, 0\n"
    "v_accvgpr_write_b32 a102, 0\n"
    "v_accvgpr_write_b32 a103, 0\n"
    "v_accvgpr_write_b32 a104, 0\n"
    "v_accvgpr_write_b32 a105, 0\n"
    "v_accvgpr_write_b32 a106, 0\n"
    "v_accvgpr_write_b32 a107, 0\n"
    "v_accvgpr_write_b32 a108, 0\n"
    "v_accvgpr_write_b32 a109, 0\n"
    "v_accvgpr_write_b32 a110, 0\n"
    "v_accvgpr_write_b32 a111, 0\n"
    "v_accvgpr_write_b32 a112, 0\n"
    "v_accvgpr_write_b32 a113, 0\n"
    "v_accvgpr_write_b32 a114, 0\n"
    "v_accvgpr_write_b32 a115, 0\n"
    "v_accvgpr_write_b32 a116, 0\n"
    "v_accvgpr_write_b32 a117, 0\n"
    "v_accvgpr_write_b32 a118, 0\n"
    "v_accvgpr_write_b32 a119, 0\n"
    "v_accvgpr_write_b32 a120, 0\n"
    "v_accvgpr_write_b32 a121, 0\n"
    "v_accvgpr_write_b32 a122, 0\n"
    "v_accvgpr_write_b32 a123, 0\n"
    "v_accvgpr_write_b32 a124, 0\n"
    "v_accvgpr_write_b32 a125, 0\n"
    "v_accvgpr_write_b32 a126, 0\n"
    "v_accvgpr_write_b32 a127, 0\n"
    "s_waitcnt vmcnt(10)\n"
    "s_barrier\n"
    "ds_read_b128 v[24:27], v15 offset:16384\n"
    "ds_read_b128 v[28:31], v15 offset:20480\n"
    "ds_read_b64_tr_b16 v[40:41], v10 offset:0\n"
    "ds_read_b64_tr_b16 v[42:43], v10 offset:2048\n"
    "ds_read_b64_tr_b16 v[44:45], v11 offset:0\n"
    "ds_read_b64_tr_b16 v[46:47], v11 offset:2048\n"
    "ds_read_b64_tr_b16 v[48:49], v12 offset:0\n"
    "ds_read_b64_tr_b16 v[50:51], v12 offset:2048\n"
    ".Lat_pk_%=:\n"
    "s_waitcnt lgkmcnt(4)\n"
    "v_mfma_f32_32x32x16_bf16 v[128:143], v[40:43], v[24:27], v[128:143]\n"
    "ds_read_b64_tr_b16 v[52:53], v13 offset:0\n"
    "ds_read_b64_tr_b16 v[54:55], v13 offset:2048\n"
    "v_mfma_f32_32x32x16_bf16 a[0:15], v[40:43], v[28:31], a[0:15]\n"
    "s_add_u32 m0, s70, 81920\n"
    "s_add_u32 s56, s66, 0\n"
    "buffer_load_dwordx4 v4, s[40:43], s56 offen lds\n"
    "s_waitcnt lgkmcnt(4)\n"
    "v_mfma_f32_32x32x16_bf16 v[144:159], v[44:47], v[24:27], v[144:159]\n"
    "ds_read_b64_tr_b16 v[56:57], v10 offset:256\n"
    "ds_read_b64_tr_b16 v[58:59], v10 offset:2304\n"
    "v_mfma_f32_32x32x16_bf16 a[16:31], v[44:47], v[28:31], a[16:31]\n"
    "s_add_u32 m0, s70, 82944\n"
    "s_add_u32 s56, s66, 16384\n"
    "buffer_load_dwordx4 v5, s[40:43], s56 offen lds\n"
    "s_waitcnt lgkmcnt(4)\n"
    "v_mfma_f32_32x32x16_bf16 v[160:175], v[48:51], v[24:27], v[160:175]\n"
    "ds_read_b64_tr_b16 v[60:61], v11 offset:256\n"
    "ds_read_b64_tr_b16 v[62:63], v11 offset:2304\n"
    "ds_read_b128 v[32:35], v16 offset:16384\n"
    "v_mfma_f32_32x32x16_bf16 a[32:47], v[48:51], v[28:31], a[32:47]\n"
    "s_add_u32 m0, s70, 83968\n"
    "s_add_u32 s56, s66, 32768\n"
    "buffer_load_dwordx4 v4, s[40:43], s56 offen lds\n"
    "s_waitcnt lgkmcnt(5)\n"
    "v_mfma_f32_32x32x16_bf16 v[176:191], v[52:55], v[24:27], v[176:191]\n"
    "ds_read_b64_tr_b16 v[40:41], v12 offset:256\n"
    "ds_read_b64_tr_b16 v[42:43], v12 offset:2304\n"
    "ds_read_b128 v[36:39], v16 offset:20480\n"
    "v_mfma_f32_32x32x16_bf16 a[48:63], v[52:55], v[28:31], a[48:63]\n"
    "s_add_u32 m0, s70, 84992\n"
    "s_add_u32 s56, s66, 49152\n"
    "buffer_load_dwordx4 v5, s[40:43], s56 offen lds\n"
    "s_waitcnt lgkmcnt(6)\n"
    "v_mfma_f32_32x32x16_bf16 v[192:207], v[56:59], v[24:27], v[192:207]\n"
    "ds_read_b64_tr_b16 v[44:45], v13 offset:256\n"
    "ds_read_b64_tr_b16 v[46:47], v13 offset:2304\n"
    "v_mfma_f32_32x32x16_bf16 a[64:79], v[56:59], v[28:31], a[64:79]\n"
    "s_add_u32 m0, s70, 86016\n"
    "s_add_u32 s56, s71, 0\n"
    "buffer_load_dwordx4 v4, s[40:43], s56 offen lds\n"
    "s_waitcnt lgkmcnt(6)\n"
    "v_mfma_f32_32x32x16_bf16 v[208:223], v[60:63], v[24:27], v[208:223]\n"
    "ds_read_b64_tr_b16 v[48:49], v14 offset:16384\n"
    "ds_read_b64_tr_b16 v[50:51], v14 offset:16640\n"
    "s_add_u32 s62, s62, 20480\n"
    "s_cmp_ge_u32 s62, 81920\n"
    "s_cselect_b32 s62, 0, s62\n"
    "v_mfma_f32_32x32x16_bf16 a[80:95], v[60:63], v[28:31], a[80:95]\n"
    "s_add_u32 m0, s70, 87040\n"
    "s_add_u32 s56, s71, 16384\n"
    "buffer_load_dwordx4 v5, s[40:43], s56 offen lds\n"
    "s_waitcnt lgkmcnt(5)\n"
    "v_mfma_f32_32x32x16_bf16 v[224:239], v[40:43], v[24:27], v[224:239]\n"
    "ds_read_b64_tr_b16 v[52:53], v10 offset:8192\n"
    "ds_read_b64_tr_b16 v[54:55], v10 offset:10240\n"
    "v_mfma_f32_32x32x16_bf16 a[96:111], v[40:43], v[28:31], a[96:111]\n"
    "s_add_u32 m0, s70, 88064\n"
    "s_add_u32 s56, s71, 32768\n"
    "buffer_load_dwordx4 v4, s[40:43], s56 offen lds\n"
    "s_waitcnt lgkmcnt(4)\n"
    "v_mfma_f32_32x32x16_bf16 v[240:255], v[44:47], v[24:27], v[240:255]\n"
    "ds_read_b64_tr_b16 v[56:57], v11 offset:8192\n"
    "ds_read_b64_tr_b16 v[58:59], v11 offset:10240\n"
    "v_mfma_f32_32x32x16_bf16 a[112:127], v[44:47], v[28:31], a[112:127]\n"
    "s_add_u32 m0, s70, 89088\n"
    "s_add_u32 s56, s71, 49152\n"
    "buffer_load_dwordx4 v5, s[40:43], s56 offen lds\n"
    "s_waitcnt lgkmcnt(4)\n"
    "v_mfma_f32_32x32x16_bf16 v[112:127], v[48:51], v[24:27], v[112:127]\n"
    "ds_read_b64_tr_b16 v[60:61], v12 offset:8192\n"
    "ds_read_b64_tr_b16 v[62:63], v12 offset:10240\n"
    "s_waitcnt lgkmcnt(4)\n"
    "v_mfma_f32_32x32x16_bf16 v[128:143], v[52:55], v[32:35], v[128:143]\n"
    "ds_read_b64_tr_b16 v[40:41], v13 offset:8192\n"
    "ds_read_b64_tr_b16 v[42:43], v13 offset:10240\n"
    "v_mfma_f32_32x32x16_bf16 a[0:15], v[52:55], v[36:39], a[0:15]\n"
    "s_add_u32 m0, s63, 0\n"
    "s_add_u32 s56, s64, 0\n"
    "buffer_load_dwordx4 v3, s[32:35], s56 offen lds\n"
    "s_waitcnt lgkmcnt(4)\n"
    "v_mfma_f32_32x32x16_bf16 v[144:159], v[56:59], v[32:35], v[144:159]\n"
    "ds_read_b64_tr_b16 v[44:45], v10 offset:8448\n"
    "ds_read_b64_tr_b16 v[46:47], v10 offset:10496\n"
    "v_mfma_f32_32x32x16_bf16 a[16:31], v[56:59], v[36:39], a[16:31]\n"
    "s_add_u32 m0, s63, 4096\n"
    "s_add_u32 s56, s64, 16384\n"
    "buffer_load_dwordx4 v3, s[32:35], s56 offen lds\n"
    "s_waitcnt lgkmcnt(4)\n"
    "v_mfma_f32_32x32x16_bf16 v[160:175], v[60:63], v[32:35], v[160:175]\n"
    "ds_read_b64_tr_b16 v[48:49], v11 offset:8448\n"
    "ds_read_b64_tr_b16 v[50:51], v11 offset:10496\n"
    "v_mfma_f32_32x32x16_bf16 a[32:47], v[60:63], v[36:39], a[32:47]\n"
    "s_add_u32 m0, s63, 8192\n"
    "s_add_u32 s56, s64, 32768\n"
    "buffer_load_dwordx4 v3, s[32:35], s56 offen lds\n"
    "s_waitcnt lgkmcnt(4)\n"
    "v_mfma_f32_32x32x16_bf16 v[176:191], v[40:43], v[32:35], v[176:191]\n"
    "ds_read_b64_tr_b16 v[52:53], v12 offset:8448\n"
    "ds_read_b64_tr_b16 v[54:55], v12 offset:10496\n"
    "v_mfma_f32_32x32x16_bf16 a[48:63], v[40:43], v[36:39], a[48:63]\n"
    "s_add_u32 m0, s63, 12288\n"
    "s_add_u32 s56, s64, 49152\n"
    "buffer_load_dwordx4 v3, s[32:35], s56 offen lds\n"
    "s_waitcnt lgkmcnt(4)\n"
    "v_mfma_f32_32x32x16_bf16 v[192:207], v[44:47], v[32:35], v[192:207]\n"
    "ds_read_b64_tr_b16 v[56:57], v13 offset:8448\n"
    "ds_read_b64_tr_b16 v[58:59], v13 offset:10496\n"
    "v_mfma_f32_32x32x16_bf16 a[64:79], v[44:47], v[36:39], a[64:79]\n"
    "s_add_u32 m0, s63, 16384\n"
    "s_nop 0\n"
    "buffer_load_dwordx4 v2, s[36:39], s65 offen lds\n"
    "s_waitcnt lgkmcnt(4)\n"
    "v_mfma_f32_32x32x16_bf16 v[208:223], v[48:51], v[32:35], v[208:223]\n"
    "ds_read_b64_tr_b16 v[60:61], v14 offset:17408\n"
    "ds_read_b64_tr_b16 v[62:63], v14 offset:17664\n"
    "v_mfma_f32_32x32x16_bf16 a[80:95], v[48:51], v[36:39], a[80:95]\n"
    "s_add_u32 s64, s64, 65536\n"
    "s_add_u32 s65, s65, 2048\n"
    "s_cmp_ge_u32 s63, 61440\n"
    "s_cselect_b32 s56, 4294905856, s58\n"
    "s_add_u32 s63, s63, s56\n"
    "s_waitcnt lgkmcnt(4)\n"
    "v_mfma_f32_32x32x16_bf16 v[224:239], v[52:55], v[32:35], v[224:239]\n"
    "v_add_u32 v10, s62, v6\n"
    "v_add_u32 v14, s62, v7\n"
    "v_xor_b32 v11, 64, v10\n"
    "v_xor_b32 v12, 128, v10\n"
    "v_xor_b32 v13, 192, v10\n"
    "v_mfma_f32_32x32x16_bf16 a[96:111], v[52:55], v[36:39], a[96:111]\n"
    "s_waitcnt vmcnt(18) lgkmcnt(0)\n"
    "s_barrier\n"
    "ds_read_b128 v[24:27], v17 offset:16384\n"
    "ds_read_b128 v[28:31], v17 offset:20480\n"
    "ds_read_b64_tr_b16 v[40:41], v10 offset:0\n"
    "ds_read_b64_tr_b16 v[42:43], v10 offset:2048\n"
    "v_mfma_f32_32x32x16_bf16 v[240:255], v[56:59], v[32:35], v[240:255]\n"
    "ds_read_b64_tr_b16 v[44:45], v11 offset:0\n"
    "ds_read_b64_tr_b16 v[46:47], v11 offset:2048\n"
    "v_mfma_f32_32x32x16_bf16 a[112:127], v[56:59], v[36:39], a[112:127]\n"
    "v_mfma_f32_32x32x16_bf16 v[112:127], v[60:63], v[32:35], v[112:127]\n"
    "ds_read_b64_tr_b16 v[48:49], v12 offset:0\n"
    "ds_read_b64_tr_b16 v[50:51], v12 offset:2048\n"
    "s_waitcnt lgkmcnt(4)\n"
    "v_mfma_f32_32x32x16_bf16 v[128:143], v[40:43], v[24:27], v[128:143]\n"
    "ds_read_b64_tr_b16 v[52:53], v13 offset:0\n"
    "ds_read_b64_tr_b16 v[54:55], v13 offset:2048\n"
    "v_mfma_f32_32x32x16_bf16 a[0:15], v[40:43], v[28:31], a[0:15]\n"
    "s_add_u32 m0, s63, 0\n"
    "s_add_u32 s56, s64, 0\n"
    "buffer_load_dwordx4 v3, s[32:35], s56 offen lds\n"
    "s_waitcnt lgkmcnt(4)\n"
    "v_mfma_f32_32x32x16_bf16 v[144:159], v[44:47], v[24:27], v[144:159]\n"
    "ds_read_b64_tr_b16 v[56:57], v10 offset:256\n"
    "ds_read_b64_tr_b16 v[58:59], v10 offset:2304\n"
    "v_mfma_f32_32x32x16_bf16 a[16:31], v[44:47], v[28:31], a[16:31]\n"
    "s_add_u32 m0, s63, 4096\n"
    "s_add_u32 s56, s64, 16384\n"
    "buffer_load_dwordx4 v3, s[32:35], s56 offen lds\n"
    "s_waitcnt lgkmcnt(4)\n"
    "v_mfma_f32_32x32x16_bf16 v[160:175], v[48:51], v[24:27], v[160:175]\n"
    "ds_read_b64_tr_b16 v[60:61], v11 offset:256\n"
    "ds_read_b64_tr_b16 v[62:63], v11 offset:2304\n"
    "ds_read_b128 v[32:35], v18 offset:16384\n"
    "v_mfma_f32_32x32x16_bf16 a[32:47], v[48:51], v[28:31], a[32:47]\n"
    "s_add_u32 m0, s63, 8192\n"
    "s_add_u32 s56, s64, 32768\n"
    "buffer_load_dwordx4 v3, s[32:35], s56 offen lds\n"
    "s_waitcnt lgkmcnt(5)\n"
    "v_mfma_f32_32x32x16_bf16 v[176:191], v[52:55], v[24:27], v[176:191]\n"
    "ds_read_b64_tr_b16 v[40:41], v12 offset:256\n"
    "ds_read_b64_tr_b16 v[42:43], v12 offset:2304\n"
    "ds_read_b128 v[36:39], v18 offset:20480\n"
    "v_mfma_f32_32x32x16_bf16 a[48:63], v[52:55], v[28:31], a[48:63]\n"
    "s_add_u32 m0, s63, 12288\n"
    "s_add_u32 s56, s64, 49152\n"
    "buffer_load_dwordx4 v3, s[32:35], s56 offen lds\n"
    "s_waitcnt lgkmcnt(6)\n"
    "v_mfma_f32_32x32x16_bf16 v[192:207], v[56:59], v[24:27], v[192:207]\n"
    "ds_read_b64_tr_b16 v[44:45], v13 offset:256\n"
    "ds_read_b64_tr_b16 v[46:47], v13 offset:2304\n"
    "v_mfma_f32_32x32x16_bf16 a[64:79], v[56:59], v[28:31], a[64:79]\n"
    "s_add_u32 m0, s63, 16384\n"
    "s_nop 0\n"
    "buffer_load_dwordx4 v2, s[36:39], s65 offen lds\n"
    "s_waitcnt lgkmcnt(6)\n"
    "v_mfma_f32_32x32x16_bf16 v[208:223], v[60:63], v[24:27], v[208:223]\n"
    "ds_read_b64_tr_b16 v[48:49], v14 offset:16384\n"
    "ds_read_b64_tr_b16 v[50:51], v14 offset:16640\n"
    "s_add_u32 s62, s62, 20480\n"
    "s_cmp_ge_u32 s62, 81920\n"
    "s_cselect_b32 s62, 0, s62\n"
    "v_mfma_f32_32x32x16_bf16 a[80:95], v[60:63], v[28:31], a[80:95]\n"
    "s_add_u32 s66, s66, 128\n"
    "s_add_u32 s71, s71, 128\n"
    "s_xor_b32 s70, s70, 32768\n"
    "s_waitcnt lgkmcnt(5)\n"
    "v_mfma_f32_32x32x16_bf16 v[224:239], v[40:43], v[24:27], v[224:239]\n"
    "ds_read_b64_tr_b16 v[52:53], v10 offset:8192\n"
    "ds_read_b64_tr_b16 v[54:55], v10 offset:10240\n"
    "v_xor_b32 v15, 32768, v15\n"
    "v_xor_b32 v16, 32768, v16\n"
    "v_xor_b32 v17, 32768, v17\n"
    "v_xor_b32 v18, 32768, v18\n"
    "v_mfma_f32_32x32x16_bf16 a[96:111], v[40:43], v[28:31], a[96:111]\n"
    "s_add_u32 s64, s64, 65536\n"
    "s_add_u32 s65, s65, 2048\n"
    "s_cmp_ge_u32 s63, 61440\n"
    "s_cselect_b32 s56, 4294905856, s58\n"
    "s_add_u32 s63, s63, s56\n"
    "s_waitcnt lgkmcnt(4)\n"
    "v_mfma_f32_32x32x16_bf16 v[240:255], v[44:47], v[24:27], v[240:255]\n"
    "ds_read_b64_tr_b16 v[56:57], v11 offset:8192\n"
    "ds_read_b64_tr_b16 v[58:59], v11 offset:10240\n"
    "v_mfma_f32_32x32x16_bf16 a[112:127], v[44:47], v[28:31], a[112:127]\n"
    "s_waitcnt lgkmcnt(4)\n"
    "v_mfma_f32_32x32x16_bf16 v[112:127], v[48:51], v[24:27], v[112:127]\n"
    "ds_read_b64_tr_b16 v[60:61], v12 offset:8192\n"
    "ds_read_b64_tr_b16 v[62:63], v12 offset:10240\n"
    "s_waitcnt lgkmcnt(4)\n"
    "v_mfma_f32_32x32x16_bf16 v[128:143], v[52:55], v[32:35], v[128:143]\n"
    "ds_read_b64_tr_b16 v[40:41], v13 offset:8192\n"
    "ds_read_b64_tr_b16 v[42:43], v13 offset:10240\n"
    "v_mfma_f32_32x32x16_bf16 a[0:15], v[52:55], v[36:39], a[0:15]\n"
    "s_waitcnt lgkmcnt(4)\n"
    "v_mfma_f32_32x32x16_bf16 v[144:159], v[56:59], v[32:35], v[144:159]\n"
    "ds_read_b64_tr_b16 v[44:45], v10 offset:8448\n"
    "ds_read_b64_tr_b16 v[46:47], v10 offset:10496\n"
    "v_mfma_f32_32x32x16_bf16 a[16:31], v[56:59], v[36:39], a[16:31]\n"
    "s_waitcnt lgkmcnt(4)\n"
    "v_mfma_f32_32x32x16_bf16 v[160:175], v[60:63], v[32:35], v[160:175]\n"
    "ds_read_b64_tr_b16 v[48:49], v11 offset:8448\n"
    "ds_read_b64_tr_b16 v[50:51], v11 offset:10496\n"
    "v_mfma_f32_32x32x16_bf16 a[32:47], v[60:63], v[36:39], a[32:47]\n"
    "s_waitcnt lgkmcnt(4)\n"
    "v_mfma_f32_32x32x16_bf16 v[176:191], v[40:43], v[32:35], v[176:191]\n"
    "ds_read_b64_tr_b16 v[52:53], v12 offset:8448\n"
    "ds_read_b64_tr_b16 v[54:55], v12 offset:10496\n"
    "v_mfma_f32_32x32x16_bf16 a[48:63], v[40:43], v[36:39], a[48:63]\n"
    "s_waitcnt lgkmcnt(4)\n"
    "v_mfma_f32_32x32x16_bf16 v[192:207], v[44:47], v[32:35], v[192:207]\n"
    "ds_read_b64_tr_b16 v[56:57], v13 offset:8448\n"
    "ds_read_b64_tr_b16 v[58:59], v13 offset:10496\n"
    "v_mfma_f32_32x32x16_bf16 a[64:79], v[44:47], v[36:39], a[64:79]\n"
    "s_waitcnt lgkmcnt(4)\n"
    "v_mfma_f32_32x32x16_bf16 v[208:223], v[48:51], v[32:35], v[208:223]\n"
    "ds_read_b64_tr_b16 v[60:61], v14 offset:17408\n"
    "ds_read_b64_tr_b16 v[62:63], v14 offset:17664\n"
    "v_mfma_f32_32x32x16_bf16 a[80:95], v[48:51], v[36:39], a[80:95]\n"
    "s_waitcnt lgkmcnt(4)\n"
    "v_mfma_f32_32x32x16_bf16 v[224:239], v[52:55], v[32:35], v[224:239]\n"
    "v_add_u32 v10, s62, v6\n"
    "v_add_u32 v14, s62, v7\n"
    "v_xor_b32 v11, 64, v10\n"
    "v_xor_b32 v12, 128, v10\n"
    "v_xor_b32 v13, 192, v10\n"
    "v_mfma_f32_32x32x16_bf16 a[96:111], v[52:55], v[36:39], a[96:111]\n"
    "s_waitcnt vmcnt(10) lgkmcnt(0)\n"
    "s_barrier\n"
    "ds_read_b128 v[24:27], v15 offset:16384\n"
    "ds_read_b128 v[28:31], v15 offset:20480\n"
    "ds_read_b64_tr_b16 v[40:41], v10 offset:0\n"
    "ds_read_b64_tr_b16 v[42:43], v10 offset:2048\n"
    "v_mfma_f32_32x32x16_bf16 v[240:255], v[56:59], v[32:35], v[240:255]\n"
    "ds_read_b64_tr_b16 v[44:45], v11 offset:0\n"
    "ds_read_b64_tr_b16 v[46:47], v11 offset:2048\n"
    "v_mfma_f32_32x32x16_bf16 a[112:127], v[56:59], v[36:39], a[112:127]\n"
    "v_mfma_f32_32x32x16_bf16 v[112:127], v[60:63], v[32:35], v[112:127]\n"
    "ds_read_b64_tr_b16 v[48:49], v12 offset:0\n"
    "ds_read_b64_tr_b16 v[50:51], v12 offset:2048\n"
    "s_add_u32 s61, s61, 1\n"
    "s_cmp_eq_u32 s61, 3\n"
    "s_cbranch_scc1 .Lat_hsdo_%=\n"
    "s_cmp_lt_u32 s61, 16\n"
    "s_cbranch_scc1 .Lat_pk_%=\n"
    "s_waitcnt lgkmcnt(0)\n"
    "s_branch .Lat_pdone_%=\n"
    ".Lat_pv0_%=:\n"
    "s_mov_b32 s64, 0\n"
    "s_lshl_b32 s65, s25, 10\n"
    "s_lshl_b32 s66, s60, 16\n"
    "s_xor_b32 s71, s66, 262144\n"
    "s_lshl_b32 s63, s25, 10\n"
    "s_lshl_b32 s70, s25, 13\n"
    "s_mov_b32 s58, 20480\n"
    "s_mov_b32 s62, 0\n"
    "s_mov_b32 s61, 0\n"
    "s_add_u32 m0, s70, 81920\n"
    "s_add_u32 s56, s66, 0\n"
    "buffer_load_dwordx4 v4, s[40:43], s56 offen lds\n"
    "s_add_u32 m0, s70, 82944\n"
    "s_add_u32 s56, s66, 16384\n"
    "buffer_load_dwordx4 v5, s[40:43], s56 offen lds\n"
    "s_add_u32 m0, s70, 83968\n"
    "s_add_u32 s56, s66, 32768\n"
    "buffer_load_dwordx4 v4, s[40:43], s56 offen lds\n"
    "s_add_u32 m0, s70, 84992\n"
    "s_add_u32 s56, s66, 49152\n"
    "buffer_load_dwordx4 v5, s[40:43], s56 offen lds\n"
    "s_add_u32 m0, s70, 86016\n"
    "s_add_u32 s56, s71, 0\n"
    "buffer_load_dwordx4 v4, s[40:43], s56 offen lds\n"
    "s_add_u32 m0, s70, 87040\n"
    "s_add_u32 s56, s71, 16384\n"
    "buffer_load_dwordx4 v5, s[40:43], s56 offen lds\n"
    "s_add_u32 m0, s70, 88064\n"
    "s_add_u32 s56, s71, 32768\n"
    "buffer_load_dwordx4 v4, s[40:43], s56 offen lds\n"
    "s_add_u32 m0, s70, 89088\n"
    "s_add_u32 s56, s71, 49152\n"
    "buffer_load_dwordx4 v5, s[40:43], s56 offen lds\n"
    "s_add_u32 s66, s66, 128\n"
    "s_add_u32 s71, s71, 128\n"
    "s_xor_b32 s70, s70, 32768\n"
    "s_add_u32 m0, s63, 0\n"
    "s_add_u32 s56, s64, 0\n"
    "buffer_load_dwordx4 v3, s[32:35], s56 offen lds\n"
    "s_add_u32 m0, s63, 4096\n"
    "s_add_u32 s56, s64, 16384\n"
    "buffer_load_dwordx4 v3, s[32:35], s56 offen lds\n"
    "s_add_u32 m0, s63, 8192\n"
    "s_add_u32 s56, s64, 32768\n"
    "buffer_load_dwordx4 v3, s[32:35], s56 offen lds\n"
    "s_add_u32 m0, s63, 12288\n"
    "s_add_u32 s56, s64, 49152\n"
    "buffer_load_dwordx4 v3, s[32:35], s56 offen lds\n"
    "s_add_u32 m0, s63, 16384\n"
    "s_nop 0\n"
    "buffer_load_dwordx4 v2, s[36:39], s65 offen lds\n"
    "s_add_u32 s64, s64, 65536\n"
    "s_add_u32 s65, s65, 2048\n"
    "s_cmp_ge_u32 s63, 61440\n"
    "s_cselect_b32 s56, 4294905856, s58\n"
    "s_add_u32 s63, s63, s56\n"
    "s_add_u32 m0, s63, 0\n"
    "s_add_u32 s56, s64, 0\n"
    "buffer_load_dwordx4 v3, s[32:35], s56 offen lds\n"
    "s_add_u32 m0, s63, 4096\n"
    "s_add_u32 s56, s64, 16384\n"
    "buffer_load_dwordx4 v3, s[32:35], s56 offen lds\n"
    "s_add_u32 m0, s63, 8192\n"
    "s_add_u32 s56, s64, 32768\n"
    "buffer_load_dwordx4 v3, s[32:35], s56 offen lds\n"
    "s_add_u32 m0, s63, 12288\n"
    "s_add_u32 s56, s64, 49152\n"
    "buffer_load_dwordx4 v3, s[32:35], s56 offen lds\n"
    "s_add_u32 m0, s63, 16384\n"
    "s_nop 0\n"
    "buffer_load_dwordx4 v2, s[36:39], s65 offen lds\n"
    "s_add_u32 s64, s64, 65536\n"
    "s_add_u32 s65, s65, 2048\n"
    "s_cmp_ge_u32 s63, 61440\n"
    "s_cselect_b32 s56, 4294905856, s58\n"
    "s_add_u32 s63, s63, s56\n"
    "s_add_u32 m0, s63, 0\n"
    "s_add_u32 s56, s64, 0\n"
    "buffer_load_dwordx4 v3, s[32:35], s56 offen lds\n"
    "s_add_u32 m0, s63, 4096\n"
    "s_add_u32 s56, s64, 16384\n"
    "buffer_load_dwordx4 v3, s[32:35], s56 offen lds\n"
    "s_add_u32 m0, s63, 8192\n"
    "s_add_u32 s56, s64, 32768\n"
    "buffer_load_dwordx4 v3, s[32:35], s56 offen lds\n"
    "s_add_u32 m0, s63, 12288\n"
    "s_add_u32 s56, s64, 49152\n"
    "buffer_load_dwordx4 v3, s[32:35], s56 offen lds\n"
    "s_add_u32 m0, s63, 16384\n"
    "s_nop 0\n"
    "buffer_load_dwordx4 v2, s[36:39], s65 offen lds\n"
    "s_add_u32 s64, s64, 65536\n"
    "s_add_u32 s65, s65, 2048\n"
    "s_cmp_ge_u32 s63, 61440\n"
    "s_cselect_b32 s56, 4294905856, s58\n"
    "s_add_u32 s63, s63, s56\n"
    "v_add_u32 v10, s62, v6\n"
    "v_add_u32 v14, s62, v7\n"
    "v_xor_b32 v11, 64, v10\n"
    "v_xor_b32 v12, 128, v10\n"
    "v_xor_b32 v13, 192, v10\n"
    "v_xor_b32 v15, 65536, v8\n"
    "v_xor_b32 v16, 65568, v8\n"
    "v_xor_b32 v17, 65600, v8\n"
    "v_xor_b32 v18, 65632, v8\n"
    "v_mov_b64 v[112:113], 0\n"
    "v_mov_b64 v[114:115], 0\n"
    "v_mov_b64 v[116:117], 0\n"
    "v_mov_b64 v[118:119], 0\n"
    "v_mov_b64 v[120:121], 0\n"
    "v_mov_b64 v[122:123], 0\n"
    "v_mov_b64 v[124:125], 0\n"
    "v_mov_b64 v[126:127], 0\n"
    "v_mov_b64 v[128:129], 0\n"
    "v_mov_b64 v[130:131], 0\n"
    "v_mov_b64 v[132:133], 0\n"
    "v_mov_b64 v[134:135], 0\n"
    "v_mov_b64 v[136:137], 0\n"
    "v_mov_b64 v[138:139], 0\n"
    "v_mov_b64 v[140:141], 0\n"
    "v_mov_b64 v[142:143], 0\n"
    "v_mov_b64 v[144:145], 0\n"
    "v_mov_b64 v[146:147], 0\n"
    "v_mov_b64 v[148:149], 0\n"
    "v_mov_b64 v[150:151], 0\n"
    "v_mov_b64 v[152:153], 0\n"
    "v_mov_b64 v[154:155], 0\n"
    "v_mov_b64 v[156:157], 0\n"
    "v_mov_b64 v[158:159], 0\n"
    "v_mov_b64 v[160:161], 0\n"
    "v_mov_b64 v[162:163], 0\n"
    "v_mov_b64 v[164:165], 0\n"
    "v_mov_b64 v[166:167], 0\n"
    "v_mov_b64 v[168:169], 0\n"
    "v_mov_b64 v[170:171], 0\n"
    "v_mov_b64 v[172:173], 0\n"
    "v_mov_b64 v[174:175], 0\n"
    "v_mov_b64 v[176:177], 0\n"
    "v_mov_b64 v[178:179], 0\n"
    "v_mov_b64 v[180:181], 0\n"
    "v_mov_b64 v[182:183], 0\n"
    "v_mov_b64 v[184:185], 0\n"
    "v_mov_b64 v[186:187], 0\n"
    "v_mov_b64 v[188:189], 0\n"
    "v_mov_b64 v[190:191], 0\n"
    "v_mov_b64 v[192:193], 0\n"
    "v_mov_b64 v[194:195], 0\n"
    "v_mov_b64 v[196:197], 0\n"
    "v_mov_b64 v[198:199], 0\n"
    "v_mov_b64 v[200:201], 0\n"
    "v_mov_b64 v[202:203], 0\n"
    "v_mov_b64 v[204:205], 0\n"
    "v_mov_b64 v[206:207], 0\n"
    "v_mov_b64 v[208:209], 0\n"
    "v_mov_b64 v[210:211], 0\n"
    "v_mov_b64 v[212:213], 0\n"
    "v_mov_b64 v[214:215], 0\n"
    "v_mov_b64 v[216:217], 0\n"
    "v_mov_b64 v[218:219], 0\n"
    "v_mov_b64 v[220:221], 0\n"
    "v_mov_b64 v[222:223], 0\n"
    "v_mov_b64 v[224:225], 0\n"
    "v_mov_b64 v[226:227], 0\n"
    "v_mov_b64 v[228:229], 0\n"
    "v_mov_b64 v[230:231], 0\n"
    "v_mov_b64 v[232:233], 0\n"
    "v_mov_b64 v[234:235], 0\n"
    "v_mov_b64 v[236:237], 0\n"
    "v_mov_b64 v[238:239], 0\n"
    "v_mov_b64 v[240:241], 0\n"
    "v_mov_b64 v[242:243], 0\n"
    "v_mov_b64 v[244:245], 0\n"
    "v_mov_b64 v[246:247], 0\n"
    "v_mov_b64 v[248:249], 0\n"
    "v_mov_b64 v[250:251], 0\n"
    "v_mov_b64 v[252:253], 0\n"
    "v_mov_b64 v[254:255], 0\n"
    "v_accvgpr_write_b32 a0, 0\n"
    "v_accvgpr_write_b32 a1, 0\n"
    "v_accvgpr_write_b32 a2, 0\n"
    "v_accvgpr_write_b32 a3, 0\n"
    "v_accvgpr_write_b32 a4, 0\n"
    "v_accvgpr_write_b32 a5, 0\n"
    "v_accvgpr_write_b32 a6, 0\n"
    "v_accvgpr_write_b32 a7, 0\n"
    "v_accvgpr_write_b32 a8, 0\n"
    "v_accvgpr_write_b32 a9, 0\n"
    "v_accvgpr_write_b32 a10, 0\n"
    "v_accvgpr_write_b32 a11, 0\n"
    "v_accvgpr_write_b32 a12, 0\n"
    "v_accvgpr_write_b32 a13, 0\n"
    "v_accvgpr_write_b32 a14, 0\n"
    "v_accvgpr_write_b32 a15, 0\n"
    "v_accvgpr_write_b32 a16, 0\n"
    "v_accvgpr_write_b32 a17, 0\n"
    "v_accvgpr_write_b32 a18, 0\n"
    "v_accvgpr_write_b32 a19, 0\n"
    "v_accvgpr_write_b32 a20, 0\n"
    "v_accvgpr_write_b32 a21, 0\n"
    "v_accvgpr_write_b32 a22, 0\n"
    "v_accvgpr_write_b32 a23, 0\n"
    "v_accvgpr_write_b32 a24, 0\n"
    "v_accvgpr_write_b32 a25, 0\n"
    "v_accvgpr_write_b32 a26, 0\n"
    "v_accvgpr_write_b32 a27, 0\n"
    "v_accvgpr_write_b32 a28, 0\n"
    "v_accvgpr_write_b32 a29, 0\n"
    "v_accvgpr_write_b32 a30, 0\n"
    "v_accvgpr_write_b32 a31, 0\n"
    "v_accvgpr_write_b32 a32, 0\n"
    "v_accvgpr_write_b32 a33, 0\n"
    "v_accvgpr_write_b32 a34, 0\n"
    "v_accvgpr_write_b32 a35, 0\n"
    "v_accvgpr_write_b32 a36, 0\n"
    "v_accvgpr_write_b32 a37, 0\n"
    "v_accvgpr_write_b32 a38, 0\n"
    "v_accvgpr_write_b32 a39, 0\n"
    "v_accvgpr_write_b32 a40, 0\n"
    "v_accvgpr_write_b32 a41, 0\n"
    "v_accvgpr_write_b32 a42, 0\n"
    "v_accvgpr_write_b32 a43, 0\n"
    "v_accvgpr_write_b32 a44, 0\n"
    "v_accvgpr_write_b32 a45, 0\n"
    "v_accvgpr_write_b32 a46, 0\n"
    "v_accvgpr_write_b32 a47, 0\n"
    "v_accvgpr_write_b32 a48, 0\n"
    "v_accvgpr_write_b32 a49, 0\n"
    "v_accvgpr_write_b32 a50, 0\n"
    "v_accvgpr_write_b32 a51, 0\n"
    "v_accvgpr_write_b32 a52, 0\n"
    "v_accvgpr_write_b32 a53, 0\n"
    "v_accvgpr_write_b32 a54, 0\n"
    "v_accvgpr_write_b32 a55, 0\n"
    "v_accvgpr_write_b32 a56, 0\n"
    "v_accvgpr_write_b32 a57, 0\n"
    "v_accvgpr_write_b32 a58, 0\n"
    "v_accvgpr_write_b32 a59, 0\n"
    "v_accvgpr_write_b32 a60, 0\n"
    "v_accvgpr_write_b32 a61, 0\n"
    "v_accvgpr_write_b32 a62, 0\n"
    "v_accvgpr_write_b32 a63, 0\n"
    "v_accvgpr_write_b32 a64, 0\n"
    "v_accvgpr_write_b32 a65, 0\n"
    "v_accvgpr_write_b32 a66, 0\n"
    "v_accvgpr_write_b32 a67, 0\n"
    "v_accvgpr_write_b32 a68, 0\n"
    "v_accvgpr_write_b32 a69, 0\n"
    "v_accvgpr_write_b32 a70, 0\n"
    "v_accvgpr_write_b32 a71, 0\n"
    "v_accvgpr_write_b32 a72, 0\n"
    "v_accvgpr_write_b32 a73, 0\n"
    "v_accvgpr_write_b32 a74, 0\n"
    "v_accvgpr_write_b32 a75, 0\n"
    "v_accvgpr_write_b32 a76, 0\n"
    "v_accvgpr_write_b32 a77, 0\n"
    "v_accvgpr_write_b32 a78, 0\n"
    "v_accvgpr_write_b32 a79, 0\n"
    "v_accvgpr_write_b32 a80, 0\n"
    "v_accvgpr_write_b32 a81, 0\n"
    "v_accvgpr_write_b32 a82, 0\n"
    "v_accvgpr_write_b32 a83, 0\n"
    "v_accvgpr_write_b32 a84, 0\n"
    "v_accvgpr_write_b32 a85, 0\n"
    "v_accvgpr_write_b32 a86, 0\n"
    "v_accvgpr_write_b32 a87, 0\n"
    "v_accvgpr_write_b32 a88, 0\n"
    "v_accvgpr_write_b32 a89, 0\n"
    "v_accvgpr_write_b32 a90, 0\n"
    "v_accvgpr_write_b32 a91, 0\n"
    "v_accvgpr_write_b32 a92, 0\n"
    "v_accvgpr_write_b32 a93, 0\n"
    "v_accvgpr_write_b32 a94, 0\n"
    "v_accvgpr_write_b32 a95, 0\n"
    "v_accvgpr_write_b32 a96, 0\n"
    "v_accvgpr_write_b32 a97, 0\n"
    "v_accvgpr_write_b32 a98, 0\n"
    "v_accvgpr_write_b32 a99, 0\n"
    "v_accvgpr_write_b32 a100, 0\n"
    "v_accvgpr_write_b32 a101, 0\n"
    "v_accvgpr_write_b32 a102, 0\n"
    "v_accvgpr_write_b32 a103, 0\n"
    "v_accvgpr_write_b32 a104, 0\n"
    "v_accvgpr_write_b32 a105, 0\n"
    "v_accvgpr_write_b32 a106, 0\n"
    "v_accvgpr_write_b32 a107, 0\n"
    "v_accvgpr_write_b32 a108, 0\n"
    "v_accvgpr_write_b32 a109, 0\n"
    "v_accvgpr_write_b32 a110, 0\n"
    "v_accvgpr_write_b32 a111, 0\n"
    "v_accvgpr_write_b32 a112, 0\n"
    "v_accvgpr_write_b32 a113, 0\n"
    "v_accvgpr_write_b32 a114, 0\n"
    "v_accvgpr_write_b32 a115, 0\n"
    "v_accvgpr_write_b32 a116, 0\n"
    "v_accvgpr_write_b32 a117, 0\n"
    "v_accvgpr_write_b32 a118, 0\n"
    "v_accvgpr_write_b32 a119, 0\n"
    "v_accvgpr_write_b32 a120, 0\n"
    "v_accvgpr_write_b32 a121, 0\n"
    "v_accvgpr_write_b32 a122, 0\n"
    "v_accvgpr_write_b32 a123, 0\n"
    "v_accvgpr_write_b32 a124, 0\n"
    "v_accvgpr_write_b32 a125, 0\n"
    "v_accvgpr_write_b32 a126, 0\n"
    "v_accvgpr_write_b32 a127, 0\n"
    "s_waitcnt vmcnt(10)\n"
    "s_barrier\n"
    "ds_read_b128 v[24:27], v15 offset:16384\n"
    "ds_read_b128 v[28:31], v15 offset:20480\n"
    "ds_read_b64_tr_b16 v[40:41], v10 offset:0\n"
    "ds_read_b64_tr_b16 v[42:43], v10 offset:2048\n"
    "ds_read_b64_tr_b16 v[44:45], v11 offset:0\n"
    "ds_read_b64_tr_b16 v[46:47], v11 offset:2048\n"
    "ds_read_b64_tr_b16 v[48:49], v12 offset:0\n"
    "ds_read_b64_tr_b16 v[50:51], v12 offset:2048\n"
    ".Lat_pv_%=:\n"
    "s_waitcnt lgkmcnt(4)\n"
    "v_mfma_f32_32x32x16_bf16 v[128:143], v[40:43], v[24:27], v[128:143]\n"
    "ds_read_b64_tr_b16 v[52:53], v13 offset:0\n"
    "ds_read_b64_tr_b16 v[54:55], v13 offset:2048\n"
    "v_mfma_f32_32x32x16_bf16 a[0:15], v[40:43], v[28:31], a[0:15]\n"
    "s_add_u32 m0, s70, 81920\n"
    "s_add_u32 s56, s66, 0\n"
    "buffer_load_dwordx4 v4, s[40:43], s56 offen lds\n"
    "s_waitcnt lgkmcnt(4)\n"
    "v_mfma_f32_32x32x16_bf16 v[144:159], v[44:47], v[24:27], v[144:159]\n"
    "ds_read_b64_tr_b16 v[56:57], v10 offset:256\n"
    "ds_read_b64_tr_b16 v[58:59], v10 offset:2304\n"
    "v_mfma_f32_32x32x16_bf16 a[16:31], v[44:47], v[28:31], a[16:31]\n"
    "s_add_u32 m0, s70, 82944\n"
    "s_add_u32 s56, s66, 16384\n"
    "buffer_load_dwordx4 v5, s[40:43], s56 offen lds\n"
    "s_waitcnt lgkmcnt(4)\n"
    "v_mfma_f32_32x32x16_bf16 v[160:175], v[48:51], v[24:27], v[160:175]\n"
    "ds_read_b64_tr_b16 v[60:61], v11 offset:256\n"
    "ds_read_b64_tr_b16 v[62:63], v11 offset:2304\n"
    "ds_read_b128 v[32:35], v16 offset:16384\n"
    "v_mfma_f32_32x32x16_bf16 a[32:47], v[48:51], v[28:31], a[32:47]\n"
    "s_add_u32 m0, s70, 83968\n"
    "s_add_u32 s56, s66, 32768\n"
    "buffer_load_dwordx4 v4, s[40:43], s56 offen lds\n"
    "s_waitcnt lgkmcnt(5)\n"
    "v_mfma_f32_32x32x16_bf16 v[176:191], v[52:55], v[24:27], v[176:191]\n"
    "ds_read_b64_tr_b16 v[40:41], v12 offset:256\n"
    "ds_read_b64_tr_b16 v[42:43], v12 offset:2304\n"
    "ds_read_b128 v[36:39], v16 offset:20480\n"
    "v_mfma_f32_32x32x16_bf16 a[48:63], v[52:55], v[28:31], a[48:63]\n"
    "s_add_u32 m0, s70, 84992\n"
    "s_add_u32 s56, s66, 49152\n"
    "buffer_load_dwordx4 v5, s[40:43], s56 offen lds\n"
    "s_waitcnt lgkmcnt(6)\n"
    "v_mfma_f32_32x32x16_bf16 v[192:207], v[56:59], v[24:27], v[192:207]\n"
    "ds_read_b64_tr_b16 v[44:45], v13 offset:256\n"
    "ds_read_b64_tr_b16 v[46:47], v13 offset:2304\n"
    "v_mfma_f32_32x32x16_bf16 a[64:79], v[56:59], v[28:31], a[64:79]\n"
    "s_add_u32 m0, s70, 86016\n"
    "s_add_u32 s56, s71, 0\n"
    "buffer_load_dwordx4 v4, s[40:43], s56 offen lds\n"
    "s_waitcnt lgkmcnt(6)\n"
    "v_mfma_f32_32x32x16_bf16 v[208:223], v[60:63], v[24:27], v[208:223]\n"
    "ds_read_b64_tr_b16 v[48:49], v14 offset:16384\n"
    "ds_read_b64_tr_b16 v[50:51], v14 offset:16640\n"
    "s_add_u32 s62, s62, 20480\n"
    "s_cmp_ge_u32 s62, 81920\n"
    "s_cselect_b32 s62, 0, s62\n"
    "v_mfma_f32_32x32x16_bf16 a[80:95], v[60:63], v[28:31], a[80:95]\n"
    "s_add_u32 m0, s70, 87040\n"
    "s_add_u32 s56, s71, 16384\n"
    "buffer_load_dwordx4 v5, s[40:43], s56 offen lds\n"
    "s_waitcnt lgkmcnt(5)\n"
    "v_mfma_f32_32x32x16_bf16 v[224:239], v[40:43], v[24:27], v[224:239]\n"
    "ds_read_b64_tr_b16 v[52:53], v10 offset:8192\n"
    "ds_read_b64_tr_b16 v[54:55], v10 offset:10240\n"
    "v_mfma_f32_32x32x16_bf16 a[96:111], v[40:43], v[28:31], a[96:111]\n"
    "s_add_u32 m0, s70, 88064\n"
    "s_add_u32 s56, s71, 32768\n"
    "buffer_load_dwordx4 v4, s[40:43], s56 offen lds\n"
    "s_waitcnt lgkmcnt(4)\n"
    "v_mfma_f32_32x32x16_bf16 v[240:255], v[44:47], v[24:27], v[240:255]\n"
    "ds_read_b64_tr_b16 v[56:57], v11 offset:8192\n"
    "ds_read_b64_tr_b16 v[58:59], v11 offset:10240\n"
    "v_mfma_f32_32x32x16_bf16 a[112:127], v[44:47], v[28:31], a[112:127]\n"
    "s_add_u32 m0, s70, 89088\n"
    "s_add_u32 s56, s71, 49152\n"
    "buffer_load_dwordx4 v5, s[40:43], s56 offen lds\n"
    "s_waitcnt lgkmcnt(4)\n"
    "v_mfma_f32_32x32x16_bf16 v[112:127], v[24:27], v[48:51], v[112:127]\n"
    "ds_read_b64_tr_b16 v[60:61], v12 offset:8192\n"
    "ds_read_b64_tr_b16 v[62:63], v12 offset:10240\n"
    "s_waitcnt lgkmcnt(4)\n"
    "v_mfma_f32_32x32x16_bf16 v[128:143], v[52:55], v[32:35], v[128:143]\n"
    "ds_read_b64_tr_b16 v[40:41], v13 offset:8192\n"
    "ds_read_b64_tr_b16 v[42:43], v13 offset:10240\n"
    "v_mfma_f32_32x32x16_bf16 a[0:15], v[52:55], v[36:39], a[0:15]\n"
    "s_add_u32 m0, s63, 0\n"
    "s_add_u32 s56, s64, 0\n"
    "buffer_load_dwordx4 v3, s[32:35], s56 offen lds\n"
    "s_waitcnt lgkmcnt(4)\n"
    "v_mfma_f32_32x32x16_bf16 v[144:159], v[56:59], v[32:35], v[144:159]\n"
    "ds_read_b64_tr_b16 v[44:45], v10 offset:8448\n"
    "ds_read_b64_tr_b16 v[46:47], v10 offset:10496\n"
    "v_mfma_f32_32x32x16_bf16 a[16:31], v[56:59], v[36:39], a[16:31]\n"
    "s_add_u32 m0, s63, 4096\n"
    "s_add_u32 s56, s64, 16384\n"
    "buffer_load_dwordx4 v3, s[32:35], s56 offen lds\n"
    "s_waitcnt lgkmcnt(4)\n"
    "v_mfma_f32_32x32x16_bf16 v[160:175], v[60:63], v[32:35], v[160:175]\n"
    "ds_read_b64_tr_b16 v[48:49], v11 offset:8448\n"
    "ds_read_b64_tr_b16 v[50:51], v11 offset:10496\n"
    "v_mfma_f32_32x32x16_bf16 a[32:47], v[60:63], v[36:39], a[32:47]\n"
    "s_add_u32 m0, s63, 8192\n"
    "s_add_u32 s56, s64, 32768\n"
    "buffer_load_dwordx4 v3, s[32:35], s56 offen lds\n"
    "s_waitcnt lgkmcnt(4)\n"
    "v_mfma_f32_32x32x16_bf16 v[176:191], v[40:43], v[32:35], v[176:191]\n"
    "ds_read_b64_tr_b16 v[52:53], v12 offset:8448\n"
    "ds_read_b64_tr_b16 v[54:55], v12 offset:10496\n"
    "v_mfma_f32_32x32x16_bf16 a[48:63], v[40:43], v[36:39], a[48:63]\n"
    "s_add_u32 m0, s63, 12288\n"
    "s_add_u32 s56, s64, 49152\n"
    "buffer_load_dwordx4 v3, s[32:35], s56 offen lds\n"
    "s_waitcnt lgkmcnt(4)\n"
    "v_mfma_f32_32x32x16_bf16 v[192:207], v[44:47], v[32:35], v[192:207]\n"
    "ds_read_b64_tr_b16 v[56:57], v13 offset:8448\n"
    "ds_read_b64_tr_b16 v[58:59], v13 offset:10496\n"
    "v_mfma_f32_32x32x16_bf16 a[64:79], v[44:47], v[36:39], a[64:79]\n"
    "s_add_u32 m0, s63, 16384\n"
    "s_nop 0\n"
    "buffer_load_dwordx4 v2, s[36:39], s65 offen lds\n"
    "s_waitcnt lgkmcnt(4)\n"
    "v_mfma_f32_32x32x16_bf16 v[208:223], v[48:51], v[32:35], v[208:223]\n"
    "ds_read_b64_tr_b16 v[60:61], v14 offset:17408\n"
    "ds_read_b64_tr_b16 v[62:63], v14 offset:17664\n"
    "v_mfma_f32_32x32x16_bf16 a[80:95], v[48:51], v[36:39], a[80:95]\n"
    "s_add_u32 s64, s64, 65536\n"
    "s_add_u32 s65, s65, 2048\n"
    "s_cmp_ge_u32 s63, 61440\n"
    "s_cselect_b32 s56, 4294905856, s58\n"
    "s_add_u32 s63, s63, s56\n"
    "s_waitcnt lgkmcnt(4)\n"
    "v_mfma_f32_32x32x16_bf16 v[224:239], v[52:55], v[32:35], v[224:239]\n"
    "v_add_u32 v10, s62, v6\n"
    "v_add_u32 v14, s62, v7\n"
    "v_xor_b32 v11, 64, v10\n"
    "v_xor_b32 v12, 128, v10\n"
    "v_xor_b32 v13, 192, v10\n"
    "v_mfma_f32_32x32x16_bf16 a[96:111], v[52:55], v[36:39], a[96:111]\n"
    "s_waitcnt vmcnt(18) lgkmcnt(0)\n"
    "s_barrier\n"
    "ds_read_b128 v[24:27], v17 offset:16384\n"
    "ds_read_b128 v[28:31], v17 offset:20480\n"
    "ds_read_b64_tr_b16 v[40:41], v10 offset:0\n"
    "ds_read_b64_tr_b16 v[42:43], v10 offset:2048\n"
    "v_mfma_f32_32x32x16_bf16 v[240:255], v[56:59], v[32:35], v[240:255]\n"
    "ds_read_b64_tr_b16 v[44:45], v11 offset:0\n"
    "ds_read_b64_tr_b16 v[46:47], v11 offset:2048\n"
    "v_mfma_f32_32x32x16_bf16 a[112:127], v[56:59], v[36:39], a[112:127]\n"
    "v_mfma_f32_32x32x16_bf16 v[112:127], v[32:35], v[60:63], v[112:127]\n"
    "ds_read_b64_tr_b16 v[48:49], v12 offset:0\n"
    "ds_read_b64_tr_b16 v[50:51], v12 offset:2048\n"
    "s_waitcnt lgkmcnt(4)\n"
    "v_mfma_f32_32x32x16_bf16 v[128:143], v[40:43], v[24:27], v[128:143]\n"
    "ds_read_b64_tr_b16 v[52:53], v13 offset:0\n"
    "ds_read_b64_tr_b16 v[54:55], v13 offset:2048\n"
    "v_mfma_f32_32x32x16_bf16 a[0:15], v[40:43], v[28:31], a[0:15]\n"
    "s_add_u32 m0, s63, 0\n"
    "s_add_u32 s56, s64, 0\n"
    "buffer_load_dwordx4 v3, s[32:35], s56 offen lds\n"
    "s_waitcnt lgkmcnt(4)\n"
    "v_mfma_f32_32x32x16_bf16 v[144:159], v[44:47], v[24:27], v[144:159]\n"
    "ds_read_b64_tr_b16 v[56:57], v10 offset:256\n"
    "ds_read_b64_tr_b16 v[58:59], v10 offset:2304\n"
    "v_mfma_f32_32x32x16_bf16 a[16:31], v[44:47], v[28:31], a[16:31]\n"
    "s_add_u32 m0, s63, 4096\n"
    "s_add_u32 s56, s64, 16384\n"
    "buffer_load_dwordx4 v3, s[32:35], s56 offen lds\n"
    "s_waitcnt lgkmcnt(4)\n"
    "v_mfma_f32_32x32x16_bf16 v[160:175], v[48:51], v[24:27], v[160:175]\n"
    "ds_read_b64_tr_b16 v[60:61], v11 offset:256\n"
    "ds_read_b64_tr_b16 v[62:63], v11 offset:2304\n"
    "ds_read_b128 v[32:35], v18 offset:16384\n"
    "v_mfma_f32_32x32x16_bf16 a[32:47], v[48:51], v[28:31], a[32:47]\n"
    "s_add_u32 m0, s63, 8192\n"
    "s_add_u32 s56, s64, 32768\n"
    "buffer_load_dwordx4 v3, s[32:35], s56 offen lds\n"
    "s_waitcnt lgkmcnt(5)\n"
    "v_mfma_f32_32x32x16_bf16 v[176:191], v[52:55], v[24:27], v[176:191]\n"
    "ds_read_b64_tr_b16 v[40:41], v12 offset:256\n"
    "ds_read_b64_tr_b16 v[42:43], v12 offset:2304\n"
    "ds_read_b128 v[36:39], v18 offset:20480\n"
    "v_mfma_f32_32x32x16_bf16 a[48:63], v[52:55], v[28:31], a[48:63]\n"
    "s_add_u32 m0, s63, 12288\n"
    "s_add_u32 s56, s64, 49152\n"
    "buffer_load_dwordx4 v3, s[32:35], s56 offen lds\n"
    "s_waitcnt lgkmcnt(6)\n"
    "v_mfma_f32_32x32x16_bf16 v[192:207], v[56:59], v[24:27], v[192:207]\n"
    "ds_read_b64_tr_b16 v[44:45], v13 offset:256\n"
    "ds_read_b64_tr_b16 v[46:47], v13 offset:2304\n"
    "v_mfma_f32_32x32x16_bf16 a[64:79], v[56:59], v[28:31], a[64:79]\n"
    "s_add_u32 m0, s63, 16384\n"
    "s_nop 0\n"
    "buffer_load_dwordx4 v2, s[36:39], s65 offen lds\n"
    "s_waitcnt lgkmcnt(6)\n"
    "v_mfma_f32_32x32x16_bf16 v[208:223], v[60:63], v[24:27], v[208:223]\n"
    "ds_read_b64_tr_b16 v[48:49], v14 offset:16384\n"
    "ds_read_b64_tr_b16 v[50:51], v14 offset:16640\n"
    "s_add_u32 s62, s62, 20480\n"
    "s_cmp_ge_u32 s62, 81920\n"
    "s_cselect_b32 s62, 0, s62\n"
    "v_mfma_f32_32x32x16_bf16 a[80:95], v[60:63], v[28:31], a[80:95]\n"
    "s_add_u32 s66, s66, 128\n"
    "s_add_u32 s71, s71, 128\n"
    "s_xor_b32 s70, s70, 32768\n"
    "s_waitcnt lgkmcnt(5)\n"
    "v_mfma_f32_32x32x16_bf16 v[224:239], v[40:43], v[24:27], v[224:239]\n"
    "ds_read_b64_tr_b16 v[52:53], v10 offset:8192\n"
    "ds_read_b64_tr_b16 v[54:55], v10 offset:10240\n"
    "v_xor_b32 v15, 32768, v15\n"
    "v_xor_b32 v16, 32768, v16\n"
    "v_xor_b32 v17, 32768, v17\n"
    "v_xor_b32 v18, 32768, v18\n"
    "v_mfma_f32_32x32x16_bf16 a[96:111], v[40:43], v[28:31], a[96:111]\n"
    "s_add_u32 s64, s64, 65536\n"
    "s_add_u32 s65, s65, 2048\n"
    "s_cmp_ge_u32 s63, 61440\n"
    "s_cselect_b32 s56, 4294905856, s58\n"
    "s_add_u32 s63, s63, s56\n"
    "s_waitcnt lgkmcnt(4)\n"
    "v_mfma_f32_32x32x16_bf16 v[240:255], v[44:47], v[24:27], v[240:255]\n"
    "ds_read_b64_tr_b16 v[56:57], v11 offset:8192\n"
    "ds_read_b64_tr_b16 v[58:59], v11 offset:10240\n"
    "v_mfma_f32_32x32x16_bf16 a[112:127], v[44:47], v[28:31], a[112:127]\n"
    "s_waitcnt lgkmcnt(4)\n"
    "v_mfma_f32_32x32x16_bf16 v[112:127], v[24:27], v[48:51], v[112:127]\n"
    "ds_read_b64_tr_b16 v[60:61], v12 offset:8192\n"
    "ds_read_b64_tr_b16 v[62:63], v12 offset:10240\n"
    "s_waitcnt lgkmcnt(4)\n"
    "v_mfma_f32_32x32x16_bf16 v[128:143], v[52:55], v[32:35], v[128:143]\n"
    "ds_read_b64_tr_b16 v[40:41], v13 offset:8192\n"
    "ds_read_b64_tr_b16 v[42:43], v13 offset:10240\n"
    "v_mfma_f32_32x32x16_bf16 a[0:15], v[52:55], v[36:39], a[0:15]\n"
    "s_waitcnt lgkmcnt(4)\n"
    "v_mfma_f32_32x32x16_bf16 v[144:159], v[56:59], v[32:35], v[144:159]\n"
    "ds_read_b64_tr_b16 v[44:45], v10 offset:8448\n"
    "ds_read_b64_tr_b16 v[46:47], v10 offset:10496\n"
    "v_mfma_f32_32x32x16_bf16 a[16:31], v[56:59], v[36:39], a[16:31]\n"
    "s_waitcnt lgkmcnt(4)\n"
    "v_mfma_f32_32x32x16_bf16 v[160:175], v[60:63], v[32:35], v[160:175]\n"
    "ds_read_b64_tr_b16 v[48:49], v11 offset:8448\n"
    "ds_read_b64_tr_b16 v[50:51], v11 offset:10496\n"
    "v_mfma_f32_32x32x16_bf16 a[32:47], v[60:63], v[36:39], a[32:47]\n"
    "s_waitcnt lgkmcnt(4)\n"
    "v_mfma_f32_32x32x16_bf16 v[176:191], v[40:43], v[32:35], v[176:191]\n"
    "ds_read_b64_tr_b16 v[52:53], v12 offset:8448\n"
    "ds_read_b64_tr_b16 v[54:55], v12 offset:10496\n"
    "v_mfma_f32_32x32x16_bf16 a[48:63], v[40:43], v[36:39], a[48:63]\n"
    "s_waitcnt lgkmcnt(4)\n"
    "v_mfma_f32_32x32x16_bf16 v[192:207], v[44:47], v[32:35], v[192:207]\n"
    "ds_read_b64_tr_b16 v[56:57], v13 offset:8448\n"
    "ds_read_b64_tr_b16 v[58:59], v13 offset:10496\n"
    "v_mfma_f32_32x32x16_bf16 a[64:79], v[44:47], v[36:39], a[64:79]\n"
    "s_waitcnt lgkmcnt(4)\n"
    "v_mfma_f32_32x32x16_bf16 v[208:223], v[48:51], v[32:35], v[208:223]\n"
    "ds_read_b64_tr_b16 v[60:61], v14 offset:17408\n"
    "ds_read_b64_tr_b16 v[62:63], v14 offset:17664\n"
    "v_mfma_f32_32x32x16_bf16 a[80:95], v[48:51], v[36:39], a[80:95]\n"
    "s_waitcnt lgkmcnt(4)\n"
    "v_mfma_f32_32x32x16_bf16 v[224:239], v[52:55], v[32:35], v[224:239]\n"
    "v_add_u32 v10, s62, v6\n"
    "v_add_u32 v14, s62, v7\n"
    "v_xor_b32 v11, 64, v10\n"
    "v_xor_b32 v12, 128, v10\n"
    "v_xor_b32 v13, 192, v10\n"
    "v_mfma_f32_32x32x16_bf16 a[96:111], v[52:55], v[36:39], a[96:111]\n"
    "s_waitcnt vmcnt(10) lgkmcnt(0)\n"
    "s_barrier\n"
    "ds_read_b128 v[24:27], v15 offset:16384\n"
    "ds_read_b128 v[28:31], v15 offset:20480\n"
    "ds_read_b64_tr_b16 v[40:41], v10 offset:0\n"
    "ds_read_b64_tr_b16 v[42:43], v10 offset:2048\n"
    "v_mfma_f32_32x32x16_bf16 v[240:255], v[56:59], v[32:35], v[240:255]\n"
    "ds_read_b64_tr_b16 v[44:45], v11 offset:0\n"
    "ds_read_b64_tr_b16 v[46:47], v11 offset:2048\n"
    "v_mfma_f32_32x32x16_bf16 a[112:127], v[56:59], v[36:39], a[112:127]\n"
    "v_mfma_f32_32x32x16_bf16 v[112:127], v[32:35], v[60:63], v[112:127]\n"
    "ds_read_b64_tr_b16 v[48:49], v12 offset:0\n"
    "ds_read_b64_tr_b16 v[50:51], v12 offset:2048\n"
    "s_add_u32 s61, s61, 1\n"
    "s_cmp_eq_u32 s61, 3\n"
    "s_cbranch_scc1 .Lat_hsdo_%=\n"
    "s_cmp_lt_u32 s61, 16\n"
    "s_cbranch_scc1 .Lat_pv_%=\n"
    "s_waitcnt lgkmcnt(0)\n"
    ".Lat_pdone_%=:\n"
    "s_nop 7\n"
    "v_cvt_pk_bf16_f32 v24, v112, v113\n"
    "v_cvt_pk_bf16_f32 v25, v114, v115\n"
    "v_cvt_pk_bf16_f32 v26, v116, v117\n"
    "v_cvt_pk_bf16_f32 v27, v118, v119\n"
    "v_cvt_pk_bf16_f32 v28, v120, v121\n"
    "v_cvt_pk_bf16_f32 v29, v122, v123\n"
    "v_cvt_pk_bf16_f32 v30, v124, v125\n"
    "v_cvt_pk_bf16_f32 v31, v126, v127\n"
    "s_lshl_b32 s56, s60, 1\n"
    "s_and_b32 s57, s59, 1\n"
    "s_add_u32 s56, s56, s57\n"
    "s_lshl_b32 s67, s56, 2\n"
    "s_xor_b32 s57, s27, 8192\n"
    "s_add_u32 s67, s67, s57\n"
    "s_lshl_b32 s56, s56, 11\n"
    "buffer_store_dwordx4 v[24:27], v2, s[44:47], s56 offen offset:0 sc1\n"
    "buffer_store_dwordx4 v[28:31], v2, s[44:47], s56 offen offset:1024 sc1\n"
    "s_mov_b32 s68, 1048744942\n"
    "s_mov_b32 s69, 1048744942\n"
    "v_pk_mul_f32 v[128:129], v[128:129], s[68:69]\n"
    "v_cvt_pk_bf16_f32 v128, v128, v129\n"
    "v_accvgpr_write_b32 a128, v128\n"
    "v_pk_mul_f32 v[130:131], v[130:131], s[68:69]\n"
    "v_cvt_pk_bf16_f32 v130, v130, v131\n"
    "v_accvgpr_write_b32 a129, v130\n"
    "v_pk_mul_f32 v[132:133], v[132:133], s[68:69]\n"
    "v_cvt_pk_bf16_f32 v132, v132, v133\n"
    "v_accvgpr_write_b32 a130, v132\n"
    "v_pk_mul_f32 v[134:135], v[134:135], s[68:69]\n"
    "v_cvt_pk_bf16_f32 v134, v134, v135\n"
    "v_accvgpr_write_b32 a131, v134\n"
    "v_pk_mul_f32 v[136:137], v[136:137], s[68:69]\n"
    "v_cvt_pk_bf16_f32 v136, v136, v137\n"
    "v_accvgpr_write_b32 a132, v136\n"
    "v_pk_mul_f32 v[138:139], v[138:139], s[68:69]\n"
    "v_cvt_pk_bf16_f32 v138, v138, v139\n"
    "v_accvgpr_write_b32 a133, v138\n"
    "v_pk_mul_f32 v[140:141], v[140:141], s[68:69]\n"
    "v_cvt_pk_bf16_f32 v140, v140, v141\n"
    "v_accvgpr_write_b32 a134, v140\n"
    "v_pk_mul_f32 v[142:143], v[142:143], s[68:69]\n"
    "v_cvt_pk_bf16_f32 v142, v142, v143\n"
    "v_accvgpr_write_b32 a135, v142\n"
    "v_pk_mul_f32 v[144:145], v[144:145], s[68:69]\n"
    "v_cvt_pk_bf16_f32 v144, v144, v145\n"
    "v_accvgpr_write_b32 a136, v144\n"
    "v_pk_mul_f32 v[146:147], v[146:147], s[68:69]\n"
    "v_cvt_pk_bf16_f32 v146, v146, v147\n"
    "v_accvgpr_write_b32 a137, v146\n"
    "v_pk_mul_f32 v[148:149], v[148:149], s[68:69]\n"
    "v_cvt_pk_bf16_f32 v148, v148, v149\n"
    "v_accvgpr_write_b32 a138, v148\n"
    "v_pk_mul_f32 v[150:151], v[150:151], s[68:69]\n"
    "v_cvt_pk_bf16_f32 v150, v150, v151\n"
    "v_accvgpr_write_b32 a139, v150\n"
    "v_pk_mul_f32 v[152:153], v[152:153], s[68:69]\n"
    "v_cvt_pk_bf16_f32 v152, v152, v153\n"
    "v_accvgpr_write_b32 a140, v152\n"
    "v_pk_mul_f32 v[154:155], v[154:155], s[68:69]\n"
    "v_cvt_pk_bf16_f32 v154, v154, v155\n"
    "v_accvgpr_write_b32 a141, v154\n"
    "v_pk_mul_f32 v[156:157], v[156:157], s[68:69]\n"
    "v_cvt_pk_bf16_f32 v156, v156, v157\n"
    "v_accvgpr_write_b32 a142, v156\n"
    "v_pk_mul_f32 v[158:159], v[158:159], s[68:69]\n"
    "v_cvt_pk_bf16_f32 v158, v158, v159\n"
    "v_accvgpr_write_b32 a143, v158\n"
    "v_pk_mul_f32 v[160:161], v[160:161], s[68:69]\n"
    "v_cvt_pk_bf16_f32 v160, v160, v161\n"
    "v_accvgpr_write_b32 a144, v160\n"
    "v_pk_mul_f32 v[162:163], v[162:163], s[68:69]\n"
    "v_cvt_pk_bf16_f32 v162, v162, v163\n"
    "v_accvgpr_write_b32 a145, v162\n"
    "v_pk_mul_f32 v[164:165], v[164:165], s[68:69]\n"
    "v_cvt_pk_bf16_f32 v164, v164, v165\n"
    "v_accvgpr_write_b32 a146, v164\n"
    "v_pk_mul_f32 v[166:167], v[166:167], s[68:69]\n"
    "v_cvt_pk_bf16_f32 v166, v166, v167\n"
    "v_accvgpr_write_b32 a147, v166\n"
    "v_pk_mul_f32 v[168:169], v[168:169], s[68:69]\n"
    "v_cvt_pk_bf16_f32 v168, v168, v169\n"
    "v_accvgpr_write_b32 a148, v168\n"
    "v_pk_mul_f32 v[170:171], v[170:171], s[68:69]\n"
    "v_cvt_pk_bf16_f32 v170, v170, v171\n"
    "v_accvgpr_write_b32 a149, v170\n"
    "v_pk_mul_f32 v[172:173], v[172:173], s[68:69]\n"
    "v_cvt_pk_bf16_f32 v172, v172, v173\n"
    "v_accvgpr_write_b32 a150, v172\n"
    "v_pk_mul_f32 v[174:175], v[174:175], s[68:69]\n"
    "v_cvt_pk_bf16_f32 v174, v174, v175\n"
    "v_accvgpr_write_b32 a151, v174\n"
    "v_pk_mul_f32 v[176:177], v[176:177], s[68:69]\n"
    "v_cvt_pk_bf16_f32 v176, v176, v177\n"
    "v_accvgpr_write_b32 a152, v176\n"
    "v_pk_mul_f32 v[178:179], v[178:179], s[68:69]\n"
    "v_cvt_pk_bf16_f32 v178, v178, v179\n"
    "v_accvgpr_write_b32 a153, v178\n"
    "v_pk_mul_f32 v[180:181], v[180:181], s[68:69]\n"
    "v_cvt_pk_bf16_f32 v180, v180, v181\n"
    "v_accvgpr_write_b32 a154, v180\n"
    "v_pk_mul_f32 v[182:183], v[182:183], s[68:69]\n"
    "v_cvt_pk_bf16_f32 v182, v182, v183\n"
    "v_accvgpr_write_b32 a155, v182\n"
    "v_pk_mul_f32 v[184:185], v[184:185], s[68:69]\n"
    "v_cvt_pk_bf16_f32 v184, v184, v185\n"
    "v_accvgpr_write_b32 a156, v184\n"
    "v_pk_mul_f32 v[186:187], v[186:187], s[68:69]\n"
    "v_cvt_pk_bf16_f32 v186, v186, v187\n"
    "v_accvgpr_write_b32 a157, v186\n"
    "v_pk_mul_f32 v[188:189], v[188:189], s[68:69]\n"
    "v_cvt_pk_bf16_f32 v188, v188, v189\n"
    "v_accvgpr_write_b32 a158, v188\n"
    "v_pk_mul_f32 v[190:191], v[190:191], s[68:69]\n"
    "v_cvt_pk_bf16_f32 v190, v190, v191\n"
    "v_accvgpr_write_b32 a159, v190\n"
    "v_pk_mul_f32 v[192:193], v[192:193], s[68:69]\n"
    "v_cvt_pk_bf16_f32 v192, v192, v193\n"
    "v_accvgpr_write_b32 a160, v192\n"
    "v_pk_mul_f32 v[194:195], v[194:195], s[68:69]\n"
    "v_cvt_pk_bf16_f32 v194, v194, v195\n"
    "v_accvgpr_write_b32 a161, v194\n"
    "v_pk_mul_f32 v[196:197], v[196:197], s[68:69]\n"
    "v_cvt_pk_bf16_f32 v196, v196, v197\n"
    "v_accvgpr_write_b32 a162, v196\n"
    "v_pk_mul_f32 v[198:199], v[198:199], s[68:69]\n"
    "v_cvt_pk_bf16_f32 v198, v198, v199\n"
    "v_accvgpr_write_b32 a163, v198\n"
    "v_pk_mul_f32 v[200:201], v[200:201], s[68:69]\n"
    "v_cvt_pk_bf16_f32 v200, v200, v201\n"
    "v_accvgpr_write_b32 a164, v200\n"
    "v_pk_mul_f32 v[202:203], v[202:203], s[68:69]\n"
    "v_cvt_pk_bf16_f32 v202, v202, v203\n"
    "v_accvgpr_write_b32 a165, v202\n"
    "v_pk_mul_f32 v[204:205], v[204:205], s[68:69]\n"
    "v_cvt_pk_bf16_f32 v204, v204, v205\n"
    "v_accvgpr_write_b32 a166, v204\n"
    "v_pk_mul_f32 v[206:207], v[206:207], s[68:69]\n"
    "v_cvt_pk_bf16_f32 v206, v206, v207\n"
    "v_accvgpr_write_b32 a167, v206\n"
    "v_pk_mul_f32 v[208:209], v[208:209], s[68:69]\n"
    "v_cvt_pk_bf16_f32 v208, v208, v209\n"
    "v_accvgpr_write_b32 a168, v208\n"
    "v_pk_mul_f32 v[210:211], v[210:211], s[68:69]\n"
    "v_cvt_pk_bf16_f32 v210, v210, v211\n"
    "v_accvgpr_write_b32 a169, v210\n"
    "v_pk_mul_f32 v[212:213], v[212:213], s[68:69]\n"
    "v_cvt_pk_bf16_f32 v212, v212, v213\n"
    "v_accvgpr_write_b32 a170, v212\n"
    "v_pk_mul_f32 v[214:215], v[214:215], s[68:69]\n"
    "v_cvt_pk_bf16_f32 v214, v214, v215\n"
    "v_accvgpr_write_b32 a171, v214\n"
    "v_pk_mul_f32 v[216:217], v[216:217], s[68:69]\n"
    "v_cvt_pk_bf16_f32 v216, v216, v217\n"
    "v_accvgpr_write_b32 a172, v216\n"
    "v_pk_mul_f32 v[218:219], v[218:219], s[68:69]\n"
    "v_cvt_pk_bf16_f32 v218, v218, v219\n"
    "v_accvgpr_write_b32 a173, v218\n"
    "v_pk_mul_f32 v[220:221], v[220:221], s[68:69]\n"
    "v_cvt_pk_bf16_f32 v220, v220, v221\n"
    "v_accvgpr_write_b32 a174, v220\n"
    "v_pk_mul_f32 v[222:223], v[222:223], s[68:69]\n"
    "v_cvt_pk_bf16_f32 v222, v222, v223\n"
    "v_accvgpr_write_b32 a175, v222\n"
    "v_pk_mul_f32 v[224:225], v[224:225], s[68:69]\n"
    "v_cvt_pk_bf16_f32 v224, v224, v225\n"
    "v_accvgpr_write_b32 a176, v224\n"
    "v_pk_mul_f32 v[226:227], v[226:227], s[68:69]\n"
    "v_cvt_pk_bf16_f32 v226, v226, v227\n"
    "v_accvgpr_write_b32 a177, v226\n"
    "v_pk_mul_f32 v[228:229], v[228:229], s[68:69]\n"
    "v_cvt_pk_bf16_f32 v228, v228, v229\n"
    "v_accvgpr_write_b32 a178, v228\n"
    "v_pk_mul_f32 v[230:231], v[230:231], s[68:69]\n"
    "v_cvt_pk_bf16_f32 v230, v230, v231\n"
    "v_accvgpr_write_b32 a179, v230\n"
    "v_pk_mul_f32 v[232:233], v[232:233], s[68:69]\n"
    "v_cvt_pk_bf16_f32 v232, v232, v233\n"
    "v_accvgpr_write_b32 a180, v232\n"
    "v_pk_mul_f32 v[234:235], v[234:235], s[68:69]\n"
    "v_cvt_pk_bf16_f32 v234, v234, v235\n"
    "v_accvgpr_write_b32 a181, v234\n"
    "v_pk_mul_f32 v[236:237], v[236:237], s[68:69]\n"
    "v_cvt_pk_bf16_f32 v236, v236, v237\n"
    "v_accvgpr_write_b32 a182, v236\n"
    "v_pk_mul_f32 v[238:239], v[238:239], s[68:69]\n"
    "v_cvt_pk_bf16_f32 v238, v238, v239\n"
    "v_accvgpr_write_b32 a183, v238\n"
    "v_pk_mul_f32 v[240:241], v[240:241], s[68:69]\n"
    "v_cvt_pk_bf16_f32 v240, v240, v241\n"
    "v_accvgpr_write_b32 a184, v240\n"
    "v_pk_mul_f32 v[242:243], v[242:243], s[68:69]\n"
    "v_cvt_pk_bf16_f32 v242, v242, v243\n"
    "v_accvgpr_write_b32 a185, v242\n"
    "v_pk_mul_f32 v[244:245], v[244:245], s[68:69]\n"
    "v_cvt_pk_bf16_f32 v244, v244, v245\n"
    "v_accvgpr_write_b32 a186, v244\n"
    "v_pk_mul_f32 v[246:247], v[246:247], s[68:69]\n"
    "v_cvt_pk_bf16_f32 v246, v246, v247\n"
    "v_accvgpr_write_b32 a187, v246\n"
    "v_pk_mul_f32 v[248:249], v[248:249], s[68:69]\n"
    "v_cvt_pk_bf16_f32 v248, v248, v249\n"
    "v_accvgpr_write_b32 a188, v248\n"
    "v_pk_mul_f32 v[250:251], v[250:251], s[68:69]\n"
    "v_cvt_pk_bf16_f32 v250, v250, v251\n"
    "v_accvgpr_write_b32 a189, v250\n"
    "v_pk_mul_f32 v[252:253], v[252:253], s[68:69]\n"
    "v_cvt_pk_bf16_f32 v252, v252, v253\n"
    "v_accvgpr_write_b32 a190, v252\n"
    "v_pk_mul_f32 v[254:255], v[254:255], s[68:69]\n"
    "v_cvt_pk_bf16_f32 v254, v254, v255\n"
    "v_accvgpr_write_b32 a191, v254\n"
    "v_mov_b32 v19, 1\n"
    "s_waitcnt vmcnt(0)\n"
    "s_mov_b64 exec, 1\n"
    "buffer_store_dword v19, off, s[48:51], s67 sc1\n"
    "s_mov_b64 exec, -1\n"
    "s_add_u32 s40, s30, 74448896\n"
    "s_addc_u32 s41, s31, 0\n"
    "s_and_b32 s41, s41, 0xffff\n"
    "s_mov_b32 s42, 4194304\n"
    "s_mov_b32 s43, 0x20000\n"
    "s_xor_b32 s66, s60, 4\n"
    "s_lshl_b32 s66, s66, 2\n"
    "s_add_u32 s66, s66, s59\n"
    "s_lshl_b32 s66, s66, 14\n"
    "v_accvgpr_read_b32 v32, a0\n"
    "v_accvgpr_read_b32 v33, a1\n"
    "v_accvgpr_read_b32 v34, a2\n"
    "v_accvgpr_read_b32 v35, a3\n"
    "v_accvgpr_read_b32 v36, a4\n"
    "v_accvgpr_read_b32 v37, a5\n"
    "v_accvgpr_read_b32 v38, a6\n"
    "v_accvgpr_read_b32 v39, a7\n"
    "v_pk_mul_f32 v[32:33], v[32:33], s[68:69]\n"
    "v_pk_mul_f32 v[34:35], v[34:35], s[68:69]\n"
    "v_pk_mul_f32 v[36:37], v[36:37], s[68:69]\n"
    "v_pk_mul_f32 v[38:39], v[38:39], s[68:69]\n"
    "v_cvt_pk_bf16_f32 v32, v32, v33\n"
    "v_cvt_pk_bf16_f32 v33, v34, v35\n"
    "v_cvt_pk_bf16_f32 v34, v36, v37\n"
    "v_cvt_pk_bf16_f32 v35, v38, v39\n"
    "s_add_u32 s56, s66, 0\n"
    "buffer_store_dwordx4 v[32:35], v2, s[40:43], s56 offen offset:0 sc1\n"
    "v_accvgpr_read_b32 v40, a8\n"
    "v_accvgpr_read_b32 v41, a9\n"
    "v_accvgpr_read_b32 v42, a10\n"
    "v_accvgpr_read_b32 v43, a11\n"
    "v_accvgpr_read_b32 v44, a12\n"
    "v_accvgpr_read_b32 v45, a13\n"
    "v_accvgpr_read_b32 v46, a14\n"
    "v_accvgpr_read_b32 v47, a15\n"
    "v_pk_mul_f32 v[40:41], v[40:41], s[68:69]\n"
    "v_pk_mul_f32 v[42:43], v[42:43], s[68:69]\n"
    "v_pk_mul_f32 v[44:45], v[44:45], s[68:69]\n"
    "v_pk_mul_f32 v[46:47], v[46:47], s[68:69]\n"
    "v_cvt_pk_bf16_f32 v40, v40, v41\n"
    "v_cvt_pk_bf16_f32 v41, v42, v43\n"
    "v_cvt_pk_bf16_f32 v42, v44, v45\n"
    "v_cvt_pk_bf16_f32 v43, v46, v47\n"
    "buffer_store_dwordx4 v[40:43], v2, s[40:43], s56 offen offset:1024 sc1\n"
    "v_accvgpr_read_b32 v48, a16\n"
    "v_accvgpr_read_b32 v49, a17\n"
    "v_accvgpr_read_b32 v50, a18\n"
    "v_accvgpr_read_b32 v51, a19\n"
    "v_accvgpr_read_b32 v52, a20\n"
    "v_accvgpr_read_b32 v53, a21\n"
    "v_accvgpr_read_b32 v54, a22\n"
    "v_accvgpr_read_b32 v55, a23\n"
    "v_pk_mul_f32 v[48:49], v[48:49], s[68:69]\n"
    "v_pk_mul_f32 v[50:51], v[50:51], s[68:69]\n"
    "v_pk_mul_f32 v[52:53], v[52:53], s[68:69]\n"
    "v_pk_mul_f32 v[54:55], v[54:55], s[68:69]\n"
    "v_cvt_pk_bf16_f32 v48, v48, v49\n"
    "v_cvt_pk_bf16_f32 v49, v50, v51\n"
    "v_cvt_pk_bf16_f32 v50, v52, v53\n"
    "v_cvt_pk_bf16_f32 v51, v54, v55\n"
    "buffer_store_dwordx4 v[48:51], v2, s[40:43], s56 offen offset:2048 sc1\n"
    "v_accvgpr_read_b32 v56, a24\n"
    "v_accvgpr_read_b32 v57, a25\n"
    "v_accvgpr_read_b32 v58, a26\n"
    "v_accvgpr_read_b32 v59, a27\n"
    "v_accvgpr_read_b32 v60, a28\n"
    "v_accvgpr_read_b32 v61, a29\n"
    "v_accvgpr_read_b32 v62, a30\n"
    "v_accvgpr_read_b32 v63, a31\n"
    "v_pk_mul_f32 v[56:57], v[56:57], s[68:69]\n"
    "v_pk_mul_f32 v[58:59], v[58:59], s[68:69]\n"
    "v_pk_mul_f32 v[60:61], v[60:61], s[68:69]\n"
    "v_pk_mul_f32 v[62:63], v[62:63], s[68:69]\n"
    "v_cvt_pk_bf16_f32 v56, v56, v57\n"
    "v_cvt_pk_bf16_f32 v57, v58, v59\n"
    "v_cvt_pk_bf16_f32 v58, v60, v61\n"
    "v_cvt_pk_bf16_f32 v59, v62, v63\n"
    "buffer_store_dwordx4 v[56:59], v2, s[40:43], s56 offen offset:3072 sc1\n"
    "v_accvgpr_read_b32 v64, a32\n"
    "v_accvgpr_read_b32 v65, a33\n"
    "v_accvgpr_read_b32 v66, a34\n"
    "v_accvgpr_read_b32 v67, a35\n"
    "v_accvgpr_read_b32 v68, a36\n"
    "v_accvgpr_read_b32 v69, a37\n"
    "v_accvgpr_read_b32 v70, a38\n"
    "v_accvgpr_read_b32 v71, a39\n"
    "v_pk_mul_f32 v[64:65], v[64:65], s[68:69]\n"
    "v_pk_mul_f32 v[66:67], v[66:67], s[68:69]\n"
    "v_pk_mul_f32 v[68:69], v[68:69], s[68:69]\n"
    "v_pk_mul_f32 v[70:71], v[70:71], s[68:69]\n"
    "v_cvt_pk_bf16_f32 v64, v64, v65\n"
    "v_cvt_pk_bf16_f32 v65, v66, v67\n"
    "v_cvt_pk_bf16_f32 v66, v68, v69\n"
    "v_cvt_pk_bf16_f32 v67, v70, v71\n"
    "s_add_u32 s56, s66, 4096\n"
    "buffer_store_dwordx4 v[64:67], v2, s[40:43], s56 offen offset:0 sc1\n"
    "v_accvgpr_read_b32 v72, a40\n"
    "v_accvgpr_read_b32 v73, a41\n"
    "v_accvgpr_read_b32 v74, a42\n"
    "v_accvgpr_read_b32 v75, a43\n"
    "v_accvgpr_read_b32 v76, a44\n"
    "v_accvgpr_read_b32 v77, a45\n"
    "v_accvgpr_read_b32 v78, a46\n"
    "v_accvgpr_read_b32 v79, a47\n"
    "v_pk_mul_f32 v[72:73], v[72:73], s[68:69]\n"
    "v_pk_mul_f32 v[74:75], v[74:75], s[68:69]\n"
    "v_pk_mul_f32 v[76:77], v[76:77], s[68:69]\n"
    "v_pk_mul_f32 v[78:79], v[78:79], s[68:69]\n"
    "v_cvt_pk_bf16_f32 v72, v72, v73\n"
    "v_cvt_pk_bf16_f32 v73, v74, v75\n"
    "v_cvt_pk_bf16_f32 v74, v76, v77\n"
    "v_cvt_pk_bf16_f32 v75, v78, v79\n"
    "buffer_store_dwordx4 v[72:75], v2, s[40:43], s56 offen offset:1024 sc1\n"
    "v_accvgpr_read_b32 v80, a48\n"
    "v_accvgpr_read_b32 v81, a49\n"
    "v_accvgpr_read_b32 v82, a50\n"
    "v_accvgpr_read_b32 v83, a51\n"
    "v_accvgpr_read_b32 v84, a52\n"
    "v_accvgpr_read_b32 v85, a53\n"
    "v_accvgpr_read_b32 v86, a54\n"
    "v_accvgpr_read_b32 v87, a55\n"
    "v_pk_mul_f32 v[80:81], v[80:81], s[68:69]\n"
    "v_pk_mul_f32 v[82:83], v[82:83], s[68:69]\n"
    "v_pk_mul_f32 v[84:85], v[84:85], s[68:69]\n"
    "v_pk_mul_f32 v[86:87], v[86:87], s[68:69]\n"
    "v_cvt_pk_bf16_f32 v80, v80, v81\n"
    "v_cvt_pk_bf16_f32 v81, v82, v83\n"
    "v_cvt_pk_bf16_f32 v82, v84, v85\n"
    "v_cvt_pk_bf16_f32 v83, v86, v87\n"
    "buffer_store_dwordx4 v[80:83], v2, s[40:43], s56 offen offset:2048 sc1\n"
    "v_accvgpr_read_b32 v88, a56\n"
    "v_accvgpr_read_b32 v89, a57\n"
    "v_accvgpr_read_b32 v90, a58\n"
    "v_accvgpr_read_b32 v91, a59\n"
    "v_accvgpr_read_b32 v92, a60\n"
    "v_accvgpr_read_b32 v93, a61\n"
    "v_accvgpr_read_b32 v94, a62\n"
    "v_accvgpr_read_b32 v95, a63\n"
    "v_pk_mul_f32 v[88:89], v[88:89], s[68:69]\n"
    "v_pk_mul_f32 v[90:91], v[90:91], s[68:69]\n"
    "v_pk_mul_f32 v[92:93], v[92:93], s[68:69]\n"
    "v_pk_mul_f32 v[94:95], v[94:95], s[68:69]\n"
    "v_cvt_pk_bf16_f32 v88, v88, v89\n"
    "v_cvt_pk_bf16_f32 v89, v90, v91\n"
    "v_cvt_pk_bf16_f32 v90, v92, v93\n"
    "v_cvt_pk_bf16_f32 v91, v94, v95\n"
    "buffer_store_dwordx4 v[88:91], v2, s[40:43], s56 offen offset:3072 sc1\n"
    "v_accvgpr_read_b32 v32, a64\n"
    "v_accvgpr_read_b32 v33, a65\n"
    "v_accvgpr_read_b32 v34, a66\n"
    "v_accvgpr_read_b32 v35, a67\n"
    "v_accvgpr_read_b32 v36, a68\n"
    "v_accvgpr_read_b32 v37, a69\n"
    "v_accvgpr_read_b32 v38, a70\n"
    "v_accvgpr_read_b32 v39, a71\n"
    "v_pk_mul_f32 v[32:33], v[32:33], s[68:69]\n"
    "v_pk_mul_f32 v[34:35], v[34:35], s[68:69]\n"
    "v_pk_mul_f32 v[36:37], v[36:37], s[68:69]\n"
    "v_pk_mul_f32 v[38:39], v[38:39], s[68:69]\n"
    "v_cvt_pk_bf16_f32 v32, v32, v33\n"
    "v_cvt_pk_bf16_f32 v33, v34, v35\n"
    "v_cvt_pk_bf16_f32 v34, v36, v37\n"
    "v_cvt_pk_bf16_f32 v35, v38, v39\n"
    "s_add_u32 s56, s66, 8192\n"
    "buffer_store_dwordx4 v[32:35], v2, s[40:43], s56 offen offset:0 sc1\n"
    "v_accvgpr_read_b32 v40, a72\n"
    "v_accvgpr_read_b32 v41, a73\n"
    "v_accvgpr_read_b32 v42, a74\n"
    "v_accvgpr_read_b32 v43, a75\n"
    "v_accvgpr_read_b32 v44, a76\n"
    "v_accvgpr_read_b32 v45, a77\n"
    "v_accvgpr_read_b32 v46, a78\n"
    "v_accvgpr_read_b32 v47, a79\n"
    "v_pk_mul_f32 v[40:41], v[40:41], s[68:69]\n"
    "v_pk_mul_f32 v[42:43], v[42:43], s[68:69]\n"
    "v_pk_mul_f32 v[44:45], v[44:45], s[68:69]\n"
    "v_pk_mul_f32 v[46:47], v[46:47], s[68:69]\n"
    "v_cvt_pk_bf16_f32 v40, v40, v41\n"
    "v_cvt_pk_bf16_f32 v41, v42, v43\n"
    "v_cvt_pk_bf16_f32 v42, v44, v45\n"
    "v_cvt_pk_bf16_f32 v43, v46, v47\n"
    "buffer_store_dwordx4 v[40:43], v2, s[40:43], s56 offen offset:1024 sc1\n"
    "v_accvgpr_read_b32 v48, a80\n"
    "v_accvgpr_read_b32 v49, a81\n"
    "v_accvgpr_read_b32 v50, a82\n"
    "v_accvgpr_read_b32 v51, a83\n"
    "v_accvgpr_read_b32 v52, a84\n"
    "v_accvgpr_read_b32 v53, a85\n"
    "v_accvgpr_read_b32 v54, a86\n"
    "v_accvgpr_read_b32 v55, a87\n"
    "v_pk_mul_f32 v[48:49], v[48:49], s[68:69]\n"
    "v_pk_mul_f32 v[50:51], v[50:51], s[68:69]\n"
    "v_pk_mul_f32 v[52:53], v[52:53], s[68:69]\n"
    "v_pk_mul_f32 v[54:55], v[54:55], s[68:69]\n"
    "v_cvt_pk_bf16_f32 v48, v48, v49\n"
    "v_cvt_pk_bf16_f32 v49, v50, v51\n"
    "v_cvt_pk_bf16_f32 v50, v52, v53\n"
    "v_cvt_pk_bf16_f32 v51, v54, v55\n"
    "buffer_store_dwordx4 v[48:51], v2, s[40:43], s56 offen offset:2048 sc1\n"
    "v_accvgpr_read_b32 v56, a88\n"
    "v_accvgpr_read_b32 v57, a89\n"
    "v_accvgpr_read_b32 v58, a90\n"
    "v_accvgpr_read_b32 v59, a91\n"
    "v_accvgpr_read_b32 v60, a92\n"
    "v_accvgpr_read_b32 v61, a93\n"
    "v_accvgpr_read_b32 v62, a94\n"
    "v_accvgpr_read_b32 v63, a95\n"
    "v_pk_mul_f32 v[56:57], v[56:57], s[68:69]\n"
    "v_pk_mul_f32 v[58:59], v[58:59], s[68:69]\n"
    "v_pk_mul_f32 v[60:61], v[60:61], s[68:69]\n"
    "v_pk_mul_f32 v[62:63], v[62:63], s[68:69]\n"
    "v_cvt_pk_bf16_f32 v56, v56, v57\n"
    "v_cvt_pk_bf16_f32 v57, v58, v59\n"
    "v_cvt_pk_bf16_f32 v58, v60, v61\n"
    "v_cvt_pk_bf16_f32 v59, v62, v63\n"
    "buffer_store_dwordx4 v[56:59], v2, s[40:43], s56 offen offset:3072 sc1\n"
    "v_accvgpr_read_b32 v64, a96\n"
    "v_accvgpr_read_b32 v65, a97\n"
    "v_accvgpr_read_b32 v66, a98\n"
    "v_accvgpr_read_b32 v67, a99\n"
    "v_accvgpr_read_b32 v68, a100\n"
    "v_accvgpr_read_b32 v69, a101\n"
    "v_accvgpr_read_b32 v70, a102\n"
    "v_accvgpr_read_b32 v71, a103\n"
    "v_pk_mul_f32 v[64:65], v[64:65], s[68:69]\n"
    "v_pk_mul_f32 v[66:67], v[66:67], s[68:69]\n"
    "v_pk_mul_f32 v[68:69], v[68:69], s[68:69]\n"
    "v_pk_mul_f32 v[70:71], v[70:71], s[68:69]\n"
    "v_cvt_pk_bf16_f32 v64, v64, v65\n"
    "v_cvt_pk_bf16_f32 v65, v66, v67\n"
    "v_cvt_pk_bf16_f32 v66, v68, v69\n"
    "v_cvt_pk_bf16_f32 v67, v70, v71\n"
    "s_add_u32 s56, s66, 12288\n"
    "buffer_store_dwordx4 v[64:67], v2, s[40:43], s56 offen offset:0 sc1\n"
    "v_accvgpr_read_b32 v72, a104\n"
    "v_accvgpr_read_b32 v73, a105\n"
    "v_accvgpr_read_b32 v74, a106\n"
    "v_accvgpr_read_b32 v75, a107\n"
    "v_accvgpr_read_b32 v76, a108\n"
    "v_accvgpr_read_b32 v77, a109\n"
    "v_accvgpr_read_b32 v78, a110\n"
    "v_accvgpr_read_b32 v79, a111\n"
    "v_pk_mul_f32 v[72:73], v[72:73], s[68:69]\n"
    "v_pk_mul_f32 v[74:75], v[74:75], s[68:69]\n"
    "v_pk_mul_f32 v[76:77], v[76:77], s[68:69]\n"
    "v_pk_mul_f32 v[78:79], v[78:79], s[68:69]\n"
    "v_cvt_pk_bf16_f32 v72, v72, v73\n"
    "v_cvt_pk_bf16_f32 v73, v74, v75\n"
    "v_cvt_pk_bf16_f32 v74, v76, v77\n"
    "v_cvt_pk_bf16_f32 v75, v78, v79\n"
    "buffer_store_dwordx4 v[72:75], v2, s[40:43], s56 offen offset:1024 sc1\n"
    "v_accvgpr_read_b32 v80, a112\n"
    "v_accvgpr_read_b32 v81, a113\n"
    "v_accvgpr_read_b32 v82, a114\n"
    "v_accvgpr_read_b32 v83, a115\n"
    "v_accvgpr_read_b32 v84, a116\n"
    "v_accvgpr_read_b32 v85, a117\n"
    "v_accvgpr_read_b32 v86, a118\n"
    "v_accvgpr_read_b32 v87, a119\n"
    "v_pk_mul_f32 v[80:81], v[80:81], s[68:69]\n"
    "v_pk_mul_f32 v[82:83], v[82:83], s[68:69]\n"
    "v_pk_mul_f32 v[84:85], v[84:85], s[68:69]\n"
    "v_pk_mul_f32 v[86:87], v[86:87], s[68:69]\n"
    "v_cvt_pk_bf16_f32 v80, v80, v81\n"
    "v_cvt_pk_bf16_f32 v81, v82, v83\n"
    "v_cvt_pk_bf16_f32 v82, v84, v85\n"
    "v_cvt_pk_bf16_f32 v83, v86, v87\n"
    "buffer_store_dwordx4 v[80:83], v2, s[40:43], s56 offen offset:2048 sc1\n"
    "v_accvgpr_read_b32 v88, a120\n"
    "v_accvgpr_read_b32 v89, a121\n"
    "v_accvgpr_read_b32 v90, a122\n"
    "v_accvgpr_read_b32 v91, a123\n"
    "v_accvgpr_read_b32 v92, a124\n"
    "v_accvgpr_read_b32 v93, a125\n"
    "v_accvgpr_read_b32 v94, a126\n"
    "v_accvgpr_read_b32 v95, a127\n"
    "v_pk_mul_f32 v[88:89], v[88:89], s[68:69]\n"
    "v_pk_mul_f32 v[90:91], v[90:91], s[68:69]\n"
    "v_pk_mul_f32 v[92:93], v[92:93], s[68:69]\n"
    "v_pk_mul_f32 v[94:95], v[94:95], s[68:69]\n"
    "v_cvt_pk_bf16_f32 v88, v88, v89\n"
    "v_cvt_pk_bf16_f32 v89, v90, v91\n"
    "v_cvt_pk_bf16_f32 v90, v92, v93\n"
    "v_cvt_pk_bf16_f32 v91, v94, v95\n"
    "buffer_store_dwordx4 v[88:91], v2, s[40:43], s56 offen offset:3072 sc1\n"
    "s_mov_b32 s32, s80\n"
    "s_and_b32 s33, s81, 0xffff\n"
    "s_mov_b32 s34, 2097152\n"
    "s_mov_b32 s35, 0x20000\n"
    "s_lshl_b32 s56, s24, 2\n"
    "s_add_u32 s56, s56, s25\n"
    "s_lshl_b32 s56, s56, 14\n"
    "v_lshlrev_b32 v19, 7, v1\n"
    "s_add_u32 s57, s56, 8192\n"
    "buffer_load_dword a250, v19, s[32:35], s56 offen\n"
    "buffer_load_dword a251, v19, s[32:35], s57 offen\n"
    ".Lat_noproj_%=:\n"
    "v_accvgpr_write_b32 a0, 0\n"
    "v_accvgpr_write_b32 a1, 0\n"
    "v_accvgpr_write_b32 a2, 0\n"
    "v_accvgpr_write_b32 a3, 0\n"
    "v_accvgpr_write_b32 a4, 0\n"
    "v_accvgpr_write_b32 a5, 0\n"
    "v_accvgpr_write_b32 a6, 0\n"
    "v_accvgpr_write_b32 a7, 0\n"
    "v_accvgpr_write_b32 a8, 0\n"
    "v_accvgpr_write_b32 a9, 0\n"
    "v_accvgpr_write_b32 a10, 0\n"
    "v_accvgpr_write_b32 a11, 0\n"
    "v_accvgpr_write_b32 a12, 0\n"
    "v_accvgpr_write_b32 a13, 0\n"
    "v_accvgpr_write_b32 a14, 0\n"
    "v_accvgpr_write_b32 a15, 0\n"
    "v_accvgpr_write_b32 a16, 0\n"
    "v_accvgpr_write_b32 a17, 0\n"
    "v_accvgpr_write_b32 a18, 0\n"
    "v_accvgpr_write_b32 a19, 0\n"
    "v_accvgpr_write_b32 a20, 0\n"
    "v_accvgpr_write_b32 a21, 0\n"
    "v_accvgpr_write_b32 a22, 0\n"
    "v_accvgpr_write_b32 a23, 0\n"
    "v_accvgpr_write_b32 a24, 0\n"
    "v_accvgpr_write_b32 a25, 0\n"
    "v_accvgpr_write_b32 a26, 0\n"
    "v_accvgpr_write_b32 a27, 0\n"
    "v_accvgpr_write_b32 a28, 0\n"
    "v_accvgpr_write_b32 a29, 0\n"
    "v_accvgpr_write_b32 a30, 0\n"
    "v_accvgpr_write_b32 a31, 0\n"
    "v_accvgpr_write_b32 a32, 0\n"
    "v_accvgpr_write_b32 a33, 0\n"
    "v_accvgpr_write_b32 a34, 0\n"
    "v_accvgpr_write_b32 a35, 0\n"
    "v_accvgpr_write_b32 a36, 0\n"
    "v_accvgpr_write_b32 a37, 0\n"
    "v_accvgpr_write_b32 a38, 0\n"
    "v_accvgpr_write_b32 a39, 0\n"
    "v_accvgpr_write_b32 a40, 0\n"
    "v_accvgpr_write_b32 a41, 0\n"
    "v_accvgpr_write_b32 a42, 0\n"
    "v_accvgpr_write_b32 a43, 0\n"
    "v_accvgpr_write_b32 a44, 0\n"
    "v_accvgpr_write_b32 a45, 0\n"
    "v_accvgpr_write_b32 a46, 0\n"
    "v_accvgpr_write_b32 a47, 0\n"
    "v_accvgpr_write_b32 a48, 0\n"
    "v_accvgpr_write_b32 a49, 0\n"
    "v_accvgpr_write_b32 a50, 0\n"
    "v_accvgpr_write_b32 a51, 0\n"
    "v_accvgpr_write_b32 a52, 0\n"
    "v_accvgpr_write_b32 a53, 0\n"
    "v_accvgpr_write_b32 a54, 0\n"
    "v_accvgpr_write_b32 a55, 0\n"
    "v_accvgpr_write_b32 a56, 0\n"
    "v_accvgpr_write_b32 a57, 0\n"
    "v_accvgpr_write_b32 a58, 0\n"
    "v_accvgpr_write_b32 a59, 0\n"
    "v_accvgpr_write_b32 a60, 0\n"
    "v_accvgpr_write_b32 a61, 0\n"
    "v_accvgpr_write_b32 a62, 0\n"
    "v_accvgpr_write_b32 a63, 0\n"
    "v_accvgpr_write_b32 a64, 0\n"
    "v_accvgpr_write_b32 a65, 0\n"
    "v_accvgpr_write_b32 a66, 0\n"
    "v_accvgpr_write_b32 a67, 0\n"
    "v_accvgpr_write_b32 a68, 0\n"
    "v_accvgpr_write_b32 a69, 0\n"
    "v_accvgpr_write_b32 a70, 0\n"
    "v_accvgpr_write_b32 a71, 0\n"
    "v_accvgpr_write_b32 a72, 0\n"
    "v_accvgpr_write_b32 a73, 0\n"
    "v_accvgpr_write_b32 a74, 0\n"
    "v_accvgpr_write_b32 a75, 0\n"
    "v_accvgpr_write_b32 a76, 0\n"
    "v_accvgpr_write_b32 a77, 0\n"
    "v_accvgpr_write_b32 a78, 0\n"
    "v_accvgpr_write_b32 a79, 0\n"
    "v_accvgpr_write_b32 a80, 0\n"
    "v_accvgpr_write_b32 a81, 0\n"
    "v_accvgpr_write_b32 a82, 0\n"
    "v_accvgpr_write_b32 a83, 0\n"
    "v_accvgpr_write_b32 a84, 0\n"
    "v_accvgpr_write_b32 a85, 0\n"
    "v_accvgpr_write_b32 a86, 0\n"
    "v_accvgpr_write_b32 a87, 0\n"
    "v_accvgpr_write_b32 a88, 0\n"
    "v_accvgpr_write_b32 a89, 0\n"
    "v_accvgpr_write_b32 a90, 0\n"
    "v_accvgpr_write_b32 a91, 0\n"
    "v_accvgpr_write_b32 a92, 0\n"
    "v_accvgpr_write_b32 a93, 0\n"
    "v_accvgpr_write_b32 a94, 0\n"
    "v_accvgpr_write_b32 a95, 0\n"
    "v_accvgpr_write_b32 a96, 0\n"
    "v_accvgpr_write_b32 a97, 0\n"
    "v_accvgpr_write_b32 a98, 0\n"
    "v_accvgpr_write_b32 a99, 0\n"
    "v_accvgpr_write_b32 a100, 0\n"
    "v_accvgpr_write_b32 a101, 0\n"
    "v_accvgpr_write_b32 a102, 0\n"
    "v_accvgpr_write_b32 a103, 0\n"
    "v_accvgpr_write_b32 a104, 0\n"
    "v_accvgpr_write_b32 a105, 0\n"
    "v_accvgpr_write_b32 a106, 0\n"
    "v_accvgpr_write_b32 a107, 0\n"
    "v_accvgpr_write_b32 a108, 0\n"
    "v_accvgpr_write_b32 a109, 0\n"
    "v_accvgpr_write_b32 a110, 0\n"
    "v_accvgpr_write_b32 a111, 0\n"
    "v_accvgpr_write_b32 a112, 0\n"
    "v_accvgpr_write_b32 a113, 0\n"
    "v_accvgpr_write_b32 a114, 0\n"
    "v_accvgpr_write_b32 a115, 0\n"
    "v_accvgpr_write_b32 a116, 0\n"
    "v_accvgpr_write_b32 a117, 0\n"
    "v_accvgpr_write_b32 a118, 0\n"
    "v_accvgpr_write_b32 a119, 0\n"
    "v_accvgpr_write_b32 a120, 0\n"
    "v_accvgpr_write_b32 a121, 0\n"
    "v_accvgpr_write_b32 a122, 0\n"
    "v_accvgpr_write_b32 a123, 0\n"
    "v_accvgpr_write_b32 a124, 0\n"
    "v_accvgpr_write_b32 a125, 0\n"
    "v_accvgpr_write_b32 a126, 0\n"
    "v_accvgpr_write_b32 a127, 0\n"
    "v_accvgpr_write_b32 a192, 0\n"
    "v_accvgpr_write_b32 a193, 0\n"
    "v_accvgpr_write_b32 a194, 0\n"
    "v_accvgpr_write_b32 a195, 0\n"
    "v_accvgpr_write_b32 a196, 0\n"
    "v_accvgpr_write_b32 a197, 0\n"
    "v_accvgpr_write_b32 a198, 0\n"
    "v_accvgpr_write_b32 a199, 0\n"
    "v_accvgpr_write_b32 a200, 0\n"
    "v_accvgpr_write_b32 a201, 0\n"
    "v_accvgpr_write_b32 a202, 0\n"
    "v_accvgpr_write_b32 a203, 0\n"
    "v_accvgpr_write_b32 a204, 0\n"
    "v_accvgpr_write_b32 a205, 0\n"
    "v_accvgpr_write_b32 a206, 0\n"
    "v_accvgpr_write_b32 a207, 0\n"
    "v_accvgpr_write_b32 a208, 0\n"
    "v_accvgpr_write_b32 a209, 0\n"
    "v_accvgpr_write_b32 a210, 0\n"
    "v_accvgpr_write_b32 a211, 0\n"
    "v_accvgpr_write_b32 a212, 0\n"
    "v_accvgpr_write_b32 a213, 0\n"
    "v_accvgpr_write_b32 a214, 0\n"
    "v_accvgpr_write_b32 a215, 0\n"
    "v_accvgpr_write_b32 a216, 0\n"
    "v_accvgpr_write_b32 a217, 0\n"
    "v_accvgpr_write_b32 a218, 0\n"
    "v_accvgpr_write_b32 a219, 0\n"
    "v_accvgpr_write_b32 a220, 0\n"
    "v_accvgpr_write_b32 a221, 0\n"
    "v_accvgpr_write_b32 a222, 0\n"
    "v_accvgpr_write_b32 a223, 0\n"
    "v_lshrrev_b32 v19, 2, v1\n"
    "v_lshrrev_b32 v20, 4, v1\n"
    "v_xor_b32 v19, v19, v20\n"
    "v_and_b32 v19, 1, v19\n"
    "v_add_u32 v19, -1, v19\n"
    "v_and_b32 v76, 0x3f803f80, v19\n"
    "v_and_b32 v77, 0x3f803f80, v19\n"
    "v_and_b32 v78, 0x3f803f80, v19\n"
    "v_and_b32 v79, 0x3f803f80, v19\n"
    "s_and_b32 s67, s24, 31\n"
    "s_lshl_b32 s67, s67, 7\n"
    "s_add_u32 s67, s67, s27\n"
    "s_xor_b32 s56, s27, 8192\n"
    "s_cmp_lt_u32 s24, 32\n"
    "s_cselect_b32 s56, s56, s67\n"
    "s_cselect_b32 s57, 1, 31\n"
    "v_lshrrev_b32 v19, s57, v2\n"
    "s_cmp_lg_u32 s25, 0\n"
    "s_cbranch_scc1 .Lat_kvok_%=\n"
    ".Lat_kvpoll_%=:\n"
    "buffer_load_dwordx2 v[20:21], v19, s[48:51], s56 offen sc1\n"
    "s_waitcnt vmcnt(0)\n"
    "v_min_u32 v20, v20, v21\n"
    "v_cmp_eq_u32 vcc, 0, v20\n"
    "s_cbranch_vccz .Lat_kvok_%=\n"
    "s_sleep 16\n"
    "s_sub_u32 s26, s26, 1\n"
    "s_cbranch_scc0 .Lat_kvpoll_%=\n"
    "s_endpgm\n"
    ".Lat_kvok_%=:\n"
    "s_waitcnt vmcnt(0)\n"
    "s_barrier\n"
    "s_cmp_ge_u32 s24, 32\n"
    "s_cbranch_scc1 .Lat_needq_%=\n"
    "s_cmp_lg_u32 s25, 0\n"
    "s_cbranch_scc1 .Lat_haveq_%=\n"
    "v_mov_b64 v[20:21], -1\n"
    "s_mov_b64 exec, 1\n"
    "buffer_store_dwordx2 v[20:21], off, s[48:51], s67 sc1\n"
    "s_mov_b64 exec, -1\n"
    "s_branch .Lat_haveq_%=\n"
    ".Lat_needq_%=:\n"
    "s_and_b32 s56, s24, 7\n"
    "s_lshl_b32 s56, s56, 2\n"
    "s_add_u32 s56, s56, s25\n"
    ".Lat_stag_%=:\n"
    "s_sleep 16\n"
    "s_sub_u32 s56, s56, 1\n"
    "s_cbranch_scc0 .Lat_stag_%=\n"
    "s_add_u32 s40, s30, 74448896\n"
    "s_addc_u32 s41, s31, 0\n"
    "s_and_b32 s41, s41, 0xffff\n"
    "s_mov_b32 s42, 4194304\n"
    "s_mov_b32 s43, 0x20000\n"
    "s_lshl_b32 s66, s60, 2\n"
    "s_add_u32 s66, s66, s59\n"
    "s_lshl_b32 s66, s66, 14\n"
    "s_add_u32 s56, s66, 0\n"
    "buffer_load_dwordx4 a[128:131], v2, s[40:43], s56 offen offset:0 sc1\n"
    "buffer_load_dwordx4 a[132:135], v2, s[40:43], s56 offen offset:1024 sc1\n"
    "buffer_load_dwordx4 a[136:139], v2, s[40:43], s56 offen offset:2048 sc1\n"
    "buffer_load_dwordx4 a[140:143], v2, s[40:43], s56 offen offset:3072 sc1\n"
    "s_add_u32 s56, s66, 4096\n"
    "buffer_load_dwordx4 a[144:147], v2, s[40:43], s56 offen offset:0 sc1\n"
    "buffer_load_dwordx4 a[148:151], v2, s[40:43], s56 offen offset:1024 sc1\n"
    "buffer_load_dwordx4 a[152:155], v2, s[40:43], s56 offen offset:2048 sc1\n"
    "buffer_load_dwordx4 a[156:159], v2, s[40:43], s56 offen offset:3072 sc1\n"
    "s_add_u32 s56, s66, 8192\n"
    "buffer_load_dwordx4 a[160:163], v2, s[40:43], s56 offen offset:0 sc1\n"
    "buffer_load_dwordx4 a[164:167], v2, s[40:43], s56 offen offset:1024 sc1\n"
    "buffer_load_dwordx4 a[168:171], v2, s[40:43], s56 offen offset:2048 sc1\n"
    "buffer_load_dwordx4 a[172:175], v2, s[40:43], s56 offen offset:3072 sc1\n"
    "s_add_u32 s56, s66, 12288\n"
    "buffer_load_dwordx4 a[176:179], v2, s[40:43], s56 offen offset:0 sc1\n"
    "buffer_load_dwordx4 a[180:183], v2, s[40:43], s56 offen offset:1024 sc1\n"
    "buffer_load_dwordx4 a[184:187], v2, s[40:43], s56 offen offset:2048 sc1\n"
    "buffer_load_dwordx4 a[188:191], v2, s[40:43], s56 offen offset:3072 sc1\n"
    ".Lat_haveq_%=:\n"
    "buffer_load_dwordx4 a[224:227], v2, s[44:47], 0 offen offset:0 sc1\n"
    "buffer_load_dwordx4 a[228:231], v2, s[44:47], 0 offen offset:1024 sc1\n"
    "buffer_load_dwordx4 a[232:235], v2, s[44:47], 0 offen offset:2048 sc1\n"
    "buffer_load_dwordx4 a[236:239], v2, s[44:47], 0 offen offset:3072 sc1\n"
    "s_mov_b32 s67, 4096\n"
    "s_waitcnt vmcnt(0)\n"
    "v_mfma_f32_32x32x16_bf16 v[96:111], a[224:227], a[128:131], 0\n"
    "v_mfma_f32_32x32x16_bf16 v[96:111], a[228:231], a[132:135], v[96:111]\n"
    "s_nop 11\n"
    "v_max3_f32 v20, v96, v97, v98\n"
    "v_max3_f32 v20, v20, v99, v100\n"
    "v_max3_f32 v20, v20, v101, v102\n"
    "v_max3_f32 v20, v20, v103, v104\n"
    "v_max3_f32 v20, v20, v105, v106\n"
    "v_max3_f32 v20, v20, v107, v108\n"
    "v_max3_f32 v20, v20, v109, v110\n"
    "v_max_f32 v20, v20, v111\n"
    "v_mov_b32 v21, v20\n"
    "s_nop 1\n"
    "v_permlane32_swap_b32 v20, v21\n"
    "v_max_f32 v20, v20, v21\n"
    "v_add_f32 v20, 1109393408, v20\n"
    "v_sub_f32 v128, 0, v20\n"
    "v_sub_f32 v129, 0, v20\n"
    "v_sub_f32 v130, 0, v20\n"
    "v_sub_f32 v131, 0, v20\n"
    "v_sub_f32 v132, 0, v20\n"
    "v_sub_f32 v133, 0, v20\n"
    "v_sub_f32 v134, 0, v20\n"
    "v_sub_f32 v135, 0, v20\n"
    "v_sub_f32 v136, 0, v20\n"
    "v_sub_f32 v137, 0, v20\n"
    "v_sub_f32 v138, 0, v20\n"
    "v_sub_f32 v139, 0, v20\n"
    "v_sub_f32 v140, 0, v20\n"
    "v_sub_f32 v141, 0, v20\n"
    "v_sub_f32 v142, 0, v20\n"
    "v_sub_f32 v143, 0, v20\n"
    "v_mfma_f32_32x32x16_bf16 v[96:111], a[224:227], a[136:139], 0\n"
    "v_mfma_f32_32x32x16_bf16 v[96:111], a[228:231], a[140:143], v[96:111]\n"
    "s_nop 11\n"
    "v_max3_f32 v20, v96, v97, v98\n"
    "v_max3_f32 v20, v20, v99, v100\n"
    "v_max3_f32 v20, v20, v101, v102\n"
    "v_max3_f32 v20, v20, v103, v104\n"
    "v_max3_f32 v20, v20, v105, v106\n"
    "v_max3_f32 v20, v20, v107, v108\n"
    "v_max3_f32 v20, v20, v109, v110\n"
    "v_max_f32 v20, v20, v111\n"
    "v_mov_b32 v21, v20\n"
    "s_nop 1\n"
    "v_permlane32_swap_b32 v20, v21\n"
    "v_max_f32 v20, v20, v21\n"
    "v_add_f32 v20, 1109393408, v20\n"
    "v_sub_f32 v144, 0, v20\n"
    "v_sub_f32 v145, 0, v20\n"
    "v_sub_f32 v146, 0, v20\n"
    "v_sub_f32 v147, 0, v20\n"
    "v_sub_f32 v148, 0, v20\n"
    "v_sub_f32 v149, 0, v20\n"
    "v_sub_f32 v150, 0, v20\n"
    "v_sub_f32 v151, 0, v20\n"
    "v_sub_f32 v152, 0, v20\n"
    "v_sub_f32 v153, 0, v20\n"
    "v_sub_f32 v154, 0, v20\n"
    "v_sub_f32 v155, 0, v20\n"
    "v_sub_f32 v156, 0, v20\n"
    "v_sub_f32 v157, 0, v20\n"
    "v_sub_f32 v158, 0, v20\n"
    "v_sub_f32 v159, 0, v20\n"
    "v_mfma_f32_32x32x16_bf16 v[96:111], a[224:227], a[144:147], 0\n"
    "v_mfma_f32_32x32x16_bf16 v[96:111], a[228:231], a[148:151], v[96:111]\n"
    "s_nop 11\n"
    "v_max3_f32 v20, v96, v97, v98\n"
    "v_max3_f32 v20, v20, v99, v100\n"
    "v_max3_f32 v20, v20, v101, v102\n"
    "v_max3_f32 v20, v20, v103, v104\n"
    "v_max3_f32 v20, v20, v105, v106\n"
    "v_max3_f32 v20, v20, v107, v108\n"
    "v_max3_f32 v20, v20, v109, v110\n"
    "v_max_f32 v20, v20, v111\n"
    "v_mov_b32 v21, v20\n"
    "s_nop 1\n"
    "v_permlane32_swap_b32 v20, v21\n"
    "v_max_f32 v20, v20, v21\n"
    "v_add_f32 v20, 1109393408, v20\n"
    "v_sub_f32 v160, 0, v20\n"
    "v_sub_f32 v161, 0, v20\n"
    "v_sub_f32 v162, 0, v20\n"
    "v_sub_f32 v163, 0, v20\n"
    "v_sub_f32 v164, 0, v20\n"
    "v_sub_f32 v165, 0, v20\n"
    "v_sub_f32 v166, 0, v20\n"
    "v_sub_f32 v167, 0, v20\n"
    "v_sub_f32 v168, 0, v20\n"
    "v_sub_f32 v169, 0, v20\n"
    "v_sub_f32 v170, 0, v20\n"
    "v_sub_f32 v171, 0, v20\n"
    "v_sub_f32 v172, 0, v20\n"
    "v_sub_f32 v173, 0, v20\n"
    "v_sub_f32 v174, 0, v20\n"
    "v_sub_f32 v175, 0, v20\n"
    "v_mfma_f32_32x32x16_bf16 v[96:111], a[224:227], a[152:155], 0\n"
    "v_mfma_f32_32x32x16_bf16 v[96:111], a[228:231], a[156:159], v[96:111]\n"
    "s_nop 11\n"
    "v_max3_f32 v20, v96, v97, v98\n"
    "v_max3_f32 v20, v20, v99, v100\n"
    "v_max3_f32 v20, v20, v101, v102\n"
    "v_max3_f32 v20, v20, v103, v104\n"
    "v_max3_f32 v20, v20, v105, v106\n"
    "v_max3_f32 v20, v20, v107, v108\n"
    "v_max3_f32 v20, v20, v109, v110\n"
    "v_max_f32 v20, v20, v111\n"
    "v_mov_b32 v21, v20\n"
    "s_nop 1\n"
    "v_permlane32_swap_b32 v20, v21\n"
    "v_max_f32 v20, v20, v21\n"
    "v_add_f32 v20, 1109393408, v20\n"
    "v_sub_f32 v176, 0, v20\n"
    "v_sub_f32 v177, 0, v20\n"
    "v_sub_f32 v178, 0, v20\n"
    "v_sub_f32 v179, 0, v20\n"
    "v_sub_f32 v180, 0, v20\n"
    "v_sub_f32 v181, 0, v20\n"
    "v_sub_f32 v182, 0, v20\n"
    "v_sub_f32 v183, 0, v20\n"
    "v_sub_f32 v184, 0, v20\n"
    "v_sub_f32 v185, 0, v20\n"
    "v_sub_f32 v186, 0, v20\n"
    "v_sub_f32 v187, 0, v20\n"
    "v_sub_f32 v188, 0, v20\n"
    "v_sub_f32 v189, 0, v20\n"
    "v_sub_f32 v190, 0, v20\n"
    "v_sub_f32 v191, 0, v20\n"
    "v_mfma_f32_32x32x16_bf16 v[96:111], a[224:227], a[160:163], 0\n"
    "v_mfma_f32_32x32x16_bf16 v[96:111], a[228:231], a[164:167], v[96:111]\n"
    "s_nop 11\n"
    "v_max3_f32 v20, v96, v97, v98\n"
    "v_max3_f32 v20, v20, v99, v100\n"
    "v_max3_f32 v20, v20, v101, v102\n"
    "v_max3_f32 v20, v20, v103, v104\n"
    "v_max3_f32 v20, v20, v105, v106\n"
    "v_max3_f32 v20, v20, v107, v108\n"
    "v_max3_f32 v20, v20, v109, v110\n"
    "v_max_f32 v20, v20, v111\n"
    "v_mov_b32 v21, v20\n"
    "s_nop 1\n"
    "v_permlane32_swap_b32 v20, v21\n"
    "v_max_f32 v20, v20, v21\n"
    "v_add_f32 v20, 1109393408, v20\n"
    "v_sub_f32 v192, 0, v20\n"
    "v_sub_f32 v193, 0, v20\n"
    "v_sub_f32 v194, 0, v20\n"
    "v_sub_f32 v195, 0, v20\n"
    "v_sub_f32 v196, 0, v20\n"
    "v_sub_f32 v197, 0, v20\n"
    "v_sub_f32 v198, 0, v20\n"
    "v_sub_f32 v199, 0, v20\n"
    "v_sub_f32 v200, 0, v20\n"
    "v_sub_f32 v201, 0, v20\n"
    "v_sub_f32 v202, 0, v20\n"
    "v_sub_f32 v203, 0, v20\n"
    "v_sub_f32 v204, 0, v20\n"
    "v_sub_f32 v205, 0, v20\n"
    "v_sub_f32 v206, 0, v20\n"
    "v_sub_f32 v207, 0, v20\n"
    "v_mfma_f32_32x32x16_bf16 v[96:111], a[224:227], a[168:171], 0\n"
    "v_mfma_f32_32x32x16_bf16 v[96:111], a[228:231], a[172:175], v[96:111]\n"
    "s_nop 11\n"
    "v_max3_f32 v20, v96, v97, v98\n"
    "v_max3_f32 v20, v20, v99, v100\n"
    "v_max3_f32 v20, v20, v101, v102\n"
    "v_max3_f32 v20, v20, v103, v104\n"
    "v_max3_f32 v20, v20, v105, v106\n"
    "v_max3_f32 v20, v20, v107, v108\n"
    "v_max3_f32 v20, v20, v109, v110\n"
    "v_max_f32 v20, v20, v111\n"
    "v_mov_b32 v21, v20\n"
    "s_nop 1\n"
    "v_permlane32_swap_b32 v20, v21\n"
    "v_max_f32 v20, v20, v21\n"
    "v_add_f32 v20, 1109393408, v20\n"
    "v_sub_f32 v208, 0, v20\n"
    "v_sub_f32 v209, 0, v20\n"
    "v_sub_f32 v210, 0, v20\n"
    "v_sub_f32 v211, 0, v20\n"
    "v_sub_f32 v212, 0, v20\n"
    "v_sub_f32 v213, 0, v20\n"
    "v_sub_f32 v214, 0, v20\n"
    "v_sub_f32 v215, 0, v20\n"
    "v_sub_f32 v216, 0, v20\n"
    "v_sub_f32 v217, 0, v20\n"
    "v_sub_f32 v218, 0, v20\n"
    "v_sub_f32 v219, 0, v20\n"
    "v_sub_f32 v220, 0, v20\n"
    "v_sub_f32 v221, 0, v20\n"
    "v_sub_f32 v222, 0, v20\n"
    "v_sub_f32 v223, 0, v20\n"
    "v_mfma_f32_32x32x16_bf16 v[96:111], a[224:227], a[176:179], 0\n"
    "v_mfma_f32_32x32x16_bf16 v[96:111], a[228:231], a[180:183], v[96:111]\n"
    "s_nop 11\n"
    "v_max3_f32 v20, v96, v97, v98\n"
    "v_max3_f32 v20, v20, v99, v100\n"
    "v_max3_f32 v20, v20, v101, v102\n"
    "v_max3_f32 v20, v20, v103, v104\n"
    "v_max3_f32 v20, v20, v105, v106\n"
    "v_max3_f32 v20, v20, v107, v108\n"
    "v_max3_f32 v20, v20, v109, v110\n"
    "v_max_f32 v20, v20, v111\n"
    "v_mov_b32 v21, v20\n"
    "s_nop 1\n"
    "v_permlane32_swap_b32 v20, v21\n"
    "v_max_f32 v20, v20, v21\n"
    "v_add_f32 v20, 1109393408, v20\n"
    "v_sub_f32 v224, 0, v20\n"
    "v_sub_f32 v225, 0, v20\n"
    "v_sub_f32 v226, 0, v20\n"
    "v_sub_f32 v227, 0, v20\n"
    "v_sub_f32 v228, 0, v20\n"
    "v_sub_f32 v229, 0, v20\n"
    "v_sub_f32 v230, 0, v20\n"
    "v_sub_f32 v231, 0, v20\n"
    "v_sub_f32 v232, 0, v20\n"
    "v_sub_f32 v233, 0, v20\n"
    "v_sub_f32 v234, 0, v20\n"
    "v_sub_f32 v235, 0, v20\n"
    "v_sub_f32 v236, 0, v20\n"
    "v_sub_f32 v237, 0, v20\n"
    "v_sub_f32 v238, 0, v20\n"
    "v_sub_f32 v239, 0, v20\n"
    "v_mfma_f32_32x32x16_bf16 v[96:111], a[224:227], a[184:187], 0\n"
    "v_mfma_f32_32x32x16_bf16 v[96:111], a[228:231], a[188:191], v[96:111]\n"
    "s_nop 11\n"
    "v_max3_f32 v20, v96, v97, v98\n"
    "v_max3_f32 v20, v20, v99, v100\n"
    "v_max3_f32 v20, v20, v101, v102\n"
    "v_max3_f32 v20, v20, v103, v104\n"
    "v_max3_f32 v20, v20, v105, v106\n"
    "v_max3_f32 v20, v20, v107, v108\n"
    "v_max3_f32 v20, v20, v109, v110\n"
    "v_max_f32 v20, v20, v111\n"
    "v_mov_b32 v21, v20\n"
    "s_nop 1\n"
    "v_permlane32_swap_b32 v20, v21\n"
    "v_max_f32 v20, v20, v21\n"
    "v_add_f32 v20, 1109393408, v20\n"
    "v_sub_f32 v240, 0, v20\n"
    "v_sub_f32 v241, 0, v20\n"
    "v_sub_f32 v242, 0, v20\n"
    "v_sub_f32 v243, 0, v20\n"
    "v_sub_f32 v244, 0, v20\n"
    "v_sub_f32 v245, 0, v20\n"
    "v_sub_f32 v246, 0, v20\n"
    "v_sub_f32 v247, 0, v20\n"
    "v_sub_f32 v248, 0, v20\n"
    "v_sub_f32 v249, 0, v20\n"
    "v_sub_f32 v250, 0, v20\n"
    "v_sub_f32 v251, 0, v20\n"
    "v_sub_f32 v252, 0, v20\n"
    "v_sub_f32 v253, 0, v20\n"
    "v_sub_f32 v254, 0, v20\n"
    "v_sub_f32 v255, 0, v20\n"
    "v_mfma_f32_32x32x16_bf16 v[96:111], a[224:227], a[128:131], v[128:143]\n"
    "v_mfma_f32_32x32x16_bf16 v[96:111], a[228:231], a[132:135], v[96:111]\n"
    "v_mov_b32 v88, 0\n"
    "v_mov_b32 v89, 0\n"
    "v_mov_b32 v90, 0\n"
    "v_mov_b32 v91, 0\n"
    "v_mov_b32 v92, 0\n"
    "v_mov_b32 v93, 0\n"
    "v_mov_b32 v94, 0\n"
    "v_mov_b32 v95, 0\n"
    "v_mov_b64 v[126:127], 0\n"
    "v_accvgpr_write_b32 a248, 0\n"
    "v_accvgpr_write_b32 a249, 0\n"
    "v_accvgpr_write_b32 a250, 0\n"
    "v_accvgpr_write_b32 a251, 0\n"
    "v_accvgpr_write_b32 a252, 0\n"
    "v_accvgpr_write_b32 a253, 0\n"
    "v_accvgpr_write_b32 a254, 0\n"
    "v_accvgpr_write_b32 a255, 0\n"
    "s_mov_b32 s68, 1568693632\n"
    ".Lat_att_%=:\n"
    "v_cvt_pk_bf16_f32 v95, v126, v127\n"
    "v_mfma_f32_32x32x16_bf16 v[112:127], a[224:227], a[136:139], v[144:159]\n"
    "v_pk_maximum3_f16 v20, v88, v89, s68\n"
    "v_pk_maximum3_f16 v20, v90, v91, v20\n"
    "v_pk_maximum3_f16 v20, v92, v93, v20\n"
    "v_pk_maximum3_f16 v20, v94, v95, v20\n"
    "v_cmp_ne_u32 vcc, s68, v20\n"
    "v_exp_f32 v96, v96\n"
    "v_mfma_f32_32x32x16_bf16 v[112:127], a[228:231], a[140:143], v[112:127]\n"
    "s_cbranch_vccnz .Lat_rare0_%=\n"
    ".Lat_back0_%=:\n"
    "v_exp_f32 v97, v97\n"
    "v_exp_f32 v98, v98\n"
    "v_cvt_pk_bf16_f32 v80, v96, v97\n"
    "v_mfma_f32_32x32x16_bf16 a[112:127], a[248:251], v[88:91], a[112:127]\n"
    "v_exp_f32 v99, v99\n"
    "v_exp_f32 v100, v100\n"
    "v_cvt_pk_bf16_f32 v81, v98, v99\n"
    "v_exp_f32 v101, v101\n"
    "v_mfma_f32_32x32x16_bf16 a[112:127], a[252:255], v[92:95], a[112:127]\n"
    "buffer_load_dwordx4 a[240:243], v2, s[44:47], s67 offen offset:0 sc1\n"
    "buffer_load_dwordx4 a[244:247], v2, s[44:47], s67 offen offset:1024 sc1\n"
    "buffer_load_dwordx4 a[248:251], v2, s[44:47], s67 offen offset:2048 sc1\n"
    "buffer_load_dwordx4 a[252:255], v2, s[44:47], s67 offen offset:3072 sc1\n"
    "s_add_u32 s67, s67, 4096\n"
    "v_exp_f32 v102, v102\n"
    "v_cvt_pk_bf16_f32 v82, v100, v101\n"
    "v_exp_f32 v103, v103\n"
    "v_exp_f32 v104, v104\n"
    "v_mfma_f32_16x16x32_bf16 a[220:223], v[76:79], v[88:91], a[220:223]\n"
    "v_cvt_pk_bf16_f32 v83, v102, v103\n"
    "v_exp_f32 v105, v105\n"
    "v_mfma_f32_16x16x32_bf16 a[220:223], v[76:79], v[92:95], a[220:223]\n"
    "v_exp_f32 v106, v106\n"
    "v_cvt_pk_bf16_f32 v84, v104, v105\n"
    "v_exp_f32 v107, v107\n"
    "v_exp_f32 v108, v108\n"
    "v_cvt_pk_bf16_f32 v85, v106, v107\n"
    "v_exp_f32 v109, v109\n"
    "v_exp_f32 v110, v110\n"
    "v_exp_f32 v111, v111\n"
    "v_cvt_pk_bf16_f32 v86, v108, v109\n"
    "v_cvt_pk_bf16_f32 v87, v110, v111\n"
    "v_mfma_f32_32x32x16_bf16 v[96:111], a[224:227], a[144:147], v[160:175]\n"
    "v_pk_maximum3_f16 v20, v80, v81, s68\n"
    "v_pk_maximum3_f16 v20, v82, v83, v20\n"
    "v_pk_maximum3_f16 v20, v84, v85, v20\n"
    "v_pk_maximum3_f16 v20, v86, v87, v20\n"
    "v_cmp_ne_u32 vcc, s68, v20\n"
    "v_exp_f32 v112, v112\n"
    "v_mfma_f32_32x32x16_bf16 v[96:111], a[228:231], a[148:151], v[96:111]\n"
    "s_cbranch_vccnz .Lat_rare1_%=\n"
    ".Lat_back1_%=:\n"
    "v_exp_f32 v113, v113\n"
    "v_exp_f32 v114, v114\n"
    "v_cvt_pk_bf16_f32 v88, v112, v113\n"
    "v_mfma_f32_32x32x16_bf16 a[0:15], a[232:235], v[80:83], a[0:15]\n"
    "v_exp_f32 v115, v115\n"
    "v_exp_f32 v116, v116\n"
    "v_cvt_pk_bf16_f32 v89, v114, v115\n"
    "v_exp_f32 v117, v117\n"
    "v_mfma_f32_32x32x16_bf16 a[0:15], a[236:239], v[84:87], a[0:15]\n"
    "v_exp_f32 v118, v118\n"
    "v_cvt_pk_bf16_f32 v90, v116, v117\n"
    "v_exp_f32 v119, v119\n"
    "v_exp_f32 v120, v120\n"
    "v_mfma_f32_16x16x32_bf16 a[192:195], v[76:79], v[80:83], a[192:195]\n"
    "v_cvt_pk_bf16_f32 v91, v118, v119\n"
    "v_exp_f32 v121, v121\n"
    "v_mfma_f32_16x16x32_bf16 a[192:195], v[76:79], v[84:87], a[192:195]\n"
    "v_exp_f32 v122, v122\n"
    "v_cvt_pk_bf16_f32 v92, v120, v121\n"
    "v_exp_f32 v123, v123\n"
    "v_exp_f32 v124, v124\n"
    "v_cvt_pk_bf16_f32 v93, v122, v123\n"
    "v_exp_f32 v125, v125\n"
    "v_exp_f32 v126, v126\n"
    "v_exp_f32 v127, v127\n"
    "v_cvt_pk_bf16_f32 v94, v124, v125\n"
    "v_cvt_pk_bf16_f32 v95, v126, v127\n"
    "v_mfma_f32_32x32x16_bf16 v[112:127], a[224:227], a[152:155], v[176:191]\n"
    "v_pk_maximum3_f16 v20, v88, v89, s68\n"
    "v_pk_maximum3_f16 v20, v90, v91, v20\n"
    "v_pk_maximum3_f16 v20, v92, v93, v20\n"
    "v_pk_maximum3_f16 v20, v94, v95, v20\n"
    "v_cmp_ne_u32 vcc, s68, v20\n"
    "v_exp_f32 v96, v96\n"
    "v_mfma_f32_32x32x16_bf16 v[112:127], a[228:231], a[156:159], v[112:127]\n"
    "s_cbranch_vccnz .Lat_rare2_%=\n"
    ".Lat_back2_%=:\n"
    "v_exp_f32 v97, v97\n"
    "v_exp_f32 v98, v98\n"
    "v_cvt_pk_bf16_f32 v80, v96, v97\n"
    "v_mfma_f32_32x32x16_bf16 a[16:31], a[232:235], v[88:91], a[16:31]\n"
    "v_exp_f32 v99, v99\n"
    "v_exp_f32 v100, v100\n"
    "v_cvt_pk_bf16_f32 v81, v98, v99\n"
    "v_exp_f32 v101, v101\n"
    "v_mfma_f32_32x32x16_bf16 a[16:31], a[236:239], v[92:95], a[16:31]\n"
    "v_exp_f32 v102, v102\n"
    "v_cvt_pk_bf16_f32 v82, v100, v101\n"
    "v_exp_f32 v103, v103\n"
    "v_exp_f32 v104, v104\n"
    "v_mfma_f32_16x16x32_bf16 a[196:199], v[76:79], v[88:91], a[196:199]\n"
    "v_cvt_pk_bf16_f32 v83, v102, v103\n"
    "v_exp_f32 v105, v105\n"
    "v_mfma_f32_16x16x32_bf16 a[196:199], v[76:79], v[92:95], a[196:199]\n"
    "v_exp_f32 v106, v106\n"
    "v_cvt_pk_bf16_f32 v84, v104, v105\n"
    "v_exp_f32 v107, v107\n"
    "v_exp_f32 v108, v108\n"
    "v_cvt_pk_bf16_f32 v85, v106, v107\n"
    "v_exp_f32 v109, v109\n"
    "v_exp_f32 v110, v110\n"
    "v_exp_f32 v111, v111\n"
    "v_cvt_pk_bf16_f32 v86, v108, v109\n"
    "v_cvt_pk_bf16_f32 v87, v110, v111\n"
    "v_mfma_f32_32x32x16_bf16 v[96:111], a[224:227], a[160:163], v[192:207]\n"
    "v_pk_maximum3_f16 v20, v80, v81, s68\n"
    "v_pk_maximum3_f16 v20, v82, v83, v20\n"
    "v_pk_maximum3_f16 v20, v84, v85, v20\n"
    "v_pk_maximum3_f16 v20, v86, v87, v20\n"
    "v_cmp_ne_u32 vcc, s68, v20\n"
    "v_exp_f32 v112, v112\n"
    "v_mfma_f32_32x32x16_bf16 v[96:111], a[228:231], a[164:167], v[96:111]\n"
    "s_cbranch_vccnz .Lat_rare3_%=\n"
    ".Lat_back3_%=:\n"
    "v_exp_f32 v113, v113\n"
    "v_exp_f32 v114, v114\n"
    "v_cvt_pk_bf16_f32 v88, v112, v113\n"
    "v_mfma_f32_32x32x16_bf16 a[32:47], a[232:235], v[80:83], a[32:47]\n"
    "v_exp_f32 v115, v115\n"
    "v_exp_f32 v116, v116\n"
    "v_cvt_pk_bf16_f32 v89, v114, v115\n"
    "v_exp_f32 v117, v117\n"
    "v_mfma_f32_32x32x16_bf16 a[32:47], a[236:239], v[84:87], a[32:47]\n"
    "v_exp_f32 v118, v118\n"
    "v_cvt_pk_bf16_f32 v90, v116, v117\n"
    "v_exp_f32 v119, v119\n"
    "v_exp_f32 v120, v120\n"
    "v_mfma_f32_16x16x32_bf16 a[200:203], v[76:79], v[80:83], a[200:203]\n"
    "v_cvt_pk_bf16_f32 v91, v118, v119\n"
    "v_exp_f32 v121, v121\n"
    "v_mfma_f32_16x16x32_bf16 a[200:203], v[76:79], v[84:87], a[200:203]\n"
    "v_exp_f32 v122, v122\n"
    "v_cvt_pk_bf16_f32 v92, v120, v121\n"
    "v_exp_f32 v123, v123\n"
    "v_exp_f32 v124, v124\n"
    "v_cvt_pk_bf16_f32 v93, v122, v123\n"
    "v_exp_f32 v125, v125\n"
    "v_exp_f32 v126, v126\n"
    "v_exp_f32 v127, v127\n"
    "v_cvt_pk_bf16_f32 v94, v124, v125\n"
    "v_cvt_pk_bf16_f32 v95, v126, v127\n"
    "v_mfma_f32_32x32x16_bf16 v[112:127], a[224:227], a[168:171], v[208:223]\n"
    "v_pk_maximum3_f16 v20, v88, v89, s68\n"
    "v_pk_maximum3_f16 v20, v90, v91, v20\n"
    "v_pk_maximum3_f16 v20, v92, v93, v20\n"
    "v_pk_maximum3_f16 v20, v94, v95, v20\n"
    "v_cmp_ne_u32 vcc, s68, v20\n"
    "v_exp_f32 v96, v96\n"
    "v_mfma_f32_32x32x16_bf16 v[112:127], a[228:231], a[172:175], v[112:127]\n"
    "s_cbranch_vccnz .Lat_rare4_%=\n"
    ".Lat_back4_%=:\n"
    "v_exp_f32 v97, v97\n"
    "v_exp_f32 v98, v98\n"
    "v_cvt_pk_bf16_f32 v80, v96, v97\n"
    "v_mfma_f32_32x32x16_bf16 a[48:63], a[232:235], v[88:91], a[48:63]\n"
    "v_exp_f32 v99, v99\n"
    "v_exp_f32 v100, v100\n"
    "v_cvt_pk_bf16_f32 v81, v98, v99\n"
    "v_exp_f32 v101, v101\n"
    "v_mfma_f32_32x32x16_bf16 a[48:63], a[236:239], v[92:95], a[48:63]\n"
    "v_exp_f32 v102, v102\n"
    "v_cvt_pk_bf16_f32 v82, v100, v101\n"
    "v_exp_f32 v103, v103\n"
    "v_exp_f32 v104, v104\n"
    "v_mfma_f32_16x16x32_bf16 a[204:207], v[76:79], v[88:91], a[204:207]\n"
    "v_cvt_pk_bf16_f32 v83, v102, v103\n"
    "v_exp_f32 v105, v105\n"
    "v_mfma_f32_16x16x32_bf16 a[204:207], v[76:79], v[92:95], a[204:207]\n"
    "v_exp_f32 v106, v106\n"
    "v_cvt_pk_bf16_f32 v84, v104, v105\n"
    "v_exp_f32 v107, v107\n"
    "v_exp_f32 v108, v108\n"
    "v_cvt_pk_bf16_f32 v85, v106, v107\n"
    "v_exp_f32 v109, v109\n"
    "v_exp_f32 v110, v110\n"
    "v_exp_f32 v111, v111\n"
    "v_cvt_pk_bf16_f32 v86, v108, v109\n"
    "v_cvt_pk_bf16_f32 v87, v110, v111\n"
    "v_mfma_f32_32x32x16_bf16 v[96:111], a[224:227], a[176:179], v[224:239]\n"
    "v_pk_maximum3_f16 v20, v80, v81, s68\n"
    "v_pk_maximum3_f16 v20, v82, v83, v20\n"
    "v_pk_maximum3_f16 v20, v84, v85, v20\n"
    "v_pk_maximum3_f16 v20, v86, v87, v20\n"
    "v_cmp_ne_u32 vcc, s68, v20\n"
    "v_exp_f32 v112, v112\n"
    "v_mfma_f32_32x32x16_bf16 v[96:111], a[228:231], a[180:183], v[96:111]\n"
    "s_cbranch_vccnz .Lat_rare5_%=\n"
    ".Lat_back5_%=:\n"
    "v_exp_f32 v113, v113\n"
    "v_exp_f32 v114, v114\n"
    "v_cvt_pk_bf16_f32 v88, v112, v113\n"
    "v_mfma_f32_32x32x16_bf16 a[64:79], a[232:235], v[80:83], a[64:79]\n"
    "v_exp_f32 v115, v115\n"
    "v_exp_f32 v116, v116\n"
    "v_cvt_pk_bf16_f32 v89, v114, v115\n"
    "v_exp_f32 v117, v117\n"
    "v_mfma_f32_32x32x16_bf16 a[64:79], a[236:239], v[84:87], a[64:79]\n"
    "v_exp_f32 v118, v118\n"
    "v_cvt_pk_bf16_f32 v90, v116, v117\n"
    "v_exp_f32 v119, v119\n"
    "v_exp_f32 v120, v120\n"
    "v_mfma_f32_16x16x32_bf16 a[208:211], v[76:79], v[80:83], a[208:211]\n"
    "v_cvt_pk_bf16_f32 v91, v118, v119\n"
    "v_exp_f32 v121, v121\n"
    "v_mfma_f32_16x16x32_bf16 a[208:211], v[76:79], v[84:87], a[208:211]\n"
    "v_exp_f32 v122, v122\n"
    "v_cvt_pk_bf16_f32 v92, v120, v121\n"
    "v_exp_f32 v123, v123\n"
    "v_exp_f32 v124, v124\n"
    "v_cvt_pk_bf16_f32 v93, v122, v123\n"
    "v_exp_f32 v125, v125\n"
    "v_exp_f32 v126, v126\n"
    "v_exp_f32 v127, v127\n"
    "v_cvt_pk_bf16_f32 v94, v124, v125\n"
    "v_cvt_pk_bf16_f32 v95, v126, v127\n"
    "v_mfma_f32_32x32x16_bf16 v[112:127], a[224:227], a[184:187], v[240:255]\n"
    "v_pk_maximum3_f16 v20, v88, v89, s68\n"
    "v_pk_maximum3_f16 v20, v90, v91, v20\n"
    "v_pk_maximum3_f16 v20, v92, v93, v20\n"
    "v_pk_maximum3_f16 v20, v94, v95, v20\n"
    "v_cmp_ne_u32 vcc, s68, v20\n"
    "v_exp_f32 v96, v96\n"
    "v_mfma_f32_32x32x16_bf16 v[112:127], a[228:231], a[188:191], v[112:127]\n"
    "s_cbranch_vccnz .Lat_rare6_%=\n"
    ".Lat_back6_%=:\n"
    "v_exp_f32 v97, v97\n"
    "v_exp_f32 v98, v98\n"
    "v_cvt_pk_bf16_f32 v80, v96, v97\n"
    "v_mfma_f32_32x32x16_bf16 a[80:95], a[232:235], v[88:91], a[80:95]\n"
    "v_exp_f32 v99, v99\n"
    "v_exp_f32 v100, v100\n"
    "v_cvt_pk_bf16_f32 v81, v98, v99\n"
    "v_exp_f32 v101, v101\n"
    "v_mfma_f32_32x32x16_bf16 a[80:95], a[236:239], v[92:95], a[80:95]\n"
    "v_exp_f32 v102, v102\n"
    "v_cvt_pk_bf16_f32 v82, v100, v101\n"
    "v_exp_f32 v103, v103\n"
    "v_exp_f32 v104, v104\n"
    "v_mfma_f32_16x16x32_bf16 a[212:215], v[76:79], v[88:91], a[212:215]\n"
    "v_cvt_pk_bf16_f32 v83, v102, v103\n"
    "v_exp_f32 v105, v105\n"
    "v_mfma_f32_16x16x32_bf16 a[212:215], v[76:79], v[92:95], a[212:215]\n"
    "v_exp_f32 v106, v106\n"
    "v_cvt_pk_bf16_f32 v84, v104, v105\n"
    "v_exp_f32 v107, v107\n"
    "v_exp_f32 v108, v108\n"
    "v_cvt_pk_bf16_f32 v85, v106, v107\n"
    "v_exp_f32 v109, v109\n"
    "v_exp_f32 v110, v110\n"
    "v_exp_f32 v111, v111\n"
    "v_cvt_pk_bf16_f32 v86, v108, v109\n"
    "s_waitcnt vmcnt(0)\n"
    "v_cvt_pk_bf16_f32 v87, v110, v111\n"
    "v_mfma_f32_32x32x16_bf16 v[96:111], a[240:243], a[128:131], v[128:143]\n"
    "v_pk_maximum3_f16 v20, v80, v81, s68\n"
    "v_pk_maximum3_f16 v20, v82, v83, v20\n"
    "v_pk_maximum3_f16 v20, v84, v85, v20\n"
    "v_pk_maximum3_f16 v20, v86, v87, v20\n"
    "v_cmp_ne_u32 vcc, s68, v20\n"
    "v_exp_f32 v112, v112\n"
    "v_mfma_f32_32x32x16_bf16 v[96:111], a[244:247], a[132:135], v[96:111]\n"
    "s_cbranch_vccnz .Lat_rare7_%=\n"
    ".Lat_back7_%=:\n"
    "v_exp_f32 v113, v113\n"
    "v_exp_f32 v114, v114\n"
    "v_cvt_pk_bf16_f32 v88, v112, v113\n"
    "v_mfma_f32_32x32x16_bf16 a[96:111], a[232:235], v[80:83], a[96:111]\n"
    "v_exp_f32 v115, v115\n"
    "v_exp_f32 v116, v116\n"
    "v_cvt_pk_bf16_f32 v89, v114, v115\n"
    "v_exp_f32 v117, v117\n"
    "v_mfma_f32_32x32x16_bf16 a[96:111], a[236:239], v[84:87], a[96:111]\n"
    "v_exp_f32 v118, v118\n"
    "v_cvt_pk_bf16_f32 v90, v116, v117\n"
    "v_exp_f32 v119, v119\n"
    "v_exp_f32 v120, v120\n"
    "v_mfma_f32_16x16x32_bf16 a[216:219], v[76:79], v[80:83], a[216:219]\n"
    "v_cvt_pk_bf16_f32 v91, v118, v119\n"
    "v_exp_f32 v121, v121\n"
    "v_mfma_f32_16x16x32_bf16 a[216:219], v[76:79], v[84:87], a[216:219]\n"
    "v_exp_f32 v122, v122\n"
    "v_cvt_pk_bf16_f32 v92, v120, v121\n"
    "v_exp_f32 v123, v123\n"
    "v_exp_f32 v124, v124\n"
    "v_cvt_pk_bf16_f32 v93, v122, v123\n"
    "v_exp_f32 v125, v125\n"
    "v_exp_f32 v126, v126\n"
    "v_exp_f32 v127, v127\n"
    "v_cvt_pk_bf16_f32 v94, v124, v125\n"
    "v_cvt_pk_bf16_f32 v95, v126, v127\n"
    "v_mfma_f32_32x32x16_bf16 v[112:127], a[240:243], a[136:139], v[144:159]\n"
    "v_pk_maximum3_f16 v20, v88, v89, s68\n"
    "v_pk_maximum3_f16 v20, v90, v91, v20\n"
    "v_pk_maximum3_f16 v20, v92, v93, v20\n"
    "v_pk_maximum3_f16 v20, v94, v95, v20\n"
    "v_cmp_ne_u32 vcc, s68, v20\n"
    "v_exp_f32 v96, v96\n"
    "v_mfma_f32_32x32x16_bf16 v[112:127], a[244:247], a[140:143], v[112:127]\n"
    "s_cbranch_vccnz .Lat_rare8_%=\n"
    ".Lat_back8_%=:\n"
    "v_exp_f32 v97, v97\n"
    "v_exp_f32 v98, v98\n"
    "v_cvt_pk_bf16_f32 v80, v96, v97\n"
    "v_mfma_f32_32x32x16_bf16 a[112:127], a[232:235], v[88:91], a[112:127]\n"
    "v_exp_f32 v99, v99\n"
    "v_exp_f32 v100, v100\n"
    "v_cvt_pk_bf16_f32 v81, v98, v99\n"
    "v_exp_f32 v101, v101\n"
    "v_mfma_f32_32x32x16_bf16 a[112:127], a[236:239], v[92:95], a[112:127]\n"
    "buffer_load_dwordx4 a[224:227], v2, s[44:47], s67 offen offset:0 sc1\n"
    "buffer_load_dwordx4 a[228:231], v2, s[44:47], s67 offen offset:1024 sc1\n"
    "buffer_load_dwordx4 a[232:235], v2, s[44:47], s67 offen offset:2048 sc1\n"
    "buffer_load_dwordx4 a[236:239], v2, s[44:47], s67 offen offset:3072 sc1\n"
    "s_add_u32 s67, s67, 4096\n"
    "v_exp_f32 v102, v102\n"
    "v_cvt_pk_bf16_f32 v82, v100, v101\n"
    "v_exp_f32 v103, v103\n"
    "v_exp_f32 v104, v104\n"
    "v_mfma_f32_16x16x32_bf16 a[220:223], v[76:79], v[88:91], a[220:223]\n"
    "v_cvt_pk_bf16_f32 v83, v102, v103\n"
    "v_exp_f32 v105, v105\n"
    "v_mfma_f32_16x16x32_bf16 a[220:223], v[76:79], v[92:95], a[220:223]\n"
    "v_exp_f32 v106, v106\n"
    "v_cvt_pk_bf16_f32 v84, v104, v105\n"
    "v_exp_f32 v107, v107\n"
    "v_exp_f32 v108, v108\n"
    "v_cvt_pk_bf16_f32 v85, v106, v107\n"
    "v_exp_f32 v109, v109\n"
    "v_exp_f32 v110, v110\n"
    "v_exp_f32 v111, v111\n"
    "v_cvt_pk_bf16_f32 v86, v108, v109\n"
    "v_cvt_pk_bf16_f32 v87, v110, v111\n"
    "v_mfma_f32_32x32x16_bf16 v[96:111], a[240:243], a[144:147], v[160:175]\n"
    "v_pk_maximum3_f16 v20, v80, v81, s68\n"
    "v_pk_maximum3_f16 v20, v82, v83, v20\n"
    "v_pk_maximum3_f16 v20, v84, v85, v20\n"
    "v_pk_maximum3_f16 v20, v86, v87, v20\n"
    "v_cmp_ne_u32 vcc, s68, v20\n"
    "v_exp_f32 v112, v112\n"
    "v_mfma_f32_32x32x16_bf16 v[96:111], a[244:247], a[148:151], v[96:111]\n"
    "s_cbranch_vccnz .Lat_rare9_%=\n"
    ".Lat_back9_%=:\n"
    "v_exp_f32 v113, v113\n"
    "v_exp_f32 v114, v114\n"
    "v_cvt_pk_bf16_f32 v88, v112, v113\n"
    "v_mfma_f32_32x32x16_bf16 a[0:15], a[248:251], v[80:83], a[0:15]\n"
    "v_exp_f32 v115, v115\n"
    "v_exp_f32 v116, v116\n"
    "v_cvt_pk_bf16_f32 v89, v114, v115\n"
    "v_exp_f32 v117, v117\n"
    "v_mfma_f32_32x32x16_bf16 a[0:15], a[252:255], v[84:87], a[0:15]\n"
    "v_exp_f32 v118, v118\n"
    "v_cvt_pk_bf16_f32 v90, v116, v117\n"
    "v_exp_f32 v119, v119\n"
    "v_exp_f32 v120, v120\n"
    "v_mfma_f32_16x16x32_bf16 a[192:195], v[76:79], v[80:83], a[192:195]\n"
    "v_cvt_pk_bf16_f32 v91, v118, v119\n"
    "v_exp_f32 v121, v121\n"
    "v_mfma_f32_16x16x32_bf16 a[192:195], v[76:79], v[84:87], a[192:195]\n"
    "v_exp_f32 v122, v122\n"
    "v_cvt_pk_bf16_f32 v92, v120, v121\n"
    "v_exp_f32 v123, v123\n"
    "v_exp_f32 v124, v124\n"
    "v_cvt_pk_bf16_f32 v93, v122, v123\n"
    "v_exp_f32 v125, v125\n"
    "v_exp_f32 v126, v126\n"
    "v_exp_f32 v127, v127\n"
    "v_cvt_pk_bf16_f32 v94, v124, v125\n"
    "v_cvt_pk_bf16_f32 v95, v126, v127\n"
    "v_mfma_f32_32x32x16_bf16 v[112:127], a[240:243], a[152:155], v[176:191]\n"
    "v_pk_maximum3_f16 v20, v88, v89, s68\n"
    "v_pk_maximum3_f16 v20, v90, v91, v20\n"
    "v_pk_maximum3_f16 v20, v92, v93, v20\n"
    "v_pk_maximum3_f16 v20, v94, v95, v20\n"
    "v_cmp_ne_u32 vcc, s68, v20\n"
    "v_exp_f32 v96, v96\n"
    "v_mfma_f32_32x32x16_bf16 v[112:127], a[244:247], a[156:159], v[112:127]\n"
    "s_cbranch_vccnz .Lat_rare10_%=\n"
    ".Lat_back10_%=:\n"
    "v_exp_f32 v97, v97\n"
    "v_exp_f32 v98, v98\n"
    "v_cvt_pk_bf16_f32 v80, v96, v97\n"
    "v_mfma_f32_32x32x16_bf16 a[16:31], a[248:251], v[88:91], a[16:31]\n"
    "v_exp_f32 v99, v99\n"
    "v_exp_f32 v100, v100\n"
    "v_cvt_pk_bf16_f32 v81, v98, v99\n"
    "v_exp_f32 v101, v101\n"
    "v_mfma_f32_32x32x16_bf16 a[16:31], a[252:255], v[92:95], a[16:31]\n"
    "v_exp_f32 v102, v102\n"
    "v_cvt_pk_bf16_f32 v82, v100, v101\n"
    "v_exp_f32 v103, v103\n"
    "v_exp_f32 v104, v104\n"
    "v_mfma_f32_16x16x32_bf16 a[196:199], v[76:79], v[88:91], a[196:199]\n"
    "v_cvt_pk_bf16_f32 v83, v102, v103\n"
    "v_exp_f32 v105, v105\n"
    "v_mfma_f32_16x16x32_bf16 a[196:199], v[76:79], v[92:95], a[196:199]\n"
    "v_exp_f32 v106, v106\n"
    "v_cvt_pk_bf16_f32 v84, v104, v105\n"
    "v_exp_f32 v107, v107\n"
    "v_exp_f32 v108, v108\n"
    "v_cvt_pk_bf16_f32 v85, v106, v107\n"
    "v_exp_f32 v109, v109\n"
    "v_exp_f32 v110, v110\n"
    "v_exp_f32 v111, v111\n"
    "v_cvt_pk_bf16_f32 v86, v108, v109\n"
    "v_cvt_pk_bf16_f32 v87, v110, v111\n"
    "v_mfma_f32_32x32x16_bf16 v[96:111], a[240:243], a[160:163], v[192:207]\n"
    "v_pk_maximum3_f16 v20, v80, v81, s68\n"
    "v_pk_maximum3_f16 v20, v82, v83, v20\n"
    "v_pk_maximum3_f16 v20, v84, v85, v20\n"
    "v_pk_maximum3_f16 v20, v86, v87, v20\n"
    "v_cmp_ne_u32 vcc, s68, v20\n"
    "v_exp_f32 v112, v112\n"
    "v_mfma_f32_32x32x16_bf16 v[96:111], a[244:247], a[164:167], v[96:111]\n"
    "s_cbranch_vccnz .Lat_rare11_%=\n"
    ".Lat_back11_%=:\n"
    "v_exp_f32 v113, v113\n"
    "v_exp_f32 v114, v114\n"
    "v_cvt_pk_bf16_f32 v88, v112, v113\n"
    "v_mfma_f32_32x32x16_bf16 a[32:47], a[248:251], v[80:83], a[32:47]\n"
    "v_exp_f32 v115, v115\n"
    "v_exp_f32 v116, v116\n"
    "v_cvt_pk_bf16_f32 v89, v114, v115\n"
    "v_exp_f32 v117, v117\n"
    "v_mfma_f32_32x32x16_bf16 a[32:47], a[252:255], v[84:87], a[32:47]\n"
    "v_exp_f32 v118, v118\n"
    "v_cvt_pk_bf16_f32 v90, v116, v117\n"
    "v_exp_f32 v119, v119\n"
    "v_exp_f32 v120, v120\n"
    "v_mfma_f32_16x16x32_bf16 a[200:203], v[76:79], v[80:83], a[200:203]\n"
    "v_cvt_pk_bf16_f32 v91, v118, v119\n"
    "v_exp_f32 v121, v121\n"
    "v_mfma_f32_16x16x32_bf16 a[200:203], v[76:79], v[84:87], a[200:203]\n"
    "v_exp_f32 v122, v122\n"
    "v_cvt_pk_bf16_f32 v92, v120, v121\n"
    "v_exp_f32 v123, v123\n"
    "v_exp_f32 v124, v124\n"
    "v_cvt_pk_bf16_f32 v93, v122, v123\n"
    "v_exp_f32 v125, v125\n"
    "v_exp_f32 v126, v126\n"
    "v_exp_f32 v127, v127\n"
    "v_cvt_pk_bf16_f32 v94, v124, v125\n"
    "v_cvt_pk_bf16_f32 v95, v126, v127\n"
    "v_mfma_f32_32x32x16_bf16 v[112:127], a[240:243], a[168:171], v[208:223]\n"
    "v_pk_maximum3_f16 v20, v88, v89, s68\n"
    "v_pk_maximum3_f16 v20, v90, v91, v20\n"
    "v_pk_maximum3_f16 v20, v92, v93, v20\n"
    "v_pk_maximum3_f16 v20, v94, v95, v20\n"
    "v_cmp_ne_u32 vcc, s68, v20\n"
    "v_exp_f32 v96, v96\n"
    "v_mfma_f32_32x32x16_bf16 v[112:127], a[244:247], a[172:175], v[112:127]\n"
    "s_cbranch_vccnz .Lat_rare12_%=\n"
    ".Lat_back12_%=:\n"
    "v_exp_f32 v97, v97\n"
    "v_exp_f32 v98, v98\n"
    "v_cvt_pk_bf16_f32 v80, v96, v97\n"
    "v_mfma_f32_32x32x16_bf16 a[48:63], a[248:251], v[88:91], a[48:63]\n"
    "v_exp_f32 v99, v99\n"
    "v_exp_f32 v100, v100\n"
    "v_cvt_pk_bf16_f32 v81, v98, v99\n"
    "v_exp_f32 v101, v101\n"
    "v_mfma_f32_32x32x16_bf16 a[48:63], a[252:255], v[92:95], a[48:63]\n"
    "v_exp_f32 v102, v102\n"
    "v_cvt_pk_bf16_f32 v82, v100, v101\n"
    "v_exp_f32 v103, v103\n"
    "v_exp_f32 v104, v104\n"
    "v_mfma_f32_16x16x32_bf16 a[204:207], v[76:79], v[88:91], a[204:207]\n"
    "v_cvt_pk_bf16_f32 v83, v102, v103\n"
    "v_exp_f32 v105, v105\n"
    "v_mfma_f32_16x16x32_bf16 a[204:207], v[76:79], v[92:95], a[204:207]\n"
    "v_exp_f32 v106, v106\n"
    "v_cvt_pk_bf16_f32 v84, v104, v105\n"
    "v_exp_f32 v107, v107\n"
    "v_exp_f32 v108, v108\n"
    "v_cvt_pk_bf16_f32 v85, v106, v107\n"
    "v_exp_f32 v109, v109\n"
    "v_exp_f32 v110, v110\n"
    "v_exp_f32 v111, v111\n"
    "v_cvt_pk_bf16_f32 v86, v108, v109\n"
    "v_cvt_pk_bf16_f32 v87, v110, v111\n"
    "v_mfma_f32_32x32x16_bf16 v[96:111], a[240:243], a[176:179], v[224:239]\n"
    "v_pk_maximum3_f16 v20, v80, v81, s68\n"
    "v_pk_maximum3_f16 v20, v82, v83, v20\n"
    "v_pk_maximum3_f16 v20, v84, v85, v20\n"
    "v_pk_maximum3_f16 v20, v86, v87, v20\n"
    "v_cmp_ne_u32 vcc, s68, v20\n"
    "v_exp_f32 v112, v112\n"
    "v_mfma_f32_32x32x16_bf16 v[96:111], a[244:247], a[180:183], v[96:111]\n"
    "s_cbranch_vccnz .Lat_rare13_%=\n"
    ".Lat_back13_%=:\n"
    "v_exp_f32 v113, v113\n"
    "v_exp_f32 v114, v114\n"
    "v_cvt_pk_bf16_f32 v88, v112, v113\n"
    "v_mfma_f32_32x32x16_bf16 a[64:79], a[248:251], v[80:83], a[64:79]\n"
    "v_exp_f32 v115, v115\n"
    "v_exp_f32 v116, v116\n"
    "v_cvt_pk_bf16_f32 v89, v114, v115\n"
    "v_exp_f32 v117, v117\n"
    "v_mfma_f32_32x32x16_bf16 a[64:79], a[252:255], v[84:87], a[64:79]\n"
    "v_exp_f32 v118, v118\n"
    "v_cvt_pk_bf16_f32 v90, v116, v117\n"
    "v_exp_f32 v119, v119\n"
    "v_exp_f32 v120, v120\n"
    "v_mfma_f32_16x16x32_bf16 a[208:211], v[76:79], v[80:83], a[208:211]\n"
    "v_cvt_pk_bf16_f32 v91, v118, v119\n"
    "v_exp_f32 v121, v121\n"
    "v_mfma_f32_16x16x32_bf16 a[208:211], v[76:79], v[84:87], a[208:211]\n"
    "v_exp_f32 v122, v122\n"
    "v_cvt_pk_bf16_f32 v92, v120, v121\n"
    "v_exp_f32 v123, v123\n"
    "v_exp_f32 v124, v124\n"
    "v_cvt_pk_bf16_f32 v93, v122, v123\n"
    "v_exp_f32 v125, v125\n"
    "v_exp_f32 v126, v126\n"
    "v_exp_f32 v127, v127\n"
    "v_cvt_pk_bf16_f32 v94, v124, v125\n"
    "v_cvt_pk_bf16_f32 v95, v126, v127\n"
    "v_mfma_f32_32x32x16_bf16 v[112:127], a[240:243], a[184:187], v[240:255]\n"
    "v_pk_maximum3_f16 v20, v88, v89, s68\n"
    "v_pk_maximum3_f16 v20, v90, v91, v20\n"
    "v_pk_maximum3_f16 v20, v92, v93, v20\n"
    "v_pk_maximum3_f16 v20, v94, v95, v20\n"
    "v_cmp_ne_u32 vcc, s68, v20\n"
    "v_exp_f32 v96, v96\n"
    "v_mfma_f32_32x32x16_bf16 v[112:127], a[244:247], a[188:191], v[112:127]\n"
    "s_cbranch_vccnz .Lat_rare14_%=\n"
    ".Lat_back14_%=:\n"
    "v_exp_f32 v97, v97\n"
    "v_exp_f32 v98, v98\n"
    "v_cvt_pk_bf16_f32 v80, v96, v97\n"
    "v_mfma_f32_32x32x16_bf16 a[80:95], a[248:251], v[88:91], a[80:95]\n"
    "v_exp_f32 v99, v99\n"
    "v_exp_f32 v100, v100\n"
    "v_cvt_pk_bf16_f32 v81, v98, v99\n"
    "v_exp_f32 v101, v101\n"
    "v_mfma_f32_32x32x16_bf16 a[80:95], a[252:255], v[92:95], a[80:95]\n"
    "v_exp_f32 v102, v102\n"
    "v_cvt_pk_bf16_f32 v82, v100, v101\n"
    "v_exp_f32 v103, v103\n"
    "v_exp_f32 v104, v104\n"
    "v_mfma_f32_16x16x32_bf16 a[212:215], v[76:79], v[88:91], a[212:215]\n"
    "v_cvt_pk_bf16_f32 v83, v102, v103\n"
    "v_exp_f32 v105, v105\n"
    "v_mfma_f32_16x16x32_bf16 a[212:215], v[76:79], v[92:95], a[212:215]\n"
    "v_exp_f32 v106, v106\n"
    "v_cvt_pk_bf16_f32 v84, v104, v105\n"
    "v_exp_f32 v107, v107\n"
    "v_exp_f32 v108, v108\n"
    "v_cvt_pk_bf16_f32 v85, v106, v107\n"
    "v_exp_f32 v109, v109\n"
    "v_exp_f32 v110, v110\n"
    "v_exp_f32 v111, v111\n"
    "v_cvt_pk_bf16_f32 v86, v108, v109\n"
    "s_waitcnt vmcnt(0)\n"
    "v_cvt_pk_bf16_f32 v87, v110, v111\n"
    "v_mfma_f32_32x32x16_bf16 v[96:111], a[224:227], a[128:131], v[128:143]\n"
    "v_pk_maximum3_f16 v20, v80, v81, s68\n"
    "v_pk_maximum3_f16 v20, v82, v83, v20\n"
    "v_pk_maximum3_f16 v20, v84, v85, v20\n"
    "v_pk_maximum3_f16 v20, v86, v87, v20\n"
    "v_cmp_ne_u32 vcc, s68, v20\n"
    "v_exp_f32 v112, v112\n"
    "v_mfma_f32_32x32x16_bf16 v[96:111], a[228:231], a[132:135], v[96:111]\n"
    "s_cbranch_vccnz .Lat_rare15_%=\n"
    ".Lat_back15_%=:\n"
    "v_exp_f32 v113, v113\n"
    "v_exp_f32 v114, v114\n"
    "v_cvt_pk_bf16_f32 v88, v112, v113\n"
    "v_mfma_f32_32x32x16_bf16 a[96:111], a[248:251], v[80:83], a[96:111]\n"
    "v_exp_f32 v115, v115\n"
    "v_exp_f32 v116, v116\n"
    "v_cvt_pk_bf16_f32 v89, v114, v115\n"
    "v_exp_f32 v117, v117\n"
    "v_mfma_f32_32x32x16_bf16 a[96:111], a[252:255], v[84:87], a[96:111]\n"
    "v_exp_f32 v118, v118\n"
    "v_cvt_pk_bf16_f32 v90, v116, v117\n"
    "v_exp_f32 v119, v119\n"
    "v_exp_f32 v120, v120\n"
    "v_mfma_f32_16x16x32_bf16 a[216:219], v[76:79], v[80:83], a[216:219]\n"
    "v_cvt_pk_bf16_f32 v91, v118, v119\n"
    "v_exp_f32 v121, v121\n"
    "v_mfma_f32_16x16x32_bf16 a[216:219], v[76:79], v[84:87], a[216:219]\n"
    "s_cmp_lt_u32 s67, 266240\n"
    "v_exp_f32 v122, v122\n"
    "v_cvt_pk_bf16_f32 v92, v120, v121\n"
    "v_exp_f32 v123, v123\n"
    "v_exp_f32 v124, v124\n"
    "v_cvt_pk_bf16_f32 v93, v122, v123\n"
    "v_exp_f32 v125, v125\n"
    "v_exp_f32 v126, v126\n"
    "v_exp_f32 v127, v127\n"
    "v_cvt_pk_bf16_f32 v94, v124, v125\n"
    "s_cbranch_scc1 .Lat_att_%=\n"
    "v_cvt_pk_bf16_f32 v95, v126, v127\n"
    "v_pk_maximum3_f16 v20, v88, v89, s68\n"
    "v_pk_maximum3_f16 v20, v90, v91, v20\n"
    "v_pk_maximum3_f16 v20, v92, v93, v20\n"
    "v_pk_maximum3_f16 v20, v94, v95, v20\n"
    "v_cmp_ne_u32 vcc, s68, v20\n"
    "s_cbranch_vccnz .Lat_rare16_%=\n"
    ".Lat_back16_%=:\n"
    "s_nop 1\n"
    "v_mfma_f32_32x32x16_bf16 a[112:127], a[248:251], v[88:91], a[112:127]\n"
    "v_mfma_f32_32x32x16_bf16 a[112:127], a[252:255], v[92:95], a[112:127]\n"
    "v_mfma_f32_16x16x32_bf16 a[220:223], v[76:79], v[88:91], a[220:223]\n"
    "v_mfma_f32_16x16x32_bf16 a[220:223], v[76:79], v[92:95], a[220:223]\n"
    ".Lat_attend_%=:\n"
    "s_add_u32 s40, s30, 70254592\n"
    "s_addc_u32 s41, s31, 0\n"
    "s_and_b32 s41, s41, 0xffff\n"
    "s_mov_b32 s42, 4194304\n"
    "s_mov_b32 s43, 0x20000\n"
    "s_lshl_b32 s66, s60, 16\n"
    "s_lshl_b32 s56, s59, 14\n"
    "s_add_u32 s66, s66, s56\n"
    "v_accvgpr_read_b32 v22, a192\n"
    "v_rcp_f32 v22, v22\n"
    "v_accvgpr_read_b32 v128, a0\n"
    "v_accvgpr_read_b32 v129, a1\n"
    "v_accvgpr_read_b32 v130, a2\n"
    "v_accvgpr_read_b32 v131, a3\n"
    "v_accvgpr_read_b32 v132, a4\n"
    "v_accvgpr_read_b32 v133, a5\n"
    "v_accvgpr_read_b32 v134, a6\n"
    "v_accvgpr_read_b32 v135, a7\n"
    "v_accvgpr_read_b32 v136, a8\n"
    "v_accvgpr_read_b32 v137, a9\n"
    "v_accvgpr_read_b32 v138, a10\n"
    "v_accvgpr_read_b32 v139, a11\n"
    "v_accvgpr_read_b32 v140, a12\n"
    "v_accvgpr_read_b32 v141, a13\n"
    "v_accvgpr_read_b32 v142, a14\n"
    "v_accvgpr_read_b32 v143, a15\n"
    "v_pk_mul_f32 v[128:129], v[128:129], v[22:23] op_sel_hi:[1,0]\n"
    "v_pk_mul_f32 v[130:131], v[130:131], v[22:23] op_sel_hi:[1,0]\n"
    "v_pk_mul_f32 v[132:133], v[132:133], v[22:23] op_sel_hi:[1,0]\n"
    "v_pk_mul_f32 v[134:135], v[134:135], v[22:23] op_sel_hi:[1,0]\n"
    "v_pk_mul_f32 v[136:137], v[136:137], v[22:23] op_sel_hi:[1,0]\n"
    "v_pk_mul_f32 v[138:139], v[138:139], v[22:23] op_sel_hi:[1,0]\n"
    "v_pk_mul_f32 v[140:141], v[140:141], v[22:23] op_sel_hi:[1,0]\n"
    "v_pk_mul_f32 v[142:143], v[142:143], v[22:23] op_sel_hi:[1,0]\n"
    "v_cvt_pk_bf16_f32 v128, v128, v129\n"
    "v_cvt_pk_bf16_f32 v129, v130, v131\n"
    "v_cvt_pk_bf16_f32 v130, v132, v133\n"
    "v_cvt_pk_bf16_f32 v131, v134, v135\n"
    "v_cvt_pk_bf16_f32 v132, v136, v137\n"
    "v_cvt_pk_bf16_f32 v133, v138, v139\n"
    "v_cvt_pk_bf16_f32 v134, v140, v141\n"
    "v_cvt_pk_bf16_f32 v135, v142, v143\n"
    "s_add_u32 s56, s66, 0\n"
    "buffer_store_dwordx4 v[128:131], v2, s[40:43], s56 offen offset:0 sc1\n"
    "buffer_store_dwordx4 v[132:135], v2, s[40:43], s56 offen offset:1024 sc1\n"
    "v_accvgpr_read_b32 v22, a196\n"
    "v_rcp_f32 v22, v22\n"
    "v_accvgpr_read_b32 v144, a16\n"
    "v_accvgpr_read_b32 v145, a17\n"
    "v_accvgpr_read_b32 v146, a18\n"
    "v_accvgpr_read_b32 v147, a19\n"
    "v_accvgpr_read_b32 v148, a20\n"
    "v_accvgpr_read_b32 v149, a21\n"
    "v_accvgpr_read_b32 v150, a22\n"
    "v_accvgpr_read_b32 v151, a23\n"
    "v_accvgpr_read_b32 v152, a24\n"
    "v_accvgpr_read_b32 v153, a25\n"
    "v_accvgpr_read_b32 v154, a26\n"
    "v_accvgpr_read_b32 v155, a27\n"
    "v_accvgpr_read_b32 v156, a28\n"
    "v_accvgpr_read_b32 v157, a29\n"
    "v_accvgpr_read_b32 v158, a30\n"
    "v_accvgpr_read_b32 v159, a31\n"
    "v_pk_mul_f32 v[144:145], v[144:145], v[22:23] op_sel_hi:[1,0]\n"
    "v_pk_mul_f32 v[146:147], v[146:147], v[22:23] op_sel_hi:[1,0]\n"
    "v_pk_mul_f32 v[148:149], v[148:149], v[22:23] op_sel_hi:[1,0]\n"
    "v_pk_mul_f32 v[150:151], v[150:151], v[22:23] op_sel_hi:[1,0]\n"
    "v_pk_mul_f32 v[152:153], v[152:153], v[22:23] op_sel_hi:[1,0]\n"
    "v_pk_mul_f32 v[154:155], v[154:155], v[22:23] op_sel_hi:[1,0]\n"
    "v_pk_mul_f32 v[156:157], v[156:157], v[22:23] op_sel_hi:[1,0]\n"
    "v_pk_mul_f32 v[158:159], v[158:159], v[22:23] op_sel_hi:[1,0]\n"
    "v_cvt_pk_bf16_f32 v144, v144, v145\n"
    "v_cvt_pk_bf16_f32 v145, v146, v147\n"
    "v_cvt_pk_bf16_f32 v146, v148, v149\n"
    "v_cvt_pk_bf16_f32 v147, v150, v151\n"
    "v_cvt_pk_bf16_f32 v148, v152, v153\n"
    "v_cvt_pk_bf16_f32 v149, v154, v155\n"
    "v_cvt_pk_bf16_f32 v150, v156, v157\n"
    "v_cvt_pk_bf16_f32 v151, v158, v159\n"
    "buffer_store_dwordx4 v[144:147], v2, s[40:43], s56 offen offset:2048 sc1\n"
    "buffer_store_dwordx4 v[148:151], v2, s[40:43], s56 offen offset:3072 sc1\n"
    "v_accvgpr_read_b32 v22, a200\n"
    "v_rcp_f32 v22, v22\n"
    "v_accvgpr_read_b32 v160, a32\n"
    "v_accvgpr_read_b32 v161, a33\n"
    "v_accvgpr_read_b32 v162, a34\n"
    "v_accvgpr_read_b32 v163, a35\n"
    "v_accvgpr_read_b32 v164, a36\n"
    "v_accvgpr_read_b32 v165, a37\n"
    "v_accvgpr_read_b32 v166, a38\n"
    "v_accvgpr_read_b32 v167, a39\n"
    "v_accvgpr_read_b32 v168, a40\n"
    "v_accvgpr_read_b32 v169, a41\n"
    "v_accvgpr_read_b32 v170, a42\n"
    "v_accvgpr_read_b32 v171, a43\n"
    "v_accvgpr_read_b32 v172, a44\n"
    "v_accvgpr_read_b32 v173, a45\n"
    "v_accvgpr_read_b32 v174, a46\n"
    "v_accvgpr_read_b32 v175, a47\n"
    "v_pk_mul_f32 v[160:161], v[160:161], v[22:23] op_sel_hi:[1,0]\n"
    "v_pk_mul_f32 v[162:163], v[162:163], v[22:23] op_sel_hi:[1,0]\n"
    "v_pk_mul_f32 v[164:165], v[164:165], v[22:23] op_sel_hi:[1,0]\n"
    "v_pk_mul_f32 v[166:167], v[166:167], v[22:23] op_sel_hi:[1,0]\n"
    "v_pk_mul_f32 v[168:169], v[168:169], v[22:23] op_sel_hi:[1,0]\n"
    "v_pk_mul_f32 v[170:171], v[170:171], v[22:23] op_sel_hi:[1,0]\n"
    "v_pk_mul_f32 v[172:173], v[172:173], v[22:23] op_sel_hi:[1,0]\n"
    "v_pk_mul_f32 v[174:175], v[174:175], v[22:23] op_sel_hi:[1,0]\n"
    "v_cvt_pk_bf16_f32 v160, v160, v161\n"
    "v_cvt_pk_bf16_f32 v161, v162, v163\n"
    "v_cvt_pk_bf16_f32 v162, v164, v165\n"
    "v_cvt_pk_bf16_f32 v163, v166, v167\n"
    "v_cvt_pk_bf16_f32 v164, v168, v169\n"
    "v_cvt_pk_bf16_f32 v165, v170, v171\n"
    "v_cvt_pk_bf16_f32 v166, v172, v173\n"
    "v_cvt_pk_bf16_f32 v167, v174, v175\n"
    "s_add_u32 s56, s66, 4096\n"
    "buffer_store_dwordx4 v[160:163], v2, s[40:43], s56 offen offset:0 sc1\n"
    "buffer_store_dwordx4 v[164:167], v2, s[40:43], s56 offen offset:1024 sc1\n"
    "v_accvgpr_read_b32 v22, a204\n"
    "v_rcp_f32 v22, v22\n"
    "v_accvgpr_read_b32 v176, a48\n"
    "v_accvgpr_read_b32 v177, a49\n"
    "v_accvgpr_read_b32 v178, a50\n"
    "v_accvgpr_read_b32 v179, a51\n"
    "v_accvgpr_read_b32 v180, a52\n"
    "v_accvgpr_read_b32 v181, a53\n"
    "v_accvgpr_read_b32 v182, a54\n"
    "v_accvgpr_read_b32 v183, a55\n"
    "v_accvgpr_read_b32 v184, a56\n"
    "v_accvgpr_read_b32 v185, a57\n"
    "v_accvgpr_read_b32 v186, a58\n"
    "v_accvgpr_read_b32 v187, a59\n"
    "v_accvgpr_read_b32 v188, a60\n"
    "v_accvgpr_read_b32 v189, a61\n"
    "v_accvgpr_read_b32 v190, a62\n"
    "v_accvgpr_read_b32 v191, a63\n"
    "v_pk_mul_f32 v[176:177], v[176:177], v[22:23] op_sel_hi:[1,0]\n"
    "v_pk_mul_f32 v[178:179], v[178:179], v[22:23] op_sel_hi:[1,0]\n"
    "v_pk_mul_f32 v[180:181], v[180:181], v[22:23] op_sel_hi:[1,0]\n"
    "v_pk_mul_f32 v[182:183], v[182:183], v[22:23] op_sel_hi:[1,0]\n"
    "v_pk_mul_f32 v[184:185], v[184:185], v[22:23] op_sel_hi:[1,0]\n"
    "v_pk_mul_f32 v[186:187], v[186:187], v[22:23] op_sel_hi:[1,0]\n"
    "v_pk_mul_f32 v[188:189], v[188:189], v[22:23] op_sel_hi:[1,0]\n"
    "v_pk_mul_f32 v[190:191], v[190:191], v[22:23] op_sel_hi:[1,0]\n"
    "v_cvt_pk_bf16_f32 v176, v176, v177\n"
    "v_cvt_pk_bf16_f32 v177, v178, v179\n"
    "v_cvt_pk_bf16_f32 v178, v180, v181\n"
    "v_cvt_pk_bf16_f32 v179, v182, v183\n"
    "v_cvt_pk_bf16_f32 v180, v184, v185\n"
    "v_cvt_pk_bf16_f32 v181, v186, v187\n"
    "v_cvt_pk_bf16_f32 v182, v188, v189\n"
    "v_cvt_pk_bf16_f32 v183, v190, v191\n"
    "buffer_store_dwordx4 v[176:179], v2, s[40:43], s56 offen offset:2048 sc1\n"
    "buffer_store_dwordx4 v[180:183], v2, s[40:43], s56 offen offset:3072 sc1\n"
    "v_accvgpr_read_b32 v22, a208\n"
    "v_rcp_f32 v22, v22\n"
    "v_accvgpr_read_b32 v192, a64\n"
    "v_accvgpr_read_b32 v193, a65\n"
    "v_accvgpr_read_b32 v194, a66\n"
    "v_accvgpr_read_b32 v195, a67\n"
    "v_accvgpr_read_b32 v196, a68\n"
    "v_accvgpr_read_b32 v197, a69\n"
    "v_accvgpr_read_b32 v198, a70\n"
    "v_accvgpr_read_b32 v199, a71\n"
    "v_accvgpr_read_b32 v200, a72\n"
    "v_accvgpr_read_b32 v201, a73\n"
    "v_accvgpr_read_b32 v202, a74\n"
    "v_accvgpr_read_b32 v203, a75\n"
    "v_accvgpr_read_b32 v204, a76\n"
    "v_accvgpr_read_b32 v205, a77\n"
    "v_accvgpr_read_b32 v206, a78\n"
    "v_accvgpr_read_b32 v207, a79\n"
    "v_pk_mul_f32 v[192:193], v[192:193], v[22:23] op_sel_hi:[1,0]\n"
    "v_pk_mul_f32 v[194:195], v[194:195], v[22:23] op_sel_hi:[1,0]\n"
    "v_pk_mul_f32 v[196:197], v[196:197], v[22:23] op_sel_hi:[1,0]\n"
    "v_pk_mul_f32 v[198:199], v[198:199], v[22:23] op_sel_hi:[1,0]\n"
    "v_pk_mul_f32 v[200:201], v[200:201], v[22:23] op_sel_hi:[1,0]\n"
    "v_pk_mul_f32 v[202:203], v[202:203], v[22:23] op_sel_hi:[1,0]\n"
    "v_pk_mul_f32 v[204:205], v[204:205], v[22:23] op_sel_hi:[1,0]\n"
    "v_pk_mul_f32 v[206:207], v[206:207], v[22:23] op_sel_hi:[1,0]\n"
    "v_cvt_pk_bf16_f32 v192, v192, v193\n"
    "v_cvt_pk_bf16_f32 v193, v194, v195\n"
    "v_cvt_pk_bf16_f32 v194, v196, v197\n"
    "v_cvt_pk_bf16_f32 v195, v198, v199\n"
    "v_cvt_pk_bf16_f32 v196, v200, v201\n"
    "v_cvt_pk_bf16_f32 v197, v202, v203\n"
    "v_cvt_pk_bf16_f32 v198, v204, v205\n"
    "v_cvt_pk_bf16_f32 v199, v206, v207\n"
    "s_add_u32 s56, s66, 8192\n"
    "buffer_store_dwordx4 v[192:195], v2, s[40:43], s56 offen offset:0 sc1\n"
    "buffer_store_dwordx4 v[196:199], v2, s[40:43], s56 offen offset:1024 sc1\n"
    "v_accvgpr_read_b32 v22, a212\n"
    "v_rcp_f32 v22, v22\n"
    "v_accvgpr_read_b32 v208, a80\n"
    "v_accvgpr_read_b32 v209, a81\n"
    "v_accvgpr_read_b32 v210, a82\n"
    "v_accvgpr_read_b32 v211, a83\n"
    "v_accvgpr_read_b32 v212, a84\n"
    "v_accvgpr_read_b32 v213, a85\n"
    "v_accvgpr_read_b32 v214, a86\n"
    "v_accvgpr_read_b32 v215, a87\n"
    "v_accvgpr_read_b32 v216, a88\n"
    "v_accvgpr_read_b32 v217, a89\n"
    "v_accvgpr_read_b32 v218, a90\n"
    "v_accvgpr_read_b32 v219, a91\n"
    "v_accvgpr_read_b32 v220, a92\n"
    "v_accvgpr_read_b32 v221, a93\n"
    "v_accvgpr_read_b32 v222, a94\n"
    "v_accvgpr_read_b32 v223, a95\n"
    "v_pk_mul_f32 v[208:209], v[208:209], v[22:23] op_sel_hi:[1,0]\n"
    "v_pk_mul_f32 v[210:211], v[210:211], v[22:23] op_sel_hi:[1,0]\n"
    "v_pk_mul_f32 v[212:213], v[212:213], v[22:23] op_sel_hi:[1,0]\n"
    "v_pk_mul_f32 v[214:215], v[214:215], v[22:23] op_sel_hi:[1,0]\n"
    "v_pk_mul_f32 v[216:217], v[216:217], v[22:23] op_sel_hi:[1,0]\n"
    "v_pk_mul_f32 v[218:219], v[218:219], v[22:23] op_sel_hi:[1,0]\n"
    "v_pk_mul_f32 v[220:221], v[220:221], v[22:23] op_sel_hi:[1,0]\n"
    "v_pk_mul_f32 v[222:223], v[222:223], v[22:23] op_sel_hi:[1,0]\n"
    "v_cvt_pk_bf16_f32 v208, v208, v209\n"
    "v_cvt_pk_bf16_f32 v209, v210, v211\n"
    "v_cvt_pk_bf16_f32 v210, v212, v213\n"
    "v_cvt_pk_bf16_f32 v211, v214, v215\n"
    "v_cvt_pk_bf16_f32 v212, v216, v217\n"
    "v_cvt_pk_bf16_f32 v213, v218, v219\n"
    "v_cvt_pk_bf16_f32 v214, v220, v221\n"
    "v_cvt_pk_bf16_f32 v215, v222, v223\n"
    "buffer_store_dwordx4 v[208:211], v2, s[40:43], s56 offen offset:2048 sc1\n"
    "buffer_store_dwordx4 v[212:215], v2, s[40:43], s56 offen offset:3072 sc1\n"
    "v_accvgpr_read_b32 v22, a216\n"
    "v_rcp_f32 v22, v22\n"
    "v_accvgpr_read_b32 v224, a96\n"
    "v_accvgpr_read_b32 v225, a97\n"
    "v_accvgpr_read_b32 v226, a98\n"
    "v_accvgpr_read_b32 v227, a99\n"
    "v_accvgpr_read_b32 v228, a100\n"
    "v_accvgpr_read_b32 v229, a101\n"
    "v_accvgpr_read_b32 v230, a102\n"
    "v_accvgpr_read_b32 v231, a103\n"
    "v_accvgpr_read_b32 v232, a104\n"
    "v_accvgpr_read_b32 v233, a105\n"
    "v_accvgpr_read_b32 v234, a106\n"
    "v_accvgpr_read_b32 v235, a107\n"
    "v_accvgpr_read_b32 v236, a108\n"
    "v_accvgpr_read_b32 v237, a109\n"
    "v_accvgpr_read_b32 v238, a110\n"
    "v_accvgpr_read_b32 v239, a111\n"
    "v_pk_mul_f32 v[224:225], v[224:225], v[22:23] op_sel_hi:[1,0]\n"
    "v_pk_mul_f32 v[226:227], v[226:227], v[22:23] op_sel_hi:[1,0]\n"
    "v_pk_mul_f32 v[228:229], v[228:229], v[22:23] op_sel_hi:[1,0]\n"
    "v_pk_mul_f32 v[230:231], v[230:231], v[22:23] op_sel_hi:[1,0]\n"
    "v_pk_mul_f32 v[232:233], v[232:233], v[22:23] op_sel_hi:[1,0]\n"
    "v_pk_mul_f32 v[234:235], v[234:235], v[22:23] op_sel_hi:[1,0]\n"
    "v_pk_mul_f32 v[236:237], v[236:237], v[22:23] op_sel_hi:[1,0]\n"
    "v_pk_mul_f32 v[238:239], v[238:239], v[22:23] op_sel_hi:[1,0]\n"
    "v_cvt_pk_bf16_f32 v224, v224, v225\n"
    "v_cvt_pk_bf16_f32 v225, v226, v227\n"
    "v_cvt_pk_bf16_f32 v226, v228, v229\n"
    "v_cvt_pk_bf16_f32 v227, v230, v231\n"
    "v_cvt_pk_bf16_f32 v228, v232, v233\n"
    "v_cvt_pk_bf16_f32 v229, v234, v235\n"
    "v_cvt_pk_bf16_f32 v230, v236, v237\n"
    "v_cvt_pk_bf16_f32 v231, v238, v239\n"
    "s_add_u32 s56, s66, 12288\n"
    "buffer_store_dwordx4 v[224:227], v2, s[40:43], s56 offen offset:0 sc1\n"
    "buffer_store_dwordx4 v[228:231], v2, s[40:43], s56 offen offset:1024 sc1\n"
    "v_accvgpr_read_b32 v22, a220\n"
    "v_rcp_f32 v22, v22\n"
    "v_accvgpr_read_b32 v240, a112\n"
    "v_accvgpr_read_b32 v241, a113\n"
    "v_accvgpr_read_b32 v242, a114\n"
    "v_accvgpr_read_b32 v243, a115\n"
    "v_accvgpr_read_b32 v244, a116\n"
    "v_accvgpr_read_b32 v245, a117\n"
    "v_accvgpr_read_b32 v246, a118\n"
    "v_accvgpr_read_b32 v247, a119\n"
    "v_accvgpr_read_b32 v248, a120\n"
    "v_accvgpr_read_b32 v249, a121\n"
    "v_accvgpr_read_b32 v250, a122\n"
    "v_accvgpr_read_b32 v251, a123\n"
    "v_accvgpr_read_b32 v252, a124\n"
    "v_accvgpr_read_b32 v253, a125\n"
    "v_accvgpr_read_b32 v254, a126\n"
    "v_accvgpr_read_b32 v255, a127\n"
    "v_pk_mul_f32 v[240:241], v[240:241], v[22:23] op_sel_hi:[1,0]\n"
    "v_pk_mul_f32 v[242:243], v[242:243], v[22:23] op_sel_hi:[1,0]\n"
    "v_pk_mul_f32 v[244:245], v[244:245], v[22:23] op_sel_hi:[1,0]\n"
    "v_pk_mul_f32 v[246:247], v[246:247], v[22:23] op_sel_hi:[1,0]\n"
    "v_pk_mul_f32 v[248:249], v[248:249], v[22:23] op_sel_hi:[1,0]\n"
    "v_pk_mul_f32 v[250:251], v[250:251], v[22:23] op_sel_hi:[1,0]\n"
    "v_pk_mul_f32 v[252:253], v[252:253], v[22:23] op_sel_hi:[1,0]\n"
    "v_pk_mul_f32 v[254:255], v[254:255], v[22:23] op_sel_hi:[1,0]\n"
    "v_cvt_pk_bf16_f32 v240, v240, v241\n"
    "v_cvt_pk_bf16_f32 v241, v242, v243\n"
    "v_cvt_pk_bf16_f32 v242, v244, v245\n"
    "v_cvt_pk_bf16_f32 v243, v246, v247\n"
    "v_cvt_pk_bf16_f32 v244, v248, v249\n"
    "v_cvt_pk_bf16_f32 v245, v250, v251\n"
    "v_cvt_pk_bf16_f32 v246, v252, v253\n"
    "v_cvt_pk_bf16_f32 v247, v254, v255\n"
    "buffer_store_dwordx4 v[240:243], v2, s[40:43], s56 offen offset:2048 sc1\n"
    "buffer_store_dwordx4 v[244:247], v2, s[40:43], s56 offen offset:3072 sc1\n"
    "s_lshl_b32 s67, s60, 4\n"
    "s_xor_b32 s56, s27, 9216\n"
    "s_add_u32 s67, s67, s56\n"
    "s_lshl_b32 s56, s59, 2\n"
    "s_add_u32 s56, s56, s67\n"
    "v_mov_b32 v19, 1\n"
    "s_waitcnt vmcnt(0)\n"
    "s_mov_b64 exec, 1\n"
    "buffer_store_dword v19, off, s[48:51], s56 sc1\n"
    "s_mov_b64 exec, -1\n"
    "s_cmp_ge_u32 s24, 32\n"
    "s_cbranch_scc1 .Lat_adone_%=\n"
    "s_mov_b32 s32, s80\n"
    "s_and_b32 s33, s81, 0xffff\n"
    "s_mov_b32 s34, 2097152\n"
    "s_mov_b32 s35, 0x20000\n"
    "v_mov_b32 v4, v2\n"
    "v_bfe_u32 v6, v1, 5, 1\n"
    "v_lshlrev_b32 v6, 11, v6\n"
    "v_bfe_u32 v19, v1, 2, 2\n"
    "v_lshl_add_u32 v6, v19, 9, v6\n"
    "v_bfe_u32 v19, v1, 2, 2\n"
    "v_lshl_add_u32 v6, v19, 6, v6\n"
    "v_bfe_u32 v19, v1, 4, 1\n"
    "v_lshl_add_u32 v6, v19, 5, v6\n"
    "v_bfe_u32 v19, v1, 0, 2\n"
    "v_lshl_add_u32 v6, v19, 3, v6\n"
    "s_lshl_b32 s56, s25, 12\n"
    "v_add_u32 v8, s56, v2\n"
    "s_xor_b32 s56, s67, 64\n"
    ".Lat_opoll_%=:\n"
    "buffer_load_dwordx4 v[20:23], off, s[48:51], s67 sc1\n"
    "buffer_load_dwordx4 v[24:27], off, s[48:51], s56 sc1\n"
    "s_waitcnt vmcnt(0)\n"
    "v_min3_u32 v20, v20, v21, v22\n"
    "v_min3_u32 v20, v20, v23, v24\n"
    "v_min3_u32 v20, v20, v25, v26\n"
    "v_min_u32 v20, v20, v27\n"
    "v_cmp_eq_u32 vcc, 0, v20\n"
    "s_cbranch_vccz .Lat_ook_%=\n"
    "s_sleep 4\n"
    "s_sub_u32 s26, s26, 1\n"
    "s_cbranch_scc0 .Lat_opoll_%=\n"
    "s_endpgm\n"
    ".Lat_ook_%=:\n"
    "s_barrier\n"
    "s_mov_b32 s64, 0\n"
    "s_lshl_b32 s65, s25, 10\n"
    "s_lshl_b32 s66, s60, 16\n"
    "s_xor_b32 s71, s66, 262144\n"
    "s_lshl_b32 s63, s25, 10\n"
    "s_lshl_b32 s70, s25, 12\n"
    "s_mov_b32 s58, 36864\n"
    "s_mov_b32 s62, 0\n"
    "s_mov_b32 s61, 0\n"
    "s_add_u32 m0, s63, 0\n"
    "s_add_u32 s56, s64, 0\n"
    "buffer_load_dwordx4 v3, s[32:35], s56 offen lds\n"
    "s_add_u32 m0, s70, 20480\n"
    "s_add_u32 s56, s66, 0\n"
    "buffer_load_dwordx4 v4, s[40:43], s56 offen sc1 lds\n"
    "s_add_u32 m0, s63, 4096\n"
    "s_add_u32 s56, s64, 16384\n"
    "buffer_load_dwordx4 v3, s[32:35], s56 offen lds\n"
    "s_add_u32 m0, s70, 21504\n"
    "s_add_u32 s56, s66, 1024\n"
    "buffer_load_dwordx4 v4, s[40:43], s56 offen sc1 lds\n"
    "s_add_u32 m0, s63, 8192\n"
    "s_add_u32 s56, s64, 32768\n"
    "buffer_load_dwordx4 v3, s[32:35], s56 offen lds\n"
    "s_add_u32 m0, s70, 22528\n"
    "s_add_u32 s56, s71, 0\n"
    "buffer_load_dwordx4 v4, s[40:43], s56 offen sc1 lds\n"
    "s_add_u32 m0, s63, 12288\n"
    "s_add_u32 s56, s64, 49152\n"
    "buffer_load_dwordx4 v3, s[32:35], s56 offen lds\n"
    "s_add_u32 m0, s70, 23552\n"
    "s_add_u32 s56, s71, 1024\n"
    "buffer_load_dwordx4 v4, s[40:43], s56 offen sc1 lds\n"
    "s_add_u32 s64, s64, 65536\n"
    "s_add_u32 s65, s65, 2048\n"
    "s_add_u32 s66, s66, 2048\n"
    "s_add_u32 s71, s71, 2048\n"
    "s_cmp_ge_u32 s63, 110592\n"
    "s_cselect_b32 s56, 4294856704, s58\n"
    "s_add_u32 s63, s63, s56\n"
    "s_add_u32 s70, s70, s56\n"
    "s_add_u32 m0, s63, 0\n"
    "s_add_u32 s56, s64, 0\n"
    "buffer_load_dwordx4 v3, s[32:35], s56 offen lds\n"
    "s_add_u32 m0, s70, 20480\n"
    "s_add_u32 s56, s66, 0\n"
    "buffer_load_dwordx4 v4, s[40:43], s56 offen sc1 lds\n"
    "s_add_u32 m0, s63, 4096\n"
    "s_add_u32 s56, s64, 16384\n"
    "buffer_load_dwordx4 v3, s[32:35], s56 offen lds\n"
    "s_add_u32 m0, s70, 21504\n"
    "s_add_u32 s56, s66, 1024\n"
    "buffer_load_dwordx4 v4, s[40:43], s56 offen sc1 lds\n"
    "s_add_u32 m0, s63, 8192\n"
    "s_add_u32 s56, s64, 32768\n"
    "buffer_load_dwordx4 v3, s[32:35], s56 offen lds\n"
    "s_add_u32 m0, s70, 22528\n"
    "s_add_u32 s56, s71, 0\n"
    "buffer_load_dwordx4 v4, s[40:43], s56 offen sc1 lds\n"
    "s_add_u32 m0, s63, 12288\n"
    "s_add_u32 s56, s64, 49152\n"
    "buffer_load_dwordx4 v3, s[32:35], s56 offen lds\n"
    "s_add_u32 m0, s70, 23552\n"
    "s_add_u32 s56, s71, 1024\n"
    "buffer_load_dwordx4 v4, s[40:43], s56 offen sc1 lds\n"
    "s_add_u32 s64, s64, 65536\n"
    "s_add_u32 s65, s65, 2048\n"
    "s_add_u32 s66, s66, 2048\n"
    "s_add_u32 s71, s71, 2048\n"
    "s_cmp_ge_u32 s63, 110592\n"
    "s_cselect_b32 s56, 4294856704, s58\n"
    "s_add_u32 s63, s63, s56\n"
    "s_add_u32 s70, s70, s56\n"
    "s_add_u32 m0, s63, 0\n"
    "s_add_u32 s56, s64, 0\n"
    "buffer_load_dwordx4 v3, s[32:35], s56 offen lds\n"
    "s_add_u32 m0, s70, 20480\n"
    "s_add_u32 s56, s66, 0\n"
    "buffer_load_dwordx4 v4, s[40:43], s56 offen sc1 lds\n"
    "s_add_u32 m0, s63, 4096\n"
    "s_add_u32 s56, s64, 16384\n"
    "buffer_load_dwordx4 v3, s[32:35], s56 offen lds\n"
    "s_add_u32 m0, s70, 21504\n"
    "s_add_u32 s56, s66, 1024\n"
    "buffer_load_dwordx4 v4, s[40:43], s56 offen sc1 lds\n"
    "s_add_u32 m0, s63, 8192\n"
    "s_add_u32 s56, s64, 32768\n"
    "buffer_load_dwordx4 v3, s[32:35], s56 offen lds\n"
    "s_add_u32 m0, s70, 22528\n"
    "s_add_u32 s56, s71, 0\n"
    "buffer_load_dwordx4 v4, s[40:43], s56 offen sc1 lds\n"
    "s_add_u32 m0, s63, 12288\n"
    "s_add_u32 s56, s64, 49152\n"
    "buffer_load_dwordx4 v3, s[32:35], s56 offen lds\n"
    "s_add_u32 m0, s70, 23552\n"
    "s_add_u32 s56, s71, 1024\n"
    "buffer_load_dwordx4 v4, s[40:43], s56 offen sc1 lds\n"
    "s_add_u32 s64, s64, 65536\n"
    "s_add_u32 s65, s65, 2048\n"
    "s_add_u32 s66, s66, 2048\n"
    "s_add_u32 s71, s71, 2048\n"
    "s_cmp_ge_u32 s63, 110592\n"
    "s_cselect_b32 s56, 4294856704, s58\n"
    "s_add_u32 s63, s63, s56\n"
    "s_add_u32 s70, s70, s56\n"
    "v_add_u32 v10, s62, v6\n"
    "v_add_u32 v15, s62, v8\n"
    "v_add_u32 v14, s62, v7\n"
    "v_xor_b32 v16, 1024, v15\n"
    "v_xor_b32 v11, 64, v10\n"
    "v_xor_b32 v12, 128, v10\n"
    "v_xor_b32 v13, 192, v10\n"
    "v_mov_b64 v[112:113], 0\n"
    "v_mov_b64 v[114:115], 0\n"
    "v_mov_b64 v[116:117], 0\n"
    "v_mov_b64 v[118:119], 0\n"
    "v_mov_b64 v[120:121], 0\n"
    "v_mov_b64 v[122:123], 0\n"
    "v_mov_b64 v[124:125], 0\n"
    "v_mov_b64 v[126:127], 0\n"
    "v_mov_b64 v[128:129], 0\n"
    "v_mov_b64 v[130:131], 0\n"
    "v_mov_b64 v[132:133], 0\n"
    "v_mov_b64 v[134:135], 0\n"
    "v_mov_b64 v[136:137], 0\n"
    "v_mov_b64 v[138:139], 0\n"
    "v_mov_b64 v[140:141], 0\n"
    "v_mov_b64 v[142:143], 0\n"
    "v_mov_b64 v[144:145], 0\n"
    "v_mov_b64 v[146:147], 0\n"
    "v_mov_b64 v[148:149], 0\n"
    "v_mov_b64 v[150:151], 0\n"
    "v_mov_b64 v[152:153], 0\n"
    "v_mov_b64 v[154:155], 0\n"
    "v_mov_b64 v[156:157], 0\n"
    "v_mov_b64 v[158:159], 0\n"
    "v_mov_b64 v[160:161], 0\n"
    "v_mov_b64 v[162:163], 0\n"
    "v_mov_b64 v[164:165], 0\n"
    "v_mov_b64 v[166:167], 0\n"
    "v_mov_b64 v[168:169], 0\n"
    "v_mov_b64 v[170:171], 0\n"
    "v_mov_b64 v[172:173], 0\n"
    "v_mov_b64 v[174:175], 0\n"
    "v_mov_b64 v[176:177], 0\n"
    "v_mov_b64 v[178:179], 0\n"
    "v_mov_b64 v[180:181], 0\n"
    "v_mov_b64 v[182:183], 0\n"
    "v_mov_b64 v[184:185], 0\n"
    "v_mov_b64 v[186:187], 0\n"
    "v_mov_b64 v[188:189], 0\n"
    "v_mov_b64 v[190:191], 0\n"
    "v_mov_b64 v[192:193], 0\n"
    "v_mov_b64 v[194:195], 0\n"
    "v_mov_b64 v[196:197], 0\n"
    "v_mov_b64 v[198:199], 0\n"
    "v_mov_b64 v[200:201], 0\n"
    "v_mov_b64 v[202:203], 0\n"
    "v_mov_b64 v[204:205], 0\n"
    "v_mov_b64 v[206:207], 0\n"
    "v_mov_b64 v[208:209], 0\n"
    "v_mov_b64 v[210:211], 0\n"
    "v_mov_b64 v[212:213], 0\n"
    "v_mov_b64 v[214:215], 0\n"
    "v_mov_b64 v[216:217], 0\n"
    "v_mov_b64 v[218:219], 0\n"
    "v_mov_b64 v[220:221], 0\n"
    "v_mov_b64 v[222:223], 0\n"
    "v_mov_b64 v[224:225], 0\n"
    "v_mov_b64 v[226:227], 0\n"
    "v_mov_b64 v[228:229], 0\n"
    "v_mov_b64 v[230:231], 0\n"
    "v_mov_b64 v[232:233], 0\n"
    "v_mov_b64 v[234:235], 0\n"
    "v_mov_b64 v[236:237], 0\n"
    "v_mov_b64 v[238:239], 0\n"
    "v_mov_b64 v[240:241], 0\n"
    "v_mov_b64 v[242:243], 0\n"
    "v_mov_b64 v[244:245], 0\n"
    "v_mov_b64 v[246:247], 0\n"
    "v_mov_b64 v[248:249], 0\n"
    "v_mov_b64 v[250:251], 0\n"
    "v_mov_b64 v[252:253], 0\n"
    "v_mov_b64 v[254:255], 0\n"
    "v_accvgpr_write_b32 a0, 0\n"
    "v_accvgpr_write_b32 a1, 0\n"
    "v_accvgpr_write_b32 a2, 0\n"
    "v_accvgpr_write_b32 a3, 0\n"
    "v_accvgpr_write_b32 a4, 0\n"
    "v_accvgpr_write_b32 a5, 0\n"
    "v_accvgpr_write_b32 a6, 0\n"
    "v_accvgpr_write_b32 a7, 0\n"
    "v_accvgpr_write_b32 a8, 0\n"
    "v_accvgpr_write_b32 a9, 0\n"
    "v_accvgpr_write_b32 a10, 0\n"
    "v_accvgpr_write_b32 a11, 0\n"
    "v_accvgpr_write_b32 a12, 0\n"
    "v_accvgpr_write_b32 a13, 0\n"
    "v_accvgpr_write_b32 a14, 0\n"
    "v_accvgpr_write_b32 a15, 0\n"
    "v_accvgpr_write_b32 a16, 0\n"
    "v_accvgpr_write_b32 a17, 0\n"
    "v_accvgpr_write_b32 a18, 0\n"
    "v_accvgpr_write_b32 a19, 0\n"
    "v_accvgpr_write_b32 a20, 0\n"
    "v_accvgpr_write_b32 a21, 0\n"
    "v_accvgpr_write_b32 a22, 0\n"
    "v_accvgpr_write_b32 a23, 0\n"
    "v_accvgpr_write_b32 a24, 0\n"
    "v_accvgpr_write_b32 a25, 0\n"
    "v_accvgpr_write_b32 a26, 0\n"
    "v_accvgpr_write_b32 a27, 0\n"
    "v_accvgpr_write_b32 a28, 0\n"
    "v_accvgpr_write_b32 a29, 0\n"
    "v_accvgpr_write_b32 a30, 0\n"
    "v_accvgpr_write_b32 a31, 0\n"
    "v_accvgpr_write_b32 a32, 0\n"
    "v_accvgpr_write_b32 a33, 0\n"
    "v_accvgpr_write_b32 a34, 0\n"
    "v_accvgpr_write_b32 a35, 0\n"
    "v_accvgpr_write_b32 a36, 0\n"
    "v_accvgpr_write_b32 a37, 0\n"
    "v_accvgpr_write_b32 a38, 0\n"
    "v_accvgpr_write_b32 a39, 0\n"
    "v_accvgpr_write_b32 a40, 0\n"
    "v_accvgpr_write_b32 a41, 0\n"
    "v_accvgpr_write_b32 a42, 0\n"
    "v_accvgpr_write_b32 a43, 0\n"
    "v_accvgpr_write_b32 a44, 0\n"
    "v_accvgpr_write_b32 a45, 0\n"
    "v_accvgpr_write_b32 a46, 0\n"
    "v_accvgpr_write_b32 a47, 0\n"
    "v_accvgpr_write_b32 a48, 0\n"
    "v_accvgpr_write_b32 a49, 0\n"
    "v_accvgpr_write_b32 a50, 0\n"
    "v_accvgpr_write_b32 a51, 0\n"
    "v_accvgpr_write_b32 a52, 0\n"
    "v_accvgpr_write_b32 a53, 0\n"
    "v_accvgpr_write_b32 a54, 0\n"
    "v_accvgpr_write_b32 a55, 0\n"
    "v_accvgpr_write_b32 a56, 0\n"
    "v_accvgpr_write_b32 a57, 0\n"
    "v_accvgpr_write_b32 a58, 0\n"
    "v_accvgpr_write_b32 a59, 0\n"
    "v_accvgpr_write_b32 a60, 0\n"
    "v_accvgpr_write_b32 a61, 0\n"
    "v_accvgpr_write_b32 a62, 0\n"
    "v_accvgpr_write_b32 a63, 0\n"
    "v_accvgpr_write_b32 a64, 0\n"
    "v_accvgpr_write_b32 a65, 0\n"
    "v_accvgpr_write_b32 a66, 0\n"
    "v_accvgpr_write_b32 a67, 0\n"
    "v_accvgpr_write_b32 a68, 0\n"
    "v_accvgpr_write_b32 a69, 0\n"
    "v_accvgpr_write_b32 a70, 0\n"
    "v_accvgpr_write_b32 a71, 0\n"
    "v_accvgpr_write_b32 a72, 0\n"
    "v_accvgpr_write_b32 a73, 0\n"
    "v_accvgpr_write_b32 a74, 0\n"
    "v_accvgpr_write_b32 a75, 0\n"
    "v_accvgpr_write_b32 a76, 0\n"
    "v_accvgpr_write_b32 a77, 0\n"
    "v_accvgpr_write_b32 a78, 0\n"
    "v_accvgpr_write_b32 a79, 0\n"
    "v_accvgpr_write_b32 a80, 0\n"
    "v_accvgpr_write_b32 a81, 0\n"
    "v_accvgpr_write_b32 a82, 0\n"
    "v_accvgpr_write_b32 a83, 0\n"
    "v_accvgpr_write_b32 a84, 0\n"
    "v_accvgpr_write_b32 a85, 0\n"
    "v_accvgpr_write_b32 a86, 0\n"
    "v_accvgpr_write_b32 a87, 0\n"
    "v_accvgpr_write_b32 a88, 0\n"
    "v_accvgpr_write_b32 a89, 0\n"
    "v_accvgpr_write_b32 a90, 0\n"
    "v_accvgpr_write_b32 a91, 0\n"
    "v_accvgpr_write_b32 a92, 0\n"
    "v_accvgpr_write_b32 a93, 0\n"
    "v_accvgpr_write_b32 a94, 0\n"
    "v_accvgpr_write_b32 a95, 0\n"
    "v_accvgpr_write_b32 a96, 0\n"
    "v_accvgpr_write_b32 a97, 0\n"
    "v_accvgpr_write_b32 a98, 0\n"
    "v_accvgpr_write_b32 a99, 0\n"
    "v_accvgpr_write_b32 a100, 0\n"
    "v_accvgpr_write_b32 a101, 0\n"
    "v_accvgpr_write_b32 a102, 0\n"
    "v_accvgpr_write_b32 a103, 0\n"
    "v_accvgpr_write_b32 a104, 0\n"
    "v_accvgpr_write_b32 a105, 0\n"
    "v_accvgpr_write_b32 a106, 0\n"
    "v_accvgpr_write_b32 a107, 0\n"
    "v_accvgpr_write_b32 a108, 0\n"
    "v_accvgpr_write_b32 a109, 0\n"
    "v_accvgpr_write_b32 a110, 0\n"
    "v_accvgpr_write_b32 a111, 0\n"
    "v_accvgpr_write_b32 a112, 0\n"
    "v_accvgpr_write_b32 a113, 0\n"
    "v_accvgpr_write_b32 a114, 0\n"
    "v_accvgpr_write_b32 a115, 0\n"
    "v_accvgpr_write_b32 a116, 0\n"
    "v_accvgpr_write_b32 a117, 0\n"
    "v_accvgpr_write_b32 a118, 0\n"
    "v_accvgpr_write_b32 a119, 0\n"
    "v_accvgpr_write_b32 a120, 0\n"
    "v_accvgpr_write_b32 a121, 0\n"
    "v_accvgpr_write_b32 a122, 0\n"
    "v_accvgpr_write_b32 a123, 0\n"
    "v_accvgpr_write_b32 a124, 0\n"
    "v_accvgpr_write_b32 a125, 0\n"
    "v_accvgpr_write_b32 a126, 0\n"
    "v_accvgpr_write_b32 a127, 0\n"
    "s_waitcnt vmcnt(16)\n"
    "s_barrier\n"
    "ds_read_b128 v[24:27], v15 offset:20480\n"
    "ds_read_b128 v[28:31], v15 offset:22528\n"
    "ds_read_b64_tr_b16 v[40:41], v10 offset:0\n"
    "ds_read_b64_tr_b16 v[42:43], v10 offset:4096\n"
    "ds_read_b64_tr_b16 v[44:45], v11 offset:0\n"
    "ds_read_b64_tr_b16 v[46:47], v11 offset:4096\n"
    "ds_read_b64_tr_b16 v[48:49], v12 offset:0\n"
    "ds_read_b64_tr_b16 v[50:51], v12 offset:4096\n"
    ".Lat_po_%=:\n"
    "s_waitcnt lgkmcnt(4)\n"
    "v_mfma_f32_32x32x16_bf16 v[128:143], v[40:43], v[24:27], v[128:143]\n"
    "ds_read_b64_tr_b16 v[52:53], v13 offset:0\n"
    "ds_read_b64_tr_b16 v[54:55], v13 offset:4096\n"
    "v_mfma_f32_32x32x16_bf16 a[0:15], v[40:43], v[28:31], a[0:15]\n"
    "s_add_u32 m0, s63, 0\n"
    "s_add_u32 s56, s64, 0\n"
    "buffer_load_dwordx4 v3, s[32:35], s56 offen lds\n"
    "s_waitcnt lgkmcnt(4)\n"
    "v_mfma_f32_32x32x16_bf16 v[144:159], v[44:47], v[24:27], v[144:159]\n"
    "ds_read_b64_tr_b16 v[40:41], v10 offset:256\n"
    "ds_read_b64_tr_b16 v[42:43], v10 offset:4352\n"
    "v_mfma_f32_32x32x16_bf16 a[16:31], v[44:47], v[28:31], a[16:31]\n"
    "s_add_u32 m0, s70, 20480\n"
    "s_add_u32 s56, s66, 0\n"
    "buffer_load_dwordx4 v4, s[40:43], s56 offen sc1 lds\n"
    "s_waitcnt lgkmcnt(4)\n"
    "v_mfma_f32_32x32x16_bf16 v[160:175], v[48:51], v[24:27], v[160:175]\n"
    "ds_read_b64_tr_b16 v[44:45], v11 offset:256\n"
    "ds_read_b64_tr_b16 v[46:47], v11 offset:4352\n"
    "v_mfma_f32_32x32x16_bf16 a[32:47], v[48:51], v[28:31], a[32:47]\n"
    "ds_read_b128 v[32:35], v16 offset:20480\n"
    "s_waitcnt lgkmcnt(5)\n"
    "v_mfma_f32_32x32x16_bf16 v[176:191], v[52:55], v[24:27], v[176:191]\n"
    "ds_read_b64_tr_b16 v[48:49], v12 offset:256\n"
    "ds_read_b64_tr_b16 v[50:51], v12 offset:4352\n"
    "v_mfma_f32_32x32x16_bf16 a[48:63], v[52:55], v[28:31], a[48:63]\n"
    "ds_read_b128 v[36:39], v16 offset:22528\n"
    "s_waitcnt lgkmcnt(6)\n"
    "v_mfma_f32_32x32x16_bf16 v[192:207], v[40:43], v[24:27], v[192:207]\n"
    "ds_read_b64_tr_b16 v[52:53], v13 offset:256\n"
    "ds_read_b64_tr_b16 v[54:55], v13 offset:4352\n"
    "v_mfma_f32_32x32x16_bf16 a[64:79], v[40:43], v[28:31], a[64:79]\n"
    "s_add_u32 m0, s63, 4096\n"
    "s_add_u32 s56, s64, 16384\n"
    "buffer_load_dwordx4 v3, s[32:35], s56 offen lds\n"
    "s_waitcnt lgkmcnt(6)\n"
    "v_mfma_f32_32x32x16_bf16 v[208:223], v[44:47], v[24:27], v[208:223]\n"
    "ds_read_b64_tr_b16 v[40:41], v10 offset:8192\n"
    "ds_read_b64_tr_b16 v[42:43], v10 offset:12288\n"
    "v_mfma_f32_32x32x16_bf16 a[80:95], v[44:47], v[28:31], a[80:95]\n"
    "s_add_u32 m0, s70, 21504\n"
    "s_add_u32 s56, s66, 1024\n"
    "buffer_load_dwordx4 v4, s[40:43], s56 offen sc1 lds\n"
    "s_waitcnt lgkmcnt(5)\n"
    "v_mfma_f32_32x32x16_bf16 v[224:239], v[48:51], v[24:27], v[224:239]\n"
    "ds_read_b64_tr_b16 v[44:45], v11 offset:8192\n"
    "ds_read_b64_tr_b16 v[46:47], v11 offset:12288\n"
    "v_mfma_f32_32x32x16_bf16 a[96:111], v[48:51], v[28:31], a[96:111]\n"
    "s_add_u32 m0, s63, 8192\n"
    "s_add_u32 s56, s64, 32768\n"
    "buffer_load_dwordx4 v3, s[32:35], s56 offen lds\n"
    "s_waitcnt lgkmcnt(4)\n"
    "v_mfma_f32_32x32x16_bf16 v[240:255], v[52:55], v[24:27], v[240:255]\n"
    "ds_read_b64_tr_b16 v[48:49], v12 offset:8192\n"
    "ds_read_b64_tr_b16 v[50:51], v12 offset:12288\n"
    "v_mfma_f32_32x32x16_bf16 a[112:127], v[52:55], v[28:31], a[112:127]\n"
    "s_add_u32 m0, s70, 22528\n"
    "s_add_u32 s56, s71, 0\n"
    "buffer_load_dwordx4 v4, s[40:43], s56 offen sc1 lds\n"
    "s_waitcnt lgkmcnt(4)\n"
    "v_mfma_f32_32x32x16_bf16 v[128:143], v[40:43], v[32:35], v[128:143]\n"
    "ds_read_b64_tr_b16 v[52:53], v13 offset:8192\n"
    "ds_read_b64_tr_b16 v[54:55], v13 offset:12288\n"
    "v_mfma_f32_32x32x16_bf16 a[0:15], v[40:43], v[36:39], a[0:15]\n"
    "s_add_u32 m0, s63, 12288\n"
    "s_add_u32 s56, s64, 49152\n"
    "buffer_load_dwordx4 v3, s[32:35], s56 offen lds\n"
    "s_waitcnt lgkmcnt(4)\n"
    "v_mfma_f32_32x32x16_bf16 v[144:159], v[44:47], v[32:35], v[144:159]\n"
    "ds_read_b64_tr_b16 v[40:41], v10 offset:8448\n"
    "ds_read_b64_tr_b16 v[42:43], v10 offset:12544\n"
    "v_mfma_f32_32x32x16_bf16 a[16:31], v[44:47], v[36:39], a[16:31]\n"
    "s_add_u32 m0, s70, 23552\n"
    "s_add_u32 s56, s71, 1024\n"
    "buffer_load_dwordx4 v4, s[40:43], s56 offen sc1 lds\n"
    "s_waitcnt lgkmcnt(4)\n"
    "v_mfma_f32_32x32x16_bf16 v[160:175], v[48:51], v[32:35], v[160:175]\n"
    "ds_read_b64_tr_b16 v[44:45], v11 offset:8448\n"
    "ds_read_b64_tr_b16 v[46:47], v11 offset:12544\n"
    "v_mfma_f32_32x32x16_bf16 a[32:47], v[48:51], v[36:39], a[32:47]\n"
    "s_add_u32 s64, s64, 65536\n"
    "s_add_u32 s65, s65, 2048\n"
    "s_add_u32 s66, s66, 2048\n"
    "s_add_u32 s71, s71, 2048\n"
    "s_waitcnt lgkmcnt(4)\n"
    "v_mfma_f32_32x32x16_bf16 v[176:191], v[52:55], v[32:35], v[176:191]\n"
    "ds_read_b64_tr_b16 v[48:49], v12 offset:8448\n"
    "ds_read_b64_tr_b16 v[50:51], v12 offset:12544\n"
    "v_mfma_f32_32x32x16_bf16 a[48:63], v[52:55], v[36:39], a[48:63]\n"
    "s_cmp_ge_u32 s63, 110592\n"
    "s_cselect_b32 s56, 4294856704, s58\n"
    "s_add_u32 s63, s63, s56\n"
    "s_add_u32 s70, s70, s56\n"
    "s_waitcnt lgkmcnt(4)\n"
    "v_mfma_f32_32x32x16_bf16 v[192:207], v[40:43], v[32:35], v[192:207]\n"
    "ds_read_b64_tr_b16 v[52:53], v13 offset:8448\n"
    "ds_read_b64_tr_b16 v[54:55], v13 offset:12544\n"
    "v_mfma_f32_32x32x16_bf16 a[64:79], v[40:43], v[36:39], a[64:79]\n"
    "s_add_u32 s62, s62, 36864\n"
    "s_cmp_ge_u32 s62, 147456\n"
    "s_cselect_b32 s62, 0, s62\n"
    "s_waitcnt lgkmcnt(4)\n"
    "v_mfma_f32_32x32x16_bf16 v[208:223], v[44:47], v[32:35], v[208:223]\n"
    "v_add_u32 v10, s62, v6\n"
    "v_add_u32 v15, s62, v8\n"
    "v_add_u32 v14, s62, v7\n"
    "v_xor_b32 v16, 1024, v15\n"
    "v_xor_b32 v11, 64, v10\n"
    "v_mfma_f32_32x32x16_bf16 a[80:95], v[44:47], v[36:39], a[80:95]\n"
    "v_xor_b32 v12, 128, v10\n"
    "v_xor_b32 v13, 192, v10\n"
    "s_waitcnt vmcnt(16) lgkmcnt(0)\n"
    "s_barrier\n"
    "ds_read_b128 v[24:27], v15 offset:20480\n"
    "ds_read_b128 v[28:31], v15 offset:22528\n"
    "ds_read_b64_tr_b16 v[40:41], v10 offset:0\n"
    "ds_read_b64_tr_b16 v[42:43], v10 offset:4096\n"
    "v_mfma_f32_32x32x16_bf16 v[224:239], v[48:51], v[32:35], v[224:239]\n"
    "ds_read_b64_tr_b16 v[44:45], v11 offset:0\n"
    "ds_read_b64_tr_b16 v[46:47], v11 offset:4096\n"
    "v_mfma_f32_32x32x16_bf16 a[96:111], v[48:51], v[36:39], a[96:111]\n"
    "v_mfma_f32_32x32x16_bf16 v[240:255], v[52:55], v[32:35], v[240:255]\n"
    "ds_read_b64_tr_b16 v[48:49], v12 offset:0\n"
    "ds_read_b64_tr_b16 v[50:51], v12 offset:4096\n"
    "v_mfma_f32_32x32x16_bf16 a[112:127], v[52:55], v[36:39], a[112:127]\n"
    "s_add_u32 s61, s61, 1\n"
    "s_cmp_lt_u32 s61, 32\n"
    "s_cbranch_scc1 .Lat_po_%=\n"
    "s_waitcnt lgkmcnt(0)\n"
    "s_nop 7\n"
    "s_lshl_b32 s56, s60, 17\n"
    "s_lshl_b32 s57, s59, 9\n"
    "s_add_u32 s56, s56, s57\n"
    "s_xor_b32 s57, s56, 524288\n"
    "s_waitcnt vmcnt(0)\n"
    "s_barrier\n"
    "s_lshl_b32 s61, s25, 15\n"
    "v_bfe_u32 v10, v1, 0, 5\n"
    "v_lshlrev_b32 v10, 9, v10\n"
    "v_bfe_u32 v19, v1, 5, 1\n"
    "v_lshl_add_u32 v10, v19, 4, v10\n"
    "v_add_u32 v10, s61, v10\n"
    "v_bfe_u32 v19, v1, 0, 3\n"
    "v_lshlrev_b32 v19, 4, v19\n"
    "v_xor_b32 v10, v10, v19\n"
    "v_bfe_u32 v19, v1, 5, 1\n"
    "v_lshlrev_b32 v19, 4, v19\n"
    "v_add_u32 v19, s61, v19\n"
    "v_xor_b32 v14, v19, v2\n"
    "v_xor_b32 v11, 32, v10\n"
    "v_xor_b32 v15, 32, v14\n"
    "v_xor_b32 v12, 64, v10\n"
    "v_xor_b32 v16, 64, v14\n"
    "v_xor_b32 v13, 96, v10\n"
    "v_xor_b32 v17, 96, v14\n"
    "v_bfe_u32 v19, v1, 5, 1\n"
    "v_lshlrev_b32 v19, 12, v19\n"
    "v_bfe_u32 v20, v1, 0, 5\n"
    "v_lshl_add_u32 v19, v20, 4, v19\n"
    "v_cvt_pk_bf16_f32 v128, v128, v129\n"
    "v_cvt_pk_bf16_f32 v129, v130, v131\n"
    "v_cvt_pk_bf16_f32 v130, v132, v133\n"
    "v_cvt_pk_bf16_f32 v131, v134, v135\n"
    "v_cvt_pk_bf16_f32 v132, v136, v137\n"
    "v_cvt_pk_bf16_f32 v133, v138, v139\n"
    "v_cvt_pk_bf16_f32 v134, v140, v141\n"
    "v_cvt_pk_bf16_f32 v135, v142, v143\n"
    "v_permlane32_swap_b32 v128, v130\n"
    "v_permlane32_swap_b32 v129, v131\n"
    "v_permlane32_swap_b32 v132, v134\n"
    "v_permlane32_swap_b32 v133, v135\n"
    "ds_write_b128 v10, v[128:131] offset:0\n"
    "ds_write_b128 v11, v[132:135] offset:0\n"
    "v_cvt_pk_bf16_f32 v144, v144, v145\n"
    "v_cvt_pk_bf16_f32 v145, v146, v147\n"
    "v_cvt_pk_bf16_f32 v146, v148, v149\n"
    "v_cvt_pk_bf16_f32 v147, v150, v151\n"
    "v_cvt_pk_bf16_f32 v148, v152, v153\n"
    "v_cvt_pk_bf16_f32 v149, v154, v155\n"
    "v_cvt_pk_bf16_f32 v150, v156, v157\n"
    "v_cvt_pk_bf16_f32 v151, v158, v159\n"
    "v_permlane32_swap_b32 v144, v146\n"
    "v_permlane32_swap_b32 v145, v147\n"
    "v_permlane32_swap_b32 v148, v150\n"
    "v_permlane32_swap_b32 v149, v151\n"
    "ds_write_b128 v12, v[144:147] offset:0\n"
    "ds_write_b128 v13, v[148:151] offset:0\n"
    "v_cvt_pk_bf16_f32 v160, v160, v161\n"
    "v_cvt_pk_bf16_f32 v161, v162, v163\n"
    "v_cvt_pk_bf16_f32 v162, v164, v165\n"
    "v_cvt_pk_bf16_f32 v163, v166, v167\n"
    "v_cvt_pk_bf16_f32 v164, v168, v169\n"
    "v_cvt_pk_bf16_f32 v165, v170, v171\n"
    "v_cvt_pk_bf16_f32 v166, v172, v173\n"
    "v_cvt_pk_bf16_f32 v167, v174, v175\n"
    "v_permlane32_swap_b32 v160, v162\n"
    "v_permlane32_swap_b32 v161, v163\n"
    "v_permlane32_swap_b32 v164, v166\n"
    "v_permlane32_swap_b32 v165, v167\n"
    "ds_write_b128 v10, v[160:163] offset:128\n"
    "ds_write_b128 v11, v[164:167] offset:128\n"
    "v_cvt_pk_bf16_f32 v176, v176, v177\n"
    "v_cvt_pk_bf16_f32 v177, v178, v179\n"
    "v_cvt_pk_bf16_f32 v178, v180, v181\n"
    "v_cvt_pk_bf16_f32 v179, v182, v183\n"
    "v_cvt_pk_bf16_f32 v180, v184, v185\n"
    "v_cvt_pk_bf16_f32 v181, v186, v187\n"
    "v_cvt_pk_bf16_f32 v182, v188, v189\n"
    "v_cvt_pk_bf16_f32 v183, v190, v191\n"
    "v_permlane32_swap_b32 v176, v178\n"
    "v_permlane32_swap_b32 v177, v179\n"
    "v_permlane32_swap_b32 v180, v182\n"
    "v_permlane32_swap_b32 v181, v183\n"
    "ds_write_b128 v12, v[176:179] offset:128\n"
    "ds_write_b128 v13, v[180:183] offset:128\n"
    "v_cvt_pk_bf16_f32 v192, v192, v193\n"
    "v_cvt_pk_bf16_f32 v193, v194, v195\n"
    "v_cvt_pk_bf16_f32 v194, v196, v197\n"
    "v_cvt_pk_bf16_f32 v195, v198, v199\n"
    "v_cvt_pk_bf16_f32 v196, v200, v201\n"
    "v_cvt_pk_bf16_f32 v197, v202, v203\n"
    "v_cvt_pk_bf16_f32 v198, v204, v205\n"
    "v_cvt_pk_bf16_f32 v199, v206, v207\n"
    "v_permlane32_swap_b32 v192, v194\n"
    "v_permlane32_swap_b32 v193, v195\n"
    "v_permlane32_swap_b32 v196, v198\n"
    "v_permlane32_swap_b32 v197, v199\n"
    "ds_write_b128 v10, v[192:195] offset:256\n"
    "ds_write_b128 v11, v[196:199] offset:256\n"
    "v_cvt_pk_bf16_f32 v208, v208, v209\n"
    "v_cvt_pk_bf16_f32 v209, v210, v211\n"
    "v_cvt_pk_bf16_f32 v210, v212, v213\n"
    "v_cvt_pk_bf16_f32 v211, v214, v215\n"
    "v_cvt_pk_bf16_f32 v212, v216, v217\n"
    "v_cvt_pk_bf16_f32 v213, v218, v219\n"
    "v_cvt_pk_bf16_f32 v214, v220, v221\n"
    "v_cvt_pk_bf16_f32 v215, v222, v223\n"
    "v_permlane32_swap_b32 v208, v210\n"
    "v_permlane32_swap_b32 v209, v211\n"
    "v_permlane32_swap_b32 v212, v214\n"
    "v_permlane32_swap_b32 v213, v215\n"
    "ds_write_b128 v12, v[208:211] offset:256\n"
    "ds_write_b128 v13, v[212:215] offset:256\n"
    "v_cvt_pk_bf16_f32 v224, v224, v225\n"
    "v_cvt_pk_bf16_f32 v225, v226, v227\n"
    "v_cvt_pk_bf16_f32 v226, v228, v229\n"
    "v_cvt_pk_bf16_f32 v227, v230, v231\n"
    "v_cvt_pk_bf16_f32 v228, v232, v233\n"
    "v_cvt_pk_bf16_f32 v229, v234, v235\n"
    "v_cvt_pk_bf16_f32 v230, v236, v237\n"
    "v_cvt_pk_bf16_f32 v231, v238, v239\n"
    "v_permlane32_swap_b32 v224, v226\n"
    "v_permlane32_swap_b32 v225, v227\n"
    "v_permlane32_swap_b32 v228, v230\n"
    "v_permlane32_swap_b32 v229, v231\n"
    "ds_write_b128 v10, v[224:227] offset:384\n"
    "ds_write_b128 v11, v[228:231] offset:384\n"
    "v_cvt_pk_bf16_f32 v240, v240, v241\n"
    "v_cvt_pk_bf16_f32 v241, v242, v243\n"
    "v_cvt_pk_bf16_f32 v242, v244, v245\n"
    "v_cvt_pk_bf16_f32 v243, v246, v247\n"
    "v_cvt_pk_bf16_f32 v244, v248, v249\n"
    "v_cvt_pk_bf16_f32 v245, v250, v251\n"
    "v_cvt_pk_bf16_f32 v246, v252, v253\n"
    "v_cvt_pk_bf16_f32 v247, v254, v255\n"
    "v_permlane32_swap_b32 v240, v242\n"
    "v_permlane32_swap_b32 v241, v243\n"
    "v_permlane32_swap_b32 v244, v246\n"
    "v_permlane32_swap_b32 v245, v247\n"
    "ds_write_b128 v12, v[240:243] offset:384\n"
    "ds_write_b128 v13, v[244:247] offset:384\n"
    "s_waitcnt lgkmcnt(0)\n"
    "ds_read_b128 v[32:35], v14 offset:0\n"
    "ds_read_b128 v[36:39], v15 offset:1024\n"
    "ds_read_b128 v[40:43], v16 offset:2048\n"
    "ds_read_b128 v[44:47], v17 offset:3072\n"
    "ds_read_b128 v[48:51], v14 offset:4096\n"
    "ds_read_b128 v[52:55], v15 offset:5120\n"
    "ds_read_b128 v[56:59], v16 offset:6144\n"
    "ds_read_b128 v[60:63], v17 offset:7168\n"
    "ds_read_b128 v[64:67], v14 offset:8192\n"
    "ds_read_b128 v[68:71], v15 offset:9216\n"
    "ds_read_b128 v[72:75], v16 offset:10240\n"
    "ds_read_b128 v[76:79], v17 offset:11264\n"
    "ds_read_b128 v[80:83], v14 offset:12288\n"
    "ds_read_b128 v[84:87], v15 offset:13312\n"
    "ds_read_b128 v[88:91], v16 offset:14336\n"
    "ds_read_b128 v[92:95], v17 offset:15360\n"
    "s_waitcnt lgkmcnt(0)\n"
    "s_add_u32 s61, s56, 0\n"
    "buffer_store_dwordx4 v[32:35], v19, s[52:55], s61 offen sc1\n"
    "s_add_u32 s61, s56, 8192\n"
    "buffer_store_dwordx4 v[36:39], v19, s[52:55], s61 offen sc1\n"
    "v_accvgpr_read_b32 v96, a0\n"
    "v_accvgpr_read_b32 v97, a1\n"
    "v_accvgpr_read_b32 v98, a2\n"
    "v_accvgpr_read_b32 v99, a3\n"
    "v_accvgpr_read_b32 v100, a4\n"
    "v_accvgpr_read_b32 v101, a5\n"
    "v_accvgpr_read_b32 v102, a6\n"
    "v_accvgpr_read_b32 v103, a7\n"
    "v_accvgpr_read_b32 v104, a8\n"
    "v_accvgpr_read_b32 v105, a9\n"
    "v_accvgpr_read_b32 v106, a10\n"
    "v_accvgpr_read_b32 v107, a11\n"
    "v_accvgpr_read_b32 v108, a12\n"
    "v_accvgpr_read_b32 v109, a13\n"
    "v_accvgpr_read_b32 v110, a14\n"
    "v_accvgpr_read_b32 v111, a15\n"
    "v_cvt_pk_bf16_f32 v96, v96, v97\n"
    "v_cvt_pk_bf16_f32 v97, v98, v99\n"
    "v_cvt_pk_bf16_f32 v98, v100, v101\n"
    "v_cvt_pk_bf16_f32 v99, v102, v103\n"
    "v_cvt_pk_bf16_f32 v100, v104, v105\n"
    "v_cvt_pk_bf16_f32 v101, v106, v107\n"
    "v_cvt_pk_bf16_f32 v102, v108, v109\n"
    "v_cvt_pk_bf16_f32 v103, v110, v111\n"
    "v_permlane32_swap_b32 v96, v98\n"
    "v_permlane32_swap_b32 v97, v99\n"
    "v_permlane32_swap_b32 v100, v102\n"
    "v_permlane32_swap_b32 v101, v103\n"
    "ds_write_b128 v10, v[96:99] offset:16384\n"
    "ds_write_b128 v11, v[100:103] offset:16384\n"
    "s_add_u32 s61, s56, 16384\n"
    "buffer_store_dwordx4 v[40:43], v19, s[52:55], s61 offen sc1\n"
    "s_add_u32 s61, s56, 24576\n"
    "buffer_store_dwordx4 v[44:47], v19, s[52:55], s61 offen sc1\n"
    "v_accvgpr_read_b32 v112, a16\n"
    "v_accvgpr_read_b32 v113, a17\n"
    "v_accvgpr_read_b32 v114, a18\n"
    "v_accvgpr_read_b32 v115, a19\n"
    "v_accvgpr_read_b32 v116, a20\n"
    "v_accvgpr_read_b32 v117, a21\n"
    "v_accvgpr_read_b32 v118, a22\n"
    "v_accvgpr_read_b32 v119, a23\n"
    "v_accvgpr_read_b32 v120, a24\n"
    "v_accvgpr_read_b32 v121, a25\n"
    "v_accvgpr_read_b32 v122, a26\n"
    "v_accvgpr_read_b32 v123, a27\n"
    "v_accvgpr_read_b32 v124, a28\n"
    "v_accvgpr_read_b32 v125, a29\n"
    "v_accvgpr_read_b32 v126, a30\n"
    "v_accvgpr_read_b32 v127, a31\n"
    "v_cvt_pk_bf16_f32 v112, v112, v113\n"
    "v_cvt_pk_bf16_f32 v113, v114, v115\n"
    "v_cvt_pk_bf16_f32 v114, v116, v117\n"
    "v_cvt_pk_bf16_f32 v115, v118, v119\n"
    "v_cvt_pk_bf16_f32 v116, v120, v121\n"
    "v_cvt_pk_bf16_f32 v117, v122, v123\n"
    "v_cvt_pk_bf16_f32 v118, v124, v125\n"
    "v_cvt_pk_bf16_f32 v119, v126, v127\n"
    "v_permlane32_swap_b32 v112, v114\n"
    "v_permlane32_swap_b32 v113, v115\n"
    "v_permlane32_swap_b32 v116, v118\n"
    "v_permlane32_swap_b32 v117, v119\n"
    "ds_write_b128 v12, v[112:115] offset:16384\n"
    "ds_write_b128 v13, v[116:119] offset:16384\n"
    "s_add_u32 s61, s56, 32768\n"
    "buffer_store_dwordx4 v[48:51], v19, s[52:55], s61 offen sc1\n"
    "s_add_u32 s61, s56, 40960\n"
    "buffer_store_dwordx4 v[52:55], v19, s[52:55], s61 offen sc1\n"
    "v_accvgpr_read_b32 v96, a32\n"
    "v_accvgpr_read_b32 v97, a33\n"
    "v_accvgpr_read_b32 v98, a34\n"
    "v_accvgpr_read_b32 v99, a35\n"
    "v_accvgpr_read_b32 v100, a36\n"
    "v_accvgpr_read_b32 v101, a37\n"
    "v_accvgpr_read_b32 v102, a38\n"
    "v_accvgpr_read_b32 v103, a39\n"
    "v_accvgpr_read_b32 v104, a40\n"
    "v_accvgpr_read_b32 v105, a41\n"
    "v_accvgpr_read_b32 v106, a42\n"
    "v_accvgpr_read_b32 v107, a43\n"
    "v_accvgpr_read_b32 v108, a44\n"
    "v_accvgpr_read_b32 v109, a45\n"
    "v_accvgpr_read_b32 v110, a46\n"
    "v_accvgpr_read_b32 v111, a47\n"
    "v_cvt_pk_bf16_f32 v96, v96, v97\n"
    "v_cvt_pk_bf16_f32 v97, v98, v99\n"
    "v_cvt_pk_bf16_f32 v98, v100, v101\n"
    "v_cvt_pk_bf16_f32 v99, v102, v103\n"
    "v_cvt_pk_bf16_f32 v100, v104, v105\n"
    "v_cvt_pk_bf16_f32 v101, v106, v107\n"
    "v_cvt_pk_bf16_f32 v102, v108, v109\n"
    "v_cvt_pk_bf16_f32 v103, v110, v111\n"
    "v_permlane32_swap_b32 v96, v98\n"
    "v_permlane32_swap_b32 v97, v99\n"
    "v_permlane32_swap_b32 v100, v102\n"
    "v_permlane32_swap_b32 v101, v103\n"
    "ds_write_b128 v10, v[96:99] offset:16512\n"
    "ds_write_b128 v11, v[100:103] offset:16512\n"
    "s_add_u32 s61, s56, 49152\n"
    "buffer_store_dwordx4 v[56:59], v19, s[52:55], s61 offen sc1\n"
    "s_add_u32 s61, s56, 57344\n"
    "buffer_store_dwordx4 v[60:63], v19, s[52:55], s61 offen sc1\n"
    "v_accvgpr_read_b32 v112, a48\n"
    "v_accvgpr_read_b32 v113, a49\n"
    "v_accvgpr_read_b32 v114, a50\n"
    "v_accvgpr_read_b32 v115, a51\n"
    "v_accvgpr_read_b32 v116, a52\n"
    "v_accvgpr_read_b32 v117, a53\n"
    "v_accvgpr_read_b32 v118, a54\n"
    "v_accvgpr_read_b32 v119, a55\n"
    "v_accvgpr_read_b32 v120, a56\n"
    "v_accvgpr_read_b32 v121, a57\n"
    "v_accvgpr_read_b32 v122, a58\n"
    "v_accvgpr_read_b32 v123, a59\n"
    "v_accvgpr_read_b32 v124, a60\n"
    "v_accvgpr_read_b32 v125, a61\n"
    "v_accvgpr_read_b32 v126, a62\n"
    "v_accvgpr_read_b32 v127, a63\n"
    "v_cvt_pk_bf16_f32 v112, v112, v113\n"
    "v_cvt_pk_bf16_f32 v113, v114, v115\n"
    "v_cvt_pk_bf16_f32 v114, v116, v117\n"
    "v_cvt_pk_bf16_f32 v115, v118, v119\n"
    "v_cvt_pk_bf16_f32 v116, v120, v121\n"
    "v_cvt_pk_bf16_f32 v117, v122, v123\n"
    "v_cvt_pk_bf16_f32 v118, v124, v125\n"
    "v_cvt_pk_bf16_f32 v119, v126, v127\n"
    "v_permlane32_swap_b32 v112, v114\n"
    "v_permlane32_swap_b32 v113, v115\n"
    "v_permlane32_swap_b32 v116, v118\n"
    "v_permlane32_swap_b32 v117, v119\n"
    "ds_write_b128 v12, v[112:115] offset:16512\n"
    "ds_write_b128 v13, v[116:119] offset:16512\n"
    "s_add_u32 s61, s56, 65536\n"
    "buffer_store_dwordx4 v[64:67], v19, s[52:55], s61 offen sc1\n"
    "s_add_u32 s61, s56, 73728\n"
    "buffer_store_dwordx4 v[68:71], v19, s[52:55], s61 offen sc1\n"
    "v_accvgpr_read_b32 v96, a64\n"
    "v_accvgpr_read_b32 v97, a65\n"
    "v_accvgpr_read_b32 v98, a66\n"
    "v_accvgpr_read_b32 v99, a67\n"
    "v_accvgpr_read_b32 v100, a68\n"
    "v_accvgpr_read_b32 v101, a69\n"
    "v_accvgpr_read_b32 v102, a70\n"
    "v_accvgpr_read_b32 v103, a71\n"
    "v_accvgpr_read_b32 v104, a72\n"
    "v_accvgpr_read_b32 v105, a73\n"
    "v_accvgpr_read_b32 v106, a74\n"
    "v_accvgpr_read_b32 v107, a75\n"
    "v_accvgpr_read_b32 v108, a76\n"
    "v_accvgpr_read_b32 v109, a77\n"
    "v_accvgpr_read_b32 v110, a78\n"
    "v_accvgpr_read_b32 v111, a79\n"
    "v_cvt_pk_bf16_f32 v96, v96, v97\n"
    "v_cvt_pk_bf16_f32 v97, v98, v99\n"
    "v_cvt_pk_bf16_f32 v98, v100, v101\n"
    "v_cvt_pk_bf16_f32 v99, v102, v103\n"
    "v_cvt_pk_bf16_f32 v100, v104, v105\n"
    "v_cvt_pk_bf16_f32 v101, v106, v107\n"
    "v_cvt_pk_bf16_f32 v102, v108, v109\n"
    "v_cvt_pk_bf16_f32 v103, v110, v111\n"
    "v_permlane32_swap_b32 v96, v98\n"
    "v_permlane32_swap_b32 v97, v99\n"
    "v_permlane32_swap_b32 v100, v102\n"
    "v_permlane32_swap_b32 v101, v103\n"
    "ds_write_b128 v10, v[96:99] offset:16640\n"
    "ds_write_b128 v11, v[100:103] offset:16640\n"
    "s_add_u32 s61, s56, 81920\n"
    "buffer_store_dwordx4 v[72:75], v19, s[52:55], s61 offen sc1\n"
    "s_add_u32 s61, s56, 90112\n"
    "buffer_store_dwordx4 v[76:79], v19, s[52:55], s61 offen sc1\n"
    "v_accvgpr_read_b32 v112, a80\n"
    "v_accvgpr_read_b32 v113, a81\n"
    "v_accvgpr_read_b32 v114, a82\n"
    "v_accvgpr_read_b32 v115, a83\n"
    "v_accvgpr_read_b32 v116, a84\n"
    "v_accvgpr_read_b32 v117, a85\n"
    "v_accvgpr_read_b32 v118, a86\n"
    "v_accvgpr_read_b32 v119, a87\n"
    "v_accvgpr_read_b32 v120, a88\n"
    "v_accvgpr_read_b32 v121, a89\n"
    "v_accvgpr_read_b32 v122, a90\n"
    "v_accvgpr_read_b32 v123, a91\n"
    "v_accvgpr_read_b32 v124, a92\n"
    "v_accvgpr_read_b32 v125, a93\n"
    "v_accvgpr_read_b32 v126, a94\n"
    "v_accvgpr_read_b32 v127, a95\n"
    "v_cvt_pk_bf16_f32 v112, v112, v113\n"
    "v_cvt_pk_bf16_f32 v113, v114, v115\n"
    "v_cvt_pk_bf16_f32 v114, v116, v117\n"
    "v_cvt_pk_bf16_f32 v115, v118, v119\n"
    "v_cvt_pk_bf16_f32 v116, v120, v121\n"
    "v_cvt_pk_bf16_f32 v117, v122, v123\n"
    "v_cvt_pk_bf16_f32 v118, v124, v125\n"
    "v_cvt_pk_bf16_f32 v119, v126, v127\n"
    "v_permlane32_swap_b32 v112, v114\n"
    "v_permlane32_swap_b32 v113, v115\n"
    "v_permlane32_swap_b32 v116, v118\n"
    "v_permlane32_swap_b32 v117, v119\n"
    "ds_write_b128 v12, v[112:115] offset:16640\n"
    "ds_write_b128 v13, v[116:119] offset:16640\n"
    "s_add_u32 s61, s56, 98304\n"
    "buffer_store_dwordx4 v[80:83], v19, s[52:55], s61 offen sc1\n"
    "s_add_u32 s61, s56, 106496\n"
    "buffer_store_dwordx4 v[84:87], v19, s[52:55], s61 offen sc1\n"
    "v_accvgpr_read_b32 v96, a96\n"
    "v_accvgpr_read_b32 v97, a97\n"
    "v_accvgpr_read_b32 v98, a98\n"
    "v_accvgpr_read_b32 v99, a99\n"
    "v_accvgpr_read_b32 v100, a100\n"
    "v_accvgpr_read_b32 v101, a101\n"
    "v_accvgpr_read_b32 v102, a102\n"
    "v_accvgpr_read_b32 v103, a103\n"
    "v_accvgpr_read_b32 v104, a104\n"
    "v_accvgpr_read_b32 v105, a105\n"
    "v_accvgpr_read_b32 v106, a106\n"
    "v_accvgpr_read_b32 v107, a107\n"
    "v_accvgpr_read_b32 v108, a108\n"
    "v_accvgpr_read_b32 v109, a109\n"
    "v_accvgpr_read_b32 v110, a110\n"
    "v_accvgpr_read_b32 v111, a111\n"
    "v_cvt_pk_bf16_f32 v96, v96, v97\n"
    "v_cvt_pk_bf16_f32 v97, v98, v99\n"
    "v_cvt_pk_bf16_f32 v98, v100, v101\n"
    "v_cvt_pk_bf16_f32 v99, v102, v103\n"
    "v_cvt_pk_bf16_f32 v100, v104, v105\n"
    "v_cvt_pk_bf16_f32 v101, v106, v107\n"
    "v_cvt_pk_bf16_f32 v102, v108, v109\n"
    "v_cvt_pk_bf16_f32 v103, v110, v111\n"
    "v_permlane32_swap_b32 v96, v98\n"
    "v_permlane32_swap_b32 v97, v99\n"
    "v_permlane32_swap_b32 v100, v102\n"
    "v_permlane32_swap_b32 v101, v103\n"
    "ds_write_b128 v10, v[96:99] offset:16768\n"
    "ds_write_b128 v11, v[100:103] offset:16768\n"
    "s_add_u32 s61, s56, 114688\n"
    "buffer_store_dwordx4 v[88:91], v19, s[52:55], s61 offen sc1\n"
    "s_add_u32 s61, s56, 122880\n"
    "buffer_store_dwordx4 v[92:95], v19, s[52:55], s61 offen sc1\n"
    "v_accvgpr_read_b32 v112, a112\n"
    "v_accvgpr_read_b32 v113, a113\n"
    "v_accvgpr_read_b32 v114, a114\n"
    "v_accvgpr_read_b32 v115, a115\n"
    "v_accvgpr_read_b32 v116, a116\n"
    "v_accvgpr_read_b32 v117, a117\n"
    "v_accvgpr_read_b32 v118, a118\n"
    "v_accvgpr_read_b32 v119, a119\n"
    "v_accvgpr_read_b32 v120, a120\n"
    "v_accvgpr_read_b32 v121, a121\n"
    "v_accvgpr_read_b32 v122, a122\n"
    "v_accvgpr_read_b32 v123, a123\n"
    "v_accvgpr_read_b32 v124, a124\n"
    "v_accvgpr_read_b32 v125, a125\n"
    "v_accvgpr_read_b32 v126, a126\n"
    "v_accvgpr_read_b32 v127, a127\n"
    "v_cvt_pk_bf16_f32 v112, v112, v113\n"
    "v_cvt_pk_bf16_f32 v113, v114, v115\n"
    "v_cvt_pk_bf16_f32 v114, v116, v117\n"
    "v_cvt_pk_bf16_f32 v115, v118, v119\n"
    "v_cvt_pk_bf16_f32 v116, v120, v121\n"
    "v_cvt_pk_bf16_f32 v117, v122, v123\n"
    "v_cvt_pk_bf16_f32 v118, v124, v125\n"
    "v_cvt_pk_bf16_f32 v119, v126, v127\n"
    "v_permlane32_swap_b32 v112, v114\n"
    "v_permlane32_swap_b32 v113, v115\n"
    "v_permlane32_swap_b32 v116, v118\n"
    "v_permlane32_swap_b32 v117, v119\n"
    "ds_write_b128 v12, v[112:115] offset:16768\n"
    "ds_write_b128 v13, v[116:119] offset:16768\n"
    "s_waitcnt lgkmcnt(0)\n"
    "ds_read_b128 v[128:131], v14 offset:16384\n"
    "ds_read_b128 v[132:135], v15 offset:17408\n"
    "ds_read_b128 v[136:139], v16 offset:18432\n"
    "ds_read_b128 v[140:143], v17 offset:19456\n"
    "ds_read_b128 v[144:147], v14 offset:20480\n"
    "ds_read_b128 v[148:151], v15 offset:21504\n"
    "ds_read_b128 v[152:155], v16 offset:22528\n"
    "ds_read_b128 v[156:159], v17 offset:23552\n"
    "ds_read_b128 v[160:163], v14 offset:24576\n"
    "ds_read_b128 v[164:167], v15 offset:25600\n"
    "ds_read_b128 v[168:171], v16 offset:26624\n"
    "ds_read_b128 v[172:175], v17 offset:27648\n"
    "ds_read_b128 v[176:179], v14 offset:28672\n"
    "ds_read_b128 v[180:183], v15 offset:29696\n"
    "ds_read_b128 v[184:187], v16 offset:30720\n"
    "ds_read_b128 v[188:191], v17 offset:31744\n"
    "s_waitcnt lgkmcnt(0)\n"
    "s_add_u32 s61, s57, 0\n"
    "buffer_store_dwordx4 v[128:131], v19, s[52:55], s61 offen sc1\n"
    "s_add_u32 s61, s57, 8192\n"
    "buffer_store_dwordx4 v[132:135], v19, s[52:55], s61 offen sc1\n"
    "s_add_u32 s61, s57, 16384\n"
    "buffer_store_dwordx4 v[136:139], v19, s[52:55], s61 offen sc1\n"
    "s_add_u32 s61, s57, 24576\n"
    "buffer_store_dwordx4 v[140:143], v19, s[52:55], s61 offen sc1\n"
    "s_add_u32 s61, s57, 32768\n"
    "buffer_store_dwordx4 v[144:147], v19, s[52:55], s61 offen sc1\n"
    "s_add_u32 s61, s57, 40960\n"
    "buffer_store_dwordx4 v[148:151], v19, s[52:55], s61 offen sc1\n"
    "s_add_u32 s61, s57, 49152\n"
    "buffer_store_dwordx4 v[152:155], v19, s[52:55], s61 offen sc1\n"
    "s_add_u32 s61, s57, 57344\n"
    "buffer_store_dwordx4 v[156:159], v19, s[52:55], s61 offen sc1\n"
    "s_add_u32 s61, s57, 65536\n"
    "buffer_store_dwordx4 v[160:163], v19, s[52:55], s61 offen sc1\n"
    "s_add_u32 s61, s57, 73728\n"
    "buffer_store_dwordx4 v[164:167], v19, s[52:55], s61 offen sc1\n"
    "s_add_u32 s61, s57, 81920\n"
    "buffer_store_dwordx4 v[168:171], v19, s[52:55], s61 offen sc1\n"
    "s_add_u32 s61, s57, 90112\n"
    "buffer_store_dwordx4 v[172:175], v19, s[52:55], s61 offen sc1\n"
    "s_add_u32 s61, s57, 98304\n"
    "buffer_store_dwordx4 v[176:179], v19, s[52:55], s61 offen sc1\n"
    "s_add_u32 s61, s57, 106496\n"
    "buffer_store_dwordx4 v[180:183], v19, s[52:55], s61 offen sc1\n"
    "s_add_u32 s61, s57, 114688\n"
    "buffer_store_dwordx4 v[184:187], v19, s[52:55], s61 offen sc1\n"
    "s_add_u32 s61, s57, 122880\n"
    "buffer_store_dwordx4 v[188:191], v19, s[52:55], s61 offen sc1\n"
    ".Lat_adone_%=:\n"
    "s_endpgm\n"
    ".Lat_rare0_%=:\n"
    "v_mfma_f32_32x32x16_bf16 v[48:63], a[240:243], a[184:187], v[240:255]\n"
    "v_mfma_f32_32x32x16_bf16 v[48:63], a[244:247], a[188:191], v[48:63]\n"
    "v_accvgpr_read_b32 v24, a112\n"
    "v_accvgpr_read_b32 v25, a113\n"
    "v_accvgpr_read_b32 v26, a114\n"
    "v_accvgpr_read_b32 v27, a115\n"
    "v_accvgpr_read_b32 v28, a116\n"
    "v_accvgpr_read_b32 v29, a117\n"
    "v_accvgpr_read_b32 v30, a118\n"
    "v_accvgpr_read_b32 v31, a119\n"
    "v_accvgpr_read_b32 v32, a120\n"
    "v_accvgpr_read_b32 v33, a121\n"
    "v_accvgpr_read_b32 v34, a122\n"
    "v_accvgpr_read_b32 v35, a123\n"
    "v_accvgpr_read_b32 v36, a124\n"
    "v_accvgpr_read_b32 v37, a125\n"
    "v_accvgpr_read_b32 v38, a126\n"
    "v_accvgpr_read_b32 v39, a127\n"
    "v_accvgpr_read_b32 v40, a220\n"
    "v_accvgpr_read_b32 v41, a221\n"
    "v_accvgpr_read_b32 v42, a222\n"
    "v_accvgpr_read_b32 v43, a223\n"
    "v_max3_f32 v20, v48, v49, v50\n"
    "v_max3_f32 v20, v20, v51, v52\n"
    "v_max3_f32 v20, v20, v53, v54\n"
    "v_max3_f32 v20, v20, v55, v56\n"
    "v_max3_f32 v20, v20, v57, v58\n"
    "v_max3_f32 v20, v20, v59, v60\n"
    "v_max3_f32 v20, v20, v61, v62\n"
    "v_max_f32 v20, v20, v63\n"
    "v_mov_b32 v21, v20\n"
    "s_nop 1\n"
    "v_permlane32_swap_b32 v20, v21\n"
    "v_max_f32 v20, v20, v21\n"
    "v_add_f32 v20, 1109393408, v20\n"
    "v_max_f32 v20, 0, v20\n"
    "v_exp_f32 v22, -v20\n"
    "v_pk_add_f32 v[48:49], v[48:49], v[20:21] op_sel_hi:[1,0] neg_lo:[0,1] neg_hi:[0,1]\n"
    "v_pk_add_f32 v[50:51], v[50:51], v[20:21] op_sel_hi:[1,0] neg_lo:[0,1] neg_hi:[0,1]\n"
    "v_pk_add_f32 v[52:53], v[52:53], v[20:21] op_sel_hi:[1,0] neg_lo:[0,1] neg_hi:[0,1]\n"
    "v_pk_add_f32 v[54:55], v[54:55], v[20:21] op_sel_hi:[1,0] neg_lo:[0,1] neg_hi:[0,1]\n"
    "v_pk_add_f32 v[56:57], v[56:57], v[20:21] op_sel_hi:[1,0] neg_lo:[0,1] neg_hi:[0,1]\n"
    "v_pk_add_f32 v[58:59], v[58:59], v[20:21] op_sel_hi:[1,0] neg_lo:[0,1] neg_hi:[0,1]\n"
    "v_pk_add_f32 v[60:61], v[60:61], v[20:21] op_sel_hi:[1,0] neg_lo:[0,1] neg_hi:[0,1]\n"
    "v_pk_add_f32 v[62:63], v[62:63], v[20:21] op_sel_hi:[1,0] neg_lo:[0,1] neg_hi:[0,1]\n"
    "v_pk_add_f32 v[240:241], v[240:241], v[20:21] op_sel_hi:[1,0] neg_lo:[0,1] neg_hi:[0,1]\n"
    "v_pk_add_f32 v[242:243], v[242:243], v[20:21] op_sel_hi:[1,0] neg_lo:[0,1] neg_hi:[0,1]\n"
    "v_pk_add_f32 v[244:245], v[244:245], v[20:21] op_sel_hi:[1,0] neg_lo:[0,1] neg_hi:[0,1]\n"
    "v_pk_add_f32 v[246:247], v[246:247], v[20:21] op_sel_hi:[1,0] neg_lo:[0,1] neg_hi:[0,1]\n"
    "v_pk_add_f32 v[248:249], v[248:249], v[20:21] op_sel_hi:[1,0] neg_lo:[0,1] neg_hi:[0,1]\n"
    "v_pk_add_f32 v[250:251], v[250:251], v[20:21] op_sel_hi:[1,0] neg_lo:[0,1] neg_hi:[0,1]\n"
    "v_pk_add_f32 v[252:253], v[252:253], v[20:21] op_sel_hi:[1,0] neg_lo:[0,1] neg_hi:[0,1]\n"
    "v_pk_add_f32 v[254:255], v[254:255], v[20:21] op_sel_hi:[1,0] neg_lo:[0,1] neg_hi:[0,1]\n"
    "v_exp_f32 v48, v48\n"
    "v_exp_f32 v49, v49\n"
    "v_exp_f32 v50, v50\n"
    "v_exp_f32 v51, v51\n"
    "v_exp_f32 v52, v52\n"
    "v_exp_f32 v53, v53\n"
    "v_exp_f32 v54, v54\n"
    "v_exp_f32 v55, v55\n"
    "v_exp_f32 v56, v56\n"
    "v_exp_f32 v57, v57\n"
    "v_exp_f32 v58, v58\n"
    "v_exp_f32 v59, v59\n"
    "v_exp_f32 v60, v60\n"
    "v_exp_f32 v61, v61\n"
    "v_exp_f32 v62, v62\n"
    "v_exp_f32 v63, v63\n"
    "v_pk_mul_f32 v[24:25], v[24:25], v[22:23] op_sel_hi:[1,0]\n"
    "v_pk_mul_f32 v[26:27], v[26:27], v[22:23] op_sel_hi:[1,0]\n"
    "v_pk_mul_f32 v[28:29], v[28:29], v[22:23] op_sel_hi:[1,0]\n"
    "v_pk_mul_f32 v[30:31], v[30:31], v[22:23] op_sel_hi:[1,0]\n"
    "v_pk_mul_f32 v[32:33], v[32:33], v[22:23] op_sel_hi:[1,0]\n"
    "v_pk_mul_f32 v[34:35], v[34:35], v[22:23] op_sel_hi:[1,0]\n"
    "v_pk_mul_f32 v[36:37], v[36:37], v[22:23] op_sel_hi:[1,0]\n"
    "v_pk_mul_f32 v[38:39], v[38:39], v[22:23] op_sel_hi:[1,0]\n"
    "v_pk_mul_f32 v[40:41], v[40:41], v[22:23] op_sel_hi:[1,0]\n"
    "v_pk_mul_f32 v[42:43], v[42:43], v[22:23] op_sel_hi:[1,0]\n"
    "v_cvt_pk_bf16_f32 v88, v48, v49\n"
    "v_cvt_pk_bf16_f32 v89, v50, v51\n"
    "v_cvt_pk_bf16_f32 v90, v52, v53\n"
    "v_cvt_pk_bf16_f32 v91, v54, v55\n"
    "v_cvt_pk_bf16_f32 v92, v56, v57\n"
    "v_cvt_pk_bf16_f32 v93, v58, v59\n"
    "v_cvt_pk_bf16_f32 v94, v60, v61\n"
    "v_cvt_pk_bf16_f32 v95, v62, v63\n"
    "v_accvgpr_write_b32 a112, v24\n"
    "v_accvgpr_write_b32 a113, v25\n"
    "v_accvgpr_write_b32 a114, v26\n"
    "v_accvgpr_write_b32 a115, v27\n"
    "v_accvgpr_write_b32 a116, v28\n"
    "v_accvgpr_write_b32 a117, v29\n"
    "v_accvgpr_write_b32 a118, v30\n"
    "v_accvgpr_write_b32 a119, v31\n"
    "v_accvgpr_write_b32 a120, v32\n"
    "v_accvgpr_write_b32 a121, v33\n"
    "v_accvgpr_write_b32 a122, v34\n"
    "v_accvgpr_write_b32 a123, v35\n"
    "v_accvgpr_write_b32 a124, v36\n"
    "v_accvgpr_write_b32 a125, v37\n"
    "v_accvgpr_write_b32 a126, v38\n"
    "v_accvgpr_write_b32 a127, v39\n"
    "v_accvgpr_write_b32 a220, v40\n"
    "v_accvgpr_write_b32 a221, v41\n"
    "v_accvgpr_write_b32 a222, v42\n"
    "v_accvgpr_write_b32 a223, v43\n"
    "s_branch .Lat_back0_%=\n"
    ".Lat_rare1_%=:\n"
    "v_mfma_f32_32x32x16_bf16 v[48:63], a[224:227], a[128:131], v[128:143]\n"
    "v_mfma_f32_32x32x16_bf16 v[48:63], a[228:231], a[132:135], v[48:63]\n"
    "v_accvgpr_read_b32 v24, a0\n"
    "v_accvgpr_read_b32 v25, a1\n"
    "v_accvgpr_read_b32 v26, a2\n"
    "v_accvgpr_read_b32 v27, a3\n"
    "v_accvgpr_read_b32 v28, a4\n"
    "v_accvgpr_read_b32 v29, a5\n"
    "v_accvgpr_read_b32 v30, a6\n"
    "v_accvgpr_read_b32 v31, a7\n"
    "v_accvgpr_read_b32 v32, a8\n"
    "v_accvgpr_read_b32 v33, a9\n"
    "v_accvgpr_read_b32 v34, a10\n"
    "v_accvgpr_read_b32 v35, a11\n"
    "v_accvgpr_read_b32 v36, a12\n"
    "v_accvgpr_read_b32 v37, a13\n"
    "v_accvgpr_read_b32 v38, a14\n"
    "v_accvgpr_read_b32 v39, a15\n"
    "v_accvgpr_read_b32 v40, a192\n"
    "v_accvgpr_read_b32 v41, a193\n"
    "v_accvgpr_read_b32 v42, a194\n"
    "v_accvgpr_read_b32 v43, a195\n"
    "v_max3_f32 v20, v48, v49, v50\n"
    "v_max3_f32 v20, v20, v51, v52\n"
    "v_max3_f32 v20, v20, v53, v54\n"
    "v_max3_f32 v20, v20, v55, v56\n"
    "v_max3_f32 v20, v20, v57, v58\n"
    "v_max3_f32 v20, v20, v59, v60\n"
    "v_max3_f32 v20, v20, v61, v62\n"
    "v_max_f32 v20, v20, v63\n"
    "v_mov_b32 v21, v20\n"
    "s_nop 1\n"
    "v_permlane32_swap_b32 v20, v21\n"
    "v_max_f32 v20, v20, v21\n"
    "v_add_f32 v20, 1109393408, v20\n"
    "v_max_f32 v20, 0, v20\n"
    "v_exp_f32 v22, -v20\n"
    "v_pk_add_f32 v[48:49], v[48:49], v[20:21] op_sel_hi:[1,0] neg_lo:[0,1] neg_hi:[0,1]\n"
    "v_pk_add_f32 v[50:51], v[50:51], v[20:21] op_sel_hi:[1,0] neg_lo:[0,1] neg_hi:[0,1]\n"
    "v_pk_add_f32 v[52:53], v[52:53], v[20:21] op_sel_hi:[1,0] neg_lo:[0,1] neg_hi:[0,1]\n"
    "v_pk_add_f32 v[54:55], v[54:55], v[20:21] op_sel_hi:[1,0] neg_lo:[0,1] neg_hi:[0,1]\n"
    "v_pk_add_f32 v[56:57], v[56:57], v[20:21] op_sel_hi:[1,0] neg_lo:[0,1] neg_hi:[0,1]\n"
    "v_pk_add_f32 v[58:59], v[58:59], v[20:21] op_sel_hi:[1,0] neg_lo:[0,1] neg_hi:[0,1]\n"
    "v_pk_add_f32 v[60:61], v[60:61], v[20:21] op_sel_hi:[1,0] neg_lo:[0,1] neg_hi:[0,1]\n"
    "v_pk_add_f32 v[62:63], v[62:63], v[20:21] op_sel_hi:[1,0] neg_lo:[0,1] neg_hi:[0,1]\n"
    "v_pk_add_f32 v[128:129], v[128:129], v[20:21] op_sel_hi:[1,0] neg_lo:[0,1] neg_hi:[0,1]\n"
    "v_pk_add_f32 v[130:131], v[130:131], v[20:21] op_sel_hi:[1,0] neg_lo:[0,1] neg_hi:[0,1]\n"
    "v_pk_add_f32 v[132:133], v[132:133], v[20:21] op_sel_hi:[1,0] neg_lo:[0,1] neg_hi:[0,1]\n"
    "v_pk_add_f32 v[134:135], v[134:135], v[20:21] op_sel_hi:[1,0] neg_lo:[0,1] neg_hi:[0,1]\n"
    "v_pk_add_f32 v[136:137], v[136:137], v[20:21] op_sel_hi:[1,0] neg_lo:[0,1] neg_hi:[0,1]\n"
    "v_pk_add_f32 v[138:139], v[138:139], v[20:21] op_sel_hi:[1,0] neg_lo:[0,1] neg_hi:[0,1]\n"
    "v_pk_add_f32 v[140:141], v[140:141], v[20:21] op_sel_hi:[1,0] neg_lo:[0,1] neg_hi:[0,1]\n"
    "v_pk_add_f32 v[142:143], v[142:143], v[20:21] op_sel_hi:[1,0] neg_lo:[0,1] neg_hi:[0,1]\n"
    "v_exp_f32 v48, v48\n"
    "v_exp_f32 v49, v49\n"
    "v_exp_f32 v50, v50\n"
    "v_exp_f32 v51, v51\n"
    "v_exp_f32 v52, v52\n"
    "v_exp_f32 v53, v53\n"
    "v_exp_f32 v54, v54\n"
    "v_exp_f32 v55, v55\n"
    "v_exp_f32 v56, v56\n"
    "v_exp_f32 v57, v57\n"
    "v_exp_f32 v58, v58\n"
    "v_exp_f32 v59, v59\n"
    "v_exp_f32 v60, v60\n"
    "v_exp_f32 v61, v61\n"
    "v_exp_f32 v62, v62\n"
    "v_exp_f32 v63, v63\n"
    "v_pk_mul_f32 v[24:25], v[24:25], v[22:23] op_sel_hi:[1,0]\n"
    "v_pk_mul_f32 v[26:27], v[26:27], v[22:23] op_sel_hi:[1,0]\n"
    "v_pk_mul_f32 v[28:29], v[28:29], v[22:23] op_sel_hi:[1,0]\n"
    "v_pk_mul_f32 v[30:31], v[30:31], v[22:23] op_sel_hi:[1,0]\n"
    "v_pk_mul_f32 v[32:33], v[32:33], v[22:23] op_sel_hi:[1,0]\n"
    "v_pk_mul_f32 v[34:35], v[34:35], v[22:23] op_sel_hi:[1,0]\n"
    "v_pk_mul_f32 v[36:37], v[36:37], v[22:23] op_sel_hi:[1,0]\n"
    "v_pk_mul_f32 v[38:39], v[38:39], v[22:23] op_sel_hi:[1,0]\n"
    "v_pk_mul_f32 v[40:41], v[40:41], v[22:23] op_sel_hi:[1,0]\n"
    "v_pk_mul_f32 v[42:43], v[42:43], v[22:23] op_sel_hi:[1,0]\n"
    "v_cvt_pk_bf16_f32 v80, v48, v49\n"
    "v_cvt_pk_bf16_f32 v81, v50, v51\n"
    "v_cvt_pk_bf16_f32 v82, v52, v53\n"
    "v_cvt_pk_bf16_f32 v83, v54, v55\n"
    "v_cvt_pk_bf16_f32 v84, v56, v57\n"
    "v_cvt_pk_bf16_f32 v85, v58, v59\n"
    "v_cvt_pk_bf16_f32 v86, v60, v61\n"
    "v_cvt_pk_bf16_f32 v87, v62, v63\n"
    "v_accvgpr_write_b32 a0, v24\n"
    "v_accvgpr_write_b32 a1, v25\n"
    "v_accvgpr_write_b32 a2, v26\n"
    "v_accvgpr_write_b32 a3, v27\n"
    "v_accvgpr_write_b32 a4, v28\n"
    "v_accvgpr_write_b32 a5, v29\n"
    "v_accvgpr_write_b32 a6, v30\n"
    "v_accvgpr_write_b32 a7, v31\n"
    "v_accvgpr_write_b32 a8, v32\n"
    "v_accvgpr_write_b32 a9, v33\n"
    "v_accvgpr_write_b32 a10, v34\n"
    "v_accvgpr_write_b32 a11, v35\n"
    "v_accvgpr_write_b32 a12, v36\n"
    "v_accvgpr_write_b32 a13, v37\n"
    "v_accvgpr_write_b32 a14, v38\n"
    "v_accvgpr_write_b32 a15, v39\n"
    "v_accvgpr_write_b32 a192, v40\n"
    "v_accvgpr_write_b32 a193, v41\n"
    "v_accvgpr_write_b32 a194, v42\n"
    "v_accvgpr_write_b32 a195, v43\n"
    "s_branch .Lat_back1_%=\n"
    ".Lat_rare2_%=:\n"
    "v_mfma_f32_32x32x16_bf16 v[48:63], a[224:227], a[136:139], v[144:159]\n"
    "v_mfma_f32_32x32x16_bf16 v[48:63], a[228:231], a[140:143], v[48:63]\n"
    "v_accvgpr_read_b32 v24, a16\n"
    "v_accvgpr_read_b32 v25, a17\n"
    "v_accvgpr_read_b32 v26, a18\n"
    "v_accvgpr_read_b32 v27, a19\n"
    "v_accvgpr_read_b32 v28, a20\n"
    "v_accvgpr_read_b32 v29, a21\n"
    "v_accvgpr_read_b32 v30, a22\n"
    "v_accvgpr_read_b32 v31, a23\n"
    "v_accvgpr_read_b32 v32, a24\n"
    "v_accvgpr_read_b32 v33, a25\n"
    "v_accvgpr_read_b32 v34, a26\n"
    "v_accvgpr_read_b32 v35, a27\n"
    "v_accvgpr_read_b32 v36, a28\n"
    "v_accvgpr_read_b32 v37, a29\n"
    "v_accvgpr_read_b32 v38, a30\n"
    "v_accvgpr_read_b32 v39, a31\n"
    "v_accvgpr_read_b32 v40, a196\n"
    "v_accvgpr_read_b32 v41, a197\n"
    "v_accvgpr_read_b32 v42, a198\n"
    "v_accvgpr_read_b32 v43, a199\n"
    "v_max3_f32 v20, v48, v49, v50\n"
    "v_max3_f32 v20, v20, v51, v52\n"
    "v_max3_f32 v20, v20, v53, v54\n"
    "v_max3_f32 v20, v20, v55, v56\n"
    "v_max3_f32 v20, v20, v57, v58\n"
    "v_max3_f32 v20, v20, v59, v60\n"
    "v_max3_f32 v20, v20, v61, v62\n"
    "v_max_f32 v20, v20, v63\n"
    "v_mov_b32 v21, v20\n"
    "s_nop 1\n"
    "v_permlane32_swap_b32 v20, v21\n"
    "v_max_f32 v20, v20, v21\n"
    "v_add_f32 v20, 1109393408, v20\n"
    "v_max_f32 v20, 0, v20\n"
    "v_exp_f32 v22, -v20\n"
    "v_pk_add_f32 v[48:49], v[48:49], v[20:21] op_sel_hi:[1,0] neg_lo:[0,1] neg_hi:[0,1]\n"
    "v_pk_add_f32 v[50:51], v[50:51], v[20:21] op_sel_hi:[1,0] neg_lo:[0,1] neg_hi:[0,1]\n"
    "v_pk_add_f32 v[52:53], v[52:53], v[20:21] op_sel_hi:[1,0] neg_lo:[0,1] neg_hi:[0,1]\n"
    "v_pk_add_f32 v[54:55], v[54:55], v[20:21] op_sel_hi:[1,0] neg_lo:[0,1] neg_hi:[0,1]\n"
    "v_pk_add_f32 v[56:57], v[56:57], v[20:21] op_sel_hi:[1,0] neg_lo:[0,1] neg_hi:[0,1]\n"
    "v_pk_add_f32 v[58:59], v[58:59], v[20:21] op_sel_hi:[1,0] neg_lo:[0,1] neg_hi:[0,1]\n"
    "v_pk_add_f32 v[60:61], v[60:61], v[20:21] op_sel_hi:[1,0] neg_lo:[0,1] neg_hi:[0,1]\n"
    "v_pk_add_f32 v[62:63], v[62:63], v[20:21] op_sel_hi:[1,0] neg_lo:[0,1] neg_hi:[0,1]\n"
    "v_pk_add_f32 v[144:145], v[144:145], v[20:21] op_sel_hi:[1,0] neg_lo:[0,1] neg_hi:[0,1]\n"
    "v_pk_add_f32 v[146:147], v[146:147], v[20:21] op_sel_hi:[1,0] neg_lo:[0,1] neg_hi:[0,1]\n"
    "v_pk_add_f32 v[148:149], v[148:149], v[20:21] op_sel_hi:[1,0] neg_lo:[0,1] neg_hi:[0,1]\n"
    "v_pk_add_f32 v[150:151], v[150:151], v[20:21] op_sel_hi:[1,0] neg_lo:[0,1] neg_hi:[0,1]\n"
    "v_pk_add_f32 v[152:153], v[152:153], v[20:21] op_sel_hi:[1,0] neg_lo:[0,1] neg_hi:[0,1]\n"
    "v_pk_add_f32 v[154:155], v[154:155], v[20:21] op_sel_hi:[1,0] neg_lo:[0,1] neg_hi:[0,1]\n"
    "v_pk_add_f32 v[156:157], v[156:157], v[20:21] op_sel_hi:[1,0] neg_lo:[0,1] neg_hi:[0,1]\n"
    "v_pk_add_f32 v[158:159], v[158:159], v[20:21] op_sel_hi:[1,0] neg_lo:[0,1] neg_hi:[0,1]\n"
    "v_exp_f32 v48, v48\n"
    "v_exp_f32 v49, v49\n"
    "v_exp_f32 v50, v50\n"
    "v_exp_f32 v51, v51\n"
    "v_exp_f32 v52, v52\n"
    "v_exp_f32 v53, v53\n"
    "v_exp_f32 v54, v54\n"
    "v_exp_f32 v55, v55\n"
    "v_exp_f32 v56, v56\n"
    "v_exp_f32 v57, v57\n"
    "v_exp_f32 v58, v58\n"
    "v_exp_f32 v59, v59\n"
    "v_exp_f32 v60, v60\n"
    "v_exp_f32 v61, v61\n"
    "v_exp_f32 v62, v62\n"
    "v_exp_f32 v63, v63\n"
    "v_pk_mul_f32 v[24:25], v[24:25], v[22:23] op_sel_hi:[1,0]\n"
    "v_pk_mul_f32 v[26:27], v[26:27], v[22:23] op_sel_hi:[1,0]\n"
    "v_pk_mul_f32 v[28:29], v[28:29], v[22:23] op_sel_hi:[1,0]\n"
    "v_pk_mul_f32 v[30:31], v[30:31], v[22:23] op_sel_hi:[1,0]\n"
    "v_pk_mul_f32 v[32:33], v[32:33], v[22:23] op_sel_hi:[1,0]\n"
    "v_pk_mul_f32 v[34:35], v[34:35], v[22:23] op_sel_hi:[1,0]\n"
    "v_pk_mul_f32 v[36:37], v[36:37], v[22:23] op_sel_hi:[1,0]\n"
    "v_pk_mul_f32 v[38:39], v[38:39], v[22:23] op_sel_hi:[1,0]\n"
    "v_pk_mul_f32 v[40:41], v[40:41], v[22:23] op_sel_hi:[1,0]\n"
    "v_pk_mul_f32 v[42:43], v[42:43], v[22:23] op_sel_hi:[1,0]\n"
    "v_cvt_pk_bf16_f32 v88, v48, v49\n"
    "v_cvt_pk_bf16_f32 v89, v50, v51\n"
    "v_cvt_pk_bf16_f32 v90, v52, v53\n"
    "v_cvt_pk_bf16_f32 v91, v54, v55\n"
    "v_cvt_pk_bf16_f32 v92, v56, v57\n"
    "v_cvt_pk_bf16_f32 v93, v58, v59\n"
    "v_cvt_pk_bf16_f32 v94, v60, v61\n"
    "v_cvt_pk_bf16_f32 v95, v62, v63\n"
    "v_accvgpr_write_b32 a16, v24\n"
    "v_accvgpr_write_b32 a17, v25\n"
    "v_accvgpr_write_b32 a18, v26\n"
    "v_accvgpr_write_b32 a19, v27\n"
    "v_accvgpr_write_b32 a20, v28\n"
    "v_accvgpr_write_b32 a21, v29\n"
    "v_accvgpr_write_b32 a22, v30\n"
    "v_accvgpr_write_b32 a23, v31\n"
    "v_accvgpr_write_b32 a24, v32\n"
    "v_accvgpr_write_b32 a25, v33\n"
    "v_accvgpr_write_b32 a26, v34\n"
    "v_accvgpr_write_b32 a27, v35\n"
    "v_accvgpr_write_b32 a28, v36\n"
    "v_accvgpr_write_b32 a29, v37\n"
    "v_accvgpr_write_b32 a30, v38\n"
    "v_accvgpr_write_b32 a31, v39\n"
    "v_accvgpr_write_b32 a196, v40\n"
    "v_accvgpr_write_b32 a197, v41\n"
    "v_accvgpr_write_b32 a198, v42\n"
    "v_accvgpr_write_b32 a199, v43\n"
    "s_branch .Lat_back2_%=\n"
    ".Lat_rare3_%=:\n"
    "v_mfma_f32_32x32x16_bf16 v[48:63], a[224:227], a[144:147], v[160:175]\n"
    "v_mfma_f32_32x32x16_bf16 v[48:63], a[228:231], a[148:151], v[48:63]\n"
    "v_accvgpr_read_b32 v24, a32\n"
    "v_accvgpr_read_b32 v25, a33\n"
    "v_accvgpr_read_b32 v26, a34\n"
    "v_accvgpr_read_b32 v27, a35\n"
    "v_accvgpr_read_b32 v28, a36\n"
    "v_accvgpr_read_b32 v29, a37\n"
    "v_accvgpr_read_b32 v30, a38\n"
    "v_accvgpr_read_b32 v31, a39\n"
    "v_accvgpr_read_b32 v32, a40\n"
    "v_accvgpr_read_b32 v33, a41\n"
    "v_accvgpr_read_b32 v34, a42\n"
    "v_accvgpr_read_b32 v35, a43\n"
    "v_accvgpr_read_b32 v36, a44\n"
    "v_accvgpr_read_b32 v37, a45\n"
    "v_accvgpr_read_b32 v38, a46\n"
    "v_accvgpr_read_b32 v39, a47\n"
    "v_accvgpr_read_b32 v40, a200\n"
    "v_accvgpr_read_b32 v41, a201\n"
    "v_accvgpr_read_b32 v42, a202\n"
    "v_accvgpr_read_b32 v43, a203\n"
    "v_max3_f32 v20, v48, v49, v50\n"
    "v_max3_f32 v20, v20, v51, v52\n"
    "v_max3_f32 v20, v20, v53, v54\n"
    "v_max3_f32 v20, v20, v55, v56\n"
    "v_max3_f32 v20, v20, v57, v58\n"
    "v_max3_f32 v20, v20, v59, v60\n"
    "v_max3_f32 v20, v20, v61, v62\n"
    "v_max_f32 v20, v20, v63\n"
    "v_mov_b32 v21, v20\n"
    "s_nop 1\n"
    "v_permlane32_swap_b32 v20, v21\n"
    "v_max_f32 v20, v20, v21\n"
    "v_add_f32 v20, 1109393408, v20\n"
    "v_max_f32 v20, 0, v20\n"
    "v_exp_f32 v22, -v20\n"
    "v_pk_add_f32 v[48:49], v[48:49], v[20:21] op_sel_hi:[1,0] neg_lo:[0,1] neg_hi:[0,1]\n"
    "v_pk_add_f32 v[50:51], v[50:51], v[20:21] op_sel_hi:[1,0] neg_lo:[0,1] neg_hi:[0,1]\n"
    "v_pk_add_f32 v[52:53], v[52:53], v[20:21] op_sel_hi:[1,0] neg_lo:[0,1] neg_hi:[0,1]\n"
    "v_pk_add_f32 v[54:55], v[54:55], v[20:21] op_sel_hi:[1,0] neg_lo:[0,1] neg_hi:[0,1]\n"
    "v_pk_add_f32 v[56:57], v[56:57], v[20:21] op_sel_hi:[1,0] neg_lo:[0,1] neg_hi:[0,1]\n"
    "v_pk_add_f32 v[58:59], v[58:59], v[20:21] op_sel_hi:[1,0] neg_lo:[0,1] neg_hi:[0,1]\n"
    "v_pk_add_f32 v[60:61], v[60:61], v[20:21] op_sel_hi:[1,0] neg_lo:[0,1] neg_hi:[0,1]\n"
    "v_pk_add_f32 v[62:63], v[62:63], v[20:21] op_sel_hi:[1,0] neg_lo:[0,1] neg_hi:[0,1]\n"
    "v_pk_add_f32 v[160:161], v[160:161], v[20:21] op_sel_hi:[1,0] neg_lo:[0,1] neg_hi:[0,1]\n"
    "v_pk_add_f32 v[162:163], v[162:163], v[20:21] op_sel_hi:[1,0] neg_lo:[0,1] neg_hi:[0,1]\n"
    "v_pk_add_f32 v[164:165], v[164:165], v[20:21] op_sel_hi:[1,0] neg_lo:[0,1] neg_hi:[0,1]\n"
    "v_pk_add_f32 v[166:167], v[166:167], v[20:21] op_sel_hi:[1,0] neg_lo:[0,1] neg_hi:[0,1]\n"
    "v_pk_add_f32 v[168:169], v[168:169], v[20:21] op_sel_hi:[1,0] neg_lo:[0,1] neg_hi:[0,1]\n"
    "v_pk_add_f32 v[170:171], v[170:171], v[20:21] op_sel_hi:[1,0] neg_lo:[0,1] neg_hi:[0,1]\n"
    "v_pk_add_f32 v[172:173], v[172:173], v[20:21] op_sel_hi:[1,0] neg_lo:[0,1] neg_hi:[0,1]\n"
    "v_pk_add_f32 v[174:175], v[174:175], v[20:21] op_sel_hi:[1,0] neg_lo:[0,1] neg_hi:[0,1]\n"
    "v_exp_f32 v48, v48\n"
    "v_exp_f32 v49, v49\n"
    "v_exp_f32 v50, v50\n"
    "v_exp_f32 v51, v51\n"
    "v_exp_f32 v52, v52\n"
    "v_exp_f32 v53, v53\n"
    "v_exp_f32 v54, v54\n"
    "v_exp_f32 v55, v55\n"
    "v_exp_f32 v56, v56\n"
    "v_exp_f32 v57, v57\n"
    "v_exp_f32 v58, v58\n"
    "v_exp_f32 v59, v59\n"
    "v_exp_f32 v60, v60\n"
    "v_exp_f32 v61, v61\n"
    "v_exp_f32 v62, v62\n"
    "v_exp_f32 v63, v63\n"
    "v_pk_mul_f32 v[24:25], v[24:25], v[22:23] op_sel_hi:[1,0]\n"
    "v_pk_mul_f32 v[26:27], v[26:27], v[22:23] op_sel_hi:[1,0]\n"
    "v_pk_mul_f32 v[28:29], v[28:29], v[22:23] op_sel_hi:[1,0]\n"
    "v_pk_mul_f32 v[30:31], v[30:31], v[22:23] op_sel_hi:[1,0]\n"
    "v_pk_mul_f32 v[32:33], v[32:33], v[22:23] op_sel_hi:[1,0]\n"
    "v_pk_mul_f32 v[34:35], v[34:35], v[22:23] op_sel_hi:[1,0]\n"
    "v_pk_mul_f32 v[36:37], v[36:37], v[22:23] op_sel_hi:[1,0]\n"
    "v_pk_mul_f32 v[38:39], v[38:39], v[22:23] op_sel_hi:[1,0]\n"
    "v_pk_mul_f32 v[40:41], v[40:41], v[22:23] op_sel_hi:[1,0]\n"
    "v_pk_mul_f32 v[42:43], v[42:43], v[22:23] op_sel_hi:[1,0]\n"
    "v_cvt_pk_bf16_f32 v80, v48, v49\n"
    "v_cvt_pk_bf16_f32 v81, v50, v51\n"
    "v_cvt_pk_bf16_f32 v82, v52, v53\n"
    "v_cvt_pk_bf16_f32 v83, v54, v55\n"
    "v_cvt_pk_bf16_f32 v84, v56, v57\n"
    "v_cvt_pk_bf16_f32 v85, v58, v59\n"
    "v_cvt_pk_bf16_f32 v86, v60, v61\n"
    "v_cvt_pk_bf16_f32 v87, v62, v63\n"
    "v_accvgpr_write_b32 a32, v24\n"
    "v_accvgpr_write_b32 a33, v25\n"
    "v_accvgpr_write_b32 a34, v26\n"
    "v_accvgpr_write_b32 a35, v27\n"
    "v_accvgpr_write_b32 a36, v28\n"
    "v_accvgpr_write_b32 a37, v29\n"
    "v_accvgpr_write_b32 a38, v30\n"
    "v_accvgpr_write_b32 a39, v31\n"
    "v_accvgpr_write_b32 a40, v32\n"
    "v_accvgpr_write_b32 a41, v33\n"
    "v_accvgpr_write_b32 a42, v34\n"
    "v_accvgpr_write_b32 a43, v35\n"
    "v_accvgpr_write_b32 a44, v36\n"
    "v_accvgpr_write_b32 a45, v37\n"
    "v_accvgpr_write_b32 a46, v38\n"
    "v_accvgpr_write_b32 a47, v39\n"
    "v_accvgpr_write_b32 a200, v40\n"
    "v_accvgpr_write_b32 a201, v41\n"
    "v_accvgpr_write_b32 a202, v42\n"
    "v_accvgpr_write_b32 a203, v43\n"
    "s_branch .Lat_back3_%=\n"
    ".Lat_rare4_%=:\n"
    "v_mfma_f32_32x32x16_bf16 v[48:63], a[224:227], a[152:155], v[176:191]\n"
    "v_mfma_f32_32x32x16_bf16 v[48:63], a[228:231], a[156:159], v[48:63]\n"
    "v_accvgpr_read_b32 v24, a48\n"
    "v_accvgpr_read_b32 v25, a49\n"
    "v_accvgpr_read_b32 v26, a50\n"
    "v_accvgpr_read_b32 v27, a51\n"
    "v_accvgpr_read_b32 v28, a52\n"
    "v_accvgpr_read_b32 v29, a53\n"
    "v_accvgpr_read_b32 v30, a54\n"
    "v_accvgpr_read_b32 v31, a55\n"
    "v_accvgpr_read_b32 v32, a56\n"
    "v_accvgpr_read_b32 v33, a57\n"
    "v_accvgpr_read_b32 v34, a58\n"
    "v_accvgpr_read_b32 v35, a59\n"
    "v_accvgpr_read_b32 v36, a60\n"
    "v_accvgpr_read_b32 v37, a61\n"
    "v_accvgpr_read_b32 v38, a62\n"
    "v_accvgpr_read_b32 v39, a63\n"
    "v_accvgpr_read_b32 v40, a204\n"
    "v_accvgpr_read_b32 v41, a205\n"
    "v_accvgpr_read_b32 v42, a206\n"
    "v_accvgpr_read_b32 v43, a207\n"
    "v_max3_f32 v20, v48, v49, v50\n"
    "v_max3_f32 v20, v20, v51, v52\n"
    "v_max3_f32 v20, v20, v53, v54\n"
    "v_max3_f32 v20, v20, v55, v56\n"
    "v_max3_f32 v20, v20, v57, v58\n"
    "v_max3_f32 v20, v20, v59, v60\n"
    "v_max3_f32 v20, v20, v61, v62\n"
    "v_max_f32 v20, v20, v63\n"
    "v_mov_b32 v21, v20\n"
    "s_nop 1\n"
    "v_permlane32_swap_b32 v20, v21\n"
    "v_max_f32 v20, v20, v21\n"
    "v_add_f32 v20, 1109393408, v20\n"
    "v_max_f32 v20, 0, v20\n"
    "v_exp_f32 v22, -v20\n"
    "v_pk_add_f32 v[48:49], v[48:49], v[20:21] op_sel_hi:[1,0] neg_lo:[0,1] neg_hi:[0,1]\n"
    "v_pk_add_f32 v[50:51], v[50:51], v[20:21] op_sel_hi:[1,0] neg_lo:[0,1] neg_hi:[0,1]\n"
    "v_pk_add_f32 v[52:53], v[52:53], v[20:21] op_sel_hi:[1,0] neg_lo:[0,1] neg_hi:[0,1]\n"
    "v_pk_add_f32 v[54:55], v[54:55], v[20:21] op_sel_hi:[1,0] neg_lo:[0,1] neg_hi:[0,1]\n"
    "v_pk_add_f32 v[56:57], v[56:57], v[20:21] op_sel_hi:[1,0] neg_lo:[0,1] neg_hi:[0,1]\n"
    "v_pk_add_f32 v[58:59], v[58:59], v[20:21] op_sel_hi:[1,0] neg_lo:[0,1] neg_hi:[0,1]\n"
    "v_pk_add_f32 v[60:61], v[60:61], v[20:21] op_sel_hi:[1,0] neg_lo:[0,1] neg_hi:[0,1]\n"
    "v_pk_add_f32 v[62:63], v[62:63], v[20:21] op_sel_hi:[1,0] neg_lo:[0,1] neg_hi:[0,1]\n"
    "v_pk_add_f32 v[176:177], v[176:177], v[20:21] op_sel_hi:[1,0] neg_lo:[0,1] neg_hi:[0,1]\n"
    "v_pk_add_f32 v[178:179], v[178:179], v[20:21] op_sel_hi:[1,0] neg_lo:[0,1] neg_hi:[0,1]\n"
    "v_pk_add_f32 v[180:181], v[180:181], v[20:21] op_sel_hi:[1,0] neg_lo:[0,1] neg_hi:[0,1]\n"
    "v_pk_add_f32 v[182:183], v[182:183], v[20:21] op_sel_hi:[1,0] neg_lo:[0,1] neg_hi:[0,1]\n"
    "v_pk_add_f32 v[184:185], v[184:185], v[20:21] op_sel_hi:[1,0] neg_lo:[0,1] neg_hi:[0,1]\n"
    "v_pk_add_f32 v[186:187], v[186:187], v[20:21] op_sel_hi:[1,0] neg_lo:[0,1] neg_hi:[0,1]\n"
    "v_pk_add_f32 v[188:189], v[188:189], v[20:21] op_sel_hi:[1,0] neg_lo:[0,1] neg_hi:[0,1]\n"
    "v_pk_add_f32 v[190:191], v[190:191], v[20:21] op_sel_hi:[1,0] neg_lo:[0,1] neg_hi:[0,1]\n"
    "v_exp_f32 v48, v48\n"
    "v_exp_f32 v49, v49\n"
    "v_exp_f32 v50, v50\n"
    "v_exp_f32 v51, v51\n"
    "v_exp_f32 v52, v52\n"
    "v_exp_f32 v53, v53\n"
    "v_exp_f32 v54, v54\n"
    "v_exp_f32 v55, v55\n"
    "v_exp_f32 v56, v56\n"
    "v_exp_f32 v57, v57\n"
    "v_exp_f32 v58, v58\n"
    "v_exp_f32 v59, v59\n"
    "v_exp_f32 v60, v60\n"
    "v_exp_f32 v61, v61\n"
    "v_exp_f32 v62, v62\n"
    "v_exp_f32 v63, v63\n"
    "v_pk_mul_f32 v[24:25], v[24:25], v[22:23] op_sel_hi:[1,0]\n"
    "v_pk_mul_f32 v[26:27], v[26:27], v[22:23] op_sel_hi:[1,0]\n"
    "v_pk_mul_f32 v[28:29], v[28:29], v[22:23] op_sel_hi:[1,0]\n"
    "v_pk_mul_f32 v[30:31], v[30:31], v[22:23] op_sel_hi:[1,0]\n"
    "v_pk_mul_f32 v[32:33], v[32:33], v[22:23] op_sel_hi:[1,0]\n"
    "v_pk_mul_f32 v[34:35], v[34:35], v[22:23] op_sel_hi:[1,0]\n"
    "v_pk_mul_f32 v[36:37], v[36:37], v[22:23] op_sel_hi:[1,0]\n"
    "v_pk_mul_f32 v[38:39], v[38:39], v[22:23] op_sel_hi:[1,0]\n"
    "v_pk_mul_f32 v[40:41], v[40:41], v[22:23] op_sel_hi:[1,0]\n"
    "v_pk_mul_f32 v[42:43], v[42:43], v[22:23] op_sel_hi:[1,0]\n"
    "v_cvt_pk_bf16_f32 v88, v48, v49\n"
    "v_cvt_pk_bf16_f32 v89, v50, v51\n"
    "v_cvt_pk_bf16_f32 v90, v52, v53\n"
    "v_cvt_pk_bf16_f32 v91, v54, v55\n"
    "v_cvt_pk_bf16_f32 v92, v56, v57\n"
    "v_cvt_pk_bf16_f32 v93, v58, v59\n"
    "v_cvt_pk_bf16_f32 v94, v60, v61\n"
    "v_cvt_pk_bf16_f32 v95, v62, v63\n"
    "v_accvgpr_write_b32 a48, v24\n"
    "v_accvgpr_write_b32 a49, v25\n"
    "v_accvgpr_write_b32 a50, v26\n"
    "v_accvgpr_write_b32 a51, v27\n"
    "v_accvgpr_write_b32 a52, v28\n"
    "v_accvgpr_write_b32 a53, v29\n"
    "v_accvgpr_write_b32 a54, v30\n"
    "v_accvgpr_write_b32 a55, v31\n"
    "v_accvgpr_write_b32 a56, v32\n"
    "v_accvgpr_write_b32 a57, v33\n"
    "v_accvgpr_write_b32 a58, v34\n"
    "v_accvgpr_write_b32 a59, v35\n"
    "v_accvgpr_write_b32 a60, v36\n"
    "v_accvgpr_write_b32 a61, v37\n"
    "v_accvgpr_write_b32 a62, v38\n"
    "v_accvgpr_write_b32 a63, v39\n"
    "v_accvgpr_write_b32 a204, v40\n"
    "v_accvgpr_write_b32 a205, v41\n"
    "v_accvgpr_write_b32 a206, v42\n"
    "v_accvgpr_write_b32 a207, v43\n"
    "s_branch .Lat_back4_%=\n"
    ".Lat_rare5_%=:\n"
    "v_mfma_f32_32x32x16_bf16 v[48:63], a[224:227], a[160:163], v[192:207]\n"
    "v_mfma_f32_32x32x16_bf16 v[48:63], a[228:231], a[164:167], v[48:63]\n"
    "v_accvgpr_read_b32 v24, a64\n"
    "v_accvgpr_read_b32 v25, a65\n"
    "v_accvgpr_read_b32 v26, a66\n"
    "v_accvgpr_read_b32 v27, a67\n"
    "v_accvgpr_read_b32 v28, a68\n"
    "v_accvgpr_read_b32 v29, a69\n"
    "v_accvgpr_read_b32 v30, a70\n"
    "v_accvgpr_read_b32 v31, a71\n"
    "v_accvgpr_read_b32 v32, a72\n"
    "v_accvgpr_read_b32 v33, a73\n"
    "v_accvgpr_read_b32 v34, a74\n"
    "v_accvgpr_read_b32 v35, a75\n"
    "v_accvgpr_read_b32 v36, a76\n"
    "v_accvgpr_read_b32 v37, a77\n"
    "v_accvgpr_read_b32 v38, a78\n"
    "v_accvgpr_read_b32 v39, a79\n"
    "v_accvgpr_read_b32 v40, a208\n"
    "v_accvgpr_read_b32 v41, a209\n"
    "v_accvgpr_read_b32 v42, a210\n"
    "v_accvgpr_read_b32 v43, a211\n"
    "v_max3_f32 v20, v48, v49, v50\n"
    "v_max3_f32 v20, v20, v51, v52\n"
    "v_max3_f32 v20, v20, v53, v54\n"
    "v_max3_f32 v20, v20, v55, v56\n"
    "v_max3_f32 v20, v20, v57, v58\n"
    "v_max3_f32 v20, v20, v59, v60\n"
    "v_max3_f32 v20, v20, v61, v62\n"
    "v_max_f32 v20, v20, v63\n"
    "v_mov_b32 v21, v20\n"
    "s_nop 1\n"
    "v_permlane32_swap_b32 v20, v21\n"
    "v_max_f32 v20, v20, v21\n"
    "v_add_f32 v20, 1109393408, v20\n"
    "v_max_f32 v20, 0, v20\n"
    "v_exp_f32 v22, -v20\n"
    "v_pk_add_f32 v[48:49], v[48:49], v[20:21] op_sel_hi:[1,0] neg_lo:[0,1] neg_hi:[0,1]\n"
    "v_pk_add_f32 v[50:51], v[50:51], v[20:21] op_sel_hi:[1,0] neg_lo:[0,1] neg_hi:[0,1]\n"
    "v_pk_add_f32 v[52:53], v[52:53], v[20:21] op_sel_hi:[1,0] neg_lo:[0,1] neg_hi:[0,1]\n"
    "v_pk_add_f32 v[54:55], v[54:55], v[20:21] op_sel_hi:[1,0] neg_lo:[0,1] neg_hi:[0,1]\n"
    "v_pk_add_f32 v[56:57], v[56:57], v[20:21] op_sel_hi:[1,0] neg_lo:[0,1] neg_hi:[0,1]\n"
    "v_pk_add_f32 v[58:59], v[58:59], v[20:21] op_sel_hi:[1,0] neg_lo:[0,1] neg_hi:[0,1]\n"
    "v_pk_add_f32 v[60:61], v[60:61], v[20:21] op_sel_hi:[1,0] neg_lo:[0,1] neg_hi:[0,1]\n"
    "v_pk_add_f32 v[62:63], v[62:63], v[20:21] op_sel_hi:[1,0] neg_lo:[0,1] neg_hi:[0,1]\n"
    "v_pk_add_f32 v[192:193], v[192:193], v[20:21] op_sel_hi:[1,0] neg_lo:[0,1] neg_hi:[0,1]\n"
    "v_pk_add_f32 v[194:195], v[194:195], v[20:21] op_sel_hi:[1,0] neg_lo:[0,1] neg_hi:[0,1]\n"
    "v_pk_add_f32 v[196:197], v[196:197], v[20:21] op_sel_hi:[1,0] neg_lo:[0,1] neg_hi:[0,1]\n"
    "v_pk_add_f32 v[198:199], v[198:199], v[20:21] op_sel_hi:[1,0] neg_lo:[0,1] neg_hi:[0,1]\n"
    "v_pk_add_f32 v[200:201], v[200:201], v[20:21] op_sel_hi:[1,0] neg_lo:[0,1] neg_hi:[0,1]\n"
    "v_pk_add_f32 v[202:203], v[202:203], v[20:21] op_sel_hi:[1,0] neg_lo:[0,1] neg_hi:[0,1]\n"
    "v_pk_add_f32 v[204:205], v[204:205], v[20:21] op_sel_hi:[1,0] neg_lo:[0,1] neg_hi:[0,1]\n"
    "v_pk_add_f32 v[206:207], v[206:207], v[20:21] op_sel_hi:[1,0] neg_lo:[0,1] neg_hi:[0,1]\n"
    "v_exp_f32 v48, v48\n"
    "v_exp_f32 v49, v49\n"
    "v_exp_f32 v50, v50\n"
    "v_exp_f32 v51, v51\n"
    "v_exp_f32 v52, v52\n"
    "v_exp_f32 v53, v53\n"
    "v_exp_f32 v54, v54\n"
    "v_exp_f32 v55, v55\n"
    "v_exp_f32 v56, v56\n"
    "v_exp_f32 v57, v57\n"
    "v_exp_f32 v58, v58\n"
    "v_exp_f32 v59, v59\n"
    "v_exp_f32 v60, v60\n"
    "v_exp_f32 v61, v61\n"
    "v_exp_f32 v62, v62\n"
    "v_exp_f32 v63, v63\n"
    "v_pk_mul_f32 v[24:25], v[24:25], v[22:23] op_sel_hi:[1,0]\n"
    "v_pk_mul_f32 v[26:27], v[26:27], v[22:23] op_sel_hi:[1,0]\n"
    "v_pk_mul_f32 v[28:29], v[28:29], v[22:23] op_sel_hi:[1,0]\n"
    "v_pk_mul_f32 v[30:31], v[30:31], v[22:23] op_sel_hi:[1,0]\n"
    "v_pk_mul_f32 v[32:33], v[32:33], v[22:23] op_sel_hi:[1,0]\n"
    "v_pk_mul_f32 v[34:35], v[34:35], v[22:23] op_sel_hi:[1,0]\n"
    "v_pk_mul_f32 v[36:37], v[36:37], v[22:23] op_sel_hi:[1,0]\n"
    "v_pk_mul_f32 v[38:39], v[38:39], v[22:23] op_sel_hi:[1,0]\n"
    "v_pk_mul_f32 v[40:41], v[40:41], v[22:23] op_sel_hi:[1,0]\n"
    "v_pk_mul_f32 v[42:43], v[42:43], v[22:23] op_sel_hi:[1,0]\n"
    "v_cvt_pk_bf16_f32 v80, v48, v49\n"
    "v_cvt_pk_bf16_f32 v81, v50, v51\n"
    "v_cvt_pk_bf16_f32 v82, v52, v53\n"
    "v_cvt_pk_bf16_f32 v83, v54, v55\n"
    "v_cvt_pk_bf16_f32 v84, v56, v57\n"
    "v_cvt_pk_bf16_f32 v85, v58, v59\n"
    "v_cvt_pk_bf16_f32 v86, v60, v61\n"
    "v_cvt_pk_bf16_f32 v87, v62, v63\n"
    "v_accvgpr_write_b32 a64, v24\n"
    "v_accvgpr_write_b32 a65, v25\n"
    "v_accvgpr_write_b32 a66, v26\n"
    "v_accvgpr_write_b32 a67, v27\n"
    "v_accvgpr_write_b32 a68, v28\n"
    "v_accvgpr_write_b32 a69, v29\n"
    "v_accvgpr_write_b32 a70, v30\n"
    "v_accvgpr_write_b32 a71, v31\n"
    "v_accvgpr_write_b32 a72, v32\n"
    "v_accvgpr_write_b32 a73, v33\n"
    "v_accvgpr_write_b32 a74, v34\n"
    "v_accvgpr_write_b32 a75, v35\n"
    "v_accvgpr_write_b32 a76, v36\n"
    "v_accvgpr_write_b32 a77, v37\n"
    "v_accvgpr_write_b32 a78, v38\n"
    "v_accvgpr_write_b32 a79, v39\n"
    "v_accvgpr_write_b32 a208, v40\n"
    "v_accvgpr_write_b32 a209, v41\n"
    "v_accvgpr_write_b32 a210, v42\n"
    "v_accvgpr_write_b32 a211, v43\n"
    "s_branch .Lat_back5_%=\n"
    ".Lat_rare6_%=:\n"
    "v_mfma_f32_32x32x16_bf16 v[48:63], a[224:227], a[168:171], v[208:223]\n"
    "v_mfma_f32_32x32x16_bf16 v[48:63], a[228:231], a[172:175], v[48:63]\n"
    "v_accvgpr_read_b32 v24, a80\n"
    "v_accvgpr_read_b32 v25, a81\n"
    "v_accvgpr_read_b32 v26, a82\n"
    "v_accvgpr_read_b32 v27, a83\n"
    "v_accvgpr_read_b32 v28, a84\n"
    "v_accvgpr_read_b32 v29, a85\n"
    "v_accvgpr_read_b32 v30, a86\n"
    "v_accvgpr_read_b32 v31, a87\n"
    "v_accvgpr_read_b32 v32, a88\n"
    "v_accvgpr_read_b32 v33, a89\n"
    "v_accvgpr_read_b32 v34, a90\n"
    "v_accvgpr_read_b32 v35, a91\n"
    "v_accvgpr_read_b32 v36, a92\n"
    "v_accvgpr_read_b32 v37, a93\n"
    "v_accvgpr_read_b32 v38, a94\n"
    "v_accvgpr_read_b32 v39, a95\n"
    "v_accvgpr_read_b32 v40, a212\n"
    "v_accvgpr_read_b32 v41, a213\n"
    "v_accvgpr_read_b32 v42, a214\n"
    "v_accvgpr_read_b32 v43, a215\n"
    "v_max3_f32 v20, v48, v49, v50\n"
    "v_max3_f32 v20, v20, v51, v52\n"
    "v_max3_f32 v20, v20, v53, v54\n"
    "v_max3_f32 v20, v20, v55, v56\n"
    "v_max3_f32 v20, v20, v57, v58\n"
    "v_max3_f32 v20, v20, v59, v60\n"
    "v_max3_f32 v20, v20, v61, v62\n"
    "v_max_f32 v20, v20, v63\n"
    "v_mov_b32 v21, v20\n"
    "s_nop 1\n"
    "v_permlane32_swap_b32 v20, v21\n"
    "v_max_f32 v20, v20, v21\n"
    "v_add_f32 v20, 1109393408, v20\n"
    "v_max_f32 v20, 0, v20\n"
    "v_exp_f32 v22, -v20\n"
    "v_pk_add_f32 v[48:49], v[48:49], v[20:21] op_sel_hi:[1,0] neg_lo:[0,1] neg_hi:[0,1]\n"
    "v_pk_add_f32 v[50:51], v[50:51], v[20:21] op_sel_hi:[1,0] neg_lo:[0,1] neg_hi:[0,1]\n"
    "v_pk_add_f32 v[52:53], v[52:53], v[20:21] op_sel_hi:[1,0] neg_lo:[0,1] neg_hi:[0,1]\n"
    "v_pk_add_f32 v[54:55], v[54:55], v[20:21] op_sel_hi:[1,0] neg_lo:[0,1] neg_hi:[0,1]\n"
    "v_pk_add_f32 v[56:57], v[56:57], v[20:21] op_sel_hi:[1,0] neg_lo:[0,1] neg_hi:[0,1]\n"
    "v_pk_add_f32 v[58:59], v[58:59], v[20:21] op_sel_hi:[1,0] neg_lo:[0,1] neg_hi:[0,1]\n"
    "v_pk_add_f32 v[60:61], v[60:61], v[20:21] op_sel_hi:[1,0] neg_lo:[0,1] neg_hi:[0,1]\n"
    "v_pk_add_f32 v[62:63], v[62:63], v[20:21] op_sel_hi:[1,0] neg_lo:[0,1] neg_hi:[0,1]\n"
    "v_pk_add_f32 v[208:209], v[208:209], v[20:21] op_sel_hi:[1,0] neg_lo:[0,1] neg_hi:[0,1]\n"
    "v_pk_add_f32 v[210:211], v[210:211], v[20:21] op_sel_hi:[1,0] neg_lo:[0,1] neg_hi:[0,1]\n"
    "v_pk_add_f32 v[212:213], v[212:213], v[20:21] op_sel_hi:[1,0] neg_lo:[0,1] neg_hi:[0,1]\n"
    "v_pk_add_f32 v[214:215], v[214:215], v[20:21] op_sel_hi:[1,0] neg_lo:[0,1] neg_hi:[0,1]\n"
    "v_pk_add_f32 v[216:217], v[216:217], v[20:21] op_sel_hi:[1,0] neg_lo:[0,1] neg_hi:[0,1]\n"
    "v_pk_add_f32 v[218:219], v[218:219], v[20:21] op_sel_hi:[1,0] neg_lo:[0,1] neg_hi:[0,1]\n"
    "v_pk_add_f32 v[220:221], v[220:221], v[20:21] op_sel_hi:[1,0] neg_lo:[0,1] neg_hi:[0,1]\n"
    "v_pk_add_f32 v[222:223], v[222:223], v[20:21] op_sel_hi:[1,0] neg_lo:[0,1] neg_hi:[0,1]\n"
    "v_exp_f32 v48, v48\n"
    "v_exp_f32 v49, v49\n"
    "v_exp_f32 v50, v50\n"
    "v_exp_f32 v51, v51\n"
    "v_exp_f32 v52, v52\n"
    "v_exp_f32 v53, v53\n"
    "v_exp_f32 v54, v54\n"
    "v_exp_f32 v55, v55\n"
    "v_exp_f32 v56, v56\n"
    "v_exp_f32 v57, v57\n"
    "v_exp_f32 v58, v58\n"
    "v_exp_f32 v59, v59\n"
    "v_exp_f32 v60, v60\n"
    "v_exp_f32 v61, v61\n"
    "v_exp_f32 v62, v62\n"
    "v_exp_f32 v63, v63\n"
    "v_pk_mul_f32 v[24:25], v[24:25], v[22:23] op_sel_hi:[1,0]\n"
    "v_pk_mul_f32 v[26:27], v[26:27], v[22:23] op_sel_hi:[1,0]\n"
    "v_pk_mul_f32 v[28:29], v[28:29], v[22:23] op_sel_hi:[1,0]\n"
    "v_pk_mul_f32 v[30:31], v[30:31], v[22:23] op_sel_hi:[1,0]\n"
    "v_pk_mul_f32 v[32:33], v[32:33], v[22:23] op_sel_hi:[1,0]\n"
    "v_pk_mul_f32 v[34:35], v[34:35], v[22:23] op_sel_hi:[1,0]\n"
    "v_pk_mul_f32 v[36:37], v[36:37], v[22:23] op_sel_hi:[1,0]\n"
    "v_pk_mul_f32 v[38:39], v[38:39], v[22:23] op_sel_hi:[1,0]\n"
    "v_pk_mul_f32 v[40:41], v[40:41], v[22:23] op_sel_hi:[1,0]\n"
    "v_pk_mul_f32 v[42:43], v[42:43], v[22:23] op_sel_hi:[1,0]\n"
    "v_cvt_pk_bf16_f32 v88, v48, v49\n"
    "v_cvt_pk_bf16_f32 v89, v50, v51\n"
    "v_cvt_pk_bf16_f32 v90, v52, v53\n"
    "v_cvt_pk_bf16_f32 v91, v54, v55\n"
    "v_cvt_pk_bf16_f32 v92, v56, v57\n"
    "v_cvt_pk_bf16_f32 v93, v58, v59\n"
    "v_cvt_pk_bf16_f32 v94, v60, v61\n"
    "v_cvt_pk_bf16_f32 v95, v62, v63\n"
    "v_accvgpr_write_b32 a80, v24\n"
    "v_accvgpr_write_b32 a81, v25\n"
    "v_accvgpr_write_b32 a82, v26\n"
    "v_accvgpr_write_b32 a83, v27\n"
    "v_accvgpr_write_b32 a84, v28\n"
    "v_accvgpr_write_b32 a85, v29\n"
    "v_accvgpr_write_b32 a86, v30\n"
    "v_accvgpr_write_b32 a87, v31\n"
    "v_accvgpr_write_b32 a88, v32\n"
    "v_accvgpr_write_b32 a89, v33\n"
    "v_accvgpr_write_b32 a90, v34\n"
    "v_accvgpr_write_b32 a91, v35\n"
    "v_accvgpr_write_b32 a92, v36\n"
    "v_accvgpr_write_b32 a93, v37\n"
    "v_accvgpr_write_b32 a94, v38\n"
    "v_accvgpr_write_b32 a95, v39\n"
    "v_accvgpr_write_b32 a212, v40\n"
    "v_accvgpr_write_b32 a213, v41\n"
    "v_accvgpr_write_b32 a214, v42\n"
    "v_accvgpr_write_b32 a215, v43\n"
    "s_branch .Lat_back6_%=\n"
    ".Lat_rare7_%=:\n"
    "v_mfma_f32_32x32x16_bf16 v[48:63], a[224:227], a[176:179], v[224:239]\n"
    "v_mfma_f32_32x32x16_bf16 v[48:63], a[228:231], a[180:183], v[48:63]\n"
    "v_accvgpr_read_b32 v24, a96\n"
    "v_accvgpr_read_b32 v25, a97\n"
    "v_accvgpr_read_b32 v26, a98\n"
    "v_accvgpr_read_b32 v27, a99\n"
    "v_accvgpr_read_b32 v28, a100\n"
    "v_accvgpr_read_b32 v29, a101\n"
    "v_accvgpr_read_b32 v30, a102\n"
    "v_accvgpr_read_b32 v31, a103\n"
    "v_accvgpr_read_b32 v32, a104\n"
    "v_accvgpr_read_b32 v33, a105\n"
    "v_accvgpr_read_b32 v34, a106\n"
    "v_accvgpr_read_b32 v35, a107\n"
    "v_accvgpr_read_b32 v36, a108\n"
    "v_accvgpr_read_b32 v37, a109\n"
    "v_accvgpr_read_b32 v38, a110\n"
    "v_accvgpr_read_b32 v39, a111\n"
    "v_accvgpr_read_b32 v40, a216\n"
    "v_accvgpr_read_b32 v41, a217\n"
    "v_accvgpr_read_b32 v42, a218\n"
    "v_accvgpr_read_b32 v43, a219\n"
    "v_max3_f32 v20, v48, v49, v50\n"
    "v_max3_f32 v20, v20, v51, v52\n"
    "v_max3_f32 v20, v20, v53, v54\n"
    "v_max3_f32 v20, v20, v55, v56\n"
    "v_max3_f32 v20, v20, v57, v58\n"
    "v_max3_f32 v20, v20, v59, v60\n"
    "v_max3_f32 v20, v20, v61, v62\n"
    "v_max_f32 v20, v20, v63\n"
    "v_mov_b32 v21, v20\n"
    "s_nop 1\n"
    "v_permlane32_swap_b32 v20, v21\n"
    "v_max_f32 v20, v20, v21\n"
    "v_add_f32 v20, 1109393408, v20\n"
    "v_max_f32 v20, 0, v20\n"
    "v_exp_f32 v22, -v20\n"
    "v_pk_add_f32 v[48:49], v[48:49], v[20:21] op_sel_hi:[1,0] neg_lo:[0,1] neg_hi:[0,1]\n"
    "v_pk_add_f32 v[50:51], v[50:51], v[20:21] op_sel_hi:[1,0] neg_lo:[0,1] neg_hi:[0,1]\n"
    "v_pk_add_f32 v[52:53], v[52:53], v[20:21] op_sel_hi:[1,0] neg_lo:[0,1] neg_hi:[0,1]\n"
    "v_pk_add_f32 v[54:55], v[54:55], v[20:21] op_sel_hi:[1,0] neg_lo:[0,1] neg_hi:[0,1]\n"
    "v_pk_add_f32 v[56:57], v[56:57], v[20:21] op_sel_hi:[1,0] neg_lo:[0,1] neg_hi:[0,1]\n"
    "v_pk_add_f32 v[58:59], v[58:59], v[20:21] op_sel_hi:[1,0] neg_lo:[0,1] neg_hi:[0,1]\n"
    "v_pk_add_f32 v[60:61], v[60:61], v[20:21] op_sel_hi:[1,0] neg_lo:[0,1] neg_hi:[0,1]\n"
    "v_pk_add_f32 v[62:63], v[62:63], v[20:21] op_sel_hi:[1,0] neg_lo:[0,1] neg_hi:[0,1]\n"
    "v_pk_add_f32 v[224:225], v[224:225], v[20:21] op_sel_hi:[1,0] neg_lo:[0,1] neg_hi:[0,1]\n"
    "v_pk_add_f32 v[226:227], v[226:227], v[20:21] op_sel_hi:[1,0] neg_lo:[0,1] neg_hi:[0,1]\n"
    "v_pk_add_f32 v[228:229], v[228:229], v[20:21] op_sel_hi:[1,0] neg_lo:[0,1] neg_hi:[0,1]\n"
    "v_pk_add_f32 v[230:231], v[230:231], v[20:21] op_sel_hi:[1,0] neg_lo:[0,1] neg_hi:[0,1]\n"
    "v_pk_add_f32 v[232:233], v[232:233], v[20:21] op_sel_hi:[1,0] neg_lo:[0,1] neg_hi:[0,1]\n"
    "v_pk_add_f32 v[234:235], v[234:235], v[20:21] op_sel_hi:[1,0] neg_lo:[0,1] neg_hi:[0,1]\n"
    "v_pk_add_f32 v[236:237], v[236:237], v[20:21] op_sel_hi:[1,0] neg_lo:[0,1] neg_hi:[0,1]\n"
    "v_pk_add_f32 v[238:239], v[238:239], v[20:21] op_sel_hi:[1,0] neg_lo:[0,1] neg_hi:[0,1]\n"
    "v_exp_f32 v48, v48\n"
    "v_exp_f32 v49, v49\n"
    "v_exp_f32 v50, v50\n"
    "v_exp_f32 v51, v51\n"
    "v_exp_f32 v52, v52\n"
    "v_exp_f32 v53, v53\n"
    "v_exp_f32 v54, v54\n"
    "v_exp_f32 v55, v55\n"
    "v_exp_f32 v56, v56\n"
    "v_exp_f32 v57, v57\n"
    "v_exp_f32 v58, v58\n"
    "v_exp_f32 v59, v59\n"
    "v_exp_f32 v60, v60\n"
    "v_exp_f32 v61, v61\n"
    "v_exp_f32 v62, v62\n"
    "v_exp_f32 v63, v63\n"
    "v_pk_mul_f32 v[24:25], v[24:25], v[22:23] op_sel_hi:[1,0]\n"
    "v_pk_mul_f32 v[26:27], v[26:27], v[22:23] op_sel_hi:[1,0]\n"
    "v_pk_mul_f32 v[28:29], v[28:29], v[22:23] op_sel_hi:[1,0]\n"
    "v_pk_mul_f32 v[30:31], v[30:31], v[22:23] op_sel_hi:[1,0]\n"
    "v_pk_mul_f32 v[32:33], v[32:33], v[22:23] op_sel_hi:[1,0]\n"
    "v_pk_mul_f32 v[34:35], v[34:35], v[22:23] op_sel_hi:[1,0]\n"
    "v_pk_mul_f32 v[36:37], v[36:37], v[22:23] op_sel_hi:[1,0]\n"
    "v_pk_mul_f32 v[38:39], v[38:39], v[22:23] op_sel_hi:[1,0]\n"
    "v_pk_mul_f32 v[40:41], v[40:41], v[22:23] op_sel_hi:[1,0]\n"
    "v_pk_mul_f32 v[42:43], v[42:43], v[22:23] op_sel_hi:[1,0]\n"
    "v_cvt_pk_bf16_f32 v80, v48, v49\n"
    "v_cvt_pk_bf16_f32 v81, v50, v51\n"
    "v_cvt_pk_bf16_f32 v82, v52, v53\n"
    "v_cvt_pk_bf16_f32 v83, v54, v55\n"
    "v_cvt_pk_bf16_f32 v84, v56, v57\n"
    "v_cvt_pk_bf16_f32 v85, v58, v59\n"
    "v_cvt_pk_bf16_f32 v86, v60, v61\n"
    "v_cvt_pk_bf16_f32 v87, v62, v63\n"
    "v_accvgpr_write_b32 a96, v24\n"
    "v_accvgpr_write_b32 a97, v25\n"
    "v_accvgpr_write_b32 a98, v26\n"
    "v_accvgpr_write_b32 a99, v27\n"
    "v_accvgpr_write_b32 a100, v28\n"
    "v_accvgpr_write_b32 a101, v29\n"
    "v_accvgpr_write_b32 a102, v30\n"
    "v_accvgpr_write_b32 a103, v31\n"
    "v_accvgpr_write_b32 a104, v32\n"
    "v_accvgpr_write_b32 a105, v33\n"
    "v_accvgpr_write_b32 a106, v34\n"
    "v_accvgpr_write_b32 a107, v35\n"
    "v_accvgpr_write_b32 a108, v36\n"
    "v_accvgpr_write_b32 a109, v37\n"
    "v_accvgpr_write_b32 a110, v38\n"
    "v_accvgpr_write_b32 a111, v39\n"
    "v_accvgpr_write_b32 a216, v40\n"
    "v_accvgpr_write_b32 a217, v41\n"
    "v_accvgpr_write_b32 a218, v42\n"
    "v_accvgpr_write_b32 a219, v43\n"
    "s_branch .Lat_back7_%=\n"
    ".Lat_rare8_%=:\n"
    "v_mfma_f32_32x32x16_bf16 v[48:63], a[224:227], a[184:187], v[240:255]\n"
    "v_mfma_f32_32x32x16_bf16 v[48:63], a[228:231], a[188:191], v[48:63]\n"
    "v_accvgpr_read_b32 v24, a112\n"
    "v_accvgpr_read_b32 v25, a113\n"
    "v_accvgpr_read_b32 v26, a114\n"
    "v_accvgpr_read_b32 v27, a115\n"
    "v_accvgpr_read_b32 v28, a116\n"
    "v_accvgpr_read_b32 v29, a117\n"
    "v_accvgpr_read_b32 v30, a118\n"
    "v_accvgpr_read_b32 v31, a119\n"
    "v_accvgpr_read_b32 v32, a120\n"
    "v_accvgpr_read_b32 v33, a121\n"
    "v_accvgpr_read_b32 v34, a122\n"
    "v_accvgpr_read_b32 v35, a123\n"
    "v_accvgpr_read_b32 v36, a124\n"
    "v_accvgpr_read_b32 v37, a125\n"
    "v_accvgpr_read_b32 v38, a126\n"
    "v_accvgpr_read_b32 v39, a127\n"
    "v_accvgpr_read_b32 v40, a220\n"
    "v_accvgpr_read_b32 v41, a221\n"
    "v_accvgpr_read_b32 v42, a222\n"
    "v_accvgpr_read_b32 v43, a223\n"
    "v_max3_f32 v20, v48, v49, v50\n"
    "v_max3_f32 v20, v20, v51, v52\n"
    "v_max3_f32 v20, v20, v53, v54\n"
    "v_max3_f32 v20, v20, v55, v56\n"
    "v_max3_f32 v20, v20, v57, v58\n"
    "v_max3_f32 v20, v20, v59, v60\n"
    "v_max3_f32 v20, v20, v61, v62\n"
    "v_max_f32 v20, v20, v63\n"
    "v_mov_b32 v21, v20\n"
    "s_nop 1\n"
    "v_permlane32_swap_b32 v20, v21\n"
    "v_max_f32 v20, v20, v21\n"
    "v_add_f32 v20, 1109393408, v20\n"
    "v_max_f32 v20, 0, v20\n"
    "v_exp_f32 v22, -v20\n"
    "v_pk_add_f32 v[48:49], v[48:49], v[20:21] op_sel_hi:[1,0] neg_lo:[0,1] neg_hi:[0,1]\n"
    "v_pk_add_f32 v[50:51], v[50:51], v[20:21] op_sel_hi:[1,0] neg_lo:[0,1] neg_hi:[0,1]\n"
    "v_pk_add_f32 v[52:53], v[52:53], v[20:21] op_sel_hi:[1,0] neg_lo:[0,1] neg_hi:[0,1]\n"
    "v_pk_add_f32 v[54:55], v[54:55], v[20:21] op_sel_hi:[1,0] neg_lo:[0,1] neg_hi:[0,1]\n"
    "v_pk_add_f32 v[56:57], v[56:57], v[20:21] op_sel_hi:[1,0] neg_lo:[0,1] neg_hi:[0,1]\n"
    "v_pk_add_f32 v[58:59], v[58:59], v[20:21] op_sel_hi:[1,0] neg_lo:[0,1] neg_hi:[0,1]\n"
    "v_pk_add_f32 v[60:61], v[60:61], v[20:21] op_sel_hi:[1,0] neg_lo:[0,1] neg_hi:[0,1]\n"
    "v_pk_add_f32 v[62:63], v[62:63], v[20:21] op_sel_hi:[1,0] neg_lo:[0,1] neg_hi:[0,1]\n"
    "v_pk_add_f32 v[240:241], v[240:241], v[20:21] op_sel_hi:[1,0] neg_lo:[0,1] neg_hi:[0,1]\n"
    "v_pk_add_f32 v[242:243], v[242:243], v[20:21] op_sel_hi:[1,0] neg_lo:[0,1] neg_hi:[0,1]\n"
    "v_pk_add_f32 v[244:245], v[244:245], v[20:21] op_sel_hi:[1,0] neg_lo:[0,1] neg_hi:[0,1]\n"
    "v_pk_add_f32 v[246:247], v[246:247], v[20:21] op_sel_hi:[1,0] neg_lo:[0,1] neg_hi:[0,1]\n"
    "v_pk_add_f32 v[248:249], v[248:249], v[20:21] op_sel_hi:[1,0] neg_lo:[0,1] neg_hi:[0,1]\n"
    "v_pk_add_f32 v[250:251], v[250:251], v[20:21] op_sel_hi:[1,0] neg_lo:[0,1] neg_hi:[0,1]\n"
    "v_pk_add_f32 v[252:253], v[252:253], v[20:21] op_sel_hi:[1,0] neg_lo:[0,1] neg_hi:[0,1]\n"
    "v_pk_add_f32 v[254:255], v[254:255], v[20:21] op_sel_hi:[1,0] neg_lo:[0,1] neg_hi:[0,1]\n"
    "v_exp_f32 v48, v48\n"
    "v_exp_f32 v49, v49\n"
    "v_exp_f32 v50, v50\n"
    "v_exp_f32 v51, v51\n"
    "v_exp_f32 v52, v52\n"
    "v_exp_f32 v53, v53\n"
    "v_exp_f32 v54, v54\n"
    "v_exp_f32 v55, v55\n"
    "v_exp_f32 v56, v56\n"
    "v_exp_f32 v57, v57\n"
    "v_exp_f32 v58, v58\n"
    "v_exp_f32 v59, v59\n"
    "v_exp_f32 v60, v60\n"
    "v_exp_f32 v61, v61\n"
    "v_exp_f32 v62, v62\n"
    "v_exp_f32 v63, v63\n"
    "v_pk_mul_f32 v[24:25], v[24:25], v[22:23] op_sel_hi:[1,0]\n"
    "v_pk_mul_f32 v[26:27], v[26:27], v[22:23] op_sel_hi:[1,0]\n"
    "v_pk_mul_f32 v[28:29], v[28:29], v[22:23] op_sel_hi:[1,0]\n"
    "v_pk_mul_f32 v[30:31], v[30:31], v[22:23] op_sel_hi:[1,0]\n"
    "v_pk_mul_f32 v[32:33], v[32:33], v[22:23] op_sel_hi:[1,0]\n"
    "v_pk_mul_f32 v[34:35], v[34:35], v[22:23] op_sel_hi:[1,0]\n"
    "v_pk_mul_f32 v[36:37], v[36:37], v[22:23] op_sel_hi:[1,0]\n"
    "v_pk_mul_f32 v[38:39], v[38:39], v[22:23] op_sel_hi:[1,0]\n"
    "v_pk_mul_f32 v[40:41], v[40:41], v[22:23] op_sel_hi:[1,0]\n"
    "v_pk_mul_f32 v[42:43], v[42:43], v[22:23] op_sel_hi:[1,0]\n"
    "v_cvt_pk_bf16_f32 v88, v48, v49\n"
    "v_cvt_pk_bf16_f32 v89, v50, v51\n"
    "v_cvt_pk_bf16_f32 v90, v52, v53\n"
    "v_cvt_pk_bf16_f32 v91, v54, v55\n"
    "v_cvt_pk_bf16_f32 v92, v56, v57\n"
    "v_cvt_pk_bf16_f32 v93, v58, v59\n"
    "v_cvt_pk_bf16_f32 v94, v60, v61\n"
    "v_cvt_pk_bf16_f32 v95, v62, v63\n"
    "v_accvgpr_write_b32 a112, v24\n"
    "v_accvgpr_write_b32 a113, v25\n"
    "v_accvgpr_write_b32 a114, v26\n"
    "v_accvgpr_write_b32 a115, v27\n"
    "v_accvgpr_write_b32 a116, v28\n"
    "v_accvgpr_write_b32 a117, v29\n"
    "v_accvgpr_write_b32 a118, v30\n"
    "v_accvgpr_write_b32 a119, v31\n"
    "v_accvgpr_write_b32 a120, v32\n"
    "v_accvgpr_write_b32 a121, v33\n"
    "v_accvgpr_write_b32 a122, v34\n"
    "v_accvgpr_write_b32 a123, v35\n"
    "v_accvgpr_write_b32 a124, v36\n"
    "v_accvgpr_write_b32 a125, v37\n"
    "v_accvgpr_write_b32 a126, v38\n"
    "v_accvgpr_write_b32 a127, v39\n"
    "v_accvgpr_write_b32 a220, v40\n"
    "v_accvgpr_write_b32 a221, v41\n"
    "v_accvgpr_write_b32 a222, v42\n"
    "v_accvgpr_write_b32 a223, v43\n"
    "s_branch .Lat_back8_%=\n"
    ".Lat_rare9_%=:\n"
    "v_mfma_f32_32x32x16_bf16 v[48:63], a[240:243], a[128:131], v[128:143]\n"
    "v_mfma_f32_32x32x16_bf16 v[48:63], a[244:247], a[132:135], v[48:63]\n"
    "v_accvgpr_read_b32 v24, a0\n"
    "v_accvgpr_read_b32 v25, a1\n"
    "v_accvgpr_read_b32 v26, a2\n"
    "v_accvgpr_read_b32 v27, a3\n"
    "v_accvgpr_read_b32 v28, a4\n"
    "v_accvgpr_read_b32 v29, a5\n"
    "v_accvgpr_read_b32 v30, a6\n"
    "v_accvgpr_read_b32 v31, a7\n"
    "v_accvgpr_read_b32 v32, a8\n"
    "v_accvgpr_read_b32 v33, a9\n"
    "v_accvgpr_read_b32 v34, a10\n"
    "v_accvgpr_read_b32 v35, a11\n"
    "v_accvgpr_read_b32 v36, a12\n"
    "v_accvgpr_read_b32 v37, a13\n"
    "v_accvgpr_read_b32 v38, a14\n"
    "v_accvgpr_read_b32 v39, a15\n"
    "v_accvgpr_read_b32 v40, a192\n"
    "v_accvgpr_read_b32 v41, a193\n"
    "v_accvgpr_read_b32 v42, a194\n"
    "v_accvgpr_read_b32 v43, a195\n"
    "v_max3_f32 v20, v48, v49, v50\n"
    "v_max3_f32 v20, v20, v51, v52\n"
    "v_max3_f32 v20, v20, v53, v54\n"
    "v_max3_f32 v20, v20, v55, v56\n"
    "v_max3_f32 v20, v20, v57, v58\n"
    "v_max3_f32 v20, v20, v59, v60\n"
    "v_max3_f32 v20, v20, v61, v62\n"
    "v_max_f32 v20, v20, v63\n"
    "v_mov_b32 v21, v20\n"
    "s_nop 1\n"
    "v_permlane32_swap_b32 v20, v21\n"
    "v_max_f32 v20, v20, v21\n"
    "v_add_f32 v20, 1109393408, v20\n"
    "v_max_f32 v20, 0, v20\n"
    "v_exp_f32 v22, -v20\n"
    "v_pk_add_f32 v[48:49], v[48:49], v[20:21] op_sel_hi:[1,0] neg_lo:[0,1] neg_hi:[0,1]\n"
    "v_pk_add_f32 v[50:51], v[50:51], v[20:21] op_sel_hi:[1,0] neg_lo:[0,1] neg_hi:[0,1]\n"
    "v_pk_add_f32 v[52:53], v[52:53], v[20:21] op_sel_hi:[1,0] neg_lo:[0,1] neg_hi:[0,1]\n"
    "v_pk_add_f32 v[54:55], v[54:55], v[20:21] op_sel_hi:[1,0] neg_lo:[0,1] neg_hi:[0,1]\n"
    "v_pk_add_f32 v[56:57], v[56:57], v[20:21] op_sel_hi:[1,0] neg_lo:[0,1] neg_hi:[0,1]\n"
    "v_pk_add_f32 v[58:59], v[58:59], v[20:21] op_sel_hi:[1,0] neg_lo:[0,1] neg_hi:[0,1]\n"
    "v_pk_add_f32 v[60:61], v[60:61], v[20:21] op_sel_hi:[1,0] neg_lo:[0,1] neg_hi:[0,1]\n"
    "v_pk_add_f32 v[62:63], v[62:63], v[20:21] op_sel_hi:[1,0] neg_lo:[0,1] neg_hi:[0,1]\n"
    "v_pk_add_f32 v[128:129], v[128:129], v[20:21] op_sel_hi:[1,0] neg_lo:[0,1] neg_hi:[0,1]\n"
    "v_pk_add_f32 v[130:131], v[130:131], v[20:21] op_sel_hi:[1,0] neg_lo:[0,1] neg_hi:[0,1]\n"
    "v_pk_add_f32 v[132:133], v[132:133], v[20:21] op_sel_hi:[1,0] neg_lo:[0,1] neg_hi:[0,1]\n"
    "v_pk_add_f32 v[134:135], v[134:135], v[20:21] op_sel_hi:[1,0] neg_lo:[0,1] neg_hi:[0,1]\n"
    "v_pk_add_f32 v[136:137], v[136:137], v[20:21] op_sel_hi:[1,0] neg_lo:[0,1] neg_hi:[0,1]\n"
    "v_pk_add_f32 v[138:139], v[138:139], v[20:21] op_sel_hi:[1,0] neg_lo:[0,1] neg_hi:[0,1]\n"
    "v_pk_add_f32 v[140:141], v[140:141], v[20:21] op_sel_hi:[1,0] neg_lo:[0,1] neg_hi:[0,1]\n"
    "v_pk_add_f32 v[142:143], v[142:143], v[20:21] op_sel_hi:[1,0] neg_lo:[0,1] neg_hi:[0,1]\n"
    "v_exp_f32 v48, v48\n"
    "v_exp_f32 v49, v49\n"
    "v_exp_f32 v50, v50\n"
    "v_exp_f32 v51, v51\n"
    "v_exp_f32 v52, v52\n"
    "v_exp_f32 v53, v53\n"
    "v_exp_f32 v54, v54\n"
    "v_exp_f32 v55, v55\n"
    "v_exp_f32 v56, v56\n"
    "v_exp_f32 v57, v57\n"
    "v_exp_f32 v58, v58\n"
    "v_exp_f32 v59, v59\n"
    "v_exp_f32 v60, v60\n"
    "v_exp_f32 v61, v61\n"
    "v_exp_f32 v62, v62\n"
    "v_exp_f32 v63, v63\n"
    "v_pk_mul_f32 v[24:25], v[24:25], v[22:23] op_sel_hi:[1,0]\n"
    "v_pk_mul_f32 v[26:27], v[26:27], v[22:23] op_sel_hi:[1,0]\n"
    "v_pk_mul_f32 v[28:29], v[28:29], v[22:23] op_sel_hi:[1,0]\n"
    "v_pk_mul_f32 v[30:31], v[30:31], v[22:23] op_sel_hi:[1,0]\n"
    "v_pk_mul_f32 v[32:33], v[32:33], v[22:23] op_sel_hi:[1,0]\n"
    "v_pk_mul_f32 v[34:35], v[34:35], v[22:23] op_sel_hi:[1,0]\n"
    "v_pk_mul_f32 v[36:37], v[36:37], v[22:23] op_sel_hi:[1,0]\n"
    "v_pk_mul_f32 v[38:39], v[38:39], v[22:23] op_sel_hi:[1,0]\n"
    "v_pk_mul_f32 v[40:41], v[40:41], v[22:23] op_sel_hi:[1,0]\n"
    "v_pk_mul_f32 v[42:43], v[42:43], v[22:23] op_sel_hi:[1,0]\n"
    "v_cvt_pk_bf16_f32 v80, v48, v49\n"
    "v_cvt_pk_bf16_f32 v81, v50, v51\n"
    "v_cvt_pk_bf16_f32 v82, v52, v53\n"
    "v_cvt_pk_bf16_f32 v83, v54, v55\n"
    "v_cvt_pk_bf16_f32 v84, v56, v57\n"
    "v_cvt_pk_bf16_f32 v85, v58, v59\n"
    "v_cvt_pk_bf16_f32 v86, v60, v61\n"
    "v_cvt_pk_bf16_f32 v87, v62, v63\n"
    "v_accvgpr_write_b32 a0, v24\n"
    "v_accvgpr_write_b32 a1, v25\n"
    "v_accvgpr_write_b32 a2, v26\n"
    "v_accvgpr_write_b32 a3, v27\n"
    "v_accvgpr_write_b32 a4, v28\n"
    "v_accvgpr_write_b32 a5, v29\n"
    "v_accvgpr_write_b32 a6, v30\n"
    "v_accvgpr_write_b32 a7, v31\n"
    "v_accvgpr_write_b32 a8, v32\n"
    "v_accvgpr_write_b32 a9, v33\n"
    "v_accvgpr_write_b32 a10, v34\n"
    "v_accvgpr_write_b32 a11, v35\n"
    "v_accvgpr_write_b32 a12, v36\n"
    "v_accvgpr_write_b32 a13, v37\n"
    "v_accvgpr_write_b32 a14, v38\n"
    "v_accvgpr_write_b32 a15, v39\n"
    "v_accvgpr_write_b32 a192, v40\n"
    "v_accvgpr_write_b32 a193, v41\n"
    "v_accvgpr_write_b32 a194, v42\n"
    "v_accvgpr_write_b32 a195, v43\n"
    "s_branch .Lat_back9_%=\n"
    ".Lat_rare10_%=:\n"
    "v_mfma_f32_32x32x16_bf16 v[48:63], a[240:243], a[136:139], v[144:159]\n"
    "v_mfma_f32_32x32x16_bf16 v[48:63], a[244:247], a[140:143], v[48:63]\n"
    "v_accvgpr_read_b32 v24, a16\n"
    "v_accvgpr_read_b32 v25, a17\n"
    "v_accvgpr_read_b32 v26, a18\n"
    "v_accvgpr_read_b32 v27, a19\n"
    "v_accvgpr_read_b32 v28, a20\n"
    "v_accvgpr_read_b32 v29, a21\n"
    "v_accvgpr_read_b32 v30, a22\n"
    "v_accvgpr_read_b32 v31, a23\n"
    "v_accvgpr_read_b32 v32, a24\n"
    "v_accvgpr_read_b32 v33, a25\n"
    "v_accvgpr_read_b32 v34, a26\n"
    "v_accvgpr_read_b32 v35, a27\n"
    "v_accvgpr_read_b32 v36, a28\n"
    "v_accvgpr_read_b32 v37, a29\n"
    "v_accvgpr_read_b32 v38, a30\n"
    "v_accvgpr_read_b32 v39, a31\n"
    "v_accvgpr_read_b32 v40, a196\n"
    "v_accvgpr_read_b32 v41, a197\n"
    "v_accvgpr_read_b32 v42, a198\n"
    "v_accvgpr_read_b32 v43, a199\n"
    "v_max3_f32 v20, v48, v49, v50\n"
    "v_max3_f32 v20, v20, v51, v52\n"
    "v_max3_f32 v20, v20, v53, v54\n"
    "v_max3_f32 v20, v20, v55, v56\n"
    "v_max3_f32 v20, v20, v57, v58\n"
    "v_max3_f32 v20, v20, v59, v60\n"
    "v_max3_f32 v20, v20, v61, v62\n"
    "v_max_f32 v20, v20, v63\n"
    "v_mov_b32 v21, v20\n"
    "s_nop 1\n"
    "v_permlane32_swap_b32 v20, v21\n"
    "v_max_f32 v20, v20, v21\n"
    "v_add_f32 v20, 1109393408, v20\n"
    "v_max_f32 v20, 0, v20\n"
    "v_exp_f32 v22, -v20\n"
    "v_pk_add_f32 v[48:49], v[48:49], v[20:21] op_sel_hi:[1,0] neg_lo:[0,1] neg_hi:[0,1]\n"
    "v_pk_add_f32 v[50:51], v[50:51], v[20:21] op_sel_hi:[1,0] neg_lo:[0,1] neg_hi:[0,1]\n"
    "v_pk_add_f32 v[52:53], v[52:53], v[20:21] op_sel_hi:[1,0] neg_lo:[0,1] neg_hi:[0,1]\n"
    "v_pk_add_f32 v[54:55], v[54:55], v[20:21] op_sel_hi:[1,0] neg_lo:[0,1] neg_hi:[0,1]\n"
    "v_pk_add_f32 v[56:57], v[56:57], v[20:21] op_sel_hi:[1,0] neg_lo:[0,1] neg_hi:[0,1]\n"
    "v_pk_add_f32 v[58:59], v[58:59], v[20:21] op_sel_hi:[1,0] neg_lo:[0,1] neg_hi:[0,1]\n"
    "v_pk_add_f32 v[60:61], v[60:61], v[20:21] op_sel_hi:[1,0] neg_lo:[0,1] neg_hi:[0,1]\n"
    "v_pk_add_f32 v[62:63], v[62:63], v[20:21] op_sel_hi:[1,0] neg_lo:[0,1] neg_hi:[0,1]\n"
    "v_pk_add_f32 v[144:145], v[144:145], v[20:21] op_sel_hi:[1,0] neg_lo:[0,1] neg_hi:[0,1]\n"
    "v_pk_add_f32 v[146:147], v[146:147], v[20:21] op_sel_hi:[1,0] neg_lo:[0,1] neg_hi:[0,1]\n"
    "v_pk_add_f32 v[148:149], v[148:149], v[20:21] op_sel_hi:[1,0] neg_lo:[0,1] neg_hi:[0,1]\n"
    "v_pk_add_f32 v[150:151], v[150:151], v[20:21] op_sel_hi:[1,0] neg_lo:[0,1] neg_hi:[0,1]\n"
    "v_pk_add_f32 v[152:153], v[152:153], v[20:21] op_sel_hi:[1,0] neg_lo:[0,1] neg_hi:[0,1]\n"
    "v_pk_add_f32 v[154:155], v[154:155], v[20:21] op_sel_hi:[1,0] neg_lo:[0,1] neg_hi:[0,1]\n"
    "v_pk_add_f32 v[156:157], v[156:157], v[20:21] op_sel_hi:[1,0] neg_lo:[0,1] neg_hi:[0,1]\n"
    "v_pk_add_f32 v[158:159], v[158:159], v[20:21] op_sel_hi:[1,0] neg_lo:[0,1] neg_hi:[0,1]\n"
    "v_exp_f32 v48, v48\n"
    "v_exp_f32 v49, v49\n"
    "v_exp_f32 v50, v50\n"
    "v_exp_f32 v51, v51\n"
    "v_exp_f32 v52, v52\n"
    "v_exp_f32 v53, v53\n"
    "v_exp_f32 v54, v54\n"
    "v_exp_f32 v55, v55\n"
    "v_exp_f32 v56, v56\n"
    "v_exp_f32 v57, v57\n"
    "v_exp_f32 v58, v58\n"
    "v_exp_f32 v59, v59\n"
    "v_exp_f32 v60, v60\n"
    "v_exp_f32 v61, v61\n"
    "v_exp_f32 v62, v62\n"
    "v_exp_f32 v63, v63\n"
    "v_pk_mul_f32 v[24:25], v[24:25], v[22:23] op_sel_hi:[1,0]\n"
    "v_pk_mul_f32 v[26:27], v[26:27], v[22:23] op_sel_hi:[1,0]\n"
    "v_pk_mul_f32 v[28:29], v[28:29], v[22:23] op_sel_hi:[1,0]\n"
    "v_pk_mul_f32 v[30:31], v[30:31], v[22:23] op_sel_hi:[1,0]\n"
    "v_pk_mul_f32 v[32:33], v[32:33], v[22:23] op_sel_hi:[1,0]\n"
    "v_pk_mul_f32 v[34:35], v[34:35], v[22:23] op_sel_hi:[1,0]\n"
    "v_pk_mul_f32 v[36:37], v[36:37], v[22:23] op_sel_hi:[1,0]\n"
    "v_pk_mul_f32 v[38:39], v[38:39], v[22:23] op_sel_hi:[1,0]\n"
    "v_pk_mul_f32 v[40:41], v[40:41], v[22:23] op_sel_hi:[1,0]\n"
    "v_pk_mul_f32 v[42:43], v[42:43], v[22:23] op_sel_hi:[1,0]\n"
    "v_cvt_pk_bf16_f32 v88, v48, v49\n"
    "v_cvt_pk_bf16_f32 v89, v50, v51\n"
    "v_cvt_pk_bf16_f32 v90, v52, v53\n"
    "v_cvt_pk_bf16_f32 v91, v54, v55\n"
    "v_cvt_pk_bf16_f32 v92, v56, v57\n"
    "v_cvt_pk_bf16_f32 v93, v58, v59\n"
    "v_cvt_pk_bf16_f32 v94, v60, v61\n"
    "v_cvt_pk_bf16_f32 v95, v62, v63\n"
    "v_accvgpr_write_b32 a16, v24\n"
    "v_accvgpr_write_b32 a17, v25\n"
    "v_accvgpr_write_b32 a18, v26\n"
    "v_accvgpr_write_b32 a19, v27\n"
    "v_accvgpr_write_b32 a20, v28\n"
    "v_accvgpr_write_b32 a21, v29\n"
    "v_accvgpr_write_b32 a22, v30\n"
    "v_accvgpr_write_b32 a23, v31\n"
    "v_accvgpr_write_b32 a24, v32\n"
    "v_accvgpr_write_b32 a25, v33\n"
    "v_accvgpr_write_b32 a26, v34\n"
    "v_accvgpr_write_b32 a27, v35\n"
    "v_accvgpr_write_b32 a28, v36\n"
    "v_accvgpr_write_b32 a29, v37\n"
    "v_accvgpr_write_b32 a30, v38\n"
    "v_accvgpr_write_b32 a31, v39\n"
    "v_accvgpr_write_b32 a196, v40\n"
    "v_accvgpr_write_b32 a197, v41\n"
    "v_accvgpr_write_b32 a198, v42\n"
    "v_accvgpr_write_b32 a199, v43\n"
    "s_branch .Lat_back10_%=\n"
    ".Lat_rare11_%=:\n"
    "v_mfma_f32_32x32x16_bf16 v[48:63], a[240:243], a[144:147], v[160:175]\n"
    "v_mfma_f32_32x32x16_bf16 v[48:63], a[244:247], a[148:151], v[48:63]\n"
    "v_accvgpr_read_b32 v24, a32\n"
    "v_accvgpr_read_b32 v25, a33\n"
    "v_accvgpr_read_b32 v26, a34\n"
    "v_accvgpr_read_b32 v27, a35\n"
    "v_accvgpr_read_b32 v28, a36\n"
    "v_accvgpr_read_b32 v29, a37\n"
    "v_accvgpr_read_b32 v30, a38\n"
    "v_accvgpr_read_b32 v31, a39\n"
    "v_accvgpr_read_b32 v32, a40\n"
    "v_accvgpr_read_b32 v33, a41\n"
    "v_accvgpr_read_b32 v34, a42\n"
    "v_accvgpr_read_b32 v35, a43\n"
    "v_accvgpr_read_b32 v36, a44\n"
    "v_accvgpr_read_b32 v37, a45\n"
    "v_accvgpr_read_b32 v38, a46\n"
    "v_accvgpr_read_b32 v39, a47\n"
    "v_accvgpr_read_b32 v40, a200\n"
    "v_accvgpr_read_b32 v41, a201\n"
    "v_accvgpr_read_b32 v42, a202\n"
    "v_accvgpr_read_b32 v43, a203\n"
    "v_max3_f32 v20, v48, v49, v50\n"
    "v_max3_f32 v20, v20, v51, v52\n"
    "v_max3_f32 v20, v20, v53, v54\n"
    "v_max3_f32 v20, v20, v55, v56\n"
    "v_max3_f32 v20, v20, v57, v58\n"
    "v_max3_f32 v20, v20, v59, v60\n"
    "v_max3_f32 v20, v20, v61, v62\n"
    "v_max_f32 v20, v20, v63\n"
    "v_mov_b32 v21, v20\n"
    "s_nop 1\n"
    "v_permlane32_swap_b32 v20, v21\n"
    "v_max_f32 v20, v20, v21\n"
    "v_add_f32 v20, 1109393408, v20\n"
    "v_max_f32 v20, 0, v20\n"
    "v_exp_f32 v22, -v20\n"
    "v_pk_add_f32 v[48:49], v[48:49], v[20:21] op_sel_hi:[1,0] neg_lo:[0,1] neg_hi:[0,1]\n"
    "v_pk_add_f32 v[50:51], v[50:51], v[20:21] op_sel_hi:[1,0] neg_lo:[0,1] neg_hi:[0,1]\n"
    "v_pk_add_f32 v[52:53], v[52:53], v[20:21] op_sel_hi:[1,0] neg_lo:[0,1] neg_hi:[0,1]\n"
    "v_pk_add_f32 v[54:55], v[54:55], v[20:21] op_sel_hi:[1,0] neg_lo:[0,1] neg_hi:[0,1]\n"
    "v_pk_add_f32 v[56:57], v[56:57], v[20:21] op_sel_hi:[1,0] neg_lo:[0,1] neg_hi:[0,1]\n"
    "v_pk_add_f32 v[58:59], v[58:59], v[20:21] op_sel_hi:[1,0] neg_lo:[0,1] neg_hi:[0,1]\n"
    "v_pk_add_f32 v[60:61], v[60:61], v[20:21] op_sel_hi:[1,0] neg_lo:[0,1] neg_hi:[0,1]\n"
    "v_pk_add_f32 v[62:63], v[62:63], v[20:21] op_sel_hi:[1,0] neg_lo:[0,1] neg_hi:[0,1]\n"
    "v_pk_add_f32 v[160:161], v[160:161], v[20:21] op_sel_hi:[1,0] neg_lo:[0,1] neg_hi:[0,1]\n"
    "v_pk_add_f32 v[162:163], v[162:163], v[20:21] op_sel_hi:[1,0] neg_lo:[0,1] neg_hi:[0,1]\n"
    "v_pk_add_f32 v[164:165], v[164:165], v[20:21] op_sel_hi:[1,0] neg_lo:[0,1] neg_hi:[0,1]\n"
    "v_pk_add_f32 v[166:167], v[166:167], v[20:21] op_sel_hi:[1,0] neg_lo:[0,1] neg_hi:[0,1]\n"
    "v_pk_add_f32 v[168:169], v[168:169], v[20:21] op_sel_hi:[1,0] neg_lo:[0,1] neg_hi:[0,1]\n"
    "v_pk_add_f32 v[170:171], v[170:171], v[20:21] op_sel_hi:[1,0] neg_lo:[0,1] neg_hi:[0,1]\n"
    "v_pk_add_f32 v[172:173], v[172:173], v[20:21] op_sel_hi:[1,0] neg_lo:[0,1] neg_hi:[0,1]\n"
    "v_pk_add_f32 v[174:175], v[174:175], v[20:21] op_sel_hi:[1,0] neg_lo:[0,1] neg_hi:[0,1]\n"
    "v_exp_f32 v48, v48\n"
    "v_exp_f32 v49, v49\n"
    "v_exp_f32 v50, v50\n"
    "v_exp_f32 v51, v51\n"
    "v_exp_f32 v52, v52\n"
    "v_exp_f32 v53, v53\n"
    "v_exp_f32 v54, v54\n"
    "v_exp_f32 v55, v55\n"
    "v_exp_f32 v56, v56\n"
    "v_exp_f32 v57, v57\n"
    "v_exp_f32 v58, v58\n"
    "v_exp_f32 v59, v59\n"
    "v_exp_f32 v60, v60\n"
    "v_exp_f32 v61, v61\n"
    "v_exp_f32 v62, v62\n"
    "v_exp_f32 v63, v63\n"
    "v_pk_mul_f32 v[24:25], v[24:25], v[22:23] op_sel_hi:[1,0]\n"
    "v_pk_mul_f32 v[26:27], v[26:27], v[22:23] op_sel_hi:[1,0]\n"
    "v_pk_mul_f32 v[28:29], v[28:29], v[22:23] op_sel_hi:[1,0]\n"
    "v_pk_mul_f32 v[30:31], v[30:31], v[22:23] op_sel_hi:[1,0]\n"
    "v_pk_mul_f32 v[32:33], v[32:33], v[22:23] op_sel_hi:[1,0]\n"
    "v_pk_mul_f32 v[34:35], v[34:35], v[22:23] op_sel_hi:[1,0]\n"
    "v_pk_mul_f32 v[36:37], v[36:37], v[22:23] op_sel_hi:[1,0]\n"
    "v_pk_mul_f32 v[38:39], v[38:39], v[22:23] op_sel_hi:[1,0]\n"
    "v_pk_mul_f32 v[40:41], v[40:41], v[22:23] op_sel_hi:[1,0]\n"
    "v_pk_mul_f32 v[42:43], v[42:43], v[22:23] op_sel_hi:[1,0]\n"
    "v_cvt_pk_bf16_f32 v80, v48, v49\n"
    "v_cvt_pk_bf16_f32 v81, v50, v51\n"
    "v_cvt_pk_bf16_f32 v82, v52, v53\n"
    "v_cvt_pk_bf16_f32 v83, v54, v55\n"
    "v_cvt_pk_bf16_f32 v84, v56, v57\n"
    "v_cvt_pk_bf16_f32 v85, v58, v59\n"
    "v_cvt_pk_bf16_f32 v86, v60, v61\n"
    "v_cvt_pk_bf16_f32 v87, v62, v63\n"
    "v_accvgpr_write_b32 a32, v24\n"
    "v_accvgpr_write_b32 a33, v25\n"
    "v_accvgpr_write_b32 a34, v26\n"
    "v_accvgpr_write_b32 a35, v27\n"
    "v_accvgpr_write_b32 a36, v28\n"
    "v_accvgpr_write_b32 a37, v29\n"
    "v_accvgpr_write_b32 a38, v30\n"
    "v_accvgpr_write_b32 a39, v31\n"
    "v_accvgpr_write_b32 a40, v32\n"
    "v_accvgpr_write_b32 a41, v33\n"
    "v_accvgpr_write_b32 a42, v34\n"
    "v_accvgpr_write_b32 a43, v35\n"
    "v_accvgpr_write_b32 a44, v36\n"
    "v_accvgpr_write_b32 a45, v37\n"
    "v_accvgpr_write_b32 a46, v38\n"
    "v_accvgpr_write_b32 a47, v39\n"
    "v_accvgpr_write_b32 a200, v40\n"
    "v_accvgpr_write_b32 a201, v41\n"
    "v_accvgpr_write_b32 a202, v42\n"
    "v_accvgpr_write_b32 a203, v43\n"
    "s_branch .Lat_back11_%=\n"
    ".Lat_rare12_%=:\n"
    "v_mfma_f32_32x32x16_bf16 v[48:63], a[240:243], a[152:155], v[176:191]\n"
    "v_mfma_f32_32x32x16_bf16 v[48:63], a[244:247], a[156:159], v[48:63]\n"
    "v_accvgpr_read_b32 v24, a48\n"
    "v_accvgpr_read_b32 v25, a49\n"
    "v_accvgpr_read_b32 v26, a50\n"
    "v_accvgpr_read_b32 v27, a51\n"
    "v_accvgpr_read_b32 v28, a52\n"
    "v_accvgpr_read_b32 v29, a53\n"
    "v_accvgpr_read_b32 v30, a54\n"
    "v_accvgpr_read_b32 v31, a55\n"
    "v_accvgpr_read_b32 v32, a56\n"
    "v_accvgpr_read_b32 v33, a57\n"
    "v_accvgpr_read_b32 v34, a58\n"
    "v_accvgpr_read_b32 v35, a59\n"
    "v_accvgpr_read_b32 v36, a60\n"
    "v_accvgpr_read_b32 v37, a61\n"
    "v_accvgpr_read_b32 v38, a62\n"
    "v_accvgpr_read_b32 v39, a63\n"
    "v_accvgpr_read_b32 v40, a204\n"
    "v_accvgpr_read_b32 v41, a205\n"
    "v_accvgpr_read_b32 v42, a206\n"
    "v_accvgpr_read_b32 v43, a207\n"
    "v_max3_f32 v20, v48, v49, v50\n"
    "v_max3_f32 v20, v20, v51, v52\n"
    "v_max3_f32 v20, v20, v53, v54\n"
    "v_max3_f32 v20, v20, v55, v56\n"
    "v_max3_f32 v20, v20, v57, v58\n"
    "v_max3_f32 v20, v20, v59, v60\n"
    "v_max3_f32 v20, v20, v61, v62\n"
    "v_max_f32 v20, v20, v63\n"
    "v_mov_b32 v21, v20\n"
    "s_nop 1\n"
    "v_permlane32_swap_b32 v20, v21\n"
    "v_max_f32 v20, v20, v21\n"
    "v_add_f32 v20, 1109393408, v20\n"
    "v_max_f32 v20, 0, v20\n"
    "v_exp_f32 v22, -v20\n"
    "v_pk_add_f32 v[48:49], v[48:49], v[20:21] op_sel_hi:[1,0] neg_lo:[0,1] neg_hi:[0,1]\n"
    "v_pk_add_f32 v[50:51], v[50:51], v[20:21] op_sel_hi:[1,0] neg_lo:[0,1] neg_hi:[0,1]\n"
    "v_pk_add_f32 v[52:53], v[52:53], v[20:21] op_sel_hi:[1,0] neg_lo:[0,1] neg_hi:[0,1]\n"
    "v_pk_add_f32 v[54:55], v[54:55], v[20:21] op_sel_hi:[1,0] neg_lo:[0,1] neg_hi:[0,1]\n"
    "v_pk_add_f32 v[56:57], v[56:57], v[20:21] op_sel_hi:[1,0] neg_lo:[0,1] neg_hi:[0,1]\n"
    "v_pk_add_f32 v[58:59], v[58:59], v[20:21] op_sel_hi:[1,0] neg_lo:[0,1] neg_hi:[0,1]\n"
    "v_pk_add_f32 v[60:61], v[60:61], v[20:21] op_sel_hi:[1,0] neg_lo:[0,1] neg_hi:[0,1]\n"
    "v_pk_add_f32 v[62:63], v[62:63], v[20:21] op_sel_hi:[1,0] neg_lo:[0,1] neg_hi:[0,1]\n"
    "v_pk_add_f32 v[176:177], v[176:177], v[20:21] op_sel_hi:[1,0] neg_lo:[0,1] neg_hi:[0,1]\n"
    "v_pk_add_f32 v[178:179], v[178:179], v[20:21] op_sel_hi:[1,0] neg_lo:[0,1] neg_hi:[0,1]\n"
    "v_pk_add_f32 v[180:181], v[180:181], v[20:21] op_sel_hi:[1,0] neg_lo:[0,1] neg_hi:[0,1]\n"
    "v_pk_add_f32 v[182:183], v[182:183], v[20:21] op_sel_hi:[1,0] neg_lo:[0,1] neg_hi:[0,1]\n"
    "v_pk_add_f32 v[184:185], v[184:185], v[20:21] op_sel_hi:[1,0] neg_lo:[0,1] neg_hi:[0,1]\n"
    "v_pk_add_f32 v[186:187], v[186:187], v[20:21] op_sel_hi:[1,0] neg_lo:[0,1] neg_hi:[0,1]\n"
    "v_pk_add_f32 v[188:189], v[188:189], v[20:21] op_sel_hi:[1,0] neg_lo:[0,1] neg_hi:[0,1]\n"
    "v_pk_add_f32 v[190:191], v[190:191], v[20:21] op_sel_hi:[1,0] neg_lo:[0,1] neg_hi:[0,1]\n"
    "v_exp_f32 v48, v48\n"
    "v_exp_f32 v49, v49\n"
    "v_exp_f32 v50, v50\n"
    "v_exp_f32 v51, v51\n"
    "v_exp_f32 v52, v52\n"
    "v_exp_f32 v53, v53\n"
    "v_exp_f32 v54, v54\n"
    "v_exp_f32 v55, v55\n"
    "v_exp_f32 v56, v56\n"
    "v_exp_f32 v57, v57\n"
    "v_exp_f32 v58, v58\n"
    "v_exp_f32 v59, v59\n"
    "v_exp_f32 v60, v60\n"
    "v_exp_f32 v61, v61\n"
    "v_exp_f32 v62, v62\n"
    "v_exp_f32 v63, v63\n"
    "v_pk_mul_f32 v[24:25], v[24:25], v[22:23] op_sel_hi:[1,0]\n"
    "v_pk_mul_f32 v[26:27], v[26:27], v[22:23] op_sel_hi:[1,0]\n"
    "v_pk_mul_f32 v[28:29], v[28:29], v[22:23] op_sel_hi:[1,0]\n"
    "v_pk_mul_f32 v[30:31], v[30:31], v[22:23] op_sel_hi:[1,0]\n"
    "v_pk_mul_f32 v[32:33], v[32:33], v[22:23] op_sel_hi:[1,0]\n"
    "v_pk_mul_f32 v[34:35], v[34:35], v[22:23] op_sel_hi:[1,0]\n"
    "v_pk_mul_f32 v[36:37], v[36:37], v[22:23] op_sel_hi:[1,0]\n"
    "v_pk_mul_f32 v[38:39], v[38:39], v[22:23] op_sel_hi:[1,0]\n"
    "v_pk_mul_f32 v[40:41], v[40:41], v[22:23] op_sel_hi:[1,0]\n"
    "v_pk_mul_f32 v[42:43], v[42:43], v[22:23] op_sel_hi:[1,0]\n"
    "v_cvt_pk_bf16_f32 v88, v48, v49\n"
    "v_cvt_pk_bf16_f32 v89, v50, v51\n"
    "v_cvt_pk_bf16_f32 v90, v52, v53\n"
    "v_cvt_pk_bf16_f32 v91, v54, v55\n"
    "v_cvt_pk_bf16_f32 v92, v56, v57\n"
    "v_cvt_pk_bf16_f32 v93, v58, v59\n"
    "v_cvt_pk_bf16_f32 v94, v60, v61\n"
    "v_cvt_pk_bf16_f32 v95, v62, v63\n"
    "v_accvgpr_write_b32 a48, v24\n"
    "v_accvgpr_write_b32 a49, v25\n"
    "v_accvgpr_write_b32 a50, v26\n"
    "v_accvgpr_write_b32 a51, v27\n"
    "v_accvgpr_write_b32 a52, v28\n"
    "v_accvgpr_write_b32 a53, v29\n"
    "v_accvgpr_write_b32 a54, v30\n"
    "v_accvgpr_write_b32 a55, v31\n"
    "v_accvgpr_write_b32 a56, v32\n"
    "v_accvgpr_write_b32 a57, v33\n"
    "v_accvgpr_write_b32 a58, v34\n"
    "v_accvgpr_write_b32 a59, v35\n"
    "v_accvgpr_write_b32 a60, v36\n"
    "v_accvgpr_write_b32 a61, v37\n"
    "v_accvgpr_write_b32 a62, v38\n"
    "v_accvgpr_write_b32 a63, v39\n"
    "v_accvgpr_write_b32 a204, v40\n"
    "v_accvgpr_write_b32 a205, v41\n"
    "v_accvgpr_write_b32 a206, v42\n"
    "v_accvgpr_write_b32 a207, v43\n"
    "s_branch .Lat_back12_%=\n"
    ".Lat_rare13_%=:\n"
    "v_mfma_f32_32x32x16_bf16 v[48:63], a[240:243], a[160:163], v[192:207]\n"
    "v_mfma_f32_32x32x16_bf16 v[48:63], a[244:247], a[164:167], v[48:63]\n"
    "v_accvgpr_read_b32 v24, a64\n"
    "v_accvgpr_read_b32 v25, a65\n"
    "v_accvgpr_read_b32 v26, a66\n"
    "v_accvgpr_read_b32 v27, a67\n"
    "v_accvgpr_read_b32 v28, a68\n"
    "v_accvgpr_read_b32 v29, a69\n"
    "v_accvgpr_read_b32 v30, a70\n"
    "v_accvgpr_read_b32 v31, a71\n"
    "v_accvgpr_read_b32 v32, a72\n"
    "v_accvgpr_read_b32 v33, a73\n"
    "v_accvgpr_read_b32 v34, a74\n"
    "v_accvgpr_read_b32 v35, a75\n"
    "v_accvgpr_read_b32 v36, a76\n"
    "v_accvgpr_read_b32 v37, a77\n"
    "v_accvgpr_read_b32 v38, a78\n"
    "v_accvgpr_read_b32 v39, a79\n"
    "v_accvgpr_read_b32 v40, a208\n"
    "v_accvgpr_read_b32 v41, a209\n"
    "v_accvgpr_read_b32 v42, a210\n"
    "v_accvgpr_read_b32 v43, a211\n"
    "v_max3_f32 v20, v48, v49, v50\n"
    "v_max3_f32 v20, v20, v51, v52\n"
    "v_max3_f32 v20, v20, v53, v54\n"
    "v_max3_f32 v20, v20, v55, v56\n"
    "v_max3_f32 v20, v20, v57, v58\n"
    "v_max3_f32 v20, v20, v59, v60\n"
    "v_max3_f32 v20, v20, v61, v62\n"
    "v_max_f32 v20, v20, v63\n"
    "v_mov_b32 v21, v20\n"
    "s_nop 1\n"
    "v_permlane32_swap_b32 v20, v21\n"
    "v_max_f32 v20, v20, v21\n"
    "v_add_f32 v20, 1109393408, v20\n"
    "v_max_f32 v20, 0, v20\n"
    "v_exp_f32 v22, -v20\n"
    "v_pk_add_f32 v[48:49], v[48:49], v[20:21] op_sel_hi:[1,0] neg_lo:[0,1] neg_hi:[0,1]\n"
    "v_pk_add_f32 v[50:51], v[50:51], v[20:21] op_sel_hi:[1,0] neg_lo:[0,1] neg_hi:[0,1]\n"
    "v_pk_add_f32 v[52:53], v[52:53], v[20:21] op_sel_hi:[1,0] neg_lo:[0,1] neg_hi:[0,1]\n"
    "v_pk_add_f32 v[54:55], v[54:55], v[20:21] op_sel_hi:[1,0] neg_lo:[0,1] neg_hi:[0,1]\n"
    "v_pk_add_f32 v[56:57], v[56:57], v[20:21] op_sel_hi:[1,0] neg_lo:[0,1] neg_hi:[0,1]\n"
    "v_pk_add_f32 v[58:59], v[58:59], v[20:21] op_sel_hi:[1,0] neg_lo:[0,1] neg_hi:[0,1]\n"
    "v_pk_add_f32 v[60:61], v[60:61], v[20:21] op_sel_hi:[1,0] neg_lo:[0,1] neg_hi:[0,1]\n"
    "v_pk_add_f32 v[62:63], v[62:63], v[20:21] op_sel_hi:[1,0] neg_lo:[0,1] neg_hi:[0,1]\n"
    "v_pk_add_f32 v[192:193], v[192:193], v[20:21] op_sel_hi:[1,0] neg_lo:[0,1] neg_hi:[0,1]\n"
    "v_pk_add_f32 v[194:195], v[194:195], v[20:21] op_sel_hi:[1,0] neg_lo:[0,1] neg_hi:[0,1]\n"
    "v_pk_add_f32 v[196:197], v[196:197], v[20:21] op_sel_hi:[1,0] neg_lo:[0,1] neg_hi:[0,1]\n"
    "v_pk_add_f32 v[198:199], v[198:199], v[20:21] op_sel_hi:[1,0] neg_lo:[0,1] neg_hi:[0,1]\n"
    "v_pk_add_f32 v[200:201], v[200:201], v[20:21] op_sel_hi:[1,0] neg_lo:[0,1] neg_hi:[0,1]\n"
    "v_pk_add_f32 v[202:203], v[202:203], v[20:21] op_sel_hi:[1,0] neg_lo:[0,1] neg_hi:[0,1]\n"
    "v_pk_add_f32 v[204:205], v[204:205], v[20:21] op_sel_hi:[1,0] neg_lo:[0,1] neg_hi:[0,1]\n"
    "v_pk_add_f32 v[206:207], v[206:207], v[20:21] op_sel_hi:[1,0] neg_lo:[0,1] neg_hi:[0,1]\n"
    "v_exp_f32 v48, v48\n"
    "v_exp_f32 v49, v49\n"
    "v_exp_f32 v50, v50\n"
    "v_exp_f32 v51, v51\n"
    "v_exp_f32 v52, v52\n"
    "v_exp_f32 v53, v53\n"
    "v_exp_f32 v54, v54\n"
    "v_exp_f32 v55, v55\n"
    "v_exp_f32 v56, v56\n"
    "v_exp_f32 v57, v57\n"
    "v_exp_f32 v58, v58\n"
    "v_exp_f32 v59, v59\n"
    "v_exp_f32 v60, v60\n"
    "v_exp_f32 v61, v61\n"
    "v_exp_f32 v62, v62\n"
    "v_exp_f32 v63, v63\n"
    "v_pk_mul_f32 v[24:25], v[24:25], v[22:23] op_sel_hi:[1,0]\n"
    "v_pk_mul_f32 v[26:27], v[26:27], v[22:23] op_sel_hi:[1,0]\n"
    "v_pk_mul_f32 v[28:29], v[28:29], v[22:23] op_sel_hi:[1,0]\n"
    "v_pk_mul_f32 v[30:31], v[30:31], v[22:23] op_sel_hi:[1,0]\n"
    "v_pk_mul_f32 v[32:33], v[32:33], v[22:23] op_sel_hi:[1,0]\n"
    "v_pk_mul_f32 v[34:35], v[34:35], v[22:23] op_sel_hi:[1,0]\n"
    "v_pk_mul_f32 v[36:37], v[36:37], v[22:23] op_sel_hi:[1,0]\n"
    "v_pk_mul_f32 v[38:39], v[38:39], v[22:23] op_sel_hi:[1,0]\n"
    "v_pk_mul_f32 v[40:41], v[40:41], v[22:23] op_sel_hi:[1,0]\n"
    "v_pk_mul_f32 v[42:43], v[42:43], v[22:23] op_sel_hi:[1,0]\n"
    "v_cvt_pk_bf16_f32 v80, v48, v49\n"
    "v_cvt_pk_bf16_f32 v81, v50, v51\n"
    "v_cvt_pk_bf16_f32 v82, v52, v53\n"
    "v_cvt_pk_bf16_f32 v83, v54, v55\n"
    "v_cvt_pk_bf16_f32 v84, v56, v57\n"
    "v_cvt_pk_bf16_f32 v85, v58, v59\n"
    "v_cvt_pk_bf16_f32 v86, v60, v61\n"
    "v_cvt_pk_bf16_f32 v87, v62, v63\n"
    "v_accvgpr_write_b32 a64, v24\n"
    "v_accvgpr_write_b32 a65, v25\n"
    "v_accvgpr_write_b32 a66, v26\n"
    "v_accvgpr_write_b32 a67, v27\n"
    "v_accvgpr_write_b32 a68, v28\n"
    "v_accvgpr_write_b32 a69, v29\n"
    "v_accvgpr_write_b32 a70, v30\n"
    "v_accvgpr_write_b32 a71, v31\n"
    "v_accvgpr_write_b32 a72, v32\n"
    "v_accvgpr_write_b32 a73, v33\n"
    "v_accvgpr_write_b32 a74, v34\n"
    "v_accvgpr_write_b32 a75, v35\n"
    "v_accvgpr_write_b32 a76, v36\n"
    "v_accvgpr_write_b32 a77, v37\n"
    "v_accvgpr_write_b32 a78, v38\n"
    "v_accvgpr_write_b32 a79, v39\n"
    "v_accvgpr_write_b32 a208, v40\n"
    "v_accvgpr_write_b32 a209, v41\n"
    "v_accvgpr_write_b32 a210, v42\n"
    "v_accvgpr_write_b32 a211, v43\n"
    "s_branch .Lat_back13_%=\n"
    ".Lat_rare14_%=:\n"
    "v_mfma_f32_32x32x16_bf16 v[48:63], a[240:243], a[168:171], v[208:223]\n"
    "v_mfma_f32_32x32x16_bf16 v[48:63], a[244:247], a[172:175], v[48:63]\n"
    "v_accvgpr_read_b32 v24, a80\n"
    "v_accvgpr_read_b32 v25, a81\n"
    "v_accvgpr_read_b32 v26, a82\n"
    "v_accvgpr_read_b32 v27, a83\n"
    "v_accvgpr_read_b32 v28, a84\n"
    "v_accvgpr_read_b32 v29, a85\n"
    "v_accvgpr_read_b32 v30, a86\n"
    "v_accvgpr_read_b32 v31, a87\n"
    "v_accvgpr_read_b32 v32, a88\n"
    "v_accvgpr_read_b32 v33, a89\n"
    "v_accvgpr_read_b32 v34, a90\n"
    "v_accvgpr_read_b32 v35, a91\n"
    "v_accvgpr_read_b32 v36, a92\n"
    "v_accvgpr_read_b32 v37, a93\n"
    "v_accvgpr_read_b32 v38, a94\n"
    "v_accvgpr_read_b32 v39, a95\n"
    "v_accvgpr_read_b32 v40, a212\n"
    "v_accvgpr_read_b32 v41, a213\n"
    "v_accvgpr_read_b32 v42, a214\n"
    "v_accvgpr_read_b32 v43, a215\n"
    "v_max3_f32 v20, v48, v49, v50\n"
    "v_max3_f32 v20, v20, v51, v52\n"
    "v_max3_f32 v20, v20, v53, v54\n"
    "v_max3_f32 v20, v20, v55, v56\n"
    "v_max3_f32 v20, v20, v57, v58\n"
    "v_max3_f32 v20, v20, v59, v60\n"
    "v_max3_f32 v20, v20, v61, v62\n"
    "v_max_f32 v20, v20, v63\n"
    "v_mov_b32 v21, v20\n"
    "s_nop 1\n"
    "v_permlane32_swap_b32 v20, v21\n"
    "v_max_f32 v20, v20, v21\n"
    "v_add_f32 v20, 1109393408, v20\n"
    "v_max_f32 v20, 0, v20\n"
    "v_exp_f32 v22, -v20\n"
    "v_pk_add_f32 v[48:49], v[48:49], v[20:21] op_sel_hi:[1,0] neg_lo:[0,1] neg_hi:[0,1]\n"
    "v_pk_add_f32 v[50:51], v[50:51], v[20:21] op_sel_hi:[1,0] neg_lo:[0,1] neg_hi:[0,1]\n"
    "v_pk_add_f32 v[52:53], v[52:53], v[20:21] op_sel_hi:[1,0] neg_lo:[0,1] neg_hi:[0,1]\n"
    "v_pk_add_f32 v[54:55], v[54:55], v[20:21] op_sel_hi:[1,0] neg_lo:[0,1] neg_hi:[0,1]\n"
    "v_pk_add_f32 v[56:57], v[56:57], v[20:21] op_sel_hi:[1,0] neg_lo:[0,1] neg_hi:[0,1]\n"
    "v_pk_add_f32 v[58:59], v[58:59], v[20:21] op_sel_hi:[1,0] neg_lo:[0,1] neg_hi:[0,1]\n"
    "v_pk_add_f32 v[60:61], v[60:61], v[20:21] op_sel_hi:[1,0] neg_lo:[0,1] neg_hi:[0,1]\n"
    "v_pk_add_f32 v[62:63], v[62:63], v[20:21] op_sel_hi:[1,0] neg_lo:[0,1] neg_hi:[0,1]\n"
    "v_pk_add_f32 v[208:209], v[208:209], v[20:21] op_sel_hi:[1,0] neg_lo:[0,1] neg_hi:[0,1]\n"
    "v_pk_add_f32 v[210:211], v[210:211], v[20:21] op_sel_hi:[1,0] neg_lo:[0,1] neg_hi:[0,1]\n"
    "v_pk_add_f32 v[212:213], v[212:213], v[20:21] op_sel_hi:[1,0] neg_lo:[0,1] neg_hi:[0,1]\n"
    "v_pk_add_f32 v[214:215], v[214:215], v[20:21] op_sel_hi:[1,0] neg_lo:[0,1] neg_hi:[0,1]\n"
    "v_pk_add_f32 v[216:217], v[216:217], v[20:21] op_sel_hi:[1,0] neg_lo:[0,1] neg_hi:[0,1]\n"
    "v_pk_add_f32 v[218:219], v[218:219], v[20:21] op_sel_hi:[1,0] neg_lo:[0,1] neg_hi:[0,1]\n"
    "v_pk_add_f32 v[220:221], v[220:221], v[20:21] op_sel_hi:[1,0] neg_lo:[0,1] neg_hi:[0,1]\n"
    "v_pk_add_f32 v[222:223], v[222:223], v[20:21] op_sel_hi:[1,0] neg_lo:[0,1] neg_hi:[0,1]\n"
    "v_exp_f32 v48, v48\n"
    "v_exp_f32 v49, v49\n"
    "v_exp_f32 v50, v50\n"
    "v_exp_f32 v51, v51\n"
    "v_exp_f32 v52, v52\n"
    "v_exp_f32 v53, v53\n"
    "v_exp_f32 v54, v54\n"
    "v_exp_f32 v55, v55\n"
    "v_exp_f32 v56, v56\n"
    "v_exp_f32 v57, v57\n"
    "v_exp_f32 v58, v58\n"
    "v_exp_f32 v59, v59\n"
    "v_exp_f32 v60, v60\n"
    "v_exp_f32 v61, v61\n"
    "v_exp_f32 v62, v62\n"
    "v_exp_f32 v63, v63\n"
    "v_pk_mul_f32 v[24:25], v[24:25], v[22:23] op_sel_hi:[1,0]\n"
    "v_pk_mul_f32 v[26:27], v[26:27], v[22:23] op_sel_hi:[1,0]\n"
    "v_pk_mul_f32 v[28:29], v[28:29], v[22:23] op_sel_hi:[1,0]\n"
    "v_pk_mul_f32 v[30:31], v[30:31], v[22:23] op_sel_hi:[1,0]\n"
    "v_pk_mul_f32 v[32:33], v[32:33], v[22:23] op_sel_hi:[1,0]\n"
    "v_pk_mul_f32 v[34:35], v[34:35], v[22:23] op_sel_hi:[1,0]\n"
    "v_pk_mul_f32 v[36:37], v[36:37], v[22:23] op_sel_hi:[1,0]\n"
    "v_pk_mul_f32 v[38:39], v[38:39], v[22:23] op_sel_hi:[1,0]\n"
    "v_pk_mul_f32 v[40:41], v[40:41], v[22:23] op_sel_hi:[1,0]\n"
    "v_pk_mul_f32 v[42:43], v[42:43], v[22:23] op_sel_hi:[1,0]\n"
    "v_cvt_pk_bf16_f32 v88, v48, v49\n"
    "v_cvt_pk_bf16_f32 v89, v50, v51\n"
    "v_cvt_pk_bf16_f32 v90, v52, v53\n"
    "v_cvt_pk_bf16_f32 v91, v54, v55\n"
    "v_cvt_pk_bf16_f32 v92, v56, v57\n"
    "v_cvt_pk_bf16_f32 v93, v58, v59\n"
    "v_cvt_pk_bf16_f32 v94, v60, v61\n"
    "v_cvt_pk_bf16_f32 v95, v62, v63\n"
    "v_accvgpr_write_b32 a80, v24\n"
    "v_accvgpr_write_b32 a81, v25\n"
    "v_accvgpr_write_b32 a82, v26\n"
    "v_accvgpr_write_b32 a83, v27\n"
    "v_accvgpr_write_b32 a84, v28\n"
    "v_accvgpr_write_b32 a85, v29\n"
    "v_accvgpr_write_b32 a86, v30\n"
    "v_accvgpr_write_b32 a87, v31\n"
    "v_accvgpr_write_b32 a88, v32\n"
    "v_accvgpr_write_b32 a89, v33\n"
    "v_accvgpr_write_b32 a90, v34\n"
    "v_accvgpr_write_b32 a91, v35\n"
    "v_accvgpr_write_b32 a92, v36\n"
    "v_accvgpr_write_b32 a93, v37\n"
    "v_accvgpr_write_b32 a94, v38\n"
    "v_accvgpr_write_b32 a95, v39\n"
    "v_accvgpr_write_b32 a212, v40\n"
    "v_accvgpr_write_b32 a213, v41\n"
    "v_accvgpr_write_b32 a214, v42\n"
    "v_accvgpr_write_b32 a215, v43\n"
    "s_branch .Lat_back14_%=\n"
    ".Lat_rare15_%=:\n"
    "v_mfma_f32_32x32x16_bf16 v[48:63], a[240:243], a[176:179], v[224:239]\n"
    "v_mfma_f32_32x32x16_bf16 v[48:63], a[244:247], a[180:183], v[48:63]\n"
    "v_accvgpr_read_b32 v24, a96\n"
    "v_accvgpr_read_b32 v25, a97\n"
    "v_accvgpr_read_b32 v26, a98\n"
    "v_accvgpr_read_b32 v27, a99\n"
    "v_accvgpr_read_b32 v28, a100\n"
    "v_accvgpr_read_b32 v29, a101\n"
    "v_accvgpr_read_b32 v30, a102\n"
    "v_accvgpr_read_b32 v31, a103\n"
    "v_accvgpr_read_b32 v32, a104\n"
    "v_accvgpr_read_b32 v33, a105\n"
    "v_accvgpr_read_b32 v34, a106\n"
    "v_accvgpr_read_b32 v35, a107\n"
    "v_accvgpr_read_b32 v36, a108\n"
    "v_accvgpr_read_b32 v37, a109\n"
    "v_accvgpr_read_b32 v38, a110\n"
    "v_accvgpr_read_b32 v39, a111\n"
    "v_accvgpr_read_b32 v40, a216\n"
    "v_accvgpr_read_b32 v41, a217\n"
    "v_accvgpr_read_b32 v42, a218\n"
    "v_accvgpr_read_b32 v43, a219\n"
    "v_max3_f32 v20, v48, v49, v50\n"
    "v_max3_f32 v20, v20, v51, v52\n"
    "v_max3_f32 v20, v20, v53, v54\n"
    "v_max3_f32 v20, v20, v55, v56\n"
    "v_max3_f32 v20, v20, v57, v58\n"
    "v_max3_f32 v20, v20, v59, v60\n"
    "v_max3_f32 v20, v20, v61, v62\n"
    "v_max_f32 v20, v20, v63\n"
    "v_mov_b32 v21, v20\n"
    "s_nop 1\n"
    "v_permlane32_swap_b32 v20, v21\n"
    "v_max_f32 v20, v20, v21\n"
    "v_add_f32 v20, 1109393408, v20\n"
    "v_max_f32 v20, 0, v20\n"
    "v_exp_f32 v22, -v20\n"
    "v_pk_add_f32 v[48:49], v[48:49], v[20:21] op_sel_hi:[1,0] neg_lo:[0,1] neg_hi:[0,1]\n"
    "v_pk_add_f32 v[50:51], v[50:51], v[20:21] op_sel_hi:[1,0] neg_lo:[0,1] neg_hi:[0,1]\n"
    "v_pk_add_f32 v[52:53], v[52:53], v[20:21] op_sel_hi:[1,0] neg_lo:[0,1] neg_hi:[0,1]\n"
    "v_pk_add_f32 v[54:55], v[54:55], v[20:21] op_sel_hi:[1,0] neg_lo:[0,1] neg_hi:[0,1]\n"
    "v_pk_add_f32 v[56:57], v[56:57], v[20:21] op_sel_hi:[1,0] neg_lo:[0,1] neg_hi:[0,1]\n"
    "v_pk_add_f32 v[58:59], v[58:59], v[20:21] op_sel_hi:[1,0] neg_lo:[0,1] neg_hi:[0,1]\n"
    "v_pk_add_f32 v[60:61], v[60:61], v[20:21] op_sel_hi:[1,0] neg_lo:[0,1] neg_hi:[0,1]\n"
    "v_pk_add_f32 v[62:63], v[62:63], v[20:21] op_sel_hi:[1,0] neg_lo:[0,1] neg_hi:[0,1]\n"
    "v_pk_add_f32 v[224:225], v[224:225], v[20:21] op_sel_hi:[1,0] neg_lo:[0,1] neg_hi:[0,1]\n"
    "v_pk_add_f32 v[226:227], v[226:227], v[20:21] op_sel_hi:[1,0] neg_lo:[0,1] neg_hi:[0,1]\n"
    "v_pk_add_f32 v[228:229], v[228:229], v[20:21] op_sel_hi:[1,0] neg_lo:[0,1] neg_hi:[0,1]\n"
    "v_pk_add_f32 v[230:231], v[230:231], v[20:21] op_sel_hi:[1,0] neg_lo:[0,1] neg_hi:[0,1]\n"
    "v_pk_add_f32 v[232:233], v[232:233], v[20:21] op_sel_hi:[1,0] neg_lo:[0,1] neg_hi:[0,1]\n"
    "v_pk_add_f32 v[234:235], v[234:235], v[20:21] op_sel_hi:[1,0] neg_lo:[0,1] neg_hi:[0,1]\n"
    "v_pk_add_f32 v[236:237], v[236:237], v[20:21] op_sel_hi:[1,0] neg_lo:[0,1] neg_hi:[0,1]\n"
    "v_pk_add_f32 v[238:239], v[238:239], v[20:21] op_sel_hi:[1,0] neg_lo:[0,1] neg_hi:[0,1]\n"
    "v_exp_f32 v48, v48\n"
    "v_exp_f32 v49, v49\n"
    "v_exp_f32 v50, v50\n"
    "v_exp_f32 v51, v51\n"
    "v_exp_f32 v52, v52\n"
    "v_exp_f32 v53, v53\n"
    "v_exp_f32 v54, v54\n"
    "v_exp_f32 v55, v55\n"
    "v_exp_f32 v56, v56\n"
    "v_exp_f32 v57, v57\n"
    "v_exp_f32 v58, v58\n"
    "v_exp_f32 v59, v59\n"
    "v_exp_f32 v60, v60\n"
    "v_exp_f32 v61, v61\n"
    "v_exp_f32 v62, v62\n"
    "v_exp_f32 v63, v63\n"
    "v_pk_mul_f32 v[24:25], v[24:25], v[22:23] op_sel_hi:[1,0]\n"
    "v_pk_mul_f32 v[26:27], v[26:27], v[22:23] op_sel_hi:[1,0]\n"
    "v_pk_mul_f32 v[28:29], v[28:29], v[22:23] op_sel_hi:[1,0]\n"
    "v_pk_mul_f32 v[30:31], v[30:31], v[22:23] op_sel_hi:[1,0]\n"
    "v_pk_mul_f32 v[32:33], v[32:33], v[22:23] op_sel_hi:[1,0]\n"
    "v_pk_mul_f32 v[34:35], v[34:35], v[22:23] op_sel_hi:[1,0]\n"
    "v_pk_mul_f32 v[36:37], v[36:37], v[22:23] op_sel_hi:[1,0]\n"
    "v_pk_mul_f32 v[38:39], v[38:39], v[22:23] op_sel_hi:[1,0]\n"
    "v_pk_mul_f32 v[40:41], v[40:41], v[22:23] op_sel_hi:[1,0]\n"
    "v_pk_mul_f32 v[42:43], v[42:43], v[22:23] op_sel_hi:[1,0]\n"
    "v_cvt_pk_bf16_f32 v80, v48, v49\n"
    "v_cvt_pk_bf16_f32 v81, v50, v51\n"
    "v_cvt_pk_bf16_f32 v82, v52, v53\n"
    "v_cvt_pk_bf16_f32 v83, v54, v55\n"
    "v_cvt_pk_bf16_f32 v84, v56, v57\n"
    "v_cvt_pk_bf16_f32 v85, v58, v59\n"
    "v_cvt_pk_bf16_f32 v86, v60, v61\n"
    "v_cvt_pk_bf16_f32 v87, v62, v63\n"
    "v_accvgpr_write_b32 a96, v24\n"
    "v_accvgpr_write_b32 a97, v25\n"
    "v_accvgpr_write_b32 a98, v26\n"
    "v_accvgpr_write_b32 a99, v27\n"
    "v_accvgpr_write_b32 a100, v28\n"
    "v_accvgpr_write_b32 a101, v29\n"
    "v_accvgpr_write_b32 a102, v30\n"
    "v_accvgpr_write_b32 a103, v31\n"
    "v_accvgpr_write_b32 a104, v32\n"
    "v_accvgpr_write_b32 a105, v33\n"
    "v_accvgpr_write_b32 a106, v34\n"
    "v_accvgpr_write_b32 a107, v35\n"
    "v_accvgpr_write_b32 a108, v36\n"
    "v_accvgpr_write_b32 a109, v37\n"
    "v_accvgpr_write_b32 a110, v38\n"
    "v_accvgpr_write_b32 a111, v39\n"
    "v_accvgpr_write_b32 a216, v40\n"
    "v_accvgpr_write_b32 a217, v41\n"
    "v_accvgpr_write_b32 a218, v42\n"
    "v_accvgpr_write_b32 a219, v43\n"
    "s_branch .Lat_back15_%=\n"
    ".Lat_rare16_%=:\n"
    "v_mfma_f32_32x32x16_bf16 v[48:63], a[240:243], a[184:187], v[240:255]\n"
    "v_mfma_f32_32x32x16_bf16 v[48:63], a[244:247], a[188:191], v[48:63]\n"
    "v_accvgpr_read_b32 v24, a112\n"
    "v_accvgpr_read_b32 v25, a113\n"
    "v_accvgpr_read_b32 v26, a114\n"
    "v_accvgpr_read_b32 v27, a115\n"
    "v_accvgpr_read_b32 v28, a116\n"
    "v_accvgpr_read_b32 v29, a117\n"
    "v_accvgpr_read_b32 v30, a118\n"
    "v_accvgpr_read_b32 v31, a119\n"
    "v_accvgpr_read_b32 v32, a120\n"
    "v_accvgpr_read_b32 v33, a121\n"
    "v_accvgpr_read_b32 v34, a122\n"
    "v_accvgpr_read_b32 v35, a123\n"
    "v_accvgpr_read_b32 v36, a124\n"
    "v_accvgpr_read_b32 v37, a125\n"
    "v_accvgpr_read_b32 v38, a126\n"
    "v_accvgpr_read_b32 v39, a127\n"
    "v_accvgpr_read_b32 v40, a220\n"
    "v_accvgpr_read_b32 v41, a221\n"
    "v_accvgpr_read_b32 v42, a222\n"
    "v_accvgpr_read_b32 v43, a223\n"
    "v_max3_f32 v20, v48, v49, v50\n"
    "v_max3_f32 v20, v20, v51, v52\n"
    "v_max3_f32 v20, v20, v53, v54\n"
    "v_max3_f32 v20, v20, v55, v56\n"
    "v_max3_f32 v20, v20, v57, v58\n"
    "v_max3_f32 v20, v20, v59, v60\n"
    "v_max3_f32 v20, v20, v61, v62\n"
    "v_max_f32 v20, v20, v63\n"
    "v_mov_b32 v21, v20\n"
    "s_nop 1\n"
    "v_permlane32_swap_b32 v20, v21\n"
    "v_max_f32 v20, v20, v21\n"
    "v_add_f32 v20, 1109393408, v20\n"
    "v_max_f32 v20, 0, v20\n"
    "v_exp_f32 v22, -v20\n"
    "v_pk_add_f32 v[48:49], v[48:49], v[20:21] op_sel_hi:[1,0] neg_lo:[0,1] neg_hi:[0,1]\n"
    "v_pk_add_f32 v[50:51], v[50:51], v[20:21] op_sel_hi:[1,0] neg_lo:[0,1] neg_hi:[0,1]\n"
    "v_pk_add_f32 v[52:53], v[52:53], v[20:21] op_sel_hi:[1,0] neg_lo:[0,1] neg_hi:[0,1]\n"
    "v_pk_add_f32 v[54:55], v[54:55], v[20:21] op_sel_hi:[1,0] neg_lo:[0,1] neg_hi:[0,1]\n"
    "v_pk_add_f32 v[56:57], v[56:57], v[20:21] op_sel_hi:[1,0] neg_lo:[0,1] neg_hi:[0,1]\n"
    "v_pk_add_f32 v[58:59], v[58:59], v[20:21] op_sel_hi:[1,0] neg_lo:[0,1] neg_hi:[0,1]\n"
    "v_pk_add_f32 v[60:61], v[60:61], v[20:21] op_sel_hi:[1,0] neg_lo:[0,1] neg_hi:[0,1]\n"
    "v_pk_add_f32 v[62:63], v[62:63], v[20:21] op_sel_hi:[1,0] neg_lo:[0,1] neg_hi:[0,1]\n"
    "v_pk_add_f32 v[240:241], v[240:241], v[20:21] op_sel_hi:[1,0] neg_lo:[0,1] neg_hi:[0,1]\n"
    "v_pk_add_f32 v[242:243], v[242:243], v[20:21] op_sel_hi:[1,0] neg_lo:[0,1] neg_hi:[0,1]\n"
    "v_pk_add_f32 v[244:245], v[244:245], v[20:21] op_sel_hi:[1,0] neg_lo:[0,1] neg_hi:[0,1]\n"
    "v_pk_add_f32 v[246:247], v[246:247], v[20:21] op_sel_hi:[1,0] neg_lo:[0,1] neg_hi:[0,1]\n"
    "v_pk_add_f32 v[248:249], v[248:249], v[20:21] op_sel_hi:[1,0] neg_lo:[0,1] neg_hi:[0,1]\n"
    "v_pk_add_f32 v[250:251], v[250:251], v[20:21] op_sel_hi:[1,0] neg_lo:[0,1] neg_hi:[0,1]\n"
    "v_pk_add_f32 v[252:253], v[252:253], v[20:21] op_sel_hi:[1,0] neg_lo:[0,1] neg_hi:[0,1]\n"
    "v_pk_add_f32 v[254:255], v[254:255], v[20:21] op_sel_hi:[1,0] neg_lo:[0,1] neg_hi:[0,1]\n"
    "v_exp_f32 v48, v48\n"
    "v_exp_f32 v49, v49\n"
    "v_exp_f32 v50, v50\n"
    "v_exp_f32 v51, v51\n"
    "v_exp_f32 v52, v52\n"
    "v_exp_f32 v53, v53\n"
    "v_exp_f32 v54, v54\n"
    "v_exp_f32 v55, v55\n"
    "v_exp_f32 v56, v56\n"
    "v_exp_f32 v57, v57\n"
    "v_exp_f32 v58, v58\n"
    "v_exp_f32 v59, v59\n"
    "v_exp_f32 v60, v60\n"
    "v_exp_f32 v61, v61\n"
    "v_exp_f32 v62, v62\n"
    "v_exp_f32 v63, v63\n"
    "v_pk_mul_f32 v[24:25], v[24:25], v[22:23] op_sel_hi:[1,0]\n"
    "v_pk_mul_f32 v[26:27], v[26:27], v[22:23] op_sel_hi:[1,0]\n"
    "v_pk_mul_f32 v[28:29], v[28:29], v[22:23] op_sel_hi:[1,0]\n"
    "v_pk_mul_f32 v[30:31], v[30:31], v[22:23] op_sel_hi:[1,0]\n"
    "v_pk_mul_f32 v[32:33], v[32:33], v[22:23] op_sel_hi:[1,0]\n"
    "v_pk_mul_f32 v[34:35], v[34:35], v[22:23] op_sel_hi:[1,0]\n"
    "v_pk_mul_f32 v[36:37], v[36:37], v[22:23] op_sel_hi:[1,0]\n"
    "v_pk_mul_f32 v[38:39], v[38:39], v[22:23] op_sel_hi:[1,0]\n"
    "v_pk_mul_f32 v[40:41], v[40:41], v[22:23] op_sel_hi:[1,0]\n"
    "v_pk_mul_f32 v[42:43], v[42:43], v[22:23] op_sel_hi:[1,0]\n"
    "v_cvt_pk_bf16_f32 v88, v48, v49\n"
    "v_cvt_pk_bf16_f32 v89, v50, v51\n"
    "v_cvt_pk_bf16_f32 v90, v52, v53\n"
    "v_cvt_pk_bf16_f32 v91, v54, v55\n"
    "v_cvt_pk_bf16_f32 v92, v56, v57\n"
    "v_cvt_pk_bf16_f32 v93, v58, v59\n"
    "v_cvt_pk_bf16_f32 v94, v60, v61\n"
    "v_cvt_pk_bf16_f32 v95, v62, v63\n"
    "v_accvgpr_write_b32 a112, v24\n"
    "v_accvgpr_write_b32 a113, v25\n"
    "v_accvgpr_write_b32 a114, v26\n"
    "v_accvgpr_write_b32 a115, v27\n"
    "v_accvgpr_write_b32 a116, v28\n"
    "v_accvgpr_write_b32 a117, v29\n"
    "v_accvgpr_write_b32 a118, v30\n"
    "v_accvgpr_write_b32 a119, v31\n"
    "v_accvgpr_write_b32 a120, v32\n"
    "v_accvgpr_write_b32 a121, v33\n"
    "v_accvgpr_write_b32 a122, v34\n"
    "v_accvgpr_write_b32 a123, v35\n"
    "v_accvgpr_write_b32 a124, v36\n"
    "v_accvgpr_write_b32 a125, v37\n"
    "v_accvgpr_write_b32 a126, v38\n"
    "v_accvgpr_write_b32 a127, v39\n"
    "v_accvgpr_write_b32 a220, v40\n"
    "v_accvgpr_write_b32 a221, v41\n"
    "v_accvgpr_write_b32 a222, v42\n"
    "v_accvgpr_write_b32 a223, v43\n"
    "s_branch .Lat_back16_%=\n"
    ".Lat_hsp_%=:\n"
    "s_sleep 2\n"
    "s_add_u32 s10, s19, 69632\n"
    "s_mov_b64 exec, 1\n"
    "buffer_load_dword v0, off, s[12:15], s10 sc1\n"
    "s_mov_b64 exec, -1\n"
    "s_waitcnt vmcnt(0)\n"
    "s_sub_u32 s26, s26, 1\n"
    "s_cbranch_scc0 .Lat_hsc_%=\n"
    "s_endpgm\n"
    ".Lat_hsr_%=:\n"
    "s_mov_b32 s17, s11\n"
    "s_waitcnt vmcnt(0) lgkmcnt(0)\n"
    "s_barrier\n"
    "s_nop 15\n"
    "s_branch .Lk_dispatch_%=\n"
    ".Lk_end_%=:\n"
    : : "s"(ka), "s"(blockIdx.x), "v"(threadIdx.x) : CLOBBERS);
}

extern "C" void kernel_launch(void* const* d_in, const int* in_sizes, int n_in, void* d_out, int out_size, void* d_ws, size_t ws_size, hipStream_t stream) {
  (void)hipFuncSetAttribute((const void*)fused, hipFuncAttributeMaxDynamicSharedMemorySize, 163840);
  fused<<<256, 256, 163840, stream>>>((cp16)d_in[0], (cp16)d_in[1], (cp16)d_in[2], (cp16)d_in[3], (cp16)d_in[4], (cp16)d_in[5], (cp16)d_in[6],
                                      (cp16)d_in[7], (cp16)d_in[8], (cp16)d_in[9], (cp16)d_in[10], (unsigned short*)d_out, (unsigned*)d_ws);
}
```
